```python
import torch

def _softmax(x, axis=-1):
    return torch.softmax(x, dim=axis)

def _gelu(x):
    return torch.nn.functional.gelu(x, approximate='tanh')

def _silu(x):
    return torch.nn.functional.silu(x)

def _sigmoid(x):
    return torch.sigmoid(x)

def _layernorm(x, axis=-1, eps=1e-05):
    m = x.mean(dim=axis, keepdim=True)
    v = x.var(dim=axis, keepdim=True, unbiased=False)
    return (x - m) / torch.sqrt(v + eps)

def _scatter_add(tgt, idx, src, axis=0):
    return tgt.clone().index_add_(axis, idx, src)

def _segment_sum(src, seg, n):
    out = torch.zeros((n,) + src.shape[1:], dtype=src.dtype)
    return out.index_add_(0, seg, src)

def _pad(x, pw):
    flat = tuple((v for pair in reversed(pw) for v in pair))
    return torch.nn.functional.pad(x, flat)
kernel_name = 'parallel_sum__memory_aug_recurrent_mem_tr__swiglu_ffn__prompt'

def setup_inputs():

    _w = lambda a: (lambda t: t.to(torch.bfloat16) if t.is_floating_point() else t)(torch.as_tensor(a))

    def _A_setup():
        B, S, Dm = (1, 2048, 512)
        H, Dh = (32, 128)
        M, seg = (8, 32)
        seg = min(seg, S)
        S = S // seg * seg
        return {'x': _w((lambda *s: torch.randn(*s, dtype=torch.bfloat16) * 0.05)(B, S, Dm)), 'mem0': _w((lambda *s: torch.randn(*s, dtype=torch.bfloat16) * 1.6)(B, M, Dm)), 'Wq': _w((lambda *s: torch.randn(*s, dtype=torch.bfloat16) * 0.8)(Dm, H, Dh) * Dm ** (-0.5)), 'Wk': _w((lambda *s: torch.randn(*s, dtype=torch.bfloat16) * 0.8)(Dm, H, Dh) * Dm ** (-0.5)), 'Wv': _w((lambda *s: torch.randn(*s, dtype=torch.bfloat16) * 1.6)(Dm, H, Dh) * Dm ** (-0.5)), 'Wo': _w((lambda *s: torch.randn(*s, dtype=torch.bfloat16) * 1.6)(H, Dh, Dm) * (H * Dh) ** (-0.5)), 'seg_len': seg}
    _A_in = _A_setup()

    def _B_setup():
        B, S, D, F = (1, 2048, 512, 1792)
        return {'x': _w((lambda *s: torch.randn(*s, dtype=torch.bfloat16) * 0.05)(B, S, D)), 'w_gate': _w((lambda *s: torch.randn(*s, dtype=torch.bfloat16) * 0.05)(D, F)), 'w_up': _w((lambda *s: torch.randn(*s, dtype=torch.bfloat16) * 0.05)(D, F)), 'w_down': _w((lambda *s: torch.randn(*s, dtype=torch.bfloat16) * 0.05)(F, D))}
    _B_in = _B_setup()
    return {'A_x': _A_in['x'], 'A_mem0': _A_in['mem0'], 'A_Wq': _A_in['Wq'], 'A_Wk': _A_in['Wk'], 'A_Wv': _A_in['Wv'], 'A_Wo': _A_in['Wo'], 'A_seg_len': _A_in['seg_len'], 'B_x': _B_in['x'], 'B_w_gate': _B_in['w_gate'], 'B_w_up': _B_in['w_up'], 'B_w_down': _B_in['w_down']}

def reference(A_x, A_mem0, A_Wq, A_Wk, A_Wv, A_Wo, A_seg_len, B_x, B_w_gate, B_w_up, B_w_down):

    def _A_ref(x, mem0, Wq, Wk, Wv, Wo, seg_len):
        B, S, Dm = x.shape
        H, Dh = (Wq.shape[1], Wq.shape[2])
        M = mem0.shape[1]
        scale = Dh ** (-0.5)
        n_seg = S // seg_len
        L = M + seg_len
        idx = torch.arange(L)
        mask = (idx[:, None] < M) | (idx[:, None] >= idx[None, :])
        mem = mem0
        outs = []
        for s in range(n_seg):
            xs = x[:, s * seg_len:(s + 1) * seg_len, :].clone()
            h = torch.concatenate([mem, xs], axis=1)
            q = torch.einsum('btd,dhe->bhte', h, Wq)
            k = torch.einsum('btd,dhe->bhte', h, Wk)
            v = torch.einsum('btd,dhe->bhte', h, Wv)
            scores = torch.einsum('bhqe,bhke->bhqk', q, k) * scale
            scores = torch.where(mask[None, None], scores, -1000000000.0)
            p = _softmax(scores, axis=-1)
            o = torch.einsum('bhqk,bhke->bhqe', p, v)
            o = torch.einsum('bhte,hed->btd', o, Wo)
            mem = o[:, :M, :].clone()
            outs.append(o[:, M:, :])
        return torch.concatenate(outs, axis=1)

    def _B_ref(x, w_gate, w_up, w_down):
        g = _silu(x @ w_gate)
        u = x @ w_up
        return g * u @ w_down
    x = A_x
    xn = _layernorm(x, axis=-1)
    return x + _A_ref(xn, A_mem0, A_Wq, A_Wk, A_Wv, A_Wo, A_seg_len) + _B_ref(xn, B_w_gate, B_w_up, B_w_down)
```

```cpp
#include <hip/hip_runtime.h>
#include <hip/hip_bf16.h>
#include <stdint.h>

typedef __bf16 bf16;
typedef bf16 v2bf __attribute__((ext_vector_type(2)));
typedef bf16 v8bf __attribute__((ext_vector_type(8)));
typedef bf16 v16bf __attribute__((ext_vector_type(16)));
typedef short v4s __attribute__((ext_vector_type(4)));
typedef float v2f __attribute__((ext_vector_type(2)));
typedef float v4f __attribute__((ext_vector_type(4)));
typedef unsigned v2u __attribute__((ext_vector_type(2)));
typedef unsigned v4u __attribute__((ext_vector_type(4)));
typedef unsigned v8u __attribute__((ext_vector_type(8)));
#define LAS __attribute__((address_space(3)))
#define DEVI __device__ __forceinline__

constexpr int SQ = 2048, DM = 512, NH = 32, DH = 128, NSEG = 64, FF = 1792;
constexpr unsigned SENT = 0xFFFFFFFFu;
constexpr size_t WS_COMM = 0;
constexpr size_t WS_CTRL = 2u << 20;
constexpr size_t WS_PB   = 88u << 20;
constexpr size_t WS_SYNC = 0x210000;
constexpr size_t WS_XN   = (2u << 20) + (1u << 20);
constexpr size_t WS_KV   = 8u << 20;
constexpr size_t WS_KM   = 40u << 20;
constexpr size_t WS_Q    = 100u << 20;
constexpr size_t WS_O    = 64u << 20;
constexpr size_t WS_HM   = 80u << 20;
constexpr size_t WS_DBG  = 96u << 20;
constexpr unsigned CT_NEAR = 0, CT_KVF = 1024, CT_KMF = 10240, CT_OFL = 18432, CT_HFL = 34816, CT_PF = 43008;
constexpr int L_MEM = 0, L_K = 8192, L_V = 16384, L_QK = 24576, L_VX = 28672, L_WO = 32768;
constexpr int L_O = L_MEM;
constexpr int LDS_BYTES = 163840;

struct Params {
    const bf16 *x, *mem0, *Wq, *Wk, *Wv, *Wo, *Wg, *Wu, *Wd;
    bf16* out; char* ws;
};

DEVI unsigned remap(unsigned off, unsigned nearbit) { return ((off >> 13) << 14) | (off & 8191u) | (nearbit << 13); }
DEVI unsigned pk2(float a, float b) { v2f f = {a, b}; return __builtin_bit_cast(unsigned, __builtin_convertvector(f, v2bf)); }
DEVI void bar() { asm volatile("s_waitcnt lgkmcnt(0)\n\ts_barrier" ::: "memory"); }
DEVI void glds16(const void* g, unsigned ldsdst) {
    unsigned keep;
    asm volatile("s_mov_b32 %0, m0\n\ts_mov_b32 m0, %2\n\ts_nop 0\n\tglobal_load_lds_dwordx4 %1, off\n\ts_mov_b32 m0, %0" : "=&s"(keep) : "v"(g), "s"(ldsdst) : "memory");
}
DEVI void glds16_sc1(const void* g, unsigned ldsdst) {
    unsigned keep;
    asm volatile("s_mov_b32 %0, m0\n\ts_mov_b32 m0, %2\n\ts_nop 0\n\tglobal_load_lds_dwordx4 %1, off sc1\n\ts_mov_b32 m0, %0" : "=&s"(keep) : "v"(g), "s"(ldsdst) : "memory");
}
DEVI v2u trd(unsigned a) { return __builtin_bit_cast(v2u, __builtin_amdgcn_ds_read_tr16_b64_v4i16((LAS v4s*)(size_t)a)); }
DEVI v4u lrd128(unsigned a) { return *(LAS v4u*)(size_t)a; }
DEVI v2u lrd64(unsigned a) { return *(LAS v2u*)(size_t)a; }
DEVI void lwr128(unsigned a, v4u v) { *(LAS v4u*)(size_t)a = v; }
DEVI void lwr64(unsigned a, v2u v) { *(LAS v2u*)(size_t)a = v; }
DEVI void lwr32(unsigned a, unsigned v) { *(LAS unsigned*)(size_t)a = v; }
DEVI v4f smf(v4u a, v8u b, v4f c, int idx) { return __builtin_amdgcn_smfmac_f32_16x16x64_bf16(__builtin_bit_cast(v8bf, a), __builtin_bit_cast(v16bf, b), c, idx, 0, 0); }
DEVI v4f mf32(v4u a, v4u b, v4f c) { return __builtin_amdgcn_mfma_f32_16x16x32_bf16(__builtin_bit_cast(v8bf, a), __builtin_bit_cast(v8bf, b), c, 0, 0, 0); }
DEVI v4f mf16(v2u a, v2u b, v4f c) { return __builtin_amdgcn_mfma_f32_16x16x16bf16_1k(__builtin_bit_cast(v4s, a), __builtin_bit_cast(v4s, b), c, 0, 0, 0); }
DEVI float fold32(float a, float b) {
    auto r = __builtin_amdgcn_permlane32_swap(__float_as_uint(a), __float_as_uint(b), false, false);
    return __uint_as_float(r[0]) + __uint_as_float(r[1]);
}
DEVI float xmax(float v) {
    auto r = __builtin_amdgcn_permlane32_swap(__float_as_uint(v), __float_as_uint(v), false, false);
    v = fmaxf(__uint_as_float(r[0]), __uint_as_float(r[1]));
    auto q = __builtin_amdgcn_permlane16_swap(__float_as_uint(v), __float_as_uint(v), false, false);
    return fmaxf(__uint_as_float(q[0]), __uint_as_float(q[1]));
}
DEVI float xsum(float v) {
    auto r = __builtin_amdgcn_permlane32_swap(__float_as_uint(v), __float_as_uint(v), false, false);
    v = __uint_as_float(r[0]) + __uint_as_float(r[1]);
    auto q = __builtin_amdgcn_permlane16_swap(__float_as_uint(v), __float_as_uint(v), false, false);
    return __uint_as_float(q[0]) + __uint_as_float(q[1]);
}
DEVI v8u rdfrag(unsigned base, unsigned rowbytes) {
    v2u a = trd(base), b = trd(base + 2 * rowbytes), c = trd(base + 32 * rowbytes), d = trd(base + 34 * rowbytes);
    return (v8u){a.x, a.y, b.x, b.y, c.x, c.y, d.x, d.y};
}
#define ST(i)
constexpr unsigned SPIN_MAX = 1u << 18;

#define CHAIN_UPC 1
#define H_LA 3
#define E_N 2
#define HMAP 0
#define EPW 0
#define PXOR 0
#define KM_LATE 0

#define CHAIN_RED \
    "RS_red:\n" \
    "s_getpc_b64 s[2:3]\n" \
    "v_lshlrev_b32 v0, 3, v253\n" \
    "s_mov_b32 s0, RE_red-RS_red-8\n" \
    "v_add_u32 v1, 0, v0\n" \
    "v_min_u32 v1, s0, v1\n" \
    "global_load_dword v2, v1, s[2:3]\n" \
    "v_add_u32 v1, 8192, v0\n" \
    "v_min_u32 v1, s0, v1\n" \
    "global_load_dword v3, v1, s[2:3]\n" \
    "s_mov_b32 s24, s22\n" \
    "s_mov_b32 s25, s23\n" \
    "s_memtime s[20:21]\n" \
    "s_memrealtime s[4:5]\n" \
    "s_nop 2\n" \
    "s_memrealtime s[6:7]\n" \
    "s_nop 2\n" \
    "s_memrealtime s[8:9]\n" \
    "s_nop 2\n" \
    "s_memrealtime s[10:11]\n" \
    "s_nop 2\n" \
    "s_memrealtime s[12:13]\n" \
    "s_nop 2\n" \
    "s_memrealtime s[14:15]\n" \
    "s_nop 2\n" \
    "s_memrealtime s[16:17]\n" \
    "s_nop 2\n" \
    "s_memrealtime s[18:19]\n" \
    "s_mov_b32 s29, 0\n" \
    "s_waitcnt lgkmcnt(0)\n" \
    "s_cmp_eq_u32 s6, s4\n" \
    "s_addc_u32 s29, s29, 0\n" \
    "s_cmp_eq_u32 s8, s4\n" \
    "s_addc_u32 s29, s29, 0\n" \
    "s_cmp_eq_u32 s10, s4\n" \
    "s_addc_u32 s29, s29, 0\n" \
    "s_cmp_eq_u32 s12, s4\n" \
    "s_addc_u32 s29, s29, 0\n" \
    "s_cmp_eq_u32 s14, s4\n" \
    "s_addc_u32 s29, s29, 0\n" \
    "s_cmp_eq_u32 s16, s4\n" \
    "s_addc_u32 s29, s29, 0\n" \
    "s_cmp_eq_u32 s18, s4\n" \
    "s_addc_u32 s29, s29, 0\n" \
    "s_lshl_b32 s29, s29, 4\n" \
    "s_add_u32 s23, s20, s29\n" \
    "s_lshl_b32 s22, s4, 2\n" \
    "s_lshl_b32 s28, s22, 6\n" \
    "v_mov_b32 v234, 0\n" \
    "s_mov_b32 s54, 2162688\n" \
    "buffer_load_dwordx2 v[232:233], v234, s[36:39], s54 offen sc1\n" \
    "s_mov_b32 s42, 4194304\n" \
    "s_mov_b32 s43, 0x20000\n" \
    "s_mov_b32 s40, s62\n" \
    "s_and_b32 s41, s63, 0xffff\n" \
    "s_add_u32 m0, s51, 32768\n" \
    "s_mov_b32 s54, 0\n" \
    "buffer_load_dwordx4 v216, s[40:43], s54 offen lds\n" \
    "s_add_u32 m0, s51, 36864\n" \
    "s_mov_b32 s54, 131072\n" \
    "buffer_load_dwordx4 v216, s[40:43], s54 offen lds\n" \
    "s_add_u32 m0, s51, 40960\n" \
    "s_mov_b32 s54, 262144\n" \
    "buffer_load_dwordx4 v216, s[40:43], s54 offen lds\n" \
    "s_add_u32 m0, s51, 45056\n" \
    "s_mov_b32 s54, 393216\n" \
    "buffer_load_dwordx4 v216, s[40:43], s54 offen lds\n" \
    "s_add_u32 m0, s51, 49152\n" \
    "s_mov_b32 s54, 524288\n" \
    "buffer_load_dwordx4 v216, s[40:43], s54 offen lds\n" \
    "s_add_u32 m0, s51, 53248\n" \
    "s_mov_b32 s54, 655360\n" \
    "buffer_load_dwordx4 v216, s[40:43], s54 offen lds\n" \
    "s_add_u32 m0, s51, 57344\n" \
    "s_mov_b32 s54, 786432\n" \
    "buffer_load_dwordx4 v216, s[40:43], s54 offen lds\n" \
    "s_add_u32 m0, s51, 61440\n" \
    "s_mov_b32 s54, 917504\n" \
    "buffer_load_dwordx4 v216, s[40:43], s54 offen lds\n" \
    "s_add_u32 m0, s51, 65536\n" \
    "s_mov_b32 s54, 1048576\n" \
    "buffer_load_dwordx4 v216, s[40:43], s54 offen lds\n" \
    "s_add_u32 m0, s51, 69632\n" \
    "s_mov_b32 s54, 1179648\n" \
    "buffer_load_dwordx4 v216, s[40:43], s54 offen lds\n" \
    "s_add_u32 m0, s51, 73728\n" \
    "s_mov_b32 s54, 1310720\n" \
    "buffer_load_dwordx4 v216, s[40:43], s54 offen lds\n" \
    "s_add_u32 m0, s51, 77824\n" \
    "s_mov_b32 s54, 1441792\n" \
    "buffer_load_dwordx4 v216, s[40:43], s54 offen lds\n" \
    "s_add_u32 m0, s51, 81920\n" \
    "s_mov_b32 s54, 1572864\n" \
    "buffer_load_dwordx4 v216, s[40:43], s54 offen lds\n" \
    "s_add_u32 m0, s51, 86016\n" \
    "s_mov_b32 s54, 1703936\n" \
    "buffer_load_dwordx4 v216, s[40:43], s54 offen lds\n" \
    "s_add_u32 m0, s51, 90112\n" \
    "s_mov_b32 s54, 1835008\n" \
    "buffer_load_dwordx4 v216, s[40:43], s54 offen lds\n" \
    "s_add_u32 m0, s51, 94208\n" \
    "s_mov_b32 s54, 1966080\n" \
    "buffer_load_dwordx4 v216, s[40:43], s54 offen lds\n" \
    "s_add_u32 m0, s51, 98304\n" \
    "s_mov_b32 s54, 2097152\n" \
    "buffer_load_dwordx4 v216, s[40:43], s54 offen lds\n" \
    "s_add_u32 m0, s51, 102400\n" \
    "s_mov_b32 s54, 2228224\n" \
    "buffer_load_dwordx4 v216, s[40:43], s54 offen lds\n" \
    "s_add_u32 m0, s51, 106496\n" \
    "s_mov_b32 s54, 2359296\n" \
    "buffer_load_dwordx4 v216, s[40:43], s54 offen lds\n" \
    "s_add_u32 m0, s51, 110592\n" \
    "s_mov_b32 s54, 2490368\n" \
    "buffer_load_dwordx4 v216, s[40:43], s54 offen lds\n" \
    "s_add_u32 m0, s51, 114688\n" \
    "s_mov_b32 s54, 2621440\n" \
    "buffer_load_dwordx4 v216, s[40:43], s54 offen lds\n" \
    "s_add_u32 m0, s51, 118784\n" \
    "s_mov_b32 s54, 2752512\n" \
    "buffer_load_dwordx4 v216, s[40:43], s54 offen lds\n" \
    "s_add_u32 m0, s51, 122880\n" \
    "s_mov_b32 s54, 2883584\n" \
    "buffer_load_dwordx4 v216, s[40:43], s54 offen lds\n" \
    "s_add_u32 m0, s51, 126976\n" \
    "s_mov_b32 s54, 3014656\n" \
    "buffer_load_dwordx4 v216, s[40:43], s54 offen lds\n" \
    "s_add_u32 m0, s51, 131072\n" \
    "s_mov_b32 s54, 3145728\n" \
    "buffer_load_dwordx4 v216, s[40:43], s54 offen lds\n" \
    "s_add_u32 m0, s51, 135168\n" \
    "s_mov_b32 s54, 3276800\n" \
    "buffer_load_dwordx4 v216, s[40:43], s54 offen lds\n" \
    "s_add_u32 m0, s51, 139264\n" \
    "s_mov_b32 s54, 3407872\n" \
    "buffer_load_dwordx4 v216, s[40:43], s54 offen lds\n" \
    "s_add_u32 m0, s51, 143360\n" \
    "s_mov_b32 s54, 3538944\n" \
    "buffer_load_dwordx4 v216, s[40:43], s54 offen lds\n" \
    "s_add_u32 m0, s51, 147456\n" \
    "s_mov_b32 s54, 3670016\n" \
    "buffer_load_dwordx4 v216, s[40:43], s54 offen lds\n" \
    "s_add_u32 m0, s51, 151552\n" \
    "s_mov_b32 s54, 3801088\n" \
    "buffer_load_dwordx4 v216, s[40:43], s54 offen lds\n" \
    "s_add_u32 m0, s51, 155648\n" \
    "s_mov_b32 s54, 3932160\n" \
    "buffer_load_dwordx4 v216, s[40:43], s54 offen lds\n" \
    "s_add_u32 m0, s51, 159744\n" \
    "s_mov_b32 s54, 4063232\n" \
    "buffer_load_dwordx4 v216, s[40:43], s54 offen lds\n" \
    "s_waitcnt vmcnt(16)\n" \
    "s_barrier\n" \
    "v_add_u32 v223, 32768, v217\n" \
    "ds_read_b64_tr_b16 a[128:129], v223\n" \
    "ds_read_b64_tr_b16 a[130:131], v223 offset:1024\n" \
    "ds_read_b64_tr_b16 a[132:133], v223 offset:8192\n" \
    "ds_read_b64_tr_b16 a[134:135], v223 offset:9216\n" \
    "ds_read_b64_tr_b16 a[136:137], v223 offset:16384\n" \
    "ds_read_b64_tr_b16 a[138:139], v223 offset:17408\n" \
    "ds_read_b64_tr_b16 a[140:141], v223 offset:24576\n" \
    "ds_read_b64_tr_b16 a[142:143], v223 offset:25600\n" \
    "s_waitcnt lgkmcnt(0)\n" \
    "ds_read_b64_tr_b16 a[144:145], v223 offset:32768\n" \
    "ds_read_b64_tr_b16 a[146:147], v223 offset:33792\n" \
    "ds_read_b64_tr_b16 a[148:149], v223 offset:40960\n" \
    "ds_read_b64_tr_b16 a[150:151], v223 offset:41984\n" \
    "ds_read_b64_tr_b16 a[152:153], v223 offset:49152\n" \
    "ds_read_b64_tr_b16 a[154:155], v223 offset:50176\n" \
    "ds_read_b64_tr_b16 a[156:157], v223 offset:57344\n" \
    "ds_read_b64_tr_b16 a[158:159], v223 offset:58368\n" \
    "s_waitcnt lgkmcnt(0)\n" \
    "v_add_u32 v223, 32768, v218\n" \
    "ds_read_b64_tr_b16 a[192:193], v223\n" \
    "ds_read_b64_tr_b16 a[194:195], v223 offset:1024\n" \
    "ds_read_b64_tr_b16 a[196:197], v223 offset:8192\n" \
    "ds_read_b64_tr_b16 a[198:199], v223 offset:9216\n" \
    "ds_read_b64_tr_b16 a[200:201], v223 offset:16384\n" \
    "ds_read_b64_tr_b16 a[202:203], v223 offset:17408\n" \
    "ds_read_b64_tr_b16 a[204:205], v223 offset:24576\n" \
    "ds_read_b64_tr_b16 a[206:207], v223 offset:25600\n" \
    "s_waitcnt lgkmcnt(0)\n" \
    "ds_read_b64_tr_b16 a[208:209], v223 offset:32768\n" \
    "ds_read_b64_tr_b16 a[210:211], v223 offset:33792\n" \
    "ds_read_b64_tr_b16 a[212:213], v223 offset:40960\n" \
    "ds_read_b64_tr_b16 a[214:215], v223 offset:41984\n" \
    "ds_read_b64_tr_b16 a[216:217], v223 offset:49152\n" \
    "ds_read_b64_tr_b16 a[218:219], v223 offset:50176\n" \
    "ds_read_b64_tr_b16 a[220:221], v223 offset:57344\n" \
    "ds_read_b64_tr_b16 a[222:223], v223 offset:58368\n" \
    "s_waitcnt lgkmcnt(0)\n" \
    "s_barrier\n" \
    "s_mov_b32 s40, s64\n" \
    "s_and_b32 s41, s65, 0xffff\n" \
    "s_add_u32 m0, s51, 32768\n" \
    "s_mov_b32 s54, 0\n" \
    "buffer_load_dwordx4 v216, s[40:43], s54 offen lds\n" \
    "s_add_u32 m0, s51, 36864\n" \
    "s_mov_b32 s54, 131072\n" \
    "buffer_load_dwordx4 v216, s[40:43], s54 offen lds\n" \
    "s_add_u32 m0, s51, 40960\n" \
    "s_mov_b32 s54, 262144\n" \
    "buffer_load_dwordx4 v216, s[40:43], s54 offen lds\n" \
    "s_add_u32 m0, s51, 45056\n" \
    "s_mov_b32 s54, 393216\n" \
    "buffer_load_dwordx4 v216, s[40:43], s54 offen lds\n" \
    "s_add_u32 m0, s51, 49152\n" \
    "s_mov_b32 s54, 524288\n" \
    "buffer_load_dwordx4 v216, s[40:43], s54 offen lds\n" \
    "s_add_u32 m0, s51, 53248\n" \
    "s_mov_b32 s54, 655360\n" \
    "buffer_load_dwordx4 v216, s[40:43], s54 offen lds\n" \
    "s_add_u32 m0, s51, 57344\n" \
    "s_mov_b32 s54, 786432\n" \
    "buffer_load_dwordx4 v216, s[40:43], s54 offen lds\n" \
    "s_add_u32 m0, s51, 61440\n" \
    "s_mov_b32 s54, 917504\n" \
    "buffer_load_dwordx4 v216, s[40:43], s54 offen lds\n" \
    "s_add_u32 m0, s51, 65536\n" \
    "s_mov_b32 s54, 1048576\n" \
    "buffer_load_dwordx4 v216, s[40:43], s54 offen lds\n" \
    "s_add_u32 m0, s51, 69632\n" \
    "s_mov_b32 s54, 1179648\n" \
    "buffer_load_dwordx4 v216, s[40:43], s54 offen lds\n" \
    "s_add_u32 m0, s51, 73728\n" \
    "s_mov_b32 s54, 1310720\n" \
    "buffer_load_dwordx4 v216, s[40:43], s54 offen lds\n" \
    "s_add_u32 m0, s51, 77824\n" \
    "s_mov_b32 s54, 1441792\n" \
    "buffer_load_dwordx4 v216, s[40:43], s54 offen lds\n" \
    "s_add_u32 m0, s51, 81920\n" \
    "s_mov_b32 s54, 1572864\n" \
    "buffer_load_dwordx4 v216, s[40:43], s54 offen lds\n" \
    "s_add_u32 m0, s51, 86016\n" \
    "s_mov_b32 s54, 1703936\n" \
    "buffer_load_dwordx4 v216, s[40:43], s54 offen lds\n" \
    "s_add_u32 m0, s51, 90112\n" \
    "s_mov_b32 s54, 1835008\n" \
    "buffer_load_dwordx4 v216, s[40:43], s54 offen lds\n" \
    "s_add_u32 m0, s51, 94208\n" \
    "s_mov_b32 s54, 1966080\n" \
    "buffer_load_dwordx4 v216, s[40:43], s54 offen lds\n" \
    "s_waitcnt vmcnt(16)\n" \
    "s_barrier\n" \
    "v_add_u32 v223, 98304, v217\n" \
    "ds_read_b64_tr_b16 a[160:161], v223\n" \
    "ds_read_b64_tr_b16 a[162:163], v223 offset:1024\n" \
    "ds_read_b64_tr_b16 a[164:165], v223 offset:8192\n" \
    "ds_read_b64_tr_b16 a[166:167], v223 offset:9216\n" \
    "ds_read_b64_tr_b16 a[168:169], v223 offset:16384\n" \
    "ds_read_b64_tr_b16 a[170:171], v223 offset:17408\n" \
    "ds_read_b64_tr_b16 a[172:173], v223 offset:24576\n" \
    "ds_read_b64_tr_b16 a[174:175], v223 offset:25600\n" \
    "s_waitcnt lgkmcnt(0)\n" \
    "ds_read_b64_tr_b16 a[176:177], v223 offset:32768\n" \
    "ds_read_b64_tr_b16 a[178:179], v223 offset:33792\n" \
    "ds_read_b64_tr_b16 a[180:181], v223 offset:40960\n" \
    "ds_read_b64_tr_b16 a[182:183], v223 offset:41984\n" \
    "ds_read_b64_tr_b16 a[184:185], v223 offset:49152\n" \
    "ds_read_b64_tr_b16 a[186:187], v223 offset:50176\n" \
    "ds_read_b64_tr_b16 a[188:189], v223 offset:57344\n" \
    "ds_read_b64_tr_b16 a[190:191], v223 offset:58368\n" \
    "s_waitcnt lgkmcnt(0)\n" \
    "v_add_u32 v223, 98304, v218\n" \
    "ds_read_b64_tr_b16 a[224:225], v223\n" \
    "ds_read_b64_tr_b16 a[226:227], v223 offset:1024\n" \
    "ds_read_b64_tr_b16 a[228:229], v223 offset:8192\n" \
    "ds_read_b64_tr_b16 a[230:231], v223 offset:9216\n" \
    "ds_read_b64_tr_b16 a[232:233], v223 offset:16384\n" \
    "ds_read_b64_tr_b16 a[234:235], v223 offset:17408\n" \
    "ds_read_b64_tr_b16 a[236:237], v223 offset:24576\n" \
    "ds_read_b64_tr_b16 a[238:239], v223 offset:25600\n" \
    "s_waitcnt lgkmcnt(0)\n" \
    "ds_read_b64_tr_b16 a[240:241], v223 offset:32768\n" \
    "ds_read_b64_tr_b16 a[242:243], v223 offset:33792\n" \
    "ds_read_b64_tr_b16 a[244:245], v223 offset:40960\n" \
    "ds_read_b64_tr_b16 a[246:247], v223 offset:41984\n" \
    "ds_read_b64_tr_b16 a[248:249], v223 offset:49152\n" \
    "ds_read_b64_tr_b16 a[250:251], v223 offset:50176\n" \
    "ds_read_b64_tr_b16 a[252:253], v223 offset:57344\n" \
    "ds_read_b64_tr_b16 a[254:255], v223 offset:58368\n" \
    "s_waitcnt lgkmcnt(0)\n" \
    "s_barrier\n" \
    "v_lshlrev_b32 v230, 3, v253\n" \
    "s_mov_b32 s54, 2163712\n" \
    "buffer_load_dwordx2 v[228:229], v230, s[36:39], s54 offen sc1\n" \
    "s_add_u32 m0, s51, 98304\n" \
    "s_mov_b32 s54, 2097152\n" \
    "buffer_load_dwordx4 v216, s[40:43], s54 offen lds\n" \
    "s_add_u32 m0, s51, 102400\n" \
    "s_mov_b32 s54, 2228224\n" \
    "buffer_load_dwordx4 v216, s[40:43], s54 offen lds\n" \
    "s_add_u32 m0, s51, 106496\n" \
    "s_mov_b32 s54, 2359296\n" \
    "buffer_load_dwordx4 v216, s[40:43], s54 offen lds\n" \
    "s_add_u32 m0, s51, 110592\n" \
    "s_mov_b32 s54, 2490368\n" \
    "buffer_load_dwordx4 v216, s[40:43], s54 offen lds\n" \
    "s_add_u32 m0, s51, 114688\n" \
    "s_mov_b32 s54, 2621440\n" \
    "buffer_load_dwordx4 v216, s[40:43], s54 offen lds\n" \
    "s_add_u32 m0, s51, 118784\n" \
    "s_mov_b32 s54, 2752512\n" \
    "buffer_load_dwordx4 v216, s[40:43], s54 offen lds\n" \
    "s_add_u32 m0, s51, 122880\n" \
    "s_mov_b32 s54, 2883584\n" \
    "buffer_load_dwordx4 v216, s[40:43], s54 offen lds\n" \
    "s_add_u32 m0, s51, 126976\n" \
    "s_mov_b32 s54, 3014656\n" \
    "buffer_load_dwordx4 v216, s[40:43], s54 offen lds\n" \
    "s_add_u32 m0, s51, 131072\n" \
    "s_mov_b32 s54, 3145728\n" \
    "buffer_load_dwordx4 v216, s[40:43], s54 offen lds\n" \
    "s_add_u32 m0, s51, 135168\n" \
    "s_mov_b32 s54, 3276800\n" \
    "buffer_load_dwordx4 v216, s[40:43], s54 offen lds\n" \
    "s_add_u32 m0, s51, 139264\n" \
    "s_mov_b32 s54, 3407872\n" \
    "buffer_load_dwordx4 v216, s[40:43], s54 offen lds\n" \
    "s_add_u32 m0, s51, 143360\n" \
    "s_mov_b32 s54, 3538944\n" \
    "buffer_load_dwordx4 v216, s[40:43], s54 offen lds\n" \
    "s_add_u32 m0, s51, 147456\n" \
    "s_mov_b32 s54, 3670016\n" \
    "buffer_load_dwordx4 v216, s[40:43], s54 offen lds\n" \
    "s_add_u32 m0, s51, 151552\n" \
    "s_mov_b32 s54, 3801088\n" \
    "buffer_load_dwordx4 v216, s[40:43], s54 offen lds\n" \
    "s_add_u32 m0, s51, 155648\n" \
    "s_mov_b32 s54, 3932160\n" \
    "buffer_load_dwordx4 v216, s[40:43], s54 offen lds\n" \
    "s_add_u32 m0, s51, 159744\n" \
    "s_mov_b32 s54, 4063232\n" \
    "buffer_load_dwordx4 v216, s[40:43], s54 offen lds\n" \
    "s_waitcnt vmcnt(16)\n" \
    "s_barrier\n" \
    "v_add_u32 v223, 32768, v217\n" \
    "ds_read_b64_tr_b16 v[0:1], v223\n" \
    "ds_read_b64_tr_b16 v[2:3], v223 offset:1024\n" \
    "ds_read_b64_tr_b16 v[4:5], v223 offset:8192\n" \
    "ds_read_b64_tr_b16 v[6:7], v223 offset:9216\n" \
    "ds_read_b64_tr_b16 v[8:9], v223 offset:16384\n" \
    "ds_read_b64_tr_b16 v[10:11], v223 offset:17408\n" \
    "ds_read_b64_tr_b16 v[12:13], v223 offset:24576\n" \
    "ds_read_b64_tr_b16 v[14:15], v223 offset:25600\n" \
    "s_waitcnt lgkmcnt(0)\n" \
    "ds_read_b64_tr_b16 v[16:17], v223 offset:32768\n" \
    "ds_read_b64_tr_b16 v[18:19], v223 offset:33792\n" \
    "ds_read_b64_tr_b16 v[20:21], v223 offset:40960\n" \
    "ds_read_b64_tr_b16 v[22:23], v223 offset:41984\n" \
    "ds_read_b64_tr_b16 v[24:25], v223 offset:49152\n" \
    "ds_read_b64_tr_b16 v[26:27], v223 offset:50176\n" \
    "ds_read_b64_tr_b16 v[28:29], v223 offset:57344\n" \
    "ds_read_b64_tr_b16 v[30:31], v223 offset:58368\n" \
    "s_waitcnt lgkmcnt(0)\n" \
    "v_add_u32 v223, 32768, v218\n" \
    "ds_read_b64_tr_b16 v[64:65], v223\n" \
    "ds_read_b64_tr_b16 v[66:67], v223 offset:1024\n" \
    "ds_read_b64_tr_b16 v[68:69], v223 offset:8192\n" \
    "ds_read_b64_tr_b16 v[70:71], v223 offset:9216\n" \
    "ds_read_b64_tr_b16 v[72:73], v223 offset:16384\n" \
    "ds_read_b64_tr_b16 v[74:75], v223 offset:17408\n" \
    "ds_read_b64_tr_b16 v[76:77], v223 offset:24576\n" \
    "ds_read_b64_tr_b16 v[78:79], v223 offset:25600\n" \
    "s_waitcnt lgkmcnt(0)\n" \
    "ds_read_b64_tr_b16 v[80:81], v223 offset:32768\n" \
    "ds_read_b64_tr_b16 v[82:83], v223 offset:33792\n" \
    "ds_read_b64_tr_b16 v[84:85], v223 offset:40960\n" \
    "ds_read_b64_tr_b16 v[86:87], v223 offset:41984\n" \
    "ds_read_b64_tr_b16 v[88:89], v223 offset:49152\n" \
    "ds_read_b64_tr_b16 v[90:91], v223 offset:50176\n" \
    "ds_read_b64_tr_b16 v[92:93], v223 offset:57344\n" \
    "ds_read_b64_tr_b16 v[94:95], v223 offset:58368\n" \
    "s_waitcnt lgkmcnt(0)\n" \
    "s_barrier\n" \
    "v_add_u32 v232, 1, v232\n" \
    "v_add_u32 v233, 8, v233\n" \
    "v_or_b32 v233, 7, v233\n" \
    "s_mov_b32 s54, 2163712\n" \
    "WIa1024%=:\n" \
    "v_or_b32 v229, 7, v229\n" \
    "v_cmp_ne_u64 vcc, v[232:233], v[228:229]\n" \
    "s_cbranch_vccz WIb1024%=\n" \
    "s_sleep 2\n" \
    "buffer_load_dwordx2 v[228:229], v230, s[36:39], s54 offen sc1\n" \
    "s_waitcnt vmcnt(0)\n" \
    "s_add_u32 s56, s56, 1\n" \
    "s_cmp_lt_u32 s56, 262144\n" \
    "s_cbranch_scc1 WIa1024%=\n" \
    "WIb1024%=:\n" \
    "s_add_u32 m0, s48, 0\n" \
    "s_add_u32 s54, s47, 0\n" \
    "buffer_load_dwordx4 v253, s[36:39], s54 offen sc1 lds\n" \
    "s_add_u32 m0, s48, 1024\n" \
    "s_add_u32 s54, s47, 1024\n" \
    "buffer_load_dwordx4 v253, s[36:39], s54 offen sc1 lds\n" \
    "s_add_u32 m0, s48, 2048\n" \
    "s_add_u32 s54, s47, 2048\n" \
    "buffer_load_dwordx4 v253, s[36:39], s54 offen sc1 lds\n" \
    "s_add_u32 m0, s48, 3072\n" \
    "s_add_u32 s54, s47, 3072\n" \
    "buffer_load_dwordx4 v253, s[36:39], s54 offen sc1 lds\n" \
    "s_add_u32 m0, s48, 4096\n" \
    "s_add_u32 s54, s47, 4096\n" \
    "buffer_load_dwordx4 v253, s[36:39], s54 offen sc1 lds\n" \
    "s_add_u32 m0, s48, 5120\n" \
    "s_add_u32 s54, s47, 5120\n" \
    "buffer_load_dwordx4 v253, s[36:39], s54 offen sc1 lds\n" \
    "s_add_u32 m0, s48, 6144\n" \
    "s_add_u32 s54, s47, 6144\n" \
    "buffer_load_dwordx4 v253, s[36:39], s54 offen sc1 lds\n" \
    "s_add_u32 m0, s48, 7168\n" \
    "s_add_u32 s54, s47, 7168\n" \
    "buffer_load_dwordx4 v253, s[36:39], s54 offen sc1 lds\n" \
    "s_waitcnt vmcnt(0)\n" \
    "s_barrier\n" \
    "v_add_u32 v223, 98304, v217\n" \
    "ds_read_b64_tr_b16 v[32:33], v223\n" \
    "ds_read_b64_tr_b16 v[34:35], v223 offset:1024\n" \
    "ds_read_b64_tr_b16 v[36:37], v223 offset:8192\n" \
    "ds_read_b64_tr_b16 v[38:39], v223 offset:9216\n" \
    "ds_read_b64_tr_b16 v[40:41], v223 offset:16384\n" \
    "ds_read_b64_tr_b16 v[42:43], v223 offset:17408\n" \
    "ds_read_b64_tr_b16 v[44:45], v223 offset:24576\n" \
    "ds_read_b64_tr_b16 v[46:47], v223 offset:25600\n" \
    "s_waitcnt lgkmcnt(0)\n" \
    "ds_read_b64_tr_b16 v[48:49], v223 offset:32768\n" \
    "ds_read_b64_tr_b16 v[50:51], v223 offset:33792\n" \
    "ds_read_b64_tr_b16 v[52:53], v223 offset:40960\n" \
    "ds_read_b64_tr_b16 v[54:55], v223 offset:41984\n" \
    "ds_read_b64_tr_b16 v[56:57], v223 offset:49152\n" \
    "ds_read_b64_tr_b16 v[58:59], v223 offset:50176\n" \
    "ds_read_b64_tr_b16 v[60:61], v223 offset:57344\n" \
    "ds_read_b64_tr_b16 v[62:63], v223 offset:58368\n" \
    "s_waitcnt lgkmcnt(0)\n" \
    "v_add_u32 v223, 98304, v218\n" \
    "ds_read_b64_tr_b16 v[96:97], v223\n" \
    "ds_read_b64_tr_b16 v[98:99], v223 offset:1024\n" \
    "ds_read_b64_tr_b16 v[100:101], v223 offset:8192\n" \
    "ds_read_b64_tr_b16 v[102:103], v223 offset:9216\n" \
    "ds_read_b64_tr_b16 v[104:105], v223 offset:16384\n" \
    "ds_read_b64_tr_b16 v[106:107], v223 offset:17408\n" \
    "ds_read_b64_tr_b16 v[108:109], v223 offset:24576\n" \
    "ds_read_b64_tr_b16 v[110:111], v223 offset:25600\n" \
    "s_waitcnt lgkmcnt(0)\n" \
    "ds_read_b64_tr_b16 v[112:113], v223 offset:32768\n" \
    "ds_read_b64_tr_b16 v[114:115], v223 offset:33792\n" \
    "ds_read_b64_tr_b16 v[116:117], v223 offset:40960\n" \
    "ds_read_b64_tr_b16 v[118:119], v223 offset:41984\n" \
    "ds_read_b64_tr_b16 v[120:121], v223 offset:49152\n" \
    "ds_read_b64_tr_b16 v[122:123], v223 offset:50176\n" \
    "ds_read_b64_tr_b16 v[124:125], v223 offset:57344\n" \
    "ds_read_b64_tr_b16 v[126:127], v223 offset:58368\n" \
    "s_waitcnt lgkmcnt(0)\n" \
    "s_barrier\n" \
    "v_mov_b32 v193, 1\n" \
    "s_add_u32 s48, s48, 32768\n" \
    "s_add_u32 s47, s47, 65536\n" \
    "s_add_u32 m0, s48, 0\n" \
    "s_add_u32 s54, s47, 0\n" \
    "buffer_load_dwordx4 v253, s[36:39], s54 offen sc1 lds\n" \
    "s_add_u32 m0, s48, 1024\n" \
    "s_add_u32 s54, s47, 1024\n" \
    "buffer_load_dwordx4 v253, s[36:39], s54 offen sc1 lds\n" \
    "s_add_u32 m0, s48, 2048\n" \
    "s_add_u32 s54, s47, 2048\n" \
    "buffer_load_dwordx4 v253, s[36:39], s54 offen sc1 lds\n" \
    "s_add_u32 m0, s48, 3072\n" \
    "s_add_u32 s54, s47, 3072\n" \
    "buffer_load_dwordx4 v253, s[36:39], s54 offen sc1 lds\n" \
    "s_add_u32 m0, s48, 4096\n" \
    "s_add_u32 s54, s47, 4096\n" \
    "buffer_load_dwordx4 v253, s[36:39], s54 offen sc1 lds\n" \
    "s_add_u32 m0, s48, 5120\n" \
    "s_add_u32 s54, s47, 5120\n" \
    "buffer_load_dwordx4 v253, s[36:39], s54 offen sc1 lds\n" \
    "s_add_u32 m0, s48, 6144\n" \
    "s_add_u32 s54, s47, 6144\n" \
    "buffer_load_dwordx4 v253, s[36:39], s54 offen sc1 lds\n" \
    "s_add_u32 m0, s48, 7168\n" \
    "s_add_u32 s54, s47, 7168\n" \
    "buffer_load_dwordx4 v253, s[36:39], s54 offen sc1 lds\n" \
    "v_add_u32 v192, 0, v253\n" \
    "ds_read_b128 v[176:179], v192\n" \
    "ds_read_b128 v[180:183], v192 offset:16384\n" \
    "ds_read_b128 v[184:187], v192 offset:1024\n" \
    "s_waitcnt lgkmcnt(2)\n" \
    "v_mfma_f32_16x16x32_bf16 v[136:139], a[128:131], v[176:179], 0\n" \
    "ds_read_b128 v[188:191], v192 offset:17408\n" \
    "v_mfma_f32_16x16x32_bf16 v[140:143], a[192:195], v[176:179], 0\n" \
    "v_mfma_f32_16x16x32_bf16 v[144:147], v[176:179], v[0:3], 0\n" \
    "v_mfma_f32_16x16x32_bf16 v[148:151], v[176:179], v[64:67], 0\n" \
    "s_waitcnt lgkmcnt(2)\n" \
    "v_mfma_f32_16x16x32_bf16 v[160:163], a[128:131], v[180:183], 0\n" \
    "ds_read_b128 v[176:179], v192 offset:2048\n" \
    "v_mfma_f32_16x16x32_bf16 v[164:167], a[192:195], v[180:183], 0\n" \
    "v_mfma_f32_16x16x32_bf16 v[168:171], v[180:183], v[0:3], 0\n" \
    "v_mfma_f32_16x16x32_bf16 v[172:175], v[180:183], v[64:67], 0\n" \
    "s_waitcnt lgkmcnt(2)\n" \
    "v_mfma_f32_16x16x32_bf16 v[136:139], a[132:135], v[184:187], v[136:139]\n" \
    "ds_read_b128 v[180:183], v192 offset:18432\n" \
    "v_mfma_f32_16x16x32_bf16 v[140:143], a[196:199], v[184:187], v[140:143]\n" \
    "v_mfma_f32_16x16x32_bf16 v[144:147], v[184:187], v[4:7], v[144:147]\n" \
    "v_mfma_f32_16x16x32_bf16 v[148:151], v[184:187], v[68:71], v[148:151]\n" \
    "s_waitcnt lgkmcnt(2)\n" \
    "v_mfma_f32_16x16x32_bf16 v[160:163], a[132:135], v[188:191], v[160:163]\n" \
    "ds_read_b128 v[184:187], v192 offset:3072\n" \
    "v_mfma_f32_16x16x32_bf16 v[164:167], a[196:199], v[188:191], v[164:167]\n" \
    "v_mfma_f32_16x16x32_bf16 v[168:171], v[188:191], v[4:7], v[168:171]\n" \
    "v_mfma_f32_16x16x32_bf16 v[172:175], v[188:191], v[68:71], v[172:175]\n" \
    "s_waitcnt lgkmcnt(2)\n" \
    "v_mfma_f32_16x16x32_bf16 v[136:139], a[136:139], v[176:179], v[136:139]\n" \
    "ds_read_b128 v[188:191], v192 offset:19456\n" \
    "v_mfma_f32_16x16x32_bf16 v[140:143], a[200:203], v[176:179], v[140:143]\n" \
    "v_mfma_f32_16x16x32_bf16 v[144:147], v[176:179], v[8:11], v[144:147]\n" \
    "v_mfma_f32_16x16x32_bf16 v[148:151], v[176:179], v[72:75], v[148:151]\n" \
    "s_waitcnt lgkmcnt(2)\n" \
    "v_mfma_f32_16x16x32_bf16 v[160:163], a[136:139], v[180:183], v[160:163]\n" \
    "ds_read_b128 v[176:179], v192 offset:4096\n" \
    "v_mfma_f32_16x16x32_bf16 v[164:167], a[200:203], v[180:183], v[164:167]\n" \
    "v_mfma_f32_16x16x32_bf16 v[168:171], v[180:183], v[8:11], v[168:171]\n" \
    "v_mfma_f32_16x16x32_bf16 v[172:175], v[180:183], v[72:75], v[172:175]\n" \
    "s_waitcnt lgkmcnt(2)\n" \
    "v_mfma_f32_16x16x32_bf16 v[136:139], a[140:143], v[184:187], v[136:139]\n" \
    "ds_read_b128 v[180:183], v192 offset:20480\n" \
    "v_mfma_f32_16x16x32_bf16 v[140:143], a[204:207], v[184:187], v[140:143]\n" \
    "v_mfma_f32_16x16x32_bf16 v[144:147], v[184:187], v[12:15], v[144:147]\n" \
    "v_mfma_f32_16x16x32_bf16 v[148:151], v[184:187], v[76:79], v[148:151]\n" \
    "s_waitcnt lgkmcnt(2)\n" \
    "v_mfma_f32_16x16x32_bf16 v[160:163], a[140:143], v[188:191], v[160:163]\n" \
    "ds_read_b128 v[184:187], v192 offset:5120\n" \
    "v_mfma_f32_16x16x32_bf16 v[164:167], a[204:207], v[188:191], v[164:167]\n" \
    "v_mfma_f32_16x16x32_bf16 v[168:171], v[188:191], v[12:15], v[168:171]\n" \
    "v_mfma_f32_16x16x32_bf16 v[172:175], v[188:191], v[76:79], v[172:175]\n" \
    "s_waitcnt lgkmcnt(2)\n" \
    "v_mfma_f32_16x16x32_bf16 v[136:139], a[144:147], v[176:179], v[136:139]\n" \
    "ds_read_b128 v[188:191], v192 offset:21504\n" \
    "v_mfma_f32_16x16x32_bf16 v[140:143], a[208:211], v[176:179], v[140:143]\n" \
    "v_mfma_f32_16x16x32_bf16 v[144:147], v[176:179], v[16:19], v[144:147]\n" \
    "v_mfma_f32_16x16x32_bf16 v[148:151], v[176:179], v[80:83], v[148:151]\n" \
    "s_waitcnt lgkmcnt(2)\n" \
    "v_mfma_f32_16x16x32_bf16 v[160:163], a[144:147], v[180:183], v[160:163]\n" \
    "ds_read_b128 v[176:179], v192 offset:6144\n" \
    "v_mfma_f32_16x16x32_bf16 v[164:167], a[208:211], v[180:183], v[164:167]\n" \
    "v_mfma_f32_16x16x32_bf16 v[168:171], v[180:183], v[16:19], v[168:171]\n" \
    "v_mfma_f32_16x16x32_bf16 v[172:175], v[180:183], v[80:83], v[172:175]\n" \
    "s_waitcnt lgkmcnt(2)\n" \
    "v_mfma_f32_16x16x32_bf16 v[136:139], a[148:151], v[184:187], v[136:139]\n" \
    "ds_read_b128 v[180:183], v192 offset:22528\n" \
    "v_mfma_f32_16x16x32_bf16 v[140:143], a[212:215], v[184:187], v[140:143]\n" \
    "v_mfma_f32_16x16x32_bf16 v[144:147], v[184:187], v[20:23], v[144:147]\n" \
    "v_mfma_f32_16x16x32_bf16 v[148:151], v[184:187], v[84:87], v[148:151]\n" \
    "s_waitcnt lgkmcnt(2)\n" \
    "v_mfma_f32_16x16x32_bf16 v[160:163], a[148:151], v[188:191], v[160:163]\n" \
    "ds_read_b128 v[184:187], v192 offset:7168\n" \
    "v_mfma_f32_16x16x32_bf16 v[164:167], a[212:215], v[188:191], v[164:167]\n" \
    "v_mfma_f32_16x16x32_bf16 v[168:171], v[188:191], v[20:23], v[168:171]\n" \
    "v_mfma_f32_16x16x32_bf16 v[172:175], v[188:191], v[84:87], v[172:175]\n" \
    "s_waitcnt lgkmcnt(2)\n" \
    "v_mfma_f32_16x16x32_bf16 v[136:139], a[152:155], v[176:179], v[136:139]\n" \
    "ds_read_b128 v[188:191], v192 offset:23552\n" \
    "v_mfma_f32_16x16x32_bf16 v[140:143], a[216:219], v[176:179], v[140:143]\n" \
    "v_mfma_f32_16x16x32_bf16 v[144:147], v[176:179], v[24:27], v[144:147]\n" \
    "v_mfma_f32_16x16x32_bf16 v[148:151], v[176:179], v[88:91], v[148:151]\n" \
    "s_waitcnt lgkmcnt(2)\n" \
    "v_mfma_f32_16x16x32_bf16 v[160:163], a[152:155], v[180:183], v[160:163]\n" \
    "ds_read_b128 v[176:179], v192 offset:8192\n" \
    "v_mfma_f32_16x16x32_bf16 v[164:167], a[216:219], v[180:183], v[164:167]\n" \
    "v_mfma_f32_16x16x32_bf16 v[168:171], v[180:183], v[24:27], v[168:171]\n" \
    "v_mfma_f32_16x16x32_bf16 v[172:175], v[180:183], v[88:91], v[172:175]\n" \
    "s_waitcnt lgkmcnt(2)\n" \
    "v_mfma_f32_16x16x32_bf16 v[136:139], a[156:159], v[184:187], v[136:139]\n" \
    "ds_read_b128 v[180:183], v192 offset:24576\n" \
    "v_mfma_f32_16x16x32_bf16 v[140:143], a[220:223], v[184:187], v[140:143]\n" \
    "v_mfma_f32_16x16x32_bf16 v[144:147], v[184:187], v[28:31], v[144:147]\n" \
    "v_mfma_f32_16x16x32_bf16 v[148:151], v[184:187], v[92:95], v[148:151]\n" \
    "s_waitcnt lgkmcnt(2)\n" \
    "v_mfma_f32_16x16x32_bf16 v[160:163], a[156:159], v[188:191], v[160:163]\n" \
    "ds_read_b128 v[184:187], v192 offset:9216\n" \
    "v_mfma_f32_16x16x32_bf16 v[164:167], a[220:223], v[188:191], v[164:167]\n" \
    "v_mfma_f32_16x16x32_bf16 v[168:171], v[188:191], v[28:31], v[168:171]\n" \
    "v_mfma_f32_16x16x32_bf16 v[172:175], v[188:191], v[92:95], v[172:175]\n" \
    "s_waitcnt lgkmcnt(2)\n" \
    "v_mfma_f32_16x16x32_bf16 v[136:139], a[160:163], v[176:179], v[136:139]\n" \
    "ds_read_b128 v[188:191], v192 offset:25600\n" \
    "v_mfma_f32_16x16x32_bf16 v[140:143], a[224:227], v[176:179], v[140:143]\n" \
    "v_mfma_f32_16x16x32_bf16 v[144:147], v[176:179], v[32:35], v[144:147]\n" \
    "v_mfma_f32_16x16x32_bf16 v[148:151], v[176:179], v[96:99], v[148:151]\n" \
    "s_waitcnt lgkmcnt(2)\n" \
    "v_mfma_f32_16x16x32_bf16 v[160:163], a[160:163], v[180:183], v[160:163]\n" \
    "ds_read_b128 v[176:179], v192 offset:10240\n" \
    "v_mfma_f32_16x16x32_bf16 v[164:167], a[224:227], v[180:183], v[164:167]\n" \
    "v_mfma_f32_16x16x32_bf16 v[168:171], v[180:183], v[32:35], v[168:171]\n" \
    "v_mfma_f32_16x16x32_bf16 v[172:175], v[180:183], v[96:99], v[172:175]\n" \
    "s_waitcnt lgkmcnt(2)\n" \
    "v_mfma_f32_16x16x32_bf16 v[136:139], a[164:167], v[184:187], v[136:139]\n" \
    "ds_read_b128 v[180:183], v192 offset:26624\n" \
    "v_mfma_f32_16x16x32_bf16 v[140:143], a[228:231], v[184:187], v[140:143]\n" \
    "v_mfma_f32_16x16x32_bf16 v[144:147], v[184:187], v[36:39], v[144:147]\n" \
    "v_mfma_f32_16x16x32_bf16 v[148:151], v[184:187], v[100:103], v[148:151]\n" \
    "s_waitcnt lgkmcnt(2)\n" \
    "v_mfma_f32_16x16x32_bf16 v[160:163], a[164:167], v[188:191], v[160:163]\n" \
    "ds_read_b128 v[184:187], v192 offset:11264\n" \
    "v_mfma_f32_16x16x32_bf16 v[164:167], a[228:231], v[188:191], v[164:167]\n" \
    "v_mfma_f32_16x16x32_bf16 v[168:171], v[188:191], v[36:39], v[168:171]\n" \
    "v_mfma_f32_16x16x32_bf16 v[172:175], v[188:191], v[100:103], v[172:175]\n" \
    "s_waitcnt lgkmcnt(2)\n" \
    "v_mfma_f32_16x16x32_bf16 v[136:139], a[168:171], v[176:179], v[136:139]\n" \
    "ds_read_b128 v[188:191], v192 offset:27648\n" \
    "v_mfma_f32_16x16x32_bf16 v[140:143], a[232:235], v[176:179], v[140:143]\n" \
    "v_mfma_f32_16x16x32_bf16 v[144:147], v[176:179], v[40:43], v[144:147]\n" \
    "v_mfma_f32_16x16x32_bf16 v[148:151], v[176:179], v[104:107], v[148:151]\n" \
    "s_waitcnt lgkmcnt(2)\n" \
    "v_mfma_f32_16x16x32_bf16 v[160:163], a[168:171], v[180:183], v[160:163]\n" \
    "ds_read_b128 v[176:179], v192 offset:12288\n" \
    "v_mfma_f32_16x16x32_bf16 v[164:167], a[232:235], v[180:183], v[164:167]\n" \
    "v_mfma_f32_16x16x32_bf16 v[168:171], v[180:183], v[40:43], v[168:171]\n" \
    "v_mfma_f32_16x16x32_bf16 v[172:175], v[180:183], v[104:107], v[172:175]\n" \
    "s_waitcnt lgkmcnt(2)\n" \
    "v_mfma_f32_16x16x32_bf16 v[136:139], a[172:175], v[184:187], v[136:139]\n" \
    "ds_read_b128 v[180:183], v192 offset:28672\n" \
    "v_mfma_f32_16x16x32_bf16 v[140:143], a[236:239], v[184:187], v[140:143]\n" \
    "v_mfma_f32_16x16x32_bf16 v[144:147], v[184:187], v[44:47], v[144:147]\n" \
    "v_mfma_f32_16x16x32_bf16 v[148:151], v[184:187], v[108:111], v[148:151]\n" \
    "s_waitcnt lgkmcnt(2)\n" \
    "v_mfma_f32_16x16x32_bf16 v[160:163], a[172:175], v[188:191], v[160:163]\n" \
    "ds_read_b128 v[184:187], v192 offset:13312\n" \
    "v_mfma_f32_16x16x32_bf16 v[164:167], a[236:239], v[188:191], v[164:167]\n" \
    "v_mfma_f32_16x16x32_bf16 v[168:171], v[188:191], v[44:47], v[168:171]\n" \
    "v_mfma_f32_16x16x32_bf16 v[172:175], v[188:191], v[108:111], v[172:175]\n" \
    "s_waitcnt lgkmcnt(2)\n" \
    "v_mfma_f32_16x16x32_bf16 v[136:139], a[176:179], v[176:179], v[136:139]\n" \
    "ds_read_b128 v[188:191], v192 offset:29696\n" \
    "v_mfma_f32_16x16x32_bf16 v[140:143], a[240:243], v[176:179], v[140:143]\n" \
    "v_mfma_f32_16x16x32_bf16 v[144:147], v[176:179], v[48:51], v[144:147]\n" \
    "v_mfma_f32_16x16x32_bf16 v[148:151], v[176:179], v[112:115], v[148:151]\n" \
    "s_waitcnt lgkmcnt(2)\n" \
    "v_mfma_f32_16x16x32_bf16 v[160:163], a[176:179], v[180:183], v[160:163]\n" \
    "ds_read_b128 v[176:179], v192 offset:14336\n" \
    "v_mfma_f32_16x16x32_bf16 v[164:167], a[240:243], v[180:183], v[164:167]\n" \
    "v_mfma_f32_16x16x32_bf16 v[168:171], v[180:183], v[48:51], v[168:171]\n" \
    "v_mfma_f32_16x16x32_bf16 v[172:175], v[180:183], v[112:115], v[172:175]\n" \
    "s_waitcnt lgkmcnt(2)\n" \
    "v_mfma_f32_16x16x32_bf16 v[136:139], a[180:183], v[184:187], v[136:139]\n" \
    "ds_read_b128 v[180:183], v192 offset:30720\n" \
    "v_mfma_f32_16x16x32_bf16 v[140:143], a[244:247], v[184:187], v[140:143]\n" \
    "v_mfma_f32_16x16x32_bf16 v[144:147], v[184:187], v[52:55], v[144:147]\n" \
    "v_mfma_f32_16x16x32_bf16 v[148:151], v[184:187], v[116:119], v[148:151]\n" \
    "s_waitcnt lgkmcnt(2)\n" \
    "v_mfma_f32_16x16x32_bf16 v[160:163], a[180:183], v[188:191], v[160:163]\n" \
    "ds_read_b128 v[184:187], v192 offset:15360\n" \
    "v_mfma_f32_16x16x32_bf16 v[164:167], a[244:247], v[188:191], v[164:167]\n" \
    "v_mfma_f32_16x16x32_bf16 v[168:171], v[188:191], v[52:55], v[168:171]\n" \
    "v_mfma_f32_16x16x32_bf16 v[172:175], v[188:191], v[116:119], v[172:175]\n" \
    "s_waitcnt lgkmcnt(2)\n" \
    "v_mfma_f32_16x16x32_bf16 v[136:139], a[184:187], v[176:179], v[136:139]\n" \
    "ds_read_b128 v[188:191], v192 offset:31744\n" \
    "v_mfma_f32_16x16x32_bf16 v[140:143], a[248:251], v[176:179], v[140:143]\n" \
    "v_mfma_f32_16x16x32_bf16 v[144:147], v[176:179], v[56:59], v[144:147]\n" \
    "v_mfma_f32_16x16x32_bf16 v[148:151], v[176:179], v[120:123], v[148:151]\n" \
    "s_waitcnt lgkmcnt(2)\n" \
    "v_mfma_f32_16x16x32_bf16 v[160:163], a[184:187], v[180:183], v[160:163]\n" \
    "v_mfma_f32_16x16x32_bf16 v[164:167], a[248:251], v[180:183], v[164:167]\n" \
    "v_mfma_f32_16x16x32_bf16 v[168:171], v[180:183], v[56:59], v[168:171]\n" \
    "v_mfma_f32_16x16x32_bf16 v[172:175], v[180:183], v[120:123], v[172:175]\n" \
    "s_waitcnt lgkmcnt(1)\n" \
    "v_mfma_f32_16x16x32_bf16 v[136:139], a[188:191], v[184:187], v[136:139]\n" \
    "v_mfma_f32_16x16x32_bf16 v[140:143], a[252:255], v[184:187], v[140:143]\n" \
    "v_mfma_f32_16x16x32_bf16 v[144:147], v[184:187], v[60:63], v[144:147]\n" \
    "v_mfma_f32_16x16x32_bf16 v[148:151], v[184:187], v[124:127], v[148:151]\n" \
    "s_waitcnt lgkmcnt(0)\n" \
    "v_mfma_f32_16x16x32_bf16 v[160:163], a[188:191], v[188:191], v[160:163]\n" \
    "v_mfma_f32_16x16x32_bf16 v[164:167], a[252:255], v[188:191], v[164:167]\n" \
    "v_mfma_f32_16x16x32_bf16 v[168:171], v[188:191], v[60:63], v[168:171]\n" \
    "v_mfma_f32_16x16x32_bf16 v[172:175], v[188:191], v[124:127], v[172:175]\n" \
    "s_add_u32 s54, s49, s51\n" \
    "s_add_u32 s55, s54, s51\n" \
    "s_add_u32 s55, s55, 8192\n" \
    "v_cvt_pk_bf16_f32 v196, v136, v137\n" \
    "v_cvt_pk_bf16_f32 v197, v138, v139\n" \
    "v_cvt_pk_bf16_f32 v198, v140, v141\n" \
    "v_cvt_pk_bf16_f32 v199, v142, v143\n" \
    "buffer_store_dwordx4 v[196:199], v253, s[36:39], s54 offen sc1\n" \
    "s_add_u32 s54, s54, 4096\n" \
    "v_cvt_pk_bf16_f32 v200, v160, v161\n" \
    "v_cvt_pk_bf16_f32 v201, v162, v163\n" \
    "v_cvt_pk_bf16_f32 v202, v164, v165\n" \
    "v_cvt_pk_bf16_f32 v203, v166, v167\n" \
    "buffer_store_dwordx4 v[200:203], v253, s[36:39], s54 offen sc1\n" \
    "v_cvt_pk_bf16_f32 v204, v144, v145\n" \
    "v_cvt_pk_bf16_f32 v205, v146, v147\n" \
    "v_cvt_pk_bf16_f32 v206, v168, v169\n" \
    "v_cvt_pk_bf16_f32 v207, v170, v171\n" \
    "buffer_store_dwordx4 v[204:207], v253, s[36:39], s55 offen sc1\n" \
    "v_cvt_pk_bf16_f32 v208, v148, v149\n" \
    "v_cvt_pk_bf16_f32 v209, v150, v151\n" \
    "v_cvt_pk_bf16_f32 v210, v172, v173\n" \
    "v_cvt_pk_bf16_f32 v211, v174, v175\n" \
    "buffer_store_dwordx4 v[208:211], v253, s[36:39], s55 offen offset:1024 sc1\n" \
    "s_add_u32 s49, s49, 32768\n" \
    "s_waitcnt vmcnt(0)\n" \
    "s_barrier\n" \
    "buffer_store_dword v193, v252, s[36:39], s50 offen sc1\n" \
    "s_add_u32 s50, s50, 8\n" \
    "v_add_u32 v192, 32768, v253\n" \
    "ds_read_b128 v[176:179], v192\n" \
    "ds_read_b128 v[180:183], v192 offset:16384\n" \
    "ds_read_b128 v[184:187], v192 offset:1024\n" \
    "s_waitcnt lgkmcnt(2)\n" \
    "v_mfma_f32_16x16x32_bf16 v[136:139], a[128:131], v[176:179], 0\n" \
    "ds_read_b128 v[188:191], v192 offset:17408\n" \
    "v_mfma_f32_16x16x32_bf16 v[140:143], a[192:195], v[176:179], 0\n" \
    "v_mfma_f32_16x16x32_bf16 v[144:147], v[176:179], v[0:3], 0\n" \
    "v_mfma_f32_16x16x32_bf16 v[148:151], v[176:179], v[64:67], 0\n" \
    "s_waitcnt lgkmcnt(2)\n" \
    "v_mfma_f32_16x16x32_bf16 v[160:163], a[128:131], v[180:183], 0\n" \
    "ds_read_b128 v[176:179], v192 offset:2048\n" \
    "v_mfma_f32_16x16x32_bf16 v[164:167], a[192:195], v[180:183], 0\n" \
    "v_mfma_f32_16x16x32_bf16 v[168:171], v[180:183], v[0:3], 0\n" \
    "v_mfma_f32_16x16x32_bf16 v[172:175], v[180:183], v[64:67], 0\n" \
    "s_waitcnt lgkmcnt(2)\n" \
    "v_mfma_f32_16x16x32_bf16 v[136:139], a[132:135], v[184:187], v[136:139]\n" \
    "ds_read_b128 v[180:183], v192 offset:18432\n" \
    "v_mfma_f32_16x16x32_bf16 v[140:143], a[196:199], v[184:187], v[140:143]\n" \
    "v_mfma_f32_16x16x32_bf16 v[144:147], v[184:187], v[4:7], v[144:147]\n" \
    "v_mfma_f32_16x16x32_bf16 v[148:151], v[184:187], v[68:71], v[148:151]\n" \
    "s_waitcnt lgkmcnt(2)\n" \
    "v_mfma_f32_16x16x32_bf16 v[160:163], a[132:135], v[188:191], v[160:163]\n" \
    "ds_read_b128 v[184:187], v192 offset:3072\n" \
    "v_mfma_f32_16x16x32_bf16 v[164:167], a[196:199], v[188:191], v[164:167]\n" \
    "v_mfma_f32_16x16x32_bf16 v[168:171], v[188:191], v[4:7], v[168:171]\n" \
    "v_mfma_f32_16x16x32_bf16 v[172:175], v[188:191], v[68:71], v[172:175]\n" \
    "s_waitcnt lgkmcnt(2)\n" \
    "v_mfma_f32_16x16x32_bf16 v[136:139], a[136:139], v[176:179], v[136:139]\n" \
    "ds_read_b128 v[188:191], v192 offset:19456\n" \
    "v_mfma_f32_16x16x32_bf16 v[140:143], a[200:203], v[176:179], v[140:143]\n" \
    "v_mfma_f32_16x16x32_bf16 v[144:147], v[176:179], v[8:11], v[144:147]\n" \
    "v_mfma_f32_16x16x32_bf16 v[148:151], v[176:179], v[72:75], v[148:151]\n" \
    "s_waitcnt lgkmcnt(2)\n" \
    "v_mfma_f32_16x16x32_bf16 v[160:163], a[136:139], v[180:183], v[160:163]\n" \
    "ds_read_b128 v[176:179], v192 offset:4096\n" \
    "v_mfma_f32_16x16x32_bf16 v[164:167], a[200:203], v[180:183], v[164:167]\n" \
    "v_mfma_f32_16x16x32_bf16 v[168:171], v[180:183], v[8:11], v[168:171]\n" \
    "v_mfma_f32_16x16x32_bf16 v[172:175], v[180:183], v[72:75], v[172:175]\n" \
    "s_waitcnt lgkmcnt(2)\n" \
    "v_mfma_f32_16x16x32_bf16 v[136:139], a[140:143], v[184:187], v[136:139]\n" \
    "ds_read_b128 v[180:183], v192 offset:20480\n" \
    "v_mfma_f32_16x16x32_bf16 v[140:143], a[204:207], v[184:187], v[140:143]\n" \
    "v_mfma_f32_16x16x32_bf16 v[144:147], v[184:187], v[12:15], v[144:147]\n" \
    "v_mfma_f32_16x16x32_bf16 v[148:151], v[184:187], v[76:79], v[148:151]\n" \
    "s_waitcnt lgkmcnt(2)\n" \
    "v_mfma_f32_16x16x32_bf16 v[160:163], a[140:143], v[188:191], v[160:163]\n" \
    "ds_read_b128 v[184:187], v192 offset:5120\n" \
    "v_mfma_f32_16x16x32_bf16 v[164:167], a[204:207], v[188:191], v[164:167]\n" \
    "v_mfma_f32_16x16x32_bf16 v[168:171], v[188:191], v[12:15], v[168:171]\n" \
    "v_mfma_f32_16x16x32_bf16 v[172:175], v[188:191], v[76:79], v[172:175]\n" \
    "s_waitcnt lgkmcnt(2)\n" \
    "v_mfma_f32_16x16x32_bf16 v[136:139], a[144:147], v[176:179], v[136:139]\n" \
    "ds_read_b128 v[188:191], v192 offset:21504\n" \
    "v_mfma_f32_16x16x32_bf16 v[140:143], a[208:211], v[176:179], v[140:143]\n" \
    "v_mfma_f32_16x16x32_bf16 v[144:147], v[176:179], v[16:19], v[144:147]\n" \
    "v_mfma_f32_16x16x32_bf16 v[148:151], v[176:179], v[80:83], v[148:151]\n" \
    "s_waitcnt lgkmcnt(2)\n" \
    "v_mfma_f32_16x16x32_bf16 v[160:163], a[144:147], v[180:183], v[160:163]\n" \
    "ds_read_b128 v[176:179], v192 offset:6144\n" \
    "v_mfma_f32_16x16x32_bf16 v[164:167], a[208:211], v[180:183], v[164:167]\n" \
    "v_mfma_f32_16x16x32_bf16 v[168:171], v[180:183], v[16:19], v[168:171]\n" \
    "v_mfma_f32_16x16x32_bf16 v[172:175], v[180:183], v[80:83], v[172:175]\n" \
    "s_waitcnt lgkmcnt(2)\n" \
    "v_mfma_f32_16x16x32_bf16 v[136:139], a[148:151], v[184:187], v[136:139]\n" \
    "ds_read_b128 v[180:183], v192 offset:22528\n" \
    "v_mfma_f32_16x16x32_bf16 v[140:143], a[212:215], v[184:187], v[140:143]\n" \
    "v_mfma_f32_16x16x32_bf16 v[144:147], v[184:187], v[20:23], v[144:147]\n" \
    "v_mfma_f32_16x16x32_bf16 v[148:151], v[184:187], v[84:87], v[148:151]\n" \
    "s_waitcnt lgkmcnt(2)\n" \
    "v_mfma_f32_16x16x32_bf16 v[160:163], a[148:151], v[188:191], v[160:163]\n" \
    "ds_read_b128 v[184:187], v192 offset:7168\n" \
    "v_mfma_f32_16x16x32_bf16 v[164:167], a[212:215], v[188:191], v[164:167]\n" \
    "v_mfma_f32_16x16x32_bf16 v[168:171], v[188:191], v[20:23], v[168:171]\n" \
    "v_mfma_f32_16x16x32_bf16 v[172:175], v[188:191], v[84:87], v[172:175]\n" \
    "s_waitcnt lgkmcnt(2)\n" \
    "v_mfma_f32_16x16x32_bf16 v[136:139], a[152:155], v[176:179], v[136:139]\n" \
    "ds_read_b128 v[188:191], v192 offset:23552\n" \
    "v_mfma_f32_16x16x32_bf16 v[140:143], a[216:219], v[176:179], v[140:143]\n" \
    "v_mfma_f32_16x16x32_bf16 v[144:147], v[176:179], v[24:27], v[144:147]\n" \
    "v_mfma_f32_16x16x32_bf16 v[148:151], v[176:179], v[88:91], v[148:151]\n" \
    "s_waitcnt lgkmcnt(2)\n" \
    "v_mfma_f32_16x16x32_bf16 v[160:163], a[152:155], v[180:183], v[160:163]\n" \
    "ds_read_b128 v[176:179], v192 offset:8192\n" \
    "v_mfma_f32_16x16x32_bf16 v[164:167], a[216:219], v[180:183], v[164:167]\n" \
    "v_mfma_f32_16x16x32_bf16 v[168:171], v[180:183], v[24:27], v[168:171]\n" \
    "v_mfma_f32_16x16x32_bf16 v[172:175], v[180:183], v[88:91], v[172:175]\n" \
    "s_waitcnt lgkmcnt(2)\n" \
    "v_mfma_f32_16x16x32_bf16 v[136:139], a[156:159], v[184:187], v[136:139]\n" \
    "ds_read_b128 v[180:183], v192 offset:24576\n" \
    "v_mfma_f32_16x16x32_bf16 v[140:143], a[220:223], v[184:187], v[140:143]\n" \
    "v_mfma_f32_16x16x32_bf16 v[144:147], v[184:187], v[28:31], v[144:147]\n" \
    "v_mfma_f32_16x16x32_bf16 v[148:151], v[184:187], v[92:95], v[148:151]\n" \
    "s_waitcnt lgkmcnt(2)\n" \
    "v_mfma_f32_16x16x32_bf16 v[160:163], a[156:159], v[188:191], v[160:163]\n" \
    "ds_read_b128 v[184:187], v192 offset:9216\n" \
    "v_mfma_f32_16x16x32_bf16 v[164:167], a[220:223], v[188:191], v[164:167]\n" \
    "v_mfma_f32_16x16x32_bf16 v[168:171], v[188:191], v[28:31], v[168:171]\n" \
    "v_mfma_f32_16x16x32_bf16 v[172:175], v[188:191], v[92:95], v[172:175]\n" \
    "s_waitcnt lgkmcnt(2)\n" \
    "v_mfma_f32_16x16x32_bf16 v[136:139], a[160:163], v[176:179], v[136:139]\n" \
    "ds_read_b128 v[188:191], v192 offset:25600\n" \
    "v_mfma_f32_16x16x32_bf16 v[140:143], a[224:227], v[176:179], v[140:143]\n" \
    "v_mfma_f32_16x16x32_bf16 v[144:147], v[176:179], v[32:35], v[144:147]\n" \
    "v_mfma_f32_16x16x32_bf16 v[148:151], v[176:179], v[96:99], v[148:151]\n" \
    "s_waitcnt lgkmcnt(2)\n" \
    "v_mfma_f32_16x16x32_bf16 v[160:163], a[160:163], v[180:183], v[160:163]\n" \
    "ds_read_b128 v[176:179], v192 offset:10240\n" \
    "v_mfma_f32_16x16x32_bf16 v[164:167], a[224:227], v[180:183], v[164:167]\n" \
    "v_mfma_f32_16x16x32_bf16 v[168:171], v[180:183], v[32:35], v[168:171]\n" \
    "v_mfma_f32_16x16x32_bf16 v[172:175], v[180:183], v[96:99], v[172:175]\n" \
    "s_waitcnt lgkmcnt(2)\n" \
    "v_mfma_f32_16x16x32_bf16 v[136:139], a[164:167], v[184:187], v[136:139]\n" \
    "ds_read_b128 v[180:183], v192 offset:26624\n" \
    "v_mfma_f32_16x16x32_bf16 v[140:143], a[228:231], v[184:187], v[140:143]\n" \
    "v_mfma_f32_16x16x32_bf16 v[144:147], v[184:187], v[36:39], v[144:147]\n" \
    "v_mfma_f32_16x16x32_bf16 v[148:151], v[184:187], v[100:103], v[148:151]\n" \
    "s_waitcnt lgkmcnt(2)\n" \
    "v_mfma_f32_16x16x32_bf16 v[160:163], a[164:167], v[188:191], v[160:163]\n" \
    "ds_read_b128 v[184:187], v192 offset:11264\n" \
    "v_mfma_f32_16x16x32_bf16 v[164:167], a[228:231], v[188:191], v[164:167]\n" \
    "v_mfma_f32_16x16x32_bf16 v[168:171], v[188:191], v[36:39], v[168:171]\n" \
    "v_mfma_f32_16x16x32_bf16 v[172:175], v[188:191], v[100:103], v[172:175]\n" \
    "s_waitcnt lgkmcnt(2)\n" \
    "v_mfma_f32_16x16x32_bf16 v[136:139], a[168:171], v[176:179], v[136:139]\n" \
    "ds_read_b128 v[188:191], v192 offset:27648\n" \
    "v_mfma_f32_16x16x32_bf16 v[140:143], a[232:235], v[176:179], v[140:143]\n" \
    "v_mfma_f32_16x16x32_bf16 v[144:147], v[176:179], v[40:43], v[144:147]\n" \
    "v_mfma_f32_16x16x32_bf16 v[148:151], v[176:179], v[104:107], v[148:151]\n" \
    "s_waitcnt lgkmcnt(2)\n" \
    "v_mfma_f32_16x16x32_bf16 v[160:163], a[168:171], v[180:183], v[160:163]\n" \
    "ds_read_b128 v[176:179], v192 offset:12288\n" \
    "v_mfma_f32_16x16x32_bf16 v[164:167], a[232:235], v[180:183], v[164:167]\n" \
    "v_mfma_f32_16x16x32_bf16 v[168:171], v[180:183], v[40:43], v[168:171]\n" \
    "v_mfma_f32_16x16x32_bf16 v[172:175], v[180:183], v[104:107], v[172:175]\n" \
    "s_waitcnt lgkmcnt(2)\n" \
    "v_mfma_f32_16x16x32_bf16 v[136:139], a[172:175], v[184:187], v[136:139]\n" \
    "ds_read_b128 v[180:183], v192 offset:28672\n" \
    "v_mfma_f32_16x16x32_bf16 v[140:143], a[236:239], v[184:187], v[140:143]\n" \
    "v_mfma_f32_16x16x32_bf16 v[144:147], v[184:187], v[44:47], v[144:147]\n" \
    "v_mfma_f32_16x16x32_bf16 v[148:151], v[184:187], v[108:111], v[148:151]\n" \
    "s_waitcnt lgkmcnt(2)\n" \
    "v_mfma_f32_16x16x32_bf16 v[160:163], a[172:175], v[188:191], v[160:163]\n" \
    "ds_read_b128 v[184:187], v192 offset:13312\n" \
    "v_mfma_f32_16x16x32_bf16 v[164:167], a[236:239], v[188:191], v[164:167]\n" \
    "v_mfma_f32_16x16x32_bf16 v[168:171], v[188:191], v[44:47], v[168:171]\n" \
    "v_mfma_f32_16x16x32_bf16 v[172:175], v[188:191], v[108:111], v[172:175]\n" \
    "s_waitcnt lgkmcnt(2)\n" \
    "v_mfma_f32_16x16x32_bf16 v[136:139], a[176:179], v[176:179], v[136:139]\n" \
    "ds_read_b128 v[188:191], v192 offset:29696\n" \
    "v_mfma_f32_16x16x32_bf16 v[140:143], a[240:243], v[176:179], v[140:143]\n" \
    "v_mfma_f32_16x16x32_bf16 v[144:147], v[176:179], v[48:51], v[144:147]\n" \
    "v_mfma_f32_16x16x32_bf16 v[148:151], v[176:179], v[112:115], v[148:151]\n" \
    "s_waitcnt lgkmcnt(2)\n" \
    "v_mfma_f32_16x16x32_bf16 v[160:163], a[176:179], v[180:183], v[160:163]\n" \
    "ds_read_b128 v[176:179], v192 offset:14336\n" \
    "v_mfma_f32_16x16x32_bf16 v[164:167], a[240:243], v[180:183], v[164:167]\n" \
    "v_mfma_f32_16x16x32_bf16 v[168:171], v[180:183], v[48:51], v[168:171]\n" \
    "v_mfma_f32_16x16x32_bf16 v[172:175], v[180:183], v[112:115], v[172:175]\n" \
    "s_waitcnt lgkmcnt(2)\n" \
    "v_mfma_f32_16x16x32_bf16 v[136:139], a[180:183], v[184:187], v[136:139]\n" \
    "ds_read_b128 v[180:183], v192 offset:30720\n" \
    "v_mfma_f32_16x16x32_bf16 v[140:143], a[244:247], v[184:187], v[140:143]\n" \
    "v_mfma_f32_16x16x32_bf16 v[144:147], v[184:187], v[52:55], v[144:147]\n" \
    "v_mfma_f32_16x16x32_bf16 v[148:151], v[184:187], v[116:119], v[148:151]\n" \
    "s_waitcnt lgkmcnt(2)\n" \
    "v_mfma_f32_16x16x32_bf16 v[160:163], a[180:183], v[188:191], v[160:163]\n" \
    "ds_read_b128 v[184:187], v192 offset:15360\n" \
    "v_mfma_f32_16x16x32_bf16 v[164:167], a[244:247], v[188:191], v[164:167]\n" \
    "v_mfma_f32_16x16x32_bf16 v[168:171], v[188:191], v[52:55], v[168:171]\n" \
    "v_mfma_f32_16x16x32_bf16 v[172:175], v[188:191], v[116:119], v[172:175]\n" \
    "s_waitcnt lgkmcnt(2)\n" \
    "v_mfma_f32_16x16x32_bf16 v[136:139], a[184:187], v[176:179], v[136:139]\n" \
    "ds_read_b128 v[188:191], v192 offset:31744\n" \
    "v_mfma_f32_16x16x32_bf16 v[140:143], a[248:251], v[176:179], v[140:143]\n" \
    "v_mfma_f32_16x16x32_bf16 v[144:147], v[176:179], v[56:59], v[144:147]\n" \
    "v_mfma_f32_16x16x32_bf16 v[148:151], v[176:179], v[120:123], v[148:151]\n" \
    "s_waitcnt lgkmcnt(2)\n" \
    "v_mfma_f32_16x16x32_bf16 v[160:163], a[184:187], v[180:183], v[160:163]\n" \
    "v_mfma_f32_16x16x32_bf16 v[164:167], a[248:251], v[180:183], v[164:167]\n" \
    "v_mfma_f32_16x16x32_bf16 v[168:171], v[180:183], v[56:59], v[168:171]\n" \
    "v_mfma_f32_16x16x32_bf16 v[172:175], v[180:183], v[120:123], v[172:175]\n" \
    "s_waitcnt lgkmcnt(1)\n" \
    "v_mfma_f32_16x16x32_bf16 v[136:139], a[188:191], v[184:187], v[136:139]\n" \
    "v_mfma_f32_16x16x32_bf16 v[140:143], a[252:255], v[184:187], v[140:143]\n" \
    "v_mfma_f32_16x16x32_bf16 v[144:147], v[184:187], v[60:63], v[144:147]\n" \
    "v_mfma_f32_16x16x32_bf16 v[148:151], v[184:187], v[124:127], v[148:151]\n" \
    "s_waitcnt lgkmcnt(0)\n" \
    "v_mfma_f32_16x16x32_bf16 v[160:163], a[188:191], v[188:191], v[160:163]\n" \
    "v_mfma_f32_16x16x32_bf16 v[164:167], a[252:255], v[188:191], v[164:167]\n" \
    "v_mfma_f32_16x16x32_bf16 v[168:171], v[188:191], v[60:63], v[168:171]\n" \
    "v_mfma_f32_16x16x32_bf16 v[172:175], v[188:191], v[124:127], v[172:175]\n" \
    "s_add_u32 s54, s49, s51\n" \
    "s_add_u32 s55, s54, s51\n" \
    "s_add_u32 s55, s55, 8192\n" \
    "v_cvt_pk_bf16_f32 v196, v136, v137\n" \
    "v_cvt_pk_bf16_f32 v197, v138, v139\n" \
    "v_cvt_pk_bf16_f32 v198, v140, v141\n" \
    "v_cvt_pk_bf16_f32 v199, v142, v143\n" \
    "buffer_store_dwordx4 v[196:199], v253, s[36:39], s54 offen sc1\n" \
    "s_add_u32 s54, s54, 4096\n" \
    "v_cvt_pk_bf16_f32 v200, v160, v161\n" \
    "v_cvt_pk_bf16_f32 v201, v162, v163\n" \
    "v_cvt_pk_bf16_f32 v202, v164, v165\n" \
    "v_cvt_pk_bf16_f32 v203, v166, v167\n" \
    "buffer_store_dwordx4 v[200:203], v253, s[36:39], s54 offen sc1\n" \
    "v_cvt_pk_bf16_f32 v204, v144, v145\n" \
    "v_cvt_pk_bf16_f32 v205, v146, v147\n" \
    "v_cvt_pk_bf16_f32 v206, v168, v169\n" \
    "v_cvt_pk_bf16_f32 v207, v170, v171\n" \
    "buffer_store_dwordx4 v[204:207], v253, s[36:39], s55 offen sc1\n" \
    "v_cvt_pk_bf16_f32 v208, v148, v149\n" \
    "v_cvt_pk_bf16_f32 v209, v150, v151\n" \
    "v_cvt_pk_bf16_f32 v210, v172, v173\n" \
    "v_cvt_pk_bf16_f32 v211, v174, v175\n" \
    "buffer_store_dwordx4 v[208:211], v253, s[36:39], s55 offen offset:1024 sc1\n" \
    "s_add_u32 s49, s49, 32768\n" \
    "s_waitcnt vmcnt(0)\n" \
    "s_barrier\n" \
    "buffer_store_dword v193, v252, s[36:39], s50 offen sc1\n" \
    "s_add_u32 s50, s50, 8\n" \
    "s_cmp_eq_u32 s52, 1\n" \
    "s_cbranch_scc1 EK%=\n" \
    "s_endpgm\n" \
    "EK%=:\n" \
    "v_lshlrev_b32 v230, 3, v253\n" \
    "s_mov_b32 s54, 2179072\n" \
    "buffer_load_dwordx2 v[228:229], v230, s[36:39], s54 offen sc1\n" \
    "s_waitcnt vmcnt(0)\n" \
    "s_mov_b32 s54, 2179072\n" \
    "WNa%=:\n" \
    "v_mov_b32 v231, v229\n" \
    "v_or_b32 v229, 7, v229\n" \
    "v_cmp_ne_u64 vcc, v[232:233], v[228:229]\n" \
    "s_cbranch_vccz WNb%=\n" \
    "s_sleep 2\n" \
    "buffer_load_dwordx2 v[228:229], v230, s[36:39], s54 offen sc1\n" \
    "s_waitcnt vmcnt(0)\n" \
    "s_add_u32 s56, s56, 1\n" \
    "s_cmp_lt_u32 s56, 262144\n" \
    "s_cbranch_scc1 WNa%=\n" \
    "WNb%=:\n" \
    "s_mov_b32 s32, 0\n" \
    "v_and_b32 v228, 1, v231\n" \
    "v_cmp_ne_u32 vcc, 0, v228\n" \
    "s_bcnt1_i32_b64 s54, vcc\n" \
    "s_cmp_gt_u32 s54, 32\n" \
    "s_cselect_b32 s54, 4096, 0\n" \
    "s_or_b32 s32, s32, s54\n" \
    "v_and_b32 v228, 2, v231\n" \
    "v_cmp_ne_u32 vcc, 0, v228\n" \
    "s_bcnt1_i32_b64 s54, vcc\n" \
    "s_cmp_gt_u32 s54, 32\n" \
    "s_cselect_b32 s54, 8192, 0\n" \
    "s_or_b32 s32, s32, s54\n" \
    "v_and_b32 v228, 4, v231\n" \
    "v_cmp_ne_u32 vcc, 0, v228\n" \
    "s_bcnt1_i32_b64 s54, vcc\n" \
    "s_cmp_gt_u32 s54, 32\n" \
    "s_cselect_b32 s54, 16384, 0\n" \
    "s_or_b32 s32, s32, s54\n" \
    "s_xor_b32 s45, s45, s32\n" \
    "v_xor_b32 v243, s32, v243\n" \
    "v_xor_b32 v244, s32, v244\n" \
    "v_xor_b32 v245, s32, v245\n" \
    "v_mov_b32 v100, -1\n" \
    "v_mov_b32 v101, -1\n" \
    "v_mov_b32 v102, -1\n" \
    "v_mov_b32 v103, -1\n" \
    "s_mov_b32 s24, s22\n" \
    "s_mov_b32 s25, s23\n" \
    "s_memtime s[20:21]\n" \
    "s_memrealtime s[4:5]\n" \
    "s_nop 2\n" \
    "s_memrealtime s[6:7]\n" \
    "s_nop 2\n" \
    "s_memrealtime s[8:9]\n" \
    "s_nop 2\n" \
    "s_memrealtime s[10:11]\n" \
    "s_nop 2\n" \
    "s_memrealtime s[12:13]\n" \
    "s_nop 2\n" \
    "s_memrealtime s[14:15]\n" \
    "s_nop 2\n" \
    "s_memrealtime s[16:17]\n" \
    "s_nop 2\n" \
    "s_memrealtime s[18:19]\n" \
    "s_mov_b32 s29, 0\n" \
    "s_waitcnt lgkmcnt(0)\n" \
    "s_cmp_eq_u32 s6, s4\n" \
    "s_addc_u32 s29, s29, 0\n" \
    "s_cmp_eq_u32 s8, s4\n" \
    "s_addc_u32 s29, s29, 0\n" \
    "s_cmp_eq_u32 s10, s4\n" \
    "s_addc_u32 s29, s29, 0\n" \
    "s_cmp_eq_u32 s12, s4\n" \
    "s_addc_u32 s29, s29, 0\n" \
    "s_cmp_eq_u32 s14, s4\n" \
    "s_addc_u32 s29, s29, 0\n" \
    "s_cmp_eq_u32 s16, s4\n" \
    "s_addc_u32 s29, s29, 0\n" \
    "s_cmp_eq_u32 s18, s4\n" \
    "s_addc_u32 s29, s29, 0\n" \
    "s_lshl_b32 s29, s29, 4\n" \
    "s_add_u32 s23, s20, s29\n" \
    "s_lshl_b32 s22, s4, 2\n" \
    "s_lshl_b32 s28, s22, 6\n" \
    "s_sub_u32 s29, s23, s25\n" \
    "s_sub_u32 s30, s22, s24\n" \
    "v_cvt_f32_u32 v110, s29\n" \
    "v_cvt_f32_u32 v111, s30\n" \
    "v_rcp_f32 v111, v111\n" \
    "v_or_b32 v243, v243, v243\n" \
    "v_mul_f32 v110, v110, v111\n" \
    "v_rcp_f32 v111, v110\n" \
    "s_nop 0\n" \
    "v_readfirstlane_b32 s26, v110\n" \
    "v_readfirstlane_b32 s27, v111\n" \
    "s_nop 1\n" \
    "RL%=:\n" \
    "s_add_u32 s54, s46, 393216\n" \
    "s_and_b32 s54, s54, 0x7ffff\n" \
    "s_add_u32 s46, s46, 131072\n" \
    "s_and_b32 s46, s46, 0x7ffff\n" \
    "s_add_u32 s55, s44, 1\n" \
    "v_mov_b32 v104, s55\n" \
    "s_add_u32 s55, s46, 65536\n" \
    "buffer_load_dword v108, v245, s[36:39], s59 offen sc1\n" \
    "s_waitcnt vmcnt(0)\n" \
    "SP%=:\n" \
    "v_and_b32 v109, 63, v108\n" \
    "v_cmp_ne_u32 vcc, v104, v109\n" \
    "s_cmp_eq_u32 vcc_lo, 0\n" \
    "s_cbranch_scc1 SG%=\n" \
    "s_sleep 1\n" \
    "buffer_load_dword v108, v245, s[36:39], s59 offen sc1\n" \
    "s_waitcnt vmcnt(0)\n" \
    "s_add_u32 s56, s56, 1\n" \
    "s_cmp_lt_u32 s56, 1048576\n" \
    "s_cbranch_scc1 SP%=\n" \
    "SG%=:\n" \
    "v_subrev_u32 v108, s28, v108\n" \
    "v_or_b32 v243, v243, v243\n" \
    "v_or_b32 v243, v243, v243\n" \
    "v_max_i32_dpp v108, v108, v108 row_ror:8 row_mask:0xf bank_mask:0xf\n" \
    "v_or_b32 v243, v243, v243\n" \
    "v_or_b32 v243, v243, v243\n" \
    "v_max_i32_dpp v108, v108, v108 row_ror:4 row_mask:0xf bank_mask:0xf\n" \
    "v_or_b32 v243, v243, v243\n" \
    "v_or_b32 v243, v243, v243\n" \
    "v_max_i32_dpp v108, v108, v108 row_ror:2 row_mask:0xf bank_mask:0xf\n" \
    "v_or_b32 v243, v243, v243\n" \
    "v_or_b32 v243, v243, v243\n" \
    "v_max_i32_dpp v108, v108, v108 row_ror:1 row_mask:0xf bank_mask:0xf\n" \
    "v_mov_b32 v109, v108\n" \
    "v_or_b32 v243, v243, v243\n" \
    "v_or_b32 v243, v243, v243\n" \
    "v_permlane16_swap_b32 v108, v109\n" \
    "v_max_i32 v108, v108, v109\n" \
    "v_ashrrev_i32 v108, 6, v108\n" \
    "v_add_u32 v108, 32, v108\n" \
    "v_cvt_f32_i32 v108, v108\n" \
    "v_mul_f32 v108, s26, v108\n" \
    "v_cvt_i32_f32 v108, v108\n" \
    "s_memtime s[20:21]\n" \
    "v_readfirstlane_b32 s30, v108\n" \
    "s_waitcnt lgkmcnt(0)\n" \
    "s_add_u32 s30, s30, s23\n" \
    "s_sub_u32 s29, s30, s20\n" \
    "s_min_i32 s29, s29, 4096\n" \
    "s_add_u32 s30, s20, s29\n" \
    "VL%=:\n" \
    "s_memtime s[20:21]\n" \
    "s_waitcnt lgkmcnt(0)\n" \
    "s_sub_u32 s29, s30, s20\n" \
    "s_cmp_gt_i32 s29, 192\n" \
    "s_cbranch_scc1 VL%=\n" \
    "s_max_i32 s29, s29, 0\n" \
    "s_lshr_b32 s29, s29, 3\n" \
    "s_lshl_b32 s64, s29, 2\n" \
    "s_getpc_b64 s[66:67]\n" \
    "VD%=:\n" \
    "s_sub_u32 s34, VE%=-VD%=, s64\n" \
    "s_add_u32 s66, s66, s34\n" \
    "s_addc_u32 s67, s67, 0\n" \
    "s_setpc_b64 s[66:67]\n" \
    "s_nop 1\n" \
    "s_nop 1\n" \
    "s_nop 1\n" \
    "s_nop 1\n" \
    "s_nop 1\n" \
    "s_nop 1\n" \
    "s_nop 1\n" \
    "s_nop 1\n" \
    "s_nop 1\n" \
    "s_nop 1\n" \
    "s_nop 1\n" \
    "s_nop 1\n" \
    "s_nop 1\n" \
    "s_nop 1\n" \
    "s_nop 1\n" \
    "s_nop 1\n" \
    "s_nop 1\n" \
    "s_nop 1\n" \
    "s_nop 1\n" \
    "s_nop 1\n" \
    "s_nop 1\n" \
    "s_nop 1\n" \
    "s_nop 1\n" \
    "s_nop 1\n" \
    "VE%=:\n" \
    "buffer_load_dwordx4 v[0:3], v253, s[36:39], s45 offen sc1\n" \
    "buffer_load_dwordx4 v[4:7], v253, s[36:39], s45 offen offset:1024 sc1\n" \
    "buffer_load_dwordx4 v[8:11], v253, s[36:39], s45 offen offset:2048 sc1\n" \
    "buffer_load_dwordx4 v[12:15], v253, s[36:39], s45 offen offset:3072 sc1\n" \
    "s_waitcnt vmcnt(3)\n" \
    "v_mfma_f32_16x16x32_bf16 v[64:67], v[236:239], v[0:3], 0\n" \
    "v_max3_u32 v68, v0, v1, v2\n" \
    "v_max_u32 v68, v68, v3\n" \
    "s_waitcnt vmcnt(2)\n" \
    "v_mfma_f32_16x16x32_bf16 v[64:67], v[236:239], v[4:7], v[64:67]\n" \
    "v_max3_u32 v68, v68, v4, v5\n" \
    "v_max3_u32 v68, v68, v6, v7\n" \
    "s_waitcnt vmcnt(1)\n" \
    "v_mfma_f32_16x16x32_bf16 v[64:67], v[236:239], v[8:11], v[64:67]\n" \
    "v_max3_u32 v68, v68, v8, v9\n" \
    "v_max3_u32 v68, v68, v10, v11\n" \
    "s_waitcnt vmcnt(0)\n" \
    "v_mfma_f32_16x16x32_bf16 v[64:67], v[236:239], v[12:15], v[64:67]\n" \
    "v_max3_u32 v68, v68, v12, v13\n" \
    "v_max3_u32 v68, v68, v14, v15\n" \
    "v_cmp_eq_u32 vcc, -1, v68\n" \
    "s_cbranch_vccnz RR%=\n" \
    "ROK%=:\n" \
    "v_or_b32 v243, v243, v243\n" \
    "v_or_b32 v243, v243, v243\n" \
    "v_or_b32 v243, v243, v243\n" \
    "v_or_b32 v243, v243, v243\n" \
    "v_add_f32_dpp v64, v64, v64 row_ror:8 row_mask:0xf bank_mask:0xf\n" \
    "v_add_f32_dpp v65, v65, v65 row_ror:8 row_mask:0xf bank_mask:0xf\n" \
    "v_add_f32_dpp v66, v66, v66 row_ror:8 row_mask:0xf bank_mask:0xf\n" \
    "v_add_f32_dpp v67, v67, v67 row_ror:8 row_mask:0xf bank_mask:0xf\n" \
    "v_cvt_pk_bf16_f32 v64, v64, v65\n" \
    "v_cvt_pk_bf16_f32 v65, v66, v67\n" \
    "v_min_u32 v72, -2, v64\n" \
    "v_min_u32 v73, -2, v65\n" \
    "buffer_store_dwordx2 v[72:73], v244, s[36:39], s46 offen sc1\n" \
    "buffer_store_dwordx2 v[72:73], v244, s[36:39], s55 offen sc1\n" \
    "s_nop 15\n" \
    "s_nop 15\n" \
    "s_nop 15\n" \
    "s_nop 15\n" \
    "s_nop 15\n" \
    "s_nop 15\n" \
    "s_nop 15\n" \
    "s_nop 15\n" \
    "s_nop 15\n" \
    "s_nop 15\n" \
    "s_nop 15\n" \
    "s_nop 15\n" \
    "s_nop 7\n" \
    "s_waitcnt vmcnt(0)\n" \
    "buffer_store_dwordx4 v[100:103], v253, s[36:39], s45 offen sc1\n" \
    "buffer_store_dwordx4 v[100:103], v253, s[36:39], s45 offen offset:1024 sc1\n" \
    "buffer_store_dwordx4 v[100:103], v253, s[36:39], s45 offen offset:2048 sc1\n" \
    "buffer_store_dwordx4 v[100:103], v253, s[36:39], s45 offen offset:3072 sc1\n" \
    "buffer_store_dwordx4 v[100:103], v243, s[36:39], s54 offen sc1\n" \
    "s_nop 15\n" \
    "s_nop 15\n" \
    "s_nop 15\n" \
    "s_nop 15\n" \
    "s_nop 15\n" \
    "s_nop 15\n" \
    "s_nop 15\n" \
    "s_nop 15\n" \
    "s_nop 15\n" \
    "s_nop 15\n" \
    "s_nop 15\n" \
    "s_nop 3\n" \
    "s_mov_b32 s24, s22\n" \
    "s_mov_b32 s25, s23\n" \
    "s_memtime s[20:21]\n" \
    "s_memrealtime s[4:5]\n" \
    "s_nop 2\n" \
    "s_memrealtime s[6:7]\n" \
    "s_nop 2\n" \
    "s_memrealtime s[8:9]\n" \
    "s_nop 2\n" \
    "s_memrealtime s[10:11]\n" \
    "s_nop 2\n" \
    "s_memrealtime s[12:13]\n" \
    "s_nop 2\n" \
    "s_memrealtime s[14:15]\n" \
    "s_nop 2\n" \
    "s_memrealtime s[16:17]\n" \
    "s_nop 2\n" \
    "s_memrealtime s[18:19]\n" \
    "s_mov_b32 s29, 0\n" \
    "s_waitcnt lgkmcnt(0)\n" \
    "s_cmp_eq_u32 s6, s4\n" \
    "s_addc_u32 s29, s29, 0\n" \
    "s_cmp_eq_u32 s8, s4\n" \
    "s_addc_u32 s29, s29, 0\n" \
    "s_cmp_eq_u32 s10, s4\n" \
    "s_addc_u32 s29, s29, 0\n" \
    "s_cmp_eq_u32 s12, s4\n" \
    "s_addc_u32 s29, s29, 0\n" \
    "s_cmp_eq_u32 s14, s4\n" \
    "s_addc_u32 s29, s29, 0\n" \
    "s_cmp_eq_u32 s16, s4\n" \
    "s_addc_u32 s29, s29, 0\n" \
    "s_cmp_eq_u32 s18, s4\n" \
    "s_addc_u32 s29, s29, 0\n" \
    "s_lshl_b32 s29, s29, 4\n" \
    "s_add_u32 s23, s20, s29\n" \
    "s_lshl_b32 s22, s4, 2\n" \
    "s_lshl_b32 s28, s22, 6\n" \
    "s_sub_u32 s29, s23, s25\n" \
    "s_sub_u32 s30, s22, s24\n" \
    "v_cvt_f32_u32 v110, s29\n" \
    "v_cvt_f32_u32 v111, s30\n" \
    "v_rcp_f32 v111, v111\n" \
    "v_or_b32 v243, v243, v243\n" \
    "v_mul_f32 v110, v110, v111\n" \
    "v_add_f32 v110, s26, v110\n" \
    "v_mul_f32 v110, 0.5, v110\n" \
    "v_rcp_f32 v111, v110\n" \
    "s_nop 0\n" \
    "v_readfirstlane_b32 s26, v110\n" \
    "v_readfirstlane_b32 s27, v111\n" \
    "s_nop 1\n" \
    "s_xor_b32 s45, s45, 0x84000\n" \
    "s_add_u32 s44, s44, 1\n" \
    "s_cmp_lt_u32 s44, 63\n" \
    "s_cbranch_scc1 RL%=\n" \
    "s_waitcnt vmcnt(0)\n" \
    "s_endpgm\n" \
    "RR%=:\n" \
    "s_nop 7\n" \
    "s_add_u32 s57, s57, 1\n" \
    "s_cmp_gt_u32 s57, 262144\n" \
    "s_cbranch_scc1 ROK%=\n" \
    "buffer_load_dwordx4 v[0:3], v253, s[36:39], s45 offen sc1\n" \
    "buffer_load_dwordx4 v[4:7], v253, s[36:39], s45 offen offset:1024 sc1\n" \
    "buffer_load_dwordx4 v[8:11], v253, s[36:39], s45 offen offset:2048 sc1\n" \
    "buffer_load_dwordx4 v[12:15], v253, s[36:39], s45 offen offset:3072 sc1\n" \
    "s_waitcnt vmcnt(3)\n" \
    "v_mfma_f32_16x16x32_bf16 v[64:67], v[236:239], v[0:3], 0\n" \
    "v_max3_u32 v68, v0, v1, v2\n" \
    "v_max_u32 v68, v68, v3\n" \
    "s_waitcnt vmcnt(2)\n" \
    "v_mfma_f32_16x16x32_bf16 v[64:67], v[236:239], v[4:7], v[64:67]\n" \
    "v_max3_u32 v68, v68, v4, v5\n" \
    "v_max3_u32 v68, v68, v6, v7\n" \
    "s_waitcnt vmcnt(1)\n" \
    "v_mfma_f32_16x16x32_bf16 v[64:67], v[236:239], v[8:11], v[64:67]\n" \
    "v_max3_u32 v68, v68, v8, v9\n" \
    "v_max3_u32 v68, v68, v10, v11\n" \
    "s_waitcnt vmcnt(0)\n" \
    "v_mfma_f32_16x16x32_bf16 v[64:67], v[236:239], v[12:15], v[64:67]\n" \
    "v_max3_u32 v68, v68, v12, v13\n" \
    "v_max3_u32 v68, v68, v14, v15\n" \
    "v_cmp_eq_u32 vcc, -1, v68\n" \
    "s_cbranch_vccnz RR%=\n" \
    "s_nop 3\n" \
    "s_branch ROK%=\n" \
    "RE_red:\n"

#define CHAIN_W0 \
    "RS_w0:\n" \
    "s_getpc_b64 s[2:3]\n" \
    "v_lshlrev_b32 v0, 3, v253\n" \
    "s_mov_b32 s0, RE_w0-RS_w0-8\n" \
    "v_add_u32 v1, 0, v0\n" \
    "v_min_u32 v1, s0, v1\n" \
    "global_load_dword v2, v1, s[2:3]\n" \
    "v_add_u32 v1, 8192, v0\n" \
    "v_min_u32 v1, s0, v1\n" \
    "global_load_dword v3, v1, s[2:3]\n" \
    "global_load_dwordx4 v[128:131], v[232:233], off\n" \
    "global_load_dwordx4 v[132:135], v[232:233], off offset:128\n" \
    "v_mov_b32 v242, 0\n" \
    "s_mov_b32 s54, 2162688\n" \
    "buffer_load_dwordx2 v[234:235], v242, s[36:39], s54 offen sc1\n" \
    "s_mov_b32 s42, 0x400000\n" \
    "s_mov_b32 s43, 0x20000\n" \
    "v_xor_b32 v222, 128, v219\n" \
    "s_mov_b32 s40, s62\n" \
    "s_and_b32 s41, s63, 0xffff\n" \
    "s_add_u32 m0, s51, 32768\n" \
    "s_mov_b32 s54, 0\n" \
    "buffer_load_dwordx4 v216, s[40:43], s54 offen lds\n" \
    "s_add_u32 m0, s51, 36864\n" \
    "s_mov_b32 s54, 131072\n" \
    "buffer_load_dwordx4 v216, s[40:43], s54 offen lds\n" \
    "s_add_u32 m0, s51, 40960\n" \
    "s_mov_b32 s54, 262144\n" \
    "buffer_load_dwordx4 v216, s[40:43], s54 offen lds\n" \
    "s_add_u32 m0, s51, 45056\n" \
    "s_mov_b32 s54, 393216\n" \
    "buffer_load_dwordx4 v216, s[40:43], s54 offen lds\n" \
    "s_add_u32 m0, s51, 49152\n" \
    "s_mov_b32 s54, 524288\n" \
    "buffer_load_dwordx4 v216, s[40:43], s54 offen lds\n" \
    "s_add_u32 m0, s51, 53248\n" \
    "s_mov_b32 s54, 655360\n" \
    "buffer_load_dwordx4 v216, s[40:43], s54 offen lds\n" \
    "s_add_u32 m0, s51, 57344\n" \
    "s_mov_b32 s54, 786432\n" \
    "buffer_load_dwordx4 v216, s[40:43], s54 offen lds\n" \
    "s_add_u32 m0, s51, 61440\n" \
    "s_mov_b32 s54, 917504\n" \
    "buffer_load_dwordx4 v216, s[40:43], s54 offen lds\n" \
    "s_add_u32 m0, s51, 65536\n" \
    "s_mov_b32 s54, 1048576\n" \
    "buffer_load_dwordx4 v216, s[40:43], s54 offen lds\n" \
    "s_add_u32 m0, s51, 69632\n" \
    "s_mov_b32 s54, 1179648\n" \
    "buffer_load_dwordx4 v216, s[40:43], s54 offen lds\n" \
    "s_add_u32 m0, s51, 73728\n" \
    "s_mov_b32 s54, 1310720\n" \
    "buffer_load_dwordx4 v216, s[40:43], s54 offen lds\n" \
    "s_add_u32 m0, s51, 77824\n" \
    "s_mov_b32 s54, 1441792\n" \
    "buffer_load_dwordx4 v216, s[40:43], s54 offen lds\n" \
    "s_add_u32 m0, s51, 81920\n" \
    "s_mov_b32 s54, 1572864\n" \
    "buffer_load_dwordx4 v216, s[40:43], s54 offen lds\n" \
    "s_add_u32 m0, s51, 86016\n" \
    "s_mov_b32 s54, 1703936\n" \
    "buffer_load_dwordx4 v216, s[40:43], s54 offen lds\n" \
    "s_add_u32 m0, s51, 90112\n" \
    "s_mov_b32 s54, 1835008\n" \
    "buffer_load_dwordx4 v216, s[40:43], s54 offen lds\n" \
    "s_add_u32 m0, s51, 94208\n" \
    "s_mov_b32 s54, 1966080\n" \
    "buffer_load_dwordx4 v216, s[40:43], s54 offen lds\n" \
    "s_add_u32 m0, s51, 98304\n" \
    "s_mov_b32 s54, 2097152\n" \
    "buffer_load_dwordx4 v216, s[40:43], s54 offen lds\n" \
    "s_add_u32 m0, s51, 102400\n" \
    "s_mov_b32 s54, 2228224\n" \
    "buffer_load_dwordx4 v216, s[40:43], s54 offen lds\n" \
    "s_add_u32 m0, s51, 106496\n" \
    "s_mov_b32 s54, 2359296\n" \
    "buffer_load_dwordx4 v216, s[40:43], s54 offen lds\n" \
    "s_add_u32 m0, s51, 110592\n" \
    "s_mov_b32 s54, 2490368\n" \
    "buffer_load_dwordx4 v216, s[40:43], s54 offen lds\n" \
    "s_add_u32 m0, s51, 114688\n" \
    "s_mov_b32 s54, 2621440\n" \
    "buffer_load_dwordx4 v216, s[40:43], s54 offen lds\n" \
    "s_add_u32 m0, s51, 118784\n" \
    "s_mov_b32 s54, 2752512\n" \
    "buffer_load_dwordx4 v216, s[40:43], s54 offen lds\n" \
    "s_add_u32 m0, s51, 122880\n" \
    "s_mov_b32 s54, 2883584\n" \
    "buffer_load_dwordx4 v216, s[40:43], s54 offen lds\n" \
    "s_add_u32 m0, s51, 126976\n" \
    "s_mov_b32 s54, 3014656\n" \
    "buffer_load_dwordx4 v216, s[40:43], s54 offen lds\n" \
    "s_add_u32 m0, s51, 131072\n" \
    "s_mov_b32 s54, 3145728\n" \
    "buffer_load_dwordx4 v216, s[40:43], s54 offen lds\n" \
    "s_add_u32 m0, s51, 135168\n" \
    "s_mov_b32 s54, 3276800\n" \
    "buffer_load_dwordx4 v216, s[40:43], s54 offen lds\n" \
    "s_add_u32 m0, s51, 139264\n" \
    "s_mov_b32 s54, 3407872\n" \
    "buffer_load_dwordx4 v216, s[40:43], s54 offen lds\n" \
    "s_add_u32 m0, s51, 143360\n" \
    "s_mov_b32 s54, 3538944\n" \
    "buffer_load_dwordx4 v216, s[40:43], s54 offen lds\n" \
    "s_add_u32 m0, s51, 147456\n" \
    "s_mov_b32 s54, 3670016\n" \
    "buffer_load_dwordx4 v216, s[40:43], s54 offen lds\n" \
    "s_add_u32 m0, s51, 151552\n" \
    "s_mov_b32 s54, 3801088\n" \
    "buffer_load_dwordx4 v216, s[40:43], s54 offen lds\n" \
    "s_add_u32 m0, s51, 155648\n" \
    "s_mov_b32 s54, 3932160\n" \
    "buffer_load_dwordx4 v216, s[40:43], s54 offen lds\n" \
    "s_add_u32 m0, s51, 159744\n" \
    "s_mov_b32 s54, 4063232\n" \
    "buffer_load_dwordx4 v216, s[40:43], s54 offen lds\n" \
    "s_mov_b32 s24, s22\n" \
    "s_mov_b32 s25, s23\n" \
    "s_memtime s[20:21]\n" \
    "s_memrealtime s[4:5]\n" \
    "s_nop 2\n" \
    "s_memrealtime s[6:7]\n" \
    "s_nop 2\n" \
    "s_memrealtime s[8:9]\n" \
    "s_nop 2\n" \
    "s_memrealtime s[10:11]\n" \
    "s_nop 2\n" \
    "s_memrealtime s[12:13]\n" \
    "s_nop 2\n" \
    "s_memrealtime s[14:15]\n" \
    "s_nop 2\n" \
    "s_memrealtime s[16:17]\n" \
    "s_nop 2\n" \
    "s_memrealtime s[18:19]\n" \
    "s_mov_b32 s29, 0\n" \
    "s_waitcnt lgkmcnt(0)\n" \
    "s_cmp_eq_u32 s6, s4\n" \
    "s_addc_u32 s29, s29, 0\n" \
    "s_cmp_eq_u32 s8, s4\n" \
    "s_addc_u32 s29, s29, 0\n" \
    "s_cmp_eq_u32 s10, s4\n" \
    "s_addc_u32 s29, s29, 0\n" \
    "s_cmp_eq_u32 s12, s4\n" \
    "s_addc_u32 s29, s29, 0\n" \
    "s_cmp_eq_u32 s14, s4\n" \
    "s_addc_u32 s29, s29, 0\n" \
    "s_cmp_eq_u32 s16, s4\n" \
    "s_addc_u32 s29, s29, 0\n" \
    "s_cmp_eq_u32 s18, s4\n" \
    "s_addc_u32 s29, s29, 0\n" \
    "s_lshl_b32 s29, s29, 4\n" \
    "s_add_u32 s23, s20, s29\n" \
    "s_lshl_b32 s22, s4, 2\n" \
    "s_lshl_b32 s28, s22, 6\n" \
    "s_waitcnt vmcnt(16)\n" \
    "s_barrier\n" \
    "v_xor_b32 v223, 0, v217\n" \
    "v_add_u32 v223, 32768, v223\n" \
    "ds_read_b64_tr_b16 a[128:129], v223\n" \
    "ds_read_b64_tr_b16 a[130:131], v223 offset:512\n" \
    "ds_read_b64_tr_b16 a[132:133], v223 offset:8192\n" \
    "ds_read_b64_tr_b16 a[134:135], v223 offset:8704\n" \
    "v_xor_b32 v223, 16384, v217\n" \
    "v_add_u32 v223, 32768, v223\n" \
    "ds_read_b64_tr_b16 a[136:137], v223\n" \
    "ds_read_b64_tr_b16 a[138:139], v223 offset:512\n" \
    "ds_read_b64_tr_b16 a[140:141], v223 offset:8192\n" \
    "ds_read_b64_tr_b16 a[142:143], v223 offset:8704\n" \
    "v_xor_b32 v223, 32768, v217\n" \
    "v_add_u32 v223, 32768, v223\n" \
    "ds_read_b64_tr_b16 a[144:145], v223\n" \
    "ds_read_b64_tr_b16 a[146:147], v223 offset:512\n" \
    "ds_read_b64_tr_b16 a[148:149], v223 offset:8192\n" \
    "ds_read_b64_tr_b16 a[150:151], v223 offset:8704\n" \
    "v_xor_b32 v223, 49152, v217\n" \
    "v_add_u32 v223, 32768, v223\n" \
    "ds_read_b64_tr_b16 a[152:153], v223\n" \
    "ds_read_b64_tr_b16 a[154:155], v223 offset:512\n" \
    "ds_read_b64_tr_b16 a[156:157], v223 offset:8192\n" \
    "ds_read_b64_tr_b16 a[158:159], v223 offset:8704\n" \
    "v_xor_b32 v223, 0, v218\n" \
    "v_add_u32 v223, 32768, v223\n" \
    "ds_read_b64_tr_b16 a[192:193], v223\n" \
    "ds_read_b64_tr_b16 a[194:195], v223 offset:512\n" \
    "ds_read_b64_tr_b16 a[196:197], v223 offset:8192\n" \
    "ds_read_b64_tr_b16 a[198:199], v223 offset:8704\n" \
    "v_xor_b32 v223, 16384, v218\n" \
    "v_add_u32 v223, 32768, v223\n" \
    "ds_read_b64_tr_b16 a[200:201], v223\n" \
    "ds_read_b64_tr_b16 a[202:203], v223 offset:512\n" \
    "ds_read_b64_tr_b16 a[204:205], v223 offset:8192\n" \
    "ds_read_b64_tr_b16 a[206:207], v223 offset:8704\n" \
    "v_xor_b32 v223, 32768, v218\n" \
    "v_add_u32 v223, 32768, v223\n" \
    "ds_read_b64_tr_b16 a[208:209], v223\n" \
    "ds_read_b64_tr_b16 a[210:211], v223 offset:512\n" \
    "ds_read_b64_tr_b16 a[212:213], v223 offset:8192\n" \
    "ds_read_b64_tr_b16 a[214:215], v223 offset:8704\n" \
    "v_xor_b32 v223, 49152, v218\n" \
    "v_add_u32 v223, 32768, v223\n" \
    "ds_read_b64_tr_b16 a[216:217], v223\n" \
    "ds_read_b64_tr_b16 a[218:219], v223 offset:512\n" \
    "ds_read_b64_tr_b16 a[220:221], v223 offset:8192\n" \
    "ds_read_b64_tr_b16 a[222:223], v223 offset:8704\n" \
    "s_waitcnt lgkmcnt(0)\n" \
    "s_barrier\n" \
    "s_mov_b32 s40, s64\n" \
    "s_and_b32 s41, s65, 0xffff\n" \
    "s_add_u32 m0, s51, 32768\n" \
    "s_mov_b32 s54, 0\n" \
    "buffer_load_dwordx4 v216, s[40:43], s54 offen lds\n" \
    "s_add_u32 m0, s51, 36864\n" \
    "s_mov_b32 s54, 131072\n" \
    "buffer_load_dwordx4 v216, s[40:43], s54 offen lds\n" \
    "s_add_u32 m0, s51, 40960\n" \
    "s_mov_b32 s54, 262144\n" \
    "buffer_load_dwordx4 v216, s[40:43], s54 offen lds\n" \
    "s_add_u32 m0, s51, 45056\n" \
    "s_mov_b32 s54, 393216\n" \
    "buffer_load_dwordx4 v216, s[40:43], s54 offen lds\n" \
    "s_add_u32 m0, s51, 49152\n" \
    "s_mov_b32 s54, 524288\n" \
    "buffer_load_dwordx4 v216, s[40:43], s54 offen lds\n" \
    "s_add_u32 m0, s51, 53248\n" \
    "s_mov_b32 s54, 655360\n" \
    "buffer_load_dwordx4 v216, s[40:43], s54 offen lds\n" \
    "s_add_u32 m0, s51, 57344\n" \
    "s_mov_b32 s54, 786432\n" \
    "buffer_load_dwordx4 v216, s[40:43], s54 offen lds\n" \
    "s_add_u32 m0, s51, 61440\n" \
    "s_mov_b32 s54, 917504\n" \
    "buffer_load_dwordx4 v216, s[40:43], s54 offen lds\n" \
    "s_add_u32 m0, s51, 65536\n" \
    "s_mov_b32 s54, 1048576\n" \
    "buffer_load_dwordx4 v216, s[40:43], s54 offen lds\n" \
    "s_add_u32 m0, s51, 69632\n" \
    "s_mov_b32 s54, 1179648\n" \
    "buffer_load_dwordx4 v216, s[40:43], s54 offen lds\n" \
    "s_add_u32 m0, s51, 73728\n" \
    "s_mov_b32 s54, 1310720\n" \
    "buffer_load_dwordx4 v216, s[40:43], s54 offen lds\n" \
    "s_add_u32 m0, s51, 77824\n" \
    "s_mov_b32 s54, 1441792\n" \
    "buffer_load_dwordx4 v216, s[40:43], s54 offen lds\n" \
    "s_add_u32 m0, s51, 81920\n" \
    "s_mov_b32 s54, 1572864\n" \
    "buffer_load_dwordx4 v216, s[40:43], s54 offen lds\n" \
    "s_add_u32 m0, s51, 86016\n" \
    "s_mov_b32 s54, 1703936\n" \
    "buffer_load_dwordx4 v216, s[40:43], s54 offen lds\n" \
    "s_add_u32 m0, s51, 90112\n" \
    "s_mov_b32 s54, 1835008\n" \
    "buffer_load_dwordx4 v216, s[40:43], s54 offen lds\n" \
    "s_add_u32 m0, s51, 94208\n" \
    "s_mov_b32 s54, 1966080\n" \
    "buffer_load_dwordx4 v216, s[40:43], s54 offen lds\n" \
    "s_waitcnt vmcnt(16)\n" \
    "s_barrier\n" \
    "v_xor_b32 v223, 0, v217\n" \
    "v_add_u32 v223, 98304, v223\n" \
    "ds_read_b64_tr_b16 a[160:161], v223\n" \
    "ds_read_b64_tr_b16 a[162:163], v223 offset:512\n" \
    "ds_read_b64_tr_b16 a[164:165], v223 offset:8192\n" \
    "ds_read_b64_tr_b16 a[166:167], v223 offset:8704\n" \
    "v_xor_b32 v223, 16384, v217\n" \
    "v_add_u32 v223, 98304, v223\n" \
    "ds_read_b64_tr_b16 a[168:169], v223\n" \
    "ds_read_b64_tr_b16 a[170:171], v223 offset:512\n" \
    "ds_read_b64_tr_b16 a[172:173], v223 offset:8192\n" \
    "ds_read_b64_tr_b16 a[174:175], v223 offset:8704\n" \
    "v_xor_b32 v223, 32768, v217\n" \
    "v_add_u32 v223, 98304, v223\n" \
    "ds_read_b64_tr_b16 a[176:177], v223\n" \
    "ds_read_b64_tr_b16 a[178:179], v223 offset:512\n" \
    "ds_read_b64_tr_b16 a[180:181], v223 offset:8192\n" \
    "ds_read_b64_tr_b16 a[182:183], v223 offset:8704\n" \
    "v_xor_b32 v223, 49152, v217\n" \
    "v_add_u32 v223, 98304, v223\n" \
    "ds_read_b64_tr_b16 a[184:185], v223\n" \
    "ds_read_b64_tr_b16 a[186:187], v223 offset:512\n" \
    "ds_read_b64_tr_b16 a[188:189], v223 offset:8192\n" \
    "ds_read_b64_tr_b16 a[190:191], v223 offset:8704\n" \
    "v_xor_b32 v223, 0, v218\n" \
    "v_add_u32 v223, 98304, v223\n" \
    "ds_read_b64_tr_b16 a[224:225], v223\n" \
    "ds_read_b64_tr_b16 a[226:227], v223 offset:512\n" \
    "ds_read_b64_tr_b16 a[228:229], v223 offset:8192\n" \
    "ds_read_b64_tr_b16 a[230:231], v223 offset:8704\n" \
    "v_xor_b32 v223, 16384, v218\n" \
    "v_add_u32 v223, 98304, v223\n" \
    "ds_read_b64_tr_b16 a[232:233], v223\n" \
    "ds_read_b64_tr_b16 a[234:235], v223 offset:512\n" \
    "ds_read_b64_tr_b16 a[236:237], v223 offset:8192\n" \
    "ds_read_b64_tr_b16 a[238:239], v223 offset:8704\n" \
    "v_xor_b32 v223, 32768, v218\n" \
    "v_add_u32 v223, 98304, v223\n" \
    "ds_read_b64_tr_b16 a[240:241], v223\n" \
    "ds_read_b64_tr_b16 a[242:243], v223 offset:512\n" \
    "ds_read_b64_tr_b16 a[244:245], v223 offset:8192\n" \
    "ds_read_b64_tr_b16 a[246:247], v223 offset:8704\n" \
    "v_xor_b32 v223, 49152, v218\n" \
    "v_add_u32 v223, 98304, v223\n" \
    "ds_read_b64_tr_b16 a[248:249], v223\n" \
    "ds_read_b64_tr_b16 a[250:251], v223 offset:512\n" \
    "ds_read_b64_tr_b16 a[252:253], v223 offset:8192\n" \
    "ds_read_b64_tr_b16 a[254:255], v223 offset:8704\n" \
    "s_waitcnt lgkmcnt(0)\n" \
    "s_barrier\n" \
    "s_add_u32 m0, s51, 98304\n" \
    "s_mov_b32 s54, 2097152\n" \
    "buffer_load_dwordx4 v216, s[40:43], s54 offen lds\n" \
    "s_add_u32 m0, s51, 102400\n" \
    "s_mov_b32 s54, 2228224\n" \
    "buffer_load_dwordx4 v216, s[40:43], s54 offen lds\n" \
    "s_add_u32 m0, s51, 106496\n" \
    "s_mov_b32 s54, 2359296\n" \
    "buffer_load_dwordx4 v216, s[40:43], s54 offen lds\n" \
    "s_add_u32 m0, s51, 110592\n" \
    "s_mov_b32 s54, 2490368\n" \
    "buffer_load_dwordx4 v216, s[40:43], s54 offen lds\n" \
    "s_add_u32 m0, s51, 114688\n" \
    "s_mov_b32 s54, 2621440\n" \
    "buffer_load_dwordx4 v216, s[40:43], s54 offen lds\n" \
    "s_add_u32 m0, s51, 118784\n" \
    "s_mov_b32 s54, 2752512\n" \
    "buffer_load_dwordx4 v216, s[40:43], s54 offen lds\n" \
    "s_add_u32 m0, s51, 122880\n" \
    "s_mov_b32 s54, 2883584\n" \
    "buffer_load_dwordx4 v216, s[40:43], s54 offen lds\n" \
    "s_add_u32 m0, s51, 126976\n" \
    "s_mov_b32 s54, 3014656\n" \
    "buffer_load_dwordx4 v216, s[40:43], s54 offen lds\n" \
    "s_add_u32 m0, s51, 131072\n" \
    "s_mov_b32 s54, 3145728\n" \
    "buffer_load_dwordx4 v216, s[40:43], s54 offen lds\n" \
    "s_add_u32 m0, s51, 135168\n" \
    "s_mov_b32 s54, 3276800\n" \
    "buffer_load_dwordx4 v216, s[40:43], s54 offen lds\n" \
    "s_add_u32 m0, s51, 139264\n" \
    "s_mov_b32 s54, 3407872\n" \
    "buffer_load_dwordx4 v216, s[40:43], s54 offen lds\n" \
    "s_add_u32 m0, s51, 143360\n" \
    "s_mov_b32 s54, 3538944\n" \
    "buffer_load_dwordx4 v216, s[40:43], s54 offen lds\n" \
    "s_add_u32 m0, s51, 147456\n" \
    "s_mov_b32 s54, 3670016\n" \
    "buffer_load_dwordx4 v216, s[40:43], s54 offen lds\n" \
    "s_add_u32 m0, s51, 151552\n" \
    "s_mov_b32 s54, 3801088\n" \
    "buffer_load_dwordx4 v216, s[40:43], s54 offen lds\n" \
    "s_add_u32 m0, s51, 155648\n" \
    "s_mov_b32 s54, 3932160\n" \
    "buffer_load_dwordx4 v216, s[40:43], s54 offen lds\n" \
    "s_add_u32 m0, s51, 159744\n" \
    "s_mov_b32 s54, 4063232\n" \
    "buffer_load_dwordx4 v216, s[40:43], s54 offen lds\n" \
    "s_waitcnt vmcnt(16)\n" \
    "s_barrier\n" \
    "v_xor_b32 v223, 0, v217\n" \
    "v_add_u32 v223, 32768, v223\n" \
    "ds_read_b64_tr_b16 v[0:1], v223\n" \
    "ds_read_b64_tr_b16 v[2:3], v223 offset:512\n" \
    "ds_read_b64_tr_b16 v[4:5], v223 offset:8192\n" \
    "ds_read_b64_tr_b16 v[6:7], v223 offset:8704\n" \
    "v_xor_b32 v223, 16384, v217\n" \
    "v_add_u32 v223, 32768, v223\n" \
    "ds_read_b64_tr_b16 v[8:9], v223\n" \
    "ds_read_b64_tr_b16 v[10:11], v223 offset:512\n" \
    "ds_read_b64_tr_b16 v[12:13], v223 offset:8192\n" \
    "ds_read_b64_tr_b16 v[14:15], v223 offset:8704\n" \
    "v_xor_b32 v223, 32768, v217\n" \
    "v_add_u32 v223, 32768, v223\n" \
    "ds_read_b64_tr_b16 v[16:17], v223\n" \
    "ds_read_b64_tr_b16 v[18:19], v223 offset:512\n" \
    "ds_read_b64_tr_b16 v[20:21], v223 offset:8192\n" \
    "ds_read_b64_tr_b16 v[22:23], v223 offset:8704\n" \
    "v_xor_b32 v223, 49152, v217\n" \
    "v_add_u32 v223, 32768, v223\n" \
    "ds_read_b64_tr_b16 v[24:25], v223\n" \
    "ds_read_b64_tr_b16 v[26:27], v223 offset:512\n" \
    "ds_read_b64_tr_b16 v[28:29], v223 offset:8192\n" \
    "ds_read_b64_tr_b16 v[30:31], v223 offset:8704\n" \
    "v_xor_b32 v223, 0, v218\n" \
    "v_add_u32 v223, 32768, v223\n" \
    "ds_read_b64_tr_b16 v[64:65], v223\n" \
    "ds_read_b64_tr_b16 v[66:67], v223 offset:512\n" \
    "ds_read_b64_tr_b16 v[68:69], v223 offset:8192\n" \
    "ds_read_b64_tr_b16 v[70:71], v223 offset:8704\n" \
    "v_xor_b32 v223, 16384, v218\n" \
    "v_add_u32 v223, 32768, v223\n" \
    "ds_read_b64_tr_b16 v[72:73], v223\n" \
    "ds_read_b64_tr_b16 v[74:75], v223 offset:512\n" \
    "ds_read_b64_tr_b16 v[76:77], v223 offset:8192\n" \
    "ds_read_b64_tr_b16 v[78:79], v223 offset:8704\n" \
    "v_xor_b32 v223, 32768, v218\n" \
    "v_add_u32 v223, 32768, v223\n" \
    "ds_read_b64_tr_b16 v[80:81], v223\n" \
    "ds_read_b64_tr_b16 v[82:83], v223 offset:512\n" \
    "ds_read_b64_tr_b16 v[84:85], v223 offset:8192\n" \
    "ds_read_b64_tr_b16 v[86:87], v223 offset:8704\n" \
    "v_xor_b32 v223, 49152, v218\n" \
    "v_add_u32 v223, 32768, v223\n" \
    "ds_read_b64_tr_b16 v[88:89], v223\n" \
    "ds_read_b64_tr_b16 v[90:91], v223 offset:512\n" \
    "ds_read_b64_tr_b16 v[92:93], v223 offset:8192\n" \
    "ds_read_b64_tr_b16 v[94:95], v223 offset:8704\n" \
    "s_waitcnt lgkmcnt(0)\n" \
    "s_barrier\n" \
    "s_mov_b32 s40, s66\n" \
    "s_and_b32 s41, s67, 0xffff\n" \
    "s_add_u32 m0, s51, 32768\n" \
    "s_mov_b32 s54, 0\n" \
    "buffer_load_dwordx4 v219, s[40:43], s54 offen lds\n" \
    "s_add_u32 m0, s51, 36864\n" \
    "s_mov_b32 s54, 8192\n" \
    "buffer_load_dwordx4 v219, s[40:43], s54 offen lds\n" \
    "s_add_u32 m0, s51, 40960\n" \
    "s_mov_b32 s54, 16384\n" \
    "buffer_load_dwordx4 v222, s[40:43], s54 offen lds\n" \
    "s_add_u32 m0, s51, 45056\n" \
    "s_mov_b32 s54, 24576\n" \
    "buffer_load_dwordx4 v222, s[40:43], s54 offen lds\n" \
    "s_add_u32 m0, s51, 49152\n" \
    "s_mov_b32 s54, 32768\n" \
    "buffer_load_dwordx4 v219, s[40:43], s54 offen lds\n" \
    "s_add_u32 m0, s51, 53248\n" \
    "s_mov_b32 s54, 40960\n" \
    "buffer_load_dwordx4 v219, s[40:43], s54 offen lds\n" \
    "s_add_u32 m0, s51, 57344\n" \
    "s_mov_b32 s54, 49152\n" \
    "buffer_load_dwordx4 v222, s[40:43], s54 offen lds\n" \
    "s_add_u32 m0, s51, 61440\n" \
    "s_mov_b32 s54, 57344\n" \
    "buffer_load_dwordx4 v222, s[40:43], s54 offen lds\n" \
    "s_add_u32 m0, s51, 65536\n" \
    "s_mov_b32 s54, 65536\n" \
    "buffer_load_dwordx4 v219, s[40:43], s54 offen lds\n" \
    "s_add_u32 m0, s51, 69632\n" \
    "s_mov_b32 s54, 73728\n" \
    "buffer_load_dwordx4 v219, s[40:43], s54 offen lds\n" \
    "s_add_u32 m0, s51, 73728\n" \
    "s_mov_b32 s54, 81920\n" \
    "buffer_load_dwordx4 v222, s[40:43], s54 offen lds\n" \
    "s_add_u32 m0, s51, 77824\n" \
    "s_mov_b32 s54, 90112\n" \
    "buffer_load_dwordx4 v222, s[40:43], s54 offen lds\n" \
    "s_add_u32 m0, s51, 81920\n" \
    "s_mov_b32 s54, 98304\n" \
    "buffer_load_dwordx4 v219, s[40:43], s54 offen lds\n" \
    "s_add_u32 m0, s51, 86016\n" \
    "s_mov_b32 s54, 106496\n" \
    "buffer_load_dwordx4 v219, s[40:43], s54 offen lds\n" \
    "s_add_u32 m0, s51, 90112\n" \
    "s_mov_b32 s54, 114688\n" \
    "buffer_load_dwordx4 v222, s[40:43], s54 offen lds\n" \
    "s_add_u32 m0, s51, 94208\n" \
    "s_mov_b32 s54, 122880\n" \
    "buffer_load_dwordx4 v222, s[40:43], s54 offen lds\n" \
    "s_waitcnt vmcnt(16)\n" \
    "s_barrier\n" \
    "v_xor_b32 v223, 0, v217\n" \
    "v_add_u32 v223, 98304, v223\n" \
    "ds_read_b64_tr_b16 v[32:33], v223\n" \
    "ds_read_b64_tr_b16 v[34:35], v223 offset:512\n" \
    "ds_read_b64_tr_b16 v[36:37], v223 offset:8192\n" \
    "ds_read_b64_tr_b16 v[38:39], v223 offset:8704\n" \
    "v_xor_b32 v223, 16384, v217\n" \
    "v_add_u32 v223, 98304, v223\n" \
    "ds_read_b64_tr_b16 v[40:41], v223\n" \
    "ds_read_b64_tr_b16 v[42:43], v223 offset:512\n" \
    "ds_read_b64_tr_b16 v[44:45], v223 offset:8192\n" \
    "ds_read_b64_tr_b16 v[46:47], v223 offset:8704\n" \
    "v_xor_b32 v223, 32768, v217\n" \
    "v_add_u32 v223, 98304, v223\n" \
    "ds_read_b64_tr_b16 v[48:49], v223\n" \
    "ds_read_b64_tr_b16 v[50:51], v223 offset:512\n" \
    "ds_read_b64_tr_b16 v[52:53], v223 offset:8192\n" \
    "ds_read_b64_tr_b16 v[54:55], v223 offset:8704\n" \
    "v_xor_b32 v223, 49152, v217\n" \
    "v_add_u32 v223, 98304, v223\n" \
    "ds_read_b64_tr_b16 v[56:57], v223\n" \
    "ds_read_b64_tr_b16 v[58:59], v223 offset:512\n" \
    "ds_read_b64_tr_b16 v[60:61], v223 offset:8192\n" \
    "ds_read_b64_tr_b16 v[62:63], v223 offset:8704\n" \
    "v_xor_b32 v223, 0, v218\n" \
    "v_add_u32 v223, 98304, v223\n" \
    "ds_read_b64_tr_b16 v[96:97], v223\n" \
    "ds_read_b64_tr_b16 v[98:99], v223 offset:512\n" \
    "ds_read_b64_tr_b16 v[100:101], v223 offset:8192\n" \
    "ds_read_b64_tr_b16 v[102:103], v223 offset:8704\n" \
    "v_xor_b32 v223, 16384, v218\n" \
    "v_add_u32 v223, 98304, v223\n" \
    "ds_read_b64_tr_b16 v[104:105], v223\n" \
    "ds_read_b64_tr_b16 v[106:107], v223 offset:512\n" \
    "ds_read_b64_tr_b16 v[108:109], v223 offset:8192\n" \
    "ds_read_b64_tr_b16 v[110:111], v223 offset:8704\n" \
    "v_xor_b32 v223, 32768, v218\n" \
    "v_add_u32 v223, 98304, v223\n" \
    "ds_read_b64_tr_b16 v[112:113], v223\n" \
    "ds_read_b64_tr_b16 v[114:115], v223 offset:512\n" \
    "ds_read_b64_tr_b16 v[116:117], v223 offset:8192\n" \
    "ds_read_b64_tr_b16 v[118:119], v223 offset:8704\n" \
    "v_xor_b32 v223, 49152, v218\n" \
    "v_add_u32 v223, 98304, v223\n" \
    "ds_read_b64_tr_b16 v[120:121], v223\n" \
    "ds_read_b64_tr_b16 v[122:123], v223 offset:512\n" \
    "ds_read_b64_tr_b16 v[124:125], v223 offset:8192\n" \
    "ds_read_b64_tr_b16 v[126:127], v223 offset:8704\n" \
    "s_waitcnt lgkmcnt(0)\n" \
    "s_barrier\n" \
    "s_mov_b32 s40, s60\n" \
    "s_and_b32 s41, s61, 0xffff\n" \
    "s_add_u32 m0, s51, 98304\n" \
    "s_mov_b32 s54, 0\n" \
    "buffer_load_dwordx4 v216, s[40:43], s54 offen lds\n" \
    "s_add_u32 m0, s51, 102400\n" \
    "s_mov_b32 s54, 131072\n" \
    "buffer_load_dwordx4 v216, s[40:43], s54 offen lds\n" \
    "s_add_u32 m0, s51, 106496\n" \
    "s_mov_b32 s54, 262144\n" \
    "buffer_load_dwordx4 v216, s[40:43], s54 offen lds\n" \
    "s_add_u32 m0, s51, 110592\n" \
    "s_mov_b32 s54, 393216\n" \
    "buffer_load_dwordx4 v216, s[40:43], s54 offen lds\n" \
    "s_add_u32 m0, s51, 114688\n" \
    "s_mov_b32 s54, 524288\n" \
    "buffer_load_dwordx4 v216, s[40:43], s54 offen lds\n" \
    "s_add_u32 m0, s51, 118784\n" \
    "s_mov_b32 s54, 655360\n" \
    "buffer_load_dwordx4 v216, s[40:43], s54 offen lds\n" \
    "s_add_u32 m0, s51, 122880\n" \
    "s_mov_b32 s54, 786432\n" \
    "buffer_load_dwordx4 v216, s[40:43], s54 offen lds\n" \
    "s_add_u32 m0, s51, 126976\n" \
    "s_mov_b32 s54, 917504\n" \
    "buffer_load_dwordx4 v216, s[40:43], s54 offen lds\n" \
    "s_add_u32 m0, s51, 131072\n" \
    "s_mov_b32 s54, 1048576\n" \
    "buffer_load_dwordx4 v216, s[40:43], s54 offen lds\n" \
    "s_add_u32 m0, s51, 135168\n" \
    "s_mov_b32 s54, 1179648\n" \
    "buffer_load_dwordx4 v216, s[40:43], s54 offen lds\n" \
    "s_add_u32 m0, s51, 139264\n" \
    "s_mov_b32 s54, 1310720\n" \
    "buffer_load_dwordx4 v216, s[40:43], s54 offen lds\n" \
    "s_add_u32 m0, s51, 143360\n" \
    "s_mov_b32 s54, 1441792\n" \
    "buffer_load_dwordx4 v216, s[40:43], s54 offen lds\n" \
    "s_add_u32 m0, s51, 147456\n" \
    "s_mov_b32 s54, 1572864\n" \
    "buffer_load_dwordx4 v216, s[40:43], s54 offen lds\n" \
    "s_add_u32 m0, s51, 151552\n" \
    "s_mov_b32 s54, 1703936\n" \
    "buffer_load_dwordx4 v216, s[40:43], s54 offen lds\n" \
    "s_add_u32 m0, s51, 155648\n" \
    "s_mov_b32 s54, 1835008\n" \
    "buffer_load_dwordx4 v216, s[40:43], s54 offen lds\n" \
    "s_add_u32 m0, s51, 159744\n" \
    "s_mov_b32 s54, 1966080\n" \
    "buffer_load_dwordx4 v216, s[40:43], s54 offen lds\n" \
    "s_waitcnt vmcnt(16)\n" \
    "s_barrier\n" \
    "v_xor_b32 v223, 0, v220\n" \
    "ds_read_b64_tr_b16 v[136:137], v223\n" \
    "ds_read_b64_tr_b16 v[138:139], v223 offset:1024\n" \
    "ds_read_b64_tr_b16 v[140:141], v223 offset:16384\n" \
    "ds_read_b64_tr_b16 v[142:143], v223 offset:17408\n" \
    "ds_read_b64_tr_b16 v[144:145], v223 offset:32768\n" \
    "ds_read_b64_tr_b16 v[146:147], v223 offset:33792\n" \
    "ds_read_b64_tr_b16 v[148:149], v223 offset:49152\n" \
    "ds_read_b64_tr_b16 v[150:151], v223 offset:50176\n" \
    "v_xor_b32 v223, 32, v220\n" \
    "ds_read_b64_tr_b16 v[152:153], v223\n" \
    "ds_read_b64_tr_b16 v[154:155], v223 offset:1024\n" \
    "ds_read_b64_tr_b16 v[156:157], v223 offset:16384\n" \
    "ds_read_b64_tr_b16 v[158:159], v223 offset:17408\n" \
    "ds_read_b64_tr_b16 v[160:161], v223 offset:32768\n" \
    "ds_read_b64_tr_b16 v[162:163], v223 offset:33792\n" \
    "ds_read_b64_tr_b16 v[164:165], v223 offset:49152\n" \
    "ds_read_b64_tr_b16 v[166:167], v223 offset:50176\n" \
    "v_xor_b32 v223, 64, v220\n" \
    "ds_read_b64_tr_b16 v[168:169], v223\n" \
    "ds_read_b64_tr_b16 v[170:171], v223 offset:1024\n" \
    "ds_read_b64_tr_b16 v[172:173], v223 offset:16384\n" \
    "ds_read_b64_tr_b16 v[174:175], v223 offset:17408\n" \
    "ds_read_b64_tr_b16 v[176:177], v223 offset:32768\n" \
    "ds_read_b64_tr_b16 v[178:179], v223 offset:33792\n" \
    "ds_read_b64_tr_b16 v[180:181], v223 offset:49152\n" \
    "ds_read_b64_tr_b16 v[182:183], v223 offset:50176\n" \
    "v_xor_b32 v223, 96, v220\n" \
    "ds_read_b64_tr_b16 v[184:185], v223\n" \
    "ds_read_b64_tr_b16 v[186:187], v223 offset:1024\n" \
    "ds_read_b64_tr_b16 v[188:189], v223 offset:16384\n" \
    "ds_read_b64_tr_b16 v[190:191], v223 offset:17408\n" \
    "ds_read_b64_tr_b16 v[192:193], v223 offset:32768\n" \
    "ds_read_b64_tr_b16 v[194:195], v223 offset:33792\n" \
    "ds_read_b64_tr_b16 v[196:197], v223 offset:49152\n" \
    "ds_read_b64_tr_b16 v[198:199], v223 offset:50176\n" \
    "s_waitcnt lgkmcnt(0)\n" \
    "s_barrier\n" \
    "v_lshlrev_b32 v206, 3, v253\n" \
    "s_mov_b32 s54, 2163712\n" \
    "buffer_load_dwordx2 v[204:205], v206, s[36:39], s54 offen sc1\n" \
    "s_add_u32 m0, s51, 32768\n" \
    "s_mov_b32 s54, 2097152\n" \
    "buffer_load_dwordx4 v216, s[40:43], s54 offen lds\n" \
    "s_add_u32 m0, s51, 36864\n" \
    "s_mov_b32 s54, 2228224\n" \
    "buffer_load_dwordx4 v216, s[40:43], s54 offen lds\n" \
    "s_add_u32 m0, s51, 40960\n" \
    "s_mov_b32 s54, 2359296\n" \
    "buffer_load_dwordx4 v216, s[40:43], s54 offen lds\n" \
    "s_add_u32 m0, s51, 45056\n" \
    "s_mov_b32 s54, 2490368\n" \
    "buffer_load_dwordx4 v216, s[40:43], s54 offen lds\n" \
    "s_add_u32 m0, s51, 49152\n" \
    "s_mov_b32 s54, 2621440\n" \
    "buffer_load_dwordx4 v216, s[40:43], s54 offen lds\n" \
    "s_add_u32 m0, s51, 53248\n" \
    "s_mov_b32 s54, 2752512\n" \
    "buffer_load_dwordx4 v216, s[40:43], s54 offen lds\n" \
    "s_add_u32 m0, s51, 57344\n" \
    "s_mov_b32 s54, 2883584\n" \
    "buffer_load_dwordx4 v216, s[40:43], s54 offen lds\n" \
    "s_add_u32 m0, s51, 61440\n" \
    "s_mov_b32 s54, 3014656\n" \
    "buffer_load_dwordx4 v216, s[40:43], s54 offen lds\n" \
    "s_add_u32 m0, s51, 65536\n" \
    "s_mov_b32 s54, 3145728\n" \
    "buffer_load_dwordx4 v216, s[40:43], s54 offen lds\n" \
    "s_add_u32 m0, s51, 69632\n" \
    "s_mov_b32 s54, 3276800\n" \
    "buffer_load_dwordx4 v216, s[40:43], s54 offen lds\n" \
    "s_add_u32 m0, s51, 73728\n" \
    "s_mov_b32 s54, 3407872\n" \
    "buffer_load_dwordx4 v216, s[40:43], s54 offen lds\n" \
    "s_add_u32 m0, s51, 77824\n" \
    "s_mov_b32 s54, 3538944\n" \
    "buffer_load_dwordx4 v216, s[40:43], s54 offen lds\n" \
    "s_add_u32 m0, s51, 81920\n" \
    "s_mov_b32 s54, 3670016\n" \
    "buffer_load_dwordx4 v216, s[40:43], s54 offen lds\n" \
    "s_add_u32 m0, s51, 86016\n" \
    "s_mov_b32 s54, 3801088\n" \
    "buffer_load_dwordx4 v216, s[40:43], s54 offen lds\n" \
    "s_add_u32 m0, s51, 90112\n" \
    "s_mov_b32 s54, 3932160\n" \
    "buffer_load_dwordx4 v216, s[40:43], s54 offen lds\n" \
    "s_add_u32 m0, s51, 94208\n" \
    "s_mov_b32 s54, 4063232\n" \
    "buffer_load_dwordx4 v216, s[40:43], s54 offen lds\n" \
    "s_waitcnt vmcnt(16)\n" \
    "s_barrier\n" \
    "v_xor_b32 v223, 0, v217\n" \
    "v_add_u32 v223, 98304, v223\n" \
    "ds_read_b64_tr_b16 a[0:1], v223\n" \
    "ds_read_b64_tr_b16 a[2:3], v223 offset:512\n" \
    "ds_read_b64_tr_b16 a[4:5], v223 offset:8192\n" \
    "ds_read_b64_tr_b16 a[6:7], v223 offset:8704\n" \
    "v_xor_b32 v223, 16384, v217\n" \
    "v_add_u32 v223, 98304, v223\n" \
    "ds_read_b64_tr_b16 a[8:9], v223\n" \
    "ds_read_b64_tr_b16 a[10:11], v223 offset:512\n" \
    "ds_read_b64_tr_b16 a[12:13], v223 offset:8192\n" \
    "ds_read_b64_tr_b16 a[14:15], v223 offset:8704\n" \
    "v_xor_b32 v223, 32768, v217\n" \
    "v_add_u32 v223, 98304, v223\n" \
    "ds_read_b64_tr_b16 a[16:17], v223\n" \
    "ds_read_b64_tr_b16 a[18:19], v223 offset:512\n" \
    "ds_read_b64_tr_b16 a[20:21], v223 offset:8192\n" \
    "ds_read_b64_tr_b16 a[22:23], v223 offset:8704\n" \
    "v_xor_b32 v223, 49152, v217\n" \
    "v_add_u32 v223, 98304, v223\n" \
    "ds_read_b64_tr_b16 a[24:25], v223\n" \
    "ds_read_b64_tr_b16 a[26:27], v223 offset:512\n" \
    "ds_read_b64_tr_b16 a[28:29], v223 offset:8192\n" \
    "ds_read_b64_tr_b16 a[30:31], v223 offset:8704\n" \
    "v_xor_b32 v223, 0, v218\n" \
    "v_add_u32 v223, 98304, v223\n" \
    "ds_read_b64_tr_b16 a[64:65], v223\n" \
    "ds_read_b64_tr_b16 a[66:67], v223 offset:512\n" \
    "ds_read_b64_tr_b16 a[68:69], v223 offset:8192\n" \
    "ds_read_b64_tr_b16 a[70:71], v223 offset:8704\n" \
    "v_xor_b32 v223, 16384, v218\n" \
    "v_add_u32 v223, 98304, v223\n" \
    "ds_read_b64_tr_b16 a[72:73], v223\n" \
    "ds_read_b64_tr_b16 a[74:75], v223 offset:512\n" \
    "ds_read_b64_tr_b16 a[76:77], v223 offset:8192\n" \
    "ds_read_b64_tr_b16 a[78:79], v223 offset:8704\n" \
    "v_xor_b32 v223, 32768, v218\n" \
    "v_add_u32 v223, 98304, v223\n" \
    "ds_read_b64_tr_b16 a[80:81], v223\n" \
    "ds_read_b64_tr_b16 a[82:83], v223 offset:512\n" \
    "ds_read_b64_tr_b16 a[84:85], v223 offset:8192\n" \
    "ds_read_b64_tr_b16 a[86:87], v223 offset:8704\n" \
    "v_xor_b32 v223, 49152, v218\n" \
    "v_add_u32 v223, 98304, v223\n" \
    "ds_read_b64_tr_b16 a[88:89], v223\n" \
    "ds_read_b64_tr_b16 a[90:91], v223 offset:512\n" \
    "ds_read_b64_tr_b16 a[92:93], v223 offset:8192\n" \
    "ds_read_b64_tr_b16 a[94:95], v223 offset:8704\n" \
    "s_waitcnt lgkmcnt(0)\n" \
    "s_barrier\n" \
    "v_add_u32 v234, 1, v234\n" \
    "v_add_u32 v235, 8, v235\n" \
    "v_or_b32 v235, 7, v235\n" \
    "s_mov_b32 s54, 2163712\n" \
    "WIa1024%=:\n" \
    "v_or_b32 v205, 7, v205\n" \
    "v_cmp_ne_u64 vcc, v[234:235], v[204:205]\n" \
    "s_cbranch_vccz WIb1024%=\n" \
    "s_sleep 2\n" \
    "buffer_load_dwordx2 v[204:205], v206, s[36:39], s54 offen sc1\n" \
    "s_waitcnt vmcnt(0)\n" \
    "s_add_u32 s57, s57, 1\n" \
    "s_cmp_lt_u32 s57, 262144\n" \
    "s_cbranch_scc1 WIa1024%=\n" \
    "WIb1024%=:\n" \
    "buffer_load_dwordx2 v[208:209], v241, s[36:39], s68 offen sc1\n" \
    "v_lshlrev_b32 v206, 3, v253\n" \
    "s_mov_b32 s54, 2179072\n" \
    "buffer_load_dwordx2 v[204:205], v206, s[36:39], s54 offen sc1\n" \
    "ds_write_b128 v221, v[136:139]\n" \
    "ds_write_b128 v221, v[140:143] offset:4096\n" \
    "ds_write_b128 v221, v[144:147] offset:8192\n" \
    "ds_write_b128 v221, v[148:151] offset:12288\n" \
    "ds_write_b128 v221, v[152:155] offset:64\n" \
    "ds_write_b128 v221, v[156:159] offset:4160\n" \
    "ds_write_b128 v221, v[160:163] offset:8256\n" \
    "ds_write_b128 v221, v[164:167] offset:12352\n" \
    "ds_write_b128 v221, v[168:171] offset:128\n" \
    "ds_write_b128 v221, v[172:175] offset:4224\n" \
    "ds_write_b128 v221, v[176:179] offset:8320\n" \
    "ds_write_b128 v221, v[180:183] offset:12416\n" \
    "ds_write_b128 v221, v[184:187] offset:192\n" \
    "ds_write_b128 v221, v[188:191] offset:4288\n" \
    "ds_write_b128 v221, v[192:195] offset:8384\n" \
    "ds_write_b128 v221, v[196:199] offset:12480\n" \
    "s_waitcnt lgkmcnt(0)\n" \
    "ds_read_b128 v[224:227], v247 offset:57344\n" \
    "ds_read_b128 v[228:231], v247 offset:61440\n" \
    "v_min_u32 v128, -2, v128\n" \
    "v_min_u32 v129, -2, v129\n" \
    "v_min_u32 v130, -2, v130\n" \
    "v_min_u32 v131, -2, v131\n" \
    "v_min_u32 v132, -2, v132\n" \
    "v_min_u32 v133, -2, v133\n" \
    "v_min_u32 v134, -2, v134\n" \
    "v_min_u32 v135, -2, v135\n" \
    "s_mov_b32 s24, s22\n" \
    "s_mov_b32 s25, s23\n" \
    "s_memtime s[20:21]\n" \
    "s_memrealtime s[4:5]\n" \
    "s_nop 2\n" \
    "s_memrealtime s[6:7]\n" \
    "s_nop 2\n" \
    "s_memrealtime s[8:9]\n" \
    "s_nop 2\n" \
    "s_memrealtime s[10:11]\n" \
    "s_nop 2\n" \
    "s_memrealtime s[12:13]\n" \
    "s_nop 2\n" \
    "s_memrealtime s[14:15]\n" \
    "s_nop 2\n" \
    "s_memrealtime s[16:17]\n" \
    "s_nop 2\n" \
    "s_memrealtime s[18:19]\n" \
    "s_mov_b32 s29, 0\n" \
    "s_waitcnt lgkmcnt(0)\n" \
    "s_cmp_eq_u32 s6, s4\n" \
    "s_addc_u32 s29, s29, 0\n" \
    "s_cmp_eq_u32 s8, s4\n" \
    "s_addc_u32 s29, s29, 0\n" \
    "s_cmp_eq_u32 s10, s4\n" \
    "s_addc_u32 s29, s29, 0\n" \
    "s_cmp_eq_u32 s12, s4\n" \
    "s_addc_u32 s29, s29, 0\n" \
    "s_cmp_eq_u32 s14, s4\n" \
    "s_addc_u32 s29, s29, 0\n" \
    "s_cmp_eq_u32 s16, s4\n" \
    "s_addc_u32 s29, s29, 0\n" \
    "s_cmp_eq_u32 s18, s4\n" \
    "s_addc_u32 s29, s29, 0\n" \
    "s_lshl_b32 s29, s29, 4\n" \
    "s_add_u32 s23, s20, s29\n" \
    "s_lshl_b32 s22, s4, 2\n" \
    "s_lshl_b32 s28, s22, 6\n" \
    "s_sub_u32 s29, s23, s25\n" \
    "s_sub_u32 s30, s22, s24\n" \
    "v_cvt_f32_u32 v232, s29\n" \
    "v_cvt_f32_u32 v233, s30\n" \
    "v_rcp_f32 v233, v233\n" \
    "v_or_b32 v243, v243, v243\n" \
    "v_mul_f32 v232, v232, v233\n" \
    "v_rcp_f32 v233, v232\n" \
    "s_nop 0\n" \
    "v_readfirstlane_b32 s26, v232\n" \
    "v_readfirstlane_b32 s27, v233\n" \
    "s_nop 1\n" \
    "s_waitcnt vmcnt(0)\n" \
    "s_barrier\n" \
    "s_mov_b32 s52, 8192\n" \
    "s_mov_b32 s48, 8192\n" \
    "s_mov_b32 s31, 0\n" \
    "KVa%=:\n" \
    "v_and_b32 v210, v208, v209\n" \
    "v_cmp_ne_u32 vcc, 1, v210\n" \
    "s_cbranch_vccnz KVc%=\n" \
    "s_add_u32 m0, s52, 0\n" \
    "s_add_u32 s55, s49, 0\n" \
    "buffer_load_dwordx4 v253, s[36:39], s55 offen sc1 lds\n" \
    "s_add_u32 m0, s52, 1024\n" \
    "s_add_u32 s55, s49, 1024\n" \
    "buffer_load_dwordx4 v253, s[36:39], s55 offen sc1 lds\n" \
    "s_add_u32 m0, s52, 2048\n" \
    "s_add_u32 s55, s49, 2048\n" \
    "buffer_load_dwordx4 v253, s[36:39], s55 offen sc1 lds\n" \
    "s_add_u32 m0, s52, 3072\n" \
    "s_add_u32 s55, s49, 3072\n" \
    "buffer_load_dwordx4 v253, s[36:39], s55 offen sc1 lds\n" \
    "s_mov_b32 s31, 1\n" \
    "KVc%=:\n" \
    "v_xor_b32 v223, 0, v217\n" \
    "v_add_u32 v223, 32768, v223\n" \
    "ds_read_b64_tr_b16 a[32:33], v223\n" \
    "ds_read_b64_tr_b16 a[34:35], v223 offset:512\n" \
    "ds_read_b64_tr_b16 a[36:37], v223 offset:8192\n" \
    "ds_read_b64_tr_b16 a[38:39], v223 offset:8704\n" \
    "v_xor_b32 v223, 16384, v217\n" \
    "v_add_u32 v223, 32768, v223\n" \
    "ds_read_b64_tr_b16 a[40:41], v223\n" \
    "ds_read_b64_tr_b16 a[42:43], v223 offset:512\n" \
    "ds_read_b64_tr_b16 a[44:45], v223 offset:8192\n" \
    "ds_read_b64_tr_b16 a[46:47], v223 offset:8704\n" \
    "v_xor_b32 v223, 32768, v217\n" \
    "v_add_u32 v223, 32768, v223\n" \
    "ds_read_b64_tr_b16 a[48:49], v223\n" \
    "ds_read_b64_tr_b16 a[50:51], v223 offset:512\n" \
    "ds_read_b64_tr_b16 a[52:53], v223 offset:8192\n" \
    "ds_read_b64_tr_b16 a[54:55], v223 offset:8704\n" \
    "v_xor_b32 v223, 49152, v217\n" \
    "v_add_u32 v223, 32768, v223\n" \
    "ds_read_b64_tr_b16 a[56:57], v223\n" \
    "ds_read_b64_tr_b16 a[58:59], v223 offset:512\n" \
    "ds_read_b64_tr_b16 a[60:61], v223 offset:8192\n" \
    "ds_read_b64_tr_b16 a[62:63], v223 offset:8704\n" \
    "v_xor_b32 v223, 0, v218\n" \
    "v_add_u32 v223, 32768, v223\n" \
    "ds_read_b64_tr_b16 a[96:97], v223\n" \
    "ds_read_b64_tr_b16 a[98:99], v223 offset:512\n" \
    "ds_read_b64_tr_b16 a[100:101], v223 offset:8192\n" \
    "ds_read_b64_tr_b16 a[102:103], v223 offset:8704\n" \
    "v_xor_b32 v223, 16384, v218\n" \
    "v_add_u32 v223, 32768, v223\n" \
    "ds_read_b64_tr_b16 a[104:105], v223\n" \
    "ds_read_b64_tr_b16 a[106:107], v223 offset:512\n" \
    "ds_read_b64_tr_b16 a[108:109], v223 offset:8192\n" \
    "ds_read_b64_tr_b16 a[110:111], v223 offset:8704\n" \
    "v_xor_b32 v223, 32768, v218\n" \
    "v_add_u32 v223, 32768, v223\n" \
    "ds_read_b64_tr_b16 a[112:113], v223\n" \
    "ds_read_b64_tr_b16 a[114:115], v223 offset:512\n" \
    "ds_read_b64_tr_b16 a[116:117], v223 offset:8192\n" \
    "ds_read_b64_tr_b16 a[118:119], v223 offset:8704\n" \
    "v_xor_b32 v223, 49152, v218\n" \
    "v_add_u32 v223, 32768, v223\n" \
    "ds_read_b64_tr_b16 a[120:121], v223\n" \
    "ds_read_b64_tr_b16 a[122:123], v223 offset:512\n" \
    "ds_read_b64_tr_b16 a[124:125], v223 offset:8192\n" \
    "ds_read_b64_tr_b16 a[126:127], v223 offset:8704\n" \
    "s_waitcnt lgkmcnt(0)\n" \
    "s_barrier\n" \
    "s_cmp_eq_u32 s31, 1\n" \
    "s_cbranch_scc1 KWc%=\n" \
    "KWa%=:\n" \
    "v_and_b32 v210, v208, v209\n" \
    "v_cmp_ne_u32 vcc, 1, v210\n" \
    "s_cbranch_vccz KWb%=\n" \
    "s_sleep 4\n" \
    "buffer_load_dwordx2 v[208:209], v241, s[36:39], s68 offen sc1\n" \
    "s_waitcnt vmcnt(0)\n" \
    "s_add_u32 s57, s57, 1\n" \
    "s_cmp_lt_u32 s57, 262144\n" \
    "s_cbranch_scc1 KWa%=\n" \
    "KWb%=:\n" \
    "s_add_u32 m0, s52, 0\n" \
    "s_add_u32 s55, s49, 0\n" \
    "buffer_load_dwordx4 v253, s[36:39], s55 offen sc1 lds\n" \
    "s_add_u32 m0, s52, 1024\n" \
    "s_add_u32 s55, s49, 1024\n" \
    "buffer_load_dwordx4 v253, s[36:39], s55 offen sc1 lds\n" \
    "s_add_u32 m0, s52, 2048\n" \
    "s_add_u32 s55, s49, 2048\n" \
    "buffer_load_dwordx4 v253, s[36:39], s55 offen sc1 lds\n" \
    "s_add_u32 m0, s52, 3072\n" \
    "s_add_u32 s55, s49, 3072\n" \
    "buffer_load_dwordx4 v253, s[36:39], s55 offen sc1 lds\n" \
    "s_mov_b32 s31, 1\n" \
    "KWc%=:\n" \
    "s_mov_b32 s54, 2179072\n" \
    "WNa%=:\n" \
    "v_mov_b32 v207, v205\n" \
    "v_or_b32 v205, 7, v205\n" \
    "v_cmp_ne_u64 vcc, v[234:235], v[204:205]\n" \
    "s_cbranch_vccz WNb%=\n" \
    "s_sleep 2\n" \
    "buffer_load_dwordx2 v[204:205], v206, s[36:39], s54 offen sc1\n" \
    "s_waitcnt vmcnt(0)\n" \
    "s_add_u32 s57, s57, 1\n" \
    "s_cmp_lt_u32 s57, 262144\n" \
    "s_cbranch_scc1 WNa%=\n" \
    "WNb%=:\n" \
    "s_mov_b32 s32, 0\n" \
    "v_and_b32 v204, 1, v207\n" \
    "v_cmp_ne_u32 vcc, 0, v204\n" \
    "s_bcnt1_i32_b64 s54, vcc\n" \
    "s_cmp_gt_u32 s54, 32\n" \
    "s_cselect_b32 s54, 4096, 0\n" \
    "s_or_b32 s32, s32, s54\n" \
    "v_and_b32 v204, 2, v207\n" \
    "v_cmp_ne_u32 vcc, 0, v204\n" \
    "s_bcnt1_i32_b64 s54, vcc\n" \
    "s_cmp_gt_u32 s54, 32\n" \
    "s_cselect_b32 s54, 8192, 0\n" \
    "s_or_b32 s32, s32, s54\n" \
    "v_and_b32 v204, 4, v207\n" \
    "v_cmp_ne_u32 vcc, 0, v204\n" \
    "s_bcnt1_i32_b64 s54, vcc\n" \
    "s_cmp_gt_u32 s54, 32\n" \
    "s_cselect_b32 s54, 16384, 0\n" \
    "s_or_b32 s32, s32, s54\n" \
    "s_xor_b32 s45, s45, s32\n" \
    "s_xor_b32 s46, s46, s32\n" \
    "s_mov_b32 exec_hi, 0\n" \
    "v_xor_b32 v245, s32, v245\n" \
    "s_mov_b32 exec_hi, -1\n" \
    "s_mov_b32 s40, s36\n" \
    "s_mov_b32 s41, s37\n" \
    "s_mov_b32 s43, s39\n" \
    "s_mov_b32 s60, s36\n" \
    "s_mov_b32 s61, s37\n" \
    "s_mov_b32 s63, s39\n" \
    "s_cmp_eq_u32 s58, 0\n" \
    "s_cselect_b32 s42, s38, 0\n" \
    "s_cselect_b32 s62, 0, s38\n" \
    "s_mov_b32 s33, 1750\n" \
    "s_mov_b32 s31, 179\n" \
    "s_mov_b32 s65, 0\n" \
    "v_mov_b64 v[160:161], 0\n" \
    "v_mov_b64 v[162:163], 0\n" \
    "v_mov_b64 v[164:165], 0\n" \
    "v_mov_b64 v[166:167], 0\n" \
    "v_mov_b64 v[168:169], 0\n" \
    "v_mov_b64 v[170:171], 0\n" \
    "v_mov_b64 v[172:173], 0\n" \
    "v_mov_b64 v[174:175], 0\n" \
    "v_mov_b64 v[176:177], 0\n" \
    "v_mov_b64 v[178:179], 0\n" \
    "v_mov_b64 v[180:181], 0\n" \
    "v_mov_b64 v[182:183], 0\n" \
    "v_xor_b32 v136, 2048, v255\n" \
    "v_xor_b32 v140, 3072, v255\n" \
    "v_xor_b32 v144, 4096, v255\n" \
    "v_xor_b32 v148, 5120, v255\n" \
    "v_xor_b32 v152, 6144, v255\n" \
    "v_xor_b32 v156, 7168, v255\n" \
    "v_add_u32 v216, s48, v253\n" \
    "v_add_u32 v220, s48, v255\n" \
    "s_xor_b32 s52, s48, 32768\n" \
    "s_add_u32 s49, s49, 16384\n" \
    "s_add_u32 m0, s52, 0\n" \
    "s_add_u32 s55, s49, 0\n" \
    "buffer_load_dwordx4 v253, s[36:39], s55 offen sc1 lds\n" \
    "s_add_u32 m0, s52, 1024\n" \
    "s_add_u32 s55, s49, 1024\n" \
    "buffer_load_dwordx4 v253, s[36:39], s55 offen sc1 lds\n" \
    "s_add_u32 m0, s52, 2048\n" \
    "s_add_u32 s55, s49, 2048\n" \
    "buffer_load_dwordx4 v253, s[36:39], s55 offen sc1 lds\n" \
    "s_add_u32 m0, s52, 3072\n" \
    "s_add_u32 s55, s49, 3072\n" \
    "buffer_load_dwordx4 v253, s[36:39], s55 offen sc1 lds\n" \
    "HEAD0%=:\n" \
    "s_waitcnt vmcnt(5)\n" \
    "ds_write_b128 v255, v[128:131]\n" \
    "v_smfmac_f32_16x16x64_bf16 v[160:163], v[128:131], a[0:7], v254\n" \
    "s_waitcnt vmcnt(4)\n" \
    "ds_write_b128 v255, v[132:135] offset:1024\n" \
    "v_max3_u32 v232, v128, v129, v130\n" \
    "v_smfmac_f32_16x16x64_bf16 v[168:171], v[128:131], a[128:135], v254\n" \
    "v_max3_u32 v240, v132, v133, v134\n" \
    "v_smfmac_f32_16x16x64_bf16 v[164:167], v[128:131], a[64:71], v254\n" \
    "v_max3_u32 v232, v232, v131, v135\n" \
    "v_smfmac_f32_16x16x64_bf16 v[172:175], v[128:131], a[192:199], v254\n" \
    "v_max_u32 v232, v232, v240\n" \
    "v_smfmac_f32_16x16x64_bf16 v[160:163], v[132:135], a[8:15], v254\n" \
    "v_cmp_eq_u32 vcc, -1, v232\n" \
    "v_smfmac_f32_16x16x64_bf16 v[168:171], v[132:135], a[136:143], v254\n" \
    "s_cbranch_vccnz RETRY2%=\n" \
    "OK2%=:\n" \
    "s_waitcnt lgkmcnt(0)\n" \
    "s_barrier\n" \
    "ds_read_b128 v[136:139], v136\n" \
    "ds_read_b128 v[140:143], v140\n" \
    "v_smfmac_f32_16x16x64_bf16 v[164:167], v[132:135], a[72:79], v254\n" \
    "ds_read_b128 v[144:147], v144\n" \
    "v_smfmac_f32_16x16x64_bf16 v[172:175], v[132:135], a[200:207], v254\n" \
    "ds_read_b128 v[148:151], v148\n" \
    "v_smfmac_f32_16x16x64_bf16 v[176:179], v[128:131], v[0:7], v254\n" \
    "ds_read_b128 v[152:155], v152\n" \
    "v_smfmac_f32_16x16x64_bf16 v[180:183], v[128:131], v[64:71], v254\n" \
    "ds_read_b128 v[156:159], v156\n" \
    "v_smfmac_f32_16x16x64_bf16 v[176:179], v[132:135], v[8:15], v254\n" \
    "v_smfmac_f32_16x16x64_bf16 v[180:183], v[132:135], v[72:79], v254\n" \
    "CONT%=:\n" \
    "s_waitcnt lgkmcnt(5)\n" \
    "v_smfmac_f32_16x16x64_bf16 v[160:163], v[136:139], a[16:23], v254\n" \
    "s_waitcnt lgkmcnt(4)\n" \
    "v_smfmac_f32_16x16x64_bf16 v[160:163], v[140:143], a[24:31], v254\n" \
    "s_waitcnt lgkmcnt(3)\n" \
    "v_smfmac_f32_16x16x64_bf16 v[160:163], v[144:147], a[32:39], v254\n" \
    "s_waitcnt lgkmcnt(2)\n" \
    "v_smfmac_f32_16x16x64_bf16 v[160:163], v[148:151], a[40:47], v254\n" \
    "s_waitcnt lgkmcnt(1)\n" \
    "v_smfmac_f32_16x16x64_bf16 v[160:163], v[152:155], a[48:55], v254\n" \
    "s_waitcnt lgkmcnt(0)\n" \
    "v_smfmac_f32_16x16x64_bf16 v[160:163], v[156:159], a[56:63], v254\n" \
    "v_smfmac_f32_16x16x64_bf16 v[168:171], v[136:139], a[144:151], v254\n" \
    "buffer_store_dword v244, v246, s[36:39], s35 offen sc1\n" \
    "v_smfmac_f32_16x16x64_bf16 v[168:171], v[140:143], a[152:159], v254\n" \
    "ds_read_b128 v[184:187], v216\n" \
    "v_or_b32 v243, v243, v243\n" \
    "v_smfmac_f32_16x16x64_bf16 v[168:171], v[144:147], a[160:167], v254\n" \
    "ds_read_b128 v[188:191], v216 offset:1024\n" \
    "v_or_b32 v243, v243, v243\n" \
    "v_smfmac_f32_16x16x64_bf16 v[168:171], v[148:151], a[168:175], v254\n" \
    "ds_read_b128 v[192:195], v216 offset:2048\n" \
    "v_add_f32 v160, v160, v161\n" \
    "v_smfmac_f32_16x16x64_bf16 v[168:171], v[152:155], a[176:183], v254\n" \
    "ds_read_b128 v[196:199], v216 offset:3072\n" \
    "v_add_f32 v162, v162, v163\n" \
    "v_smfmac_f32_16x16x64_bf16 v[168:171], v[156:159], a[184:191], v254\n" \
    "ds_read_b128 v[200:203], v216 offset:4096\n" \
    "v_cvt_pk_bf16_f32 v161, v160, v162\n" \
    "v_smfmac_f32_16x16x64_bf16 v[164:167], v[136:139], a[80:87], v254\n" \
    "ds_read_b128 v[204:207], v216 offset:5120\n" \
    "v_or_b32 v243, v243, v243\n" \
    "v_smfmac_f32_16x16x64_bf16 v[164:167], v[140:143], a[88:95], v254\n" \
    "ds_read_b128 v[208:211], v216 offset:6144\n" \
    "v_or_b32 v243, v243, v243\n" \
    "v_smfmac_f32_16x16x64_bf16 v[164:167], v[144:147], a[96:103], v254\n" \
    "ds_read_b128 v[212:215], v216 offset:7168\n" \
    "v_or_b32 v243, v243, v243\n" \
    "v_smfmac_f32_16x16x64_bf16 v[164:167], v[148:151], a[104:111], v254\n" \
    "ds_read_b128 v[216:219], v220 offset:8192\n" \
    "v_add_f32 v168, v168, v169\n" \
    "v_smfmac_f32_16x16x64_bf16 v[164:167], v[152:155], a[112:119], v254\n" \
    "ds_read_b128 v[220:223], v220 offset:9216\n" \
    "v_add_f32 v170, v170, v171\n" \
    "v_smfmac_f32_16x16x64_bf16 v[164:167], v[156:159], a[120:127], v254\n" \
    "v_cvt_pk_bf16_f32 v160, v168, v170\n" \
    "v_smfmac_f32_16x16x64_bf16 v[172:175], v[136:139], a[208:215], v254\n" \
    "ds_write_b64 v251, v[160:161]\n" \
    "v_smfmac_f32_16x16x64_bf16 v[172:175], v[140:143], a[216:223], v254\n" \
    "ds_read_b128 v[128:131], v247\n" \
    "v_or_b32 v243, v243, v243\n" \
    "v_smfmac_f32_16x16x64_bf16 v[172:175], v[144:147], a[224:231], v254\n" \
    "ds_read_b128 v[132:135], v247 offset:4096\n" \
    "v_or_b32 v243, v243, v243\n" \
    "v_smfmac_f32_16x16x64_bf16 v[172:175], v[148:151], a[232:239], v254\n" \
    "v_add_f32 v164, v164, v165\n" \
    "v_smfmac_f32_16x16x64_bf16 v[172:175], v[152:155], a[240:247], v254\n" \
    "v_add_f32 v166, v166, v167\n" \
    "v_smfmac_f32_16x16x64_bf16 v[172:175], v[156:159], a[248:255], v254\n" \
    "v_cvt_pk_bf16_f32 v165, v164, v166\n" \
    "v_smfmac_f32_16x16x64_bf16 v[176:179], v[136:139], v[16:23], v254\n" \
    "s_nop 1\n" \
    "v_or_b32 v243, v243, v243\n" \
    "v_smfmac_f32_16x16x64_bf16 v[180:183], v[136:139], v[80:87], v254\n" \
    "s_nop 1\n" \
    "v_add_f32 v172, v172, v173\n" \
    "v_add_f32 v174, v174, v175\n" \
    "v_cvt_pk_bf16_f32 v164, v172, v174\n" \
    "v_smfmac_f32_16x16x64_bf16 v[176:179], v[140:143], v[24:31], v254\n" \
    "ds_write_b64 v251, v[164:165] offset:512\n" \
    "s_cmp_eq_u32 s44, 63\n" \
    "v_smfmac_f32_16x16x64_bf16 v[180:183], v[140:143], v[88:95], v254\n" \
    "v_or_b32 v243, v243, v243\n" \
    "v_smfmac_f32_16x16x64_bf16 v[176:179], v[144:147], v[32:39], v254\n" \
    "v_or_b32 v243, v243, v243\n" \
    "v_smfmac_f32_16x16x64_bf16 v[180:183], v[144:147], v[96:103], v254\n" \
    "v_or_b32 v243, v243, v243\n" \
    "v_smfmac_f32_16x16x64_bf16 v[176:179], v[148:151], v[40:47], v254\n" \
    "v_or_b32 v243, v243, v243\n" \
    "v_smfmac_f32_16x16x64_bf16 v[180:183], v[148:151], v[104:111], v254\n" \
    "s_waitcnt lgkmcnt(0)\n" \
    "s_barrier\n" \
    "ds_read_b64_tr_b16 v[160:161], v250\n" \
    "ds_read_b64_tr_b16 v[162:163], v250 offset:512\n" \
    "s_cbranch_scc1 TAIL%=\n" \
    "v_smfmac_f32_16x16x64_bf16 v[176:179], v[152:155], v[48:55], v254\n" \
    "ds_read_b64_tr_b16 v[164:165], v250 offset:1024\n" \
    "ds_read_b64_tr_b16 v[166:167], v250 offset:1536\n" \
    "v_smfmac_f32_16x16x64_bf16 v[180:183], v[152:155], v[112:119], v254\n" \
    "ds_read_b64_tr_b16 v[168:169], v250 offset:2048\n" \
    "ds_read_b64_tr_b16 v[170:171], v250 offset:2560\n" \
    "v_smfmac_f32_16x16x64_bf16 v[176:179], v[156:159], v[56:63], v254\n" \
    "ds_read_b64_tr_b16 v[172:173], v250 offset:3072\n" \
    "ds_read_b64_tr_b16 v[174:175], v250 offset:3584\n" \
    "v_smfmac_f32_16x16x64_bf16 v[180:183], v[156:159], v[120:127], v254\n" \
    "v_or_b32 v243, v243, v243\n" \
    "s_waitcnt lgkmcnt(6)\n" \
    "v_mfma_f32_16x16x32_bf16 v[184:187], v[184:187], v[160:163], 0\n" \
    "ds_read_b128 v[136:139], v247 offset:8192\n" \
    "v_or_b32 v243, v243, v243\n" \
    "v_mfma_f32_16x16x32_bf16 v[200:203], v[200:203], v[160:163], 0\n" \
    "ds_read_b128 v[140:143], v247 offset:12288\n" \
    "v_or_b32 v243, v243, v243\n" \
    "s_waitcnt lgkmcnt(6)\n" \
    "v_mfma_f32_16x16x32_bf16 v[184:187], v[188:191], v[164:167], v[184:187]\n" \
    "ds_read_b128 v[144:147], v247 offset:16384\n" \
    "v_or_b32 v243, v243, v243\n" \
    "v_mfma_f32_16x16x32_bf16 v[200:203], v[204:207], v[164:167], v[200:203]\n" \
    "ds_read_b128 v[148:151], v247 offset:20480\n" \
    "v_add_f32 v176, v176, v177\n" \
    "s_waitcnt lgkmcnt(6)\n" \
    "v_mfma_f32_16x16x32_bf16 v[184:187], v[192:195], v[168:171], v[184:187]\n" \
    "ds_read_b128 v[152:155], v247 offset:24576\n" \
    "v_add_f32 v178, v178, v179\n" \
    "v_mfma_f32_16x16x32_bf16 v[200:203], v[208:211], v[168:171], v[200:203]\n" \
    "ds_read_b128 v[156:159], v247 offset:28672\n" \
    "v_add_f32 v180, v180, v181\n" \
    "s_waitcnt lgkmcnt(6)\n" \
    "v_mfma_f32_16x16x32_bf16 v[184:187], v[196:199], v[172:175], v[184:187]\n" \
    "v_add_f32 v182, v182, v183\n" \
    "v_cvt_pk_bf16_f32 v176, v176, v178\n" \
    "v_mfma_f32_16x16x32_bf16 v[200:203], v[212:215], v[172:175], v[200:203]\n" \
    "buffer_store_dwordx4 v[160:163], v253, s[40:43], s50 offen sc1\n" \
    "v_cvt_pk_bf16_f32 v177, v180, v182\n" \
    "v_or_b32 v243, v243, v243\n" \
    "v_mfma_f32_16x16x32_bf16 v[160:163], v[160:163], v[160:163], 0\n" \
    "ds_read_b128 v[188:191], v247 offset:32768\n" \
    "v_cvt_pk_bf16_f32 v178, v180, v182\n" \
    "v_mfma_f32_16x16x32_bf16 v[160:163], v[164:167], v[164:167], v[160:163]\n" \
    "ds_read_b128 v[192:195], v247 offset:36864\n" \
    "v_mov_b32 v179, 0\n" \
    "v_mfma_f32_16x16x32_bf16 v[160:163], v[168:171], v[168:171], v[160:163]\n" \
    "ds_read_b128 v[204:207], v247 offset:40960\n" \
    "v_max3_f32 v232, v184, v185, v186\n" \
    "v_mfma_f32_16x16x32_bf16 v[160:163], v[172:175], v[172:175], v[160:163]\n" \
    "ds_read_b128 v[208:211], v247 offset:45056\n" \
    "ds_read_b128 v[164:167], v247 offset:49152\n" \
    "ds_read_b128 v[168:171], v247 offset:53248\n" \
    "v_max3_f32 v233, v200, v201, v202\n" \
    "v_max3_f32 v232, v232, v187, v203\n" \
    "v_max_f32 v232, v232, v233\n" \
    "v_mov_b32 v172, 0\n" \
    "v_mov_b32 v173, 0\n" \
    "v_max3_f32 v234, v232, v160, v161\n" \
    "v_max3_f32 v235, v232, v160, v161\n" \
    "v_mov_b32 v174, 0\n" \
    "v_mov_b32 v175, 0\n" \
    "v_permlane32_swap_b32 v234, v235\n" \
    "v_max_f32 v232, v234, v235\n" \
    "v_max_f32 v233, v234, v235\n" \
    "v_or_b32 v243, v243, v243\n" \
    "v_or_b32 v243, v243, v243\n" \
    "v_permlane16_swap_b32 v232, v233\n" \
    "v_max_f32 v234, v232, v233\n" \
    "v_mul_f32 v234, s53, v234\n" \
    "v_pk_fma_f32 v[160:161], v[160:161], s[52:53], v[234:235] op_sel:[0,1,0] op_sel_hi:[1,1,0] neg_lo:[0,0,1] neg_hi:[0,0,1]\n" \
    "v_pk_fma_f32 v[184:185], v[184:185], s[52:53], v[234:235] op_sel:[0,1,0] op_sel_hi:[1,1,0] neg_lo:[0,0,1] neg_hi:[0,0,1]\n" \
    "v_pk_fma_f32 v[186:187], v[186:187], s[52:53], v[234:235] op_sel:[0,1,0] op_sel_hi:[1,1,0] neg_lo:[0,0,1] neg_hi:[0,0,1]\n" \
    "v_pk_fma_f32 v[200:201], v[200:201], s[52:53], v[234:235] op_sel:[0,1,0] op_sel_hi:[1,1,0] neg_lo:[0,0,1] neg_hi:[0,0,1]\n" \
    "v_pk_fma_f32 v[202:203], v[202:203], s[52:53], v[234:235] op_sel:[0,1,0] op_sel_hi:[1,1,0] neg_lo:[0,0,1] neg_hi:[0,0,1]\n" \
    "v_exp_f32 v160, v160\n" \
    "v_exp_f32 v161, v161\n" \
    "v_exp_f32 v184, v184\n" \
    "v_cvt_pk_bf16_f32 v240, v160, v161\n" \
    "v_exp_f32 v185, v185\n" \
    "v_exp_f32 v186, v186\n" \
    "v_mfma_f32_16x16x16_bf16 v[212:215], v[236:237], v[240:241], 0\n" \
    "v_exp_f32 v187, v187\n" \
    "v_mfma_f32_16x16x16_bf16 v[180:183], v[176:177], v[240:241], 0\n" \
    "v_exp_f32 v200, v200\n" \
    "v_mfma_f32_16x16x16_bf16 v[232:235], v[178:179], v[240:241], 0\n" \
    "v_exp_f32 v201, v201\n" \
    "v_exp_f32 v202, v202\n" \
    "v_exp_f32 v203, v203\n" \
    "v_cvt_pk_bf16_f32 v196, v184, v185\n" \
    "v_cvt_pk_bf16_f32 v197, v186, v187\n" \
    "v_cvt_pk_bf16_f32 v198, v200, v201\n" \
    "v_cvt_pk_bf16_f32 v199, v202, v203\n" \
    "v_mov_b32 v184, 0\n" \
    "v_mov_b32 v185, 0\n" \
    "v_mfma_f32_16x16x32_bf16 v[212:215], v[236:239], v[196:199], v[212:215]\n" \
    "buffer_load_dword v242, v245, s[36:39], s59 offen sc1\n" \
    "v_mov_b32 v186, 0\n" \
    "v_mov_b32 v187, 0\n" \
    "v_mfma_f32_16x16x32_bf16 v[180:183], v[216:219], v[196:199], v[180:183]\n" \
    "v_mov_b32 v200, 0\n" \
    "v_mov_b32 v201, 0\n" \
    "v_mfma_f32_16x16x32_bf16 v[232:235], v[220:223], v[196:199], v[232:235]\n" \
    "v_mov_b32 v202, 0\n" \
    "v_mov_b32 v203, 0\n" \
    "v_rcp_f32 v240, v212\n" \
    "v_mov_b32 v160, 0\n" \
    "v_mov_b32 v161, 0\n" \
    "v_mov_b32 v162, 0\n" \
    "v_mov_b32 v163, 0\n" \
    "v_pk_mul_f32 v[180:181], v[180:181], v[240:241] op_sel_hi:[1,0]\n" \
    "v_pk_mul_f32 v[182:183], v[182:183], v[240:241] op_sel_hi:[1,0]\n" \
    "v_pk_mul_f32 v[232:233], v[232:233], v[240:241] op_sel_hi:[1,0]\n" \
    "v_pk_mul_f32 v[234:235], v[234:235], v[240:241] op_sel_hi:[1,0]\n" \
    "v_cvt_pk_bf16_f32 v196, v180, v181\n" \
    "v_cvt_pk_bf16_f32 v197, v182, v183\n" \
    "v_cvt_pk_bf16_f32 v198, v232, v233\n" \
    "v_cvt_pk_bf16_f32 v199, v234, v235\n" \
    "ds_write_b128 v249, v[196:199]\n" \
    "v_lshrrev_b32 v240, 1, v253\n" \
    "buffer_store_dwordx2 v[176:177], v240, s[60:63], s50 offen sc1\n" \
    "s_waitcnt lgkmcnt(0)\n" \
    "s_barrier\n" \
    "ds_read_b128 v[216:219], v253 offset:28672\n" \
    "ds_read_b128 v[220:223], v253 offset:29696\n" \
    "s_waitcnt lgkmcnt(1)\n" \
    "v_smfmac_f32_16x16x64_bf16 v[184:187], v[216:219], v[128:135], v254\n" \
    "s_waitcnt lgkmcnt(0)\n" \
    "s_nop 0\n" \
    "v_smfmac_f32_16x16x64_bf16 v[184:187], v[220:223], v[136:143], v254\n" \
    "s_nop 1\n" \
    "s_nop 0\n" \
    "v_smfmac_f32_16x16x64_bf16 v[200:203], v[216:219], v[144:151], v254\n" \
    "s_nop 1\n" \
    "s_nop 0\n" \
    "v_smfmac_f32_16x16x64_bf16 v[200:203], v[220:223], v[152:159], v254\n" \
    "v_add_f32 v184, v184, v185\n" \
    "v_smfmac_f32_16x16x64_bf16 v[160:163], v[216:219], v[188:195], v254\n" \
    "s_nop 1\n" \
    "v_add_f32 v186, v186, v187\n" \
    "v_smfmac_f32_16x16x64_bf16 v[160:163], v[220:223], v[204:211], v254\n" \
    "s_nop 0\n" \
    "v_cvt_pk_bf16_f32 v184, v184, v186\n" \
    "v_smfmac_f32_16x16x64_bf16 v[172:175], v[216:219], v[164:171], v254\n" \
    "v_add_f32 v200, v200, v201\n" \
    "v_smfmac_f32_16x16x64_bf16 v[172:175], v[220:223], v[224:231], v254\n" \
    "v_add_f32 v202, v202, v203\n" \
    "v_min_u32 v212, -2, v184\n" \
    "v_cvt_pk_bf16_f32 v200, v200, v202\n" \
    "v_add_f32 v160, v160, v161\n" \
    "v_add_f32 v162, v162, v163\n" \
    "v_min_u32 v213, -2, v200\n" \
    "v_cvt_pk_bf16_f32 v160, v160, v162\n" \
    "v_min_u32 v214, -2, v160\n" \
    "v_add_f32 v172, v172, v173\n" \
    "v_add_f32 v174, v174, v175\n" \
    "v_cvt_pk_bf16_f32 v172, v172, v174\n" \
    "v_min_u32 v215, -2, v172\n" \
    "buffer_store_dwordx4 v[212:215], v252, s[36:39], s45 offen sc1\n" \
    "s_add_u32 s46, s46, 131072\n" \
    "s_and_b32 s46, s46, 0x7ffff\n" \
    "s_add_u32 s44, s44, 1\n" \
    "s_xor_b32 s45, s45, 0x84000\n" \
    "s_xor_b32 s48, s48, 32768\n" \
    "s_add_u32 s50, s50, 8192\n" \
    "v_mov_b64 v[160:161], 0\n" \
    "v_mov_b64 v[162:163], 0\n" \
    "v_mov_b64 v[164:165], 0\n" \
    "v_mov_b64 v[166:167], 0\n" \
    "v_mov_b64 v[168:169], 0\n" \
    "v_mov_b64 v[170:171], 0\n" \
    "v_mov_b64 v[172:173], 0\n" \
    "v_mov_b64 v[174:175], 0\n" \
    "v_mov_b64 v[176:177], 0\n" \
    "v_mov_b64 v[178:179], 0\n" \
    "v_mov_b64 v[180:181], 0\n" \
    "v_mov_b64 v[182:183], 0\n" \
    "v_xor_b32 v136, 2048, v255\n" \
    "v_xor_b32 v140, 3072, v255\n" \
    "v_xor_b32 v144, 4096, v255\n" \
    "v_xor_b32 v148, 5120, v255\n" \
    "v_xor_b32 v152, 6144, v255\n" \
    "v_xor_b32 v156, 7168, v255\n" \
    "v_add_u32 v216, s48, v253\n" \
    "v_add_u32 v220, s48, v255\n" \
    "s_xor_b32 s52, s48, 32768\n" \
    "s_add_u32 s49, s49, 16384\n" \
    "s_mov_b32 s24, s22\n" \
    "s_mov_b32 s25, s23\n" \
    "s_memtime s[20:21]\n" \
    "s_memrealtime s[4:5]\n" \
    "s_nop 2\n" \
    "s_memrealtime s[6:7]\n" \
    "s_nop 2\n" \
    "s_memrealtime s[8:9]\n" \
    "s_nop 2\n" \
    "s_memrealtime s[10:11]\n" \
    "s_nop 2\n" \
    "s_memrealtime s[12:13]\n" \
    "s_nop 2\n" \
    "s_memrealtime s[14:15]\n" \
    "s_nop 2\n" \
    "s_memrealtime s[16:17]\n" \
    "s_nop 2\n" \
    "s_memrealtime s[18:19]\n" \
    "s_mov_b32 s29, 0\n" \
    "s_waitcnt lgkmcnt(0)\n" \
    "s_cmp_eq_u32 s6, s4\n" \
    "s_addc_u32 s29, s29, 0\n" \
    "s_cmp_eq_u32 s8, s4\n" \
    "s_addc_u32 s29, s29, 0\n" \
    "s_cmp_eq_u32 s10, s4\n" \
    "s_addc_u32 s29, s29, 0\n" \
    "s_cmp_eq_u32 s12, s4\n" \
    "s_addc_u32 s29, s29, 0\n" \
    "s_cmp_eq_u32 s14, s4\n" \
    "s_addc_u32 s29, s29, 0\n" \
    "s_cmp_eq_u32 s16, s4\n" \
    "s_addc_u32 s29, s29, 0\n" \
    "s_cmp_eq_u32 s18, s4\n" \
    "s_addc_u32 s29, s29, 0\n" \
    "s_lshl_b32 s29, s29, 4\n" \
    "s_add_u32 s23, s20, s29\n" \
    "s_lshl_b32 s22, s4, 2\n" \
    "s_lshl_b32 s28, s22, 6\n" \
    "s_sub_u32 s29, s23, s25\n" \
    "s_sub_u32 s30, s22, s24\n" \
    "v_cvt_f32_u32 v234, s29\n" \
    "v_cvt_f32_u32 v235, s30\n" \
    "v_rcp_f32 v235, v235\n" \
    "v_or_b32 v243, v243, v243\n" \
    "v_mul_f32 v234, v234, v235\n" \
    "v_add_f32 v234, s26, v234\n" \
    "v_mul_f32 v234, 0.5, v234\n" \
    "v_rcp_f32 v235, v234\n" \
    "s_nop 0\n" \
    "v_readfirstlane_b32 s26, v234\n" \
    "v_readfirstlane_b32 s27, v235\n" \
    "s_nop 1\n" \
    "s_waitcnt vmcnt(2)\n" \
    "SP%=:\n" \
    "v_sub_u32 v232, v242, v244\n" \
    "v_and_b32 v232, 63, v232\n" \
    "v_cmp_ne_u32 vcc, 0, v232\n" \
    "s_cbranch_vccz SG%=\n" \
    "v_cmp_lt_u32 vcc, 1, v232\n" \
    "s_cbranch_vccz VE%=\n" \
    "buffer_load_dword v242, v245, s[36:39], s59 offen sc1\n" \
    "s_waitcnt vmcnt(0)\n" \
    "s_add_u32 s57, s57, 1\n" \
    "s_cmp_lt_u32 s57, 1048576\n" \
    "s_cbranch_scc1 SP%=\n" \
    "SG%=:\n" \
    "v_subrev_u32 v242, s28, v242\n" \
    "v_or_b32 v243, v243, v243\n" \
    "v_or_b32 v243, v243, v243\n" \
    "v_max_i32_dpp v242, v242, v242 row_ror:8 row_mask:0xf bank_mask:0xf\n" \
    "v_or_b32 v243, v243, v243\n" \
    "v_or_b32 v243, v243, v243\n" \
    "v_max_i32_dpp v242, v242, v242 row_ror:4 row_mask:0xf bank_mask:0xf\n" \
    "v_or_b32 v243, v243, v243\n" \
    "v_or_b32 v243, v243, v243\n" \
    "v_max_i32_dpp v242, v242, v242 row_ror:2 row_mask:0xf bank_mask:0xf\n" \
    "v_or_b32 v243, v243, v243\n" \
    "v_or_b32 v243, v243, v243\n" \
    "v_max_i32_dpp v242, v242, v242 row_ror:1 row_mask:0xf bank_mask:0xf\n" \
    "v_mov_b32 v232, v242\n" \
    "v_or_b32 v243, v243, v243\n" \
    "v_or_b32 v243, v243, v243\n" \
    "v_permlane16_swap_b32 v242, v232\n" \
    "v_max_i32 v242, v242, v232\n" \
    "v_ashrrev_i32 v242, 6, v242\n" \
    "v_add_u32 v242, s31, v242\n" \
    "v_cvt_f32_i32 v242, v242\n" \
    "v_mul_f32 v242, s26, v242\n" \
    "v_cvt_i32_f32 v242, v242\n" \
    "s_memtime s[20:21]\n" \
    "v_readfirstlane_b32 s30, v242\n" \
    "s_waitcnt lgkmcnt(0)\n" \
    "s_add_u32 s30, s30, s23\n" \
    "s_sub_u32 s29, s30, s20\n" \
    "s_min_i32 s29, s29, 4096\n" \
    "s_add_u32 s30, s20, s29\n" \
    "VL%=:\n" \
    "s_memtime s[20:21]\n" \
    "s_waitcnt lgkmcnt(0)\n" \
    "s_sub_u32 s29, s30, s20\n" \
    "s_cmp_gt_i32 s29, 192\n" \
    "s_cbranch_scc1 VL%=\n" \
    "s_max_i32 s29, s29, 0\n" \
    "s_lshr_b32 s29, s29, 3\n" \
    "s_lshl_b32 s64, s29, 2\n" \
    "s_getpc_b64 s[66:67]\n" \
    "VD%=:\n" \
    "s_sub_u32 s34, VE%=-VD%=, s64\n" \
    "s_add_u32 s66, s66, s34\n" \
    "s_addc_u32 s67, s67, 0\n" \
    "s_setpc_b64 s[66:67]\n" \
    "s_nop 1\n" \
    "s_nop 1\n" \
    "s_nop 1\n" \
    "s_nop 1\n" \
    "s_nop 1\n" \
    "s_nop 1\n" \
    "s_nop 1\n" \
    "s_nop 1\n" \
    "s_nop 1\n" \
    "s_nop 1\n" \
    "s_nop 1\n" \
    "s_nop 1\n" \
    "s_nop 1\n" \
    "s_nop 1\n" \
    "s_nop 1\n" \
    "s_nop 1\n" \
    "s_nop 1\n" \
    "s_nop 1\n" \
    "s_nop 1\n" \
    "s_nop 1\n" \
    "s_nop 1\n" \
    "s_nop 1\n" \
    "s_nop 1\n" \
    "s_nop 1\n" \
    "VE%=:\n" \
    "buffer_load_dwordx4 v[128:131], v248, s[36:39], s46 offen sc1\n" \
    "buffer_load_dwordx4 v[132:135], v248, s[36:39], s46 offen offset:1024 sc1\n" \
    "s_mov_b32 exec_hi, 0\n" \
    "v_add_u32 v244, 1, v244\n" \
    "s_mov_b32 exec_hi, -1\n" \
    "s_mov_b32 exec_lo, 0\n" \
    "v_add_u32 v245, 4, v245\n" \
    "s_mov_b32 exec_lo, -1\n" \
    "s_cmp_gt_u32 s44, 2\n" \
    "s_cselect_b32 s65, 4, 0\n" \
    "s_nop 15\n" \
    "s_nop 8\n" \
    "s_add_u32 m0, s52, 0\n" \
    "s_add_u32 s55, s49, 0\n" \
    "buffer_load_dwordx4 v253, s[36:39], s55 offen sc1 lds\n" \
    "s_add_u32 m0, s52, 1024\n" \
    "s_add_u32 s55, s49, 1024\n" \
    "buffer_load_dwordx4 v253, s[36:39], s55 offen sc1 lds\n" \
    "s_add_u32 m0, s52, 2048\n" \
    "s_add_u32 s55, s49, 2048\n" \
    "buffer_load_dwordx4 v253, s[36:39], s55 offen sc1 lds\n" \
    "s_add_u32 m0, s52, 3072\n" \
    "s_add_u32 s55, s49, 3072\n" \
    "buffer_load_dwordx4 v253, s[36:39], s55 offen sc1 lds\n" \
    "s_branch HEAD0%=\n" \
    "RETRY2%=:\n" \
    "s_add_u32 s31, s31, s65\n" \
    "s_min_u32 s31, s31, 239\n" \
    "s_mov_b32 s65, 0\n" \
    "s_add_u32 s56, s56, 1\n" \
    "s_cmp_gt_u32 s56, 262144\n" \
    "s_cbranch_scc1 OK2%=\n" \
    "s_waitcnt lgkmcnt(0)\n" \
    "s_add_u32 s54, s46, 65536\n" \
    "s_nop 15\n" \
    "s_nop 8\n" \
    "buffer_load_dwordx4 v[128:131], v248, s[36:39], s54 offen sc1\n" \
    "buffer_load_dwordx4 v[132:135], v248, s[36:39], s54 offen offset:1024 sc1\n" \
    "s_nop 7\n" \
    "v_mov_b64 v[160:161], 0\n" \
    "v_mov_b64 v[162:163], 0\n" \
    "v_mov_b64 v[164:165], 0\n" \
    "v_mov_b64 v[166:167], 0\n" \
    "v_mov_b64 v[168:169], 0\n" \
    "v_mov_b64 v[170:171], 0\n" \
    "v_mov_b64 v[172:173], 0\n" \
    "v_mov_b64 v[174:175], 0\n" \
    "v_mov_b64 v[176:177], 0\n" \
    "v_mov_b64 v[178:179], 0\n" \
    "v_mov_b64 v[180:181], 0\n" \
    "v_mov_b64 v[182:183], 0\n" \
    "s_waitcnt vmcnt(0)\n" \
    "s_branch HEAD0%=\n" \
    "TAIL%=:\n" \
    "v_smfmac_f32_16x16x64_bf16 v[176:179], v[152:155], v[48:55], v254\n" \
    "v_smfmac_f32_16x16x64_bf16 v[180:183], v[152:155], v[112:119], v254\n" \
    "v_smfmac_f32_16x16x64_bf16 v[176:179], v[156:159], v[56:63], v254\n" \
    "v_smfmac_f32_16x16x64_bf16 v[180:183], v[156:159], v[120:127], v254\n" \
    "s_nop 7\n" \
    "v_add_f32 v176, v176, v177\n" \
    "v_add_f32 v178, v178, v179\n" \
    "v_add_f32 v180, v180, v181\n" \
    "v_add_f32 v182, v182, v183\n" \
    "v_cvt_pk_bf16_f32 v176, v176, v178\n" \
    "v_cvt_pk_bf16_f32 v177, v180, v182\n" \
    "v_cvt_pk_bf16_f32 v178, v180, v182\n" \
    "v_mov_b32 v179, 0\n" \
    "s_waitcnt lgkmcnt(0)\n" \
    "s_cmp_eq_u32 s58, 0\n" \
    "s_cbranch_scc1 PK%=_1\n" \
    "v_lshrrev_b32 v240, 1, v253\n" \
    "buffer_store_dwordx2 v[176:177], v240, s[36:39], s50 offen sc1\n" \
    "s_branch PE%=_1\n" \
    "PK%=_1:\n" \
    "v_add_u32 v240, s51, v250\n" \
    "ds_read_b64_tr_b16 v[192:193], v240\n" \
    "ds_read_b64_tr_b16 v[194:195], v240 offset:512\n" \
    "s_waitcnt lgkmcnt(0)\n" \
    "buffer_store_dwordx4 v[192:195], v253, s[36:39], s50 offen sc1\n" \
    "PE%=_1:\n" \
    "s_nop 1\n" \
    "v_add_u32 v244, 1, v244\n" \
    "s_waitcnt vmcnt(0)\n" \
    "s_waitcnt lgkmcnt(0)\n" \
    "s_barrier\n" \
    "buffer_store_dword v244, v246, s[36:39], s35 offen sc1\n" \
    "s_waitcnt vmcnt(0)\n" \
    "s_endpgm\n" \
    "RE_w0:\n"

#define CHAIN_W1 \
    "RS_w1:\n" \
    "s_getpc_b64 s[2:3]\n" \
    "v_lshlrev_b32 v0, 3, v253\n" \
    "s_mov_b32 s0, RE_w1-RS_w1-8\n" \
    "v_add_u32 v1, 0, v0\n" \
    "v_min_u32 v1, s0, v1\n" \
    "global_load_dword v2, v1, s[2:3]\n" \
    "v_add_u32 v1, 8192, v0\n" \
    "v_min_u32 v1, s0, v1\n" \
    "global_load_dword v3, v1, s[2:3]\n" \
    "global_load_dwordx4 v[128:131], v[232:233], off\n" \
    "global_load_dwordx4 v[132:135], v[232:233], off offset:128\n" \
    "v_mov_b32 v242, 0\n" \
    "s_mov_b32 s54, 2162688\n" \
    "buffer_load_dwordx2 v[234:235], v242, s[36:39], s54 offen sc1\n" \
    "s_mov_b32 s42, 0x400000\n" \
    "s_mov_b32 s43, 0x20000\n" \
    "v_xor_b32 v222, 128, v219\n" \
    "s_mov_b32 s40, s62\n" \
    "s_and_b32 s41, s63, 0xffff\n" \
    "s_add_u32 m0, s51, 32768\n" \
    "s_mov_b32 s54, 0\n" \
    "buffer_load_dwordx4 v216, s[40:43], s54 offen lds\n" \
    "s_add_u32 m0, s51, 36864\n" \
    "s_mov_b32 s54, 131072\n" \
    "buffer_load_dwordx4 v216, s[40:43], s54 offen lds\n" \
    "s_add_u32 m0, s51, 40960\n" \
    "s_mov_b32 s54, 262144\n" \
    "buffer_load_dwordx4 v216, s[40:43], s54 offen lds\n" \
    "s_add_u32 m0, s51, 45056\n" \
    "s_mov_b32 s54, 393216\n" \
    "buffer_load_dwordx4 v216, s[40:43], s54 offen lds\n" \
    "s_add_u32 m0, s51, 49152\n" \
    "s_mov_b32 s54, 524288\n" \
    "buffer_load_dwordx4 v216, s[40:43], s54 offen lds\n" \
    "s_add_u32 m0, s51, 53248\n" \
    "s_mov_b32 s54, 655360\n" \
    "buffer_load_dwordx4 v216, s[40:43], s54 offen lds\n" \
    "s_add_u32 m0, s51, 57344\n" \
    "s_mov_b32 s54, 786432\n" \
    "buffer_load_dwordx4 v216, s[40:43], s54 offen lds\n" \
    "s_add_u32 m0, s51, 61440\n" \
    "s_mov_b32 s54, 917504\n" \
    "buffer_load_dwordx4 v216, s[40:43], s54 offen lds\n" \
    "s_add_u32 m0, s51, 65536\n" \
    "s_mov_b32 s54, 1048576\n" \
    "buffer_load_dwordx4 v216, s[40:43], s54 offen lds\n" \
    "s_add_u32 m0, s51, 69632\n" \
    "s_mov_b32 s54, 1179648\n" \
    "buffer_load_dwordx4 v216, s[40:43], s54 offen lds\n" \
    "s_add_u32 m0, s51, 73728\n" \
    "s_mov_b32 s54, 1310720\n" \
    "buffer_load_dwordx4 v216, s[40:43], s54 offen lds\n" \
    "s_add_u32 m0, s51, 77824\n" \
    "s_mov_b32 s54, 1441792\n" \
    "buffer_load_dwordx4 v216, s[40:43], s54 offen lds\n" \
    "s_add_u32 m0, s51, 81920\n" \
    "s_mov_b32 s54, 1572864\n" \
    "buffer_load_dwordx4 v216, s[40:43], s54 offen lds\n" \
    "s_add_u32 m0, s51, 86016\n" \
    "s_mov_b32 s54, 1703936\n" \
    "buffer_load_dwordx4 v216, s[40:43], s54 offen lds\n" \
    "s_add_u32 m0, s51, 90112\n" \
    "s_mov_b32 s54, 1835008\n" \
    "buffer_load_dwordx4 v216, s[40:43], s54 offen lds\n" \
    "s_add_u32 m0, s51, 94208\n" \
    "s_mov_b32 s54, 1966080\n" \
    "buffer_load_dwordx4 v216, s[40:43], s54 offen lds\n" \
    "s_add_u32 m0, s51, 98304\n" \
    "s_mov_b32 s54, 2097152\n" \
    "buffer_load_dwordx4 v216, s[40:43], s54 offen lds\n" \
    "s_add_u32 m0, s51, 102400\n" \
    "s_mov_b32 s54, 2228224\n" \
    "buffer_load_dwordx4 v216, s[40:43], s54 offen lds\n" \
    "s_add_u32 m0, s51, 106496\n" \
    "s_mov_b32 s54, 2359296\n" \
    "buffer_load_dwordx4 v216, s[40:43], s54 offen lds\n" \
    "s_add_u32 m0, s51, 110592\n" \
    "s_mov_b32 s54, 2490368\n" \
    "buffer_load_dwordx4 v216, s[40:43], s54 offen lds\n" \
    "s_add_u32 m0, s51, 114688\n" \
    "s_mov_b32 s54, 2621440\n" \
    "buffer_load_dwordx4 v216, s[40:43], s54 offen lds\n" \
    "s_add_u32 m0, s51, 118784\n" \
    "s_mov_b32 s54, 2752512\n" \
    "buffer_load_dwordx4 v216, s[40:43], s54 offen lds\n" \
    "s_add_u32 m0, s51, 122880\n" \
    "s_mov_b32 s54, 2883584\n" \
    "buffer_load_dwordx4 v216, s[40:43], s54 offen lds\n" \
    "s_add_u32 m0, s51, 126976\n" \
    "s_mov_b32 s54, 3014656\n" \
    "buffer_load_dwordx4 v216, s[40:43], s54 offen lds\n" \
    "s_add_u32 m0, s51, 131072\n" \
    "s_mov_b32 s54, 3145728\n" \
    "buffer_load_dwordx4 v216, s[40:43], s54 offen lds\n" \
    "s_add_u32 m0, s51, 135168\n" \
    "s_mov_b32 s54, 3276800\n" \
    "buffer_load_dwordx4 v216, s[40:43], s54 offen lds\n" \
    "s_add_u32 m0, s51, 139264\n" \
    "s_mov_b32 s54, 3407872\n" \
    "buffer_load_dwordx4 v216, s[40:43], s54 offen lds\n" \
    "s_add_u32 m0, s51, 143360\n" \
    "s_mov_b32 s54, 3538944\n" \
    "buffer_load_dwordx4 v216, s[40:43], s54 offen lds\n" \
    "s_add_u32 m0, s51, 147456\n" \
    "s_mov_b32 s54, 3670016\n" \
    "buffer_load_dwordx4 v216, s[40:43], s54 offen lds\n" \
    "s_add_u32 m0, s51, 151552\n" \
    "s_mov_b32 s54, 3801088\n" \
    "buffer_load_dwordx4 v216, s[40:43], s54 offen lds\n" \
    "s_add_u32 m0, s51, 155648\n" \
    "s_mov_b32 s54, 3932160\n" \
    "buffer_load_dwordx4 v216, s[40:43], s54 offen lds\n" \
    "s_add_u32 m0, s51, 159744\n" \
    "s_mov_b32 s54, 4063232\n" \
    "buffer_load_dwordx4 v216, s[40:43], s54 offen lds\n" \
    "s_mov_b32 s24, s22\n" \
    "s_mov_b32 s25, s23\n" \
    "s_memtime s[20:21]\n" \
    "s_memrealtime s[4:5]\n" \
    "s_nop 2\n" \
    "s_memrealtime s[6:7]\n" \
    "s_nop 2\n" \
    "s_memrealtime s[8:9]\n" \
    "s_nop 2\n" \
    "s_memrealtime s[10:11]\n" \
    "s_nop 2\n" \
    "s_memrealtime s[12:13]\n" \
    "s_nop 2\n" \
    "s_memrealtime s[14:15]\n" \
    "s_nop 2\n" \
    "s_memrealtime s[16:17]\n" \
    "s_nop 2\n" \
    "s_memrealtime s[18:19]\n" \
    "s_mov_b32 s29, 0\n" \
    "s_waitcnt lgkmcnt(0)\n" \
    "s_cmp_eq_u32 s6, s4\n" \
    "s_addc_u32 s29, s29, 0\n" \
    "s_cmp_eq_u32 s8, s4\n" \
    "s_addc_u32 s29, s29, 0\n" \
    "s_cmp_eq_u32 s10, s4\n" \
    "s_addc_u32 s29, s29, 0\n" \
    "s_cmp_eq_u32 s12, s4\n" \
    "s_addc_u32 s29, s29, 0\n" \
    "s_cmp_eq_u32 s14, s4\n" \
    "s_addc_u32 s29, s29, 0\n" \
    "s_cmp_eq_u32 s16, s4\n" \
    "s_addc_u32 s29, s29, 0\n" \
    "s_cmp_eq_u32 s18, s4\n" \
    "s_addc_u32 s29, s29, 0\n" \
    "s_lshl_b32 s29, s29, 4\n" \
    "s_add_u32 s23, s20, s29\n" \
    "s_lshl_b32 s22, s4, 2\n" \
    "s_lshl_b32 s28, s22, 6\n" \
    "s_waitcnt vmcnt(16)\n" \
    "s_barrier\n" \
    "v_xor_b32 v223, 0, v217\n" \
    "v_add_u32 v223, 32768, v223\n" \
    "ds_read_b64_tr_b16 a[128:129], v223\n" \
    "ds_read_b64_tr_b16 a[130:131], v223 offset:512\n" \
    "ds_read_b64_tr_b16 a[132:133], v223 offset:8192\n" \
    "ds_read_b64_tr_b16 a[134:135], v223 offset:8704\n" \
    "v_xor_b32 v223, 16384, v217\n" \
    "v_add_u32 v223, 32768, v223\n" \
    "ds_read_b64_tr_b16 a[136:137], v223\n" \
    "ds_read_b64_tr_b16 a[138:139], v223 offset:512\n" \
    "ds_read_b64_tr_b16 a[140:141], v223 offset:8192\n" \
    "ds_read_b64_tr_b16 a[142:143], v223 offset:8704\n" \
    "v_xor_b32 v223, 32768, v217\n" \
    "v_add_u32 v223, 32768, v223\n" \
    "ds_read_b64_tr_b16 a[144:145], v223\n" \
    "ds_read_b64_tr_b16 a[146:147], v223 offset:512\n" \
    "ds_read_b64_tr_b16 a[148:149], v223 offset:8192\n" \
    "ds_read_b64_tr_b16 a[150:151], v223 offset:8704\n" \
    "v_xor_b32 v223, 49152, v217\n" \
    "v_add_u32 v223, 32768, v223\n" \
    "ds_read_b64_tr_b16 a[152:153], v223\n" \
    "ds_read_b64_tr_b16 a[154:155], v223 offset:512\n" \
    "ds_read_b64_tr_b16 a[156:157], v223 offset:8192\n" \
    "ds_read_b64_tr_b16 a[158:159], v223 offset:8704\n" \
    "v_xor_b32 v223, 0, v218\n" \
    "v_add_u32 v223, 32768, v223\n" \
    "ds_read_b64_tr_b16 a[192:193], v223\n" \
    "ds_read_b64_tr_b16 a[194:195], v223 offset:512\n" \
    "ds_read_b64_tr_b16 a[196:197], v223 offset:8192\n" \
    "ds_read_b64_tr_b16 a[198:199], v223 offset:8704\n" \
    "v_xor_b32 v223, 16384, v218\n" \
    "v_add_u32 v223, 32768, v223\n" \
    "ds_read_b64_tr_b16 a[200:201], v223\n" \
    "ds_read_b64_tr_b16 a[202:203], v223 offset:512\n" \
    "ds_read_b64_tr_b16 a[204:205], v223 offset:8192\n" \
    "ds_read_b64_tr_b16 a[206:207], v223 offset:8704\n" \
    "v_xor_b32 v223, 32768, v218\n" \
    "v_add_u32 v223, 32768, v223\n" \
    "ds_read_b64_tr_b16 a[208:209], v223\n" \
    "ds_read_b64_tr_b16 a[210:211], v223 offset:512\n" \
    "ds_read_b64_tr_b16 a[212:213], v223 offset:8192\n" \
    "ds_read_b64_tr_b16 a[214:215], v223 offset:8704\n" \
    "v_xor_b32 v223, 49152, v218\n" \
    "v_add_u32 v223, 32768, v223\n" \
    "ds_read_b64_tr_b16 a[216:217], v223\n" \
    "ds_read_b64_tr_b16 a[218:219], v223 offset:512\n" \
    "ds_read_b64_tr_b16 a[220:221], v223 offset:8192\n" \
    "ds_read_b64_tr_b16 a[222:223], v223 offset:8704\n" \
    "s_waitcnt lgkmcnt(0)\n" \
    "s_barrier\n" \
    "s_mov_b32 s40, s64\n" \
    "s_and_b32 s41, s65, 0xffff\n" \
    "s_add_u32 m0, s51, 32768\n" \
    "s_mov_b32 s54, 0\n" \
    "buffer_load_dwordx4 v216, s[40:43], s54 offen lds\n" \
    "s_add_u32 m0, s51, 36864\n" \
    "s_mov_b32 s54, 131072\n" \
    "buffer_load_dwordx4 v216, s[40:43], s54 offen lds\n" \
    "s_add_u32 m0, s51, 40960\n" \
    "s_mov_b32 s54, 262144\n" \
    "buffer_load_dwordx4 v216, s[40:43], s54 offen lds\n" \
    "s_add_u32 m0, s51, 45056\n" \
    "s_mov_b32 s54, 393216\n" \
    "buffer_load_dwordx4 v216, s[40:43], s54 offen lds\n" \
    "s_add_u32 m0, s51, 49152\n" \
    "s_mov_b32 s54, 524288\n" \
    "buffer_load_dwordx4 v216, s[40:43], s54 offen lds\n" \
    "s_add_u32 m0, s51, 53248\n" \
    "s_mov_b32 s54, 655360\n" \
    "buffer_load_dwordx4 v216, s[40:43], s54 offen lds\n" \
    "s_add_u32 m0, s51, 57344\n" \
    "s_mov_b32 s54, 786432\n" \
    "buffer_load_dwordx4 v216, s[40:43], s54 offen lds\n" \
    "s_add_u32 m0, s51, 61440\n" \
    "s_mov_b32 s54, 917504\n" \
    "buffer_load_dwordx4 v216, s[40:43], s54 offen lds\n" \
    "s_add_u32 m0, s51, 65536\n" \
    "s_mov_b32 s54, 1048576\n" \
    "buffer_load_dwordx4 v216, s[40:43], s54 offen lds\n" \
    "s_add_u32 m0, s51, 69632\n" \
    "s_mov_b32 s54, 1179648\n" \
    "buffer_load_dwordx4 v216, s[40:43], s54 offen lds\n" \
    "s_add_u32 m0, s51, 73728\n" \
    "s_mov_b32 s54, 1310720\n" \
    "buffer_load_dwordx4 v216, s[40:43], s54 offen lds\n" \
    "s_add_u32 m0, s51, 77824\n" \
    "s_mov_b32 s54, 1441792\n" \
    "buffer_load_dwordx4 v216, s[40:43], s54 offen lds\n" \
    "s_add_u32 m0, s51, 81920\n" \
    "s_mov_b32 s54, 1572864\n" \
    "buffer_load_dwordx4 v216, s[40:43], s54 offen lds\n" \
    "s_add_u32 m0, s51, 86016\n" \
    "s_mov_b32 s54, 1703936\n" \
    "buffer_load_dwordx4 v216, s[40:43], s54 offen lds\n" \
    "s_add_u32 m0, s51, 90112\n" \
    "s_mov_b32 s54, 1835008\n" \
    "buffer_load_dwordx4 v216, s[40:43], s54 offen lds\n" \
    "s_add_u32 m0, s51, 94208\n" \
    "s_mov_b32 s54, 1966080\n" \
    "buffer_load_dwordx4 v216, s[40:43], s54 offen lds\n" \
    "s_waitcnt vmcnt(16)\n" \
    "s_barrier\n" \
    "v_xor_b32 v223, 0, v217\n" \
    "v_add_u32 v223, 98304, v223\n" \
    "ds_read_b64_tr_b16 a[160:161], v223\n" \
    "ds_read_b64_tr_b16 a[162:163], v223 offset:512\n" \
    "ds_read_b64_tr_b16 a[164:165], v223 offset:8192\n" \
    "ds_read_b64_tr_b16 a[166:167], v223 offset:8704\n" \
    "v_xor_b32 v223, 16384, v217\n" \
    "v_add_u32 v223, 98304, v223\n" \
    "ds_read_b64_tr_b16 a[168:169], v223\n" \
    "ds_read_b64_tr_b16 a[170:171], v223 offset:512\n" \
    "ds_read_b64_tr_b16 a[172:173], v223 offset:8192\n" \
    "ds_read_b64_tr_b16 a[174:175], v223 offset:8704\n" \
    "v_xor_b32 v223, 32768, v217\n" \
    "v_add_u32 v223, 98304, v223\n" \
    "ds_read_b64_tr_b16 a[176:177], v223\n" \
    "ds_read_b64_tr_b16 a[178:179], v223 offset:512\n" \
    "ds_read_b64_tr_b16 a[180:181], v223 offset:8192\n" \
    "ds_read_b64_tr_b16 a[182:183], v223 offset:8704\n" \
    "v_xor_b32 v223, 49152, v217\n" \
    "v_add_u32 v223, 98304, v223\n" \
    "ds_read_b64_tr_b16 a[184:185], v223\n" \
    "ds_read_b64_tr_b16 a[186:187], v223 offset:512\n" \
    "ds_read_b64_tr_b16 a[188:189], v223 offset:8192\n" \
    "ds_read_b64_tr_b16 a[190:191], v223 offset:8704\n" \
    "v_xor_b32 v223, 0, v218\n" \
    "v_add_u32 v223, 98304, v223\n" \
    "ds_read_b64_tr_b16 a[224:225], v223\n" \
    "ds_read_b64_tr_b16 a[226:227], v223 offset:512\n" \
    "ds_read_b64_tr_b16 a[228:229], v223 offset:8192\n" \
    "ds_read_b64_tr_b16 a[230:231], v223 offset:8704\n" \
    "v_xor_b32 v223, 16384, v218\n" \
    "v_add_u32 v223, 98304, v223\n" \
    "ds_read_b64_tr_b16 a[232:233], v223\n" \
    "ds_read_b64_tr_b16 a[234:235], v223 offset:512\n" \
    "ds_read_b64_tr_b16 a[236:237], v223 offset:8192\n" \
    "ds_read_b64_tr_b16 a[238:239], v223 offset:8704\n" \
    "v_xor_b32 v223, 32768, v218\n" \
    "v_add_u32 v223, 98304, v223\n" \
    "ds_read_b64_tr_b16 a[240:241], v223\n" \
    "ds_read_b64_tr_b16 a[242:243], v223 offset:512\n" \
    "ds_read_b64_tr_b16 a[244:245], v223 offset:8192\n" \
    "ds_read_b64_tr_b16 a[246:247], v223 offset:8704\n" \
    "v_xor_b32 v223, 49152, v218\n" \
    "v_add_u32 v223, 98304, v223\n" \
    "ds_read_b64_tr_b16 a[248:249], v223\n" \
    "ds_read_b64_tr_b16 a[250:251], v223 offset:512\n" \
    "ds_read_b64_tr_b16 a[252:253], v223 offset:8192\n" \
    "ds_read_b64_tr_b16 a[254:255], v223 offset:8704\n" \
    "s_waitcnt lgkmcnt(0)\n" \
    "s_barrier\n" \
    "s_add_u32 m0, s51, 98304\n" \
    "s_mov_b32 s54, 2097152\n" \
    "buffer_load_dwordx4 v216, s[40:43], s54 offen lds\n" \
    "s_add_u32 m0, s51, 102400\n" \
    "s_mov_b32 s54, 2228224\n" \
    "buffer_load_dwordx4 v216, s[40:43], s54 offen lds\n" \
    "s_add_u32 m0, s51, 106496\n" \
    "s_mov_b32 s54, 2359296\n" \
    "buffer_load_dwordx4 v216, s[40:43], s54 offen lds\n" \
    "s_add_u32 m0, s51, 110592\n" \
    "s_mov_b32 s54, 2490368\n" \
    "buffer_load_dwordx4 v216, s[40:43], s54 offen lds\n" \
    "s_add_u32 m0, s51, 114688\n" \
    "s_mov_b32 s54, 2621440\n" \
    "buffer_load_dwordx4 v216, s[40:43], s54 offen lds\n" \
    "s_add_u32 m0, s51, 118784\n" \
    "s_mov_b32 s54, 2752512\n" \
    "buffer_load_dwordx4 v216, s[40:43], s54 offen lds\n" \
    "s_add_u32 m0, s51, 122880\n" \
    "s_mov_b32 s54, 2883584\n" \
    "buffer_load_dwordx4 v216, s[40:43], s54 offen lds\n" \
    "s_add_u32 m0, s51, 126976\n" \
    "s_mov_b32 s54, 3014656\n" \
    "buffer_load_dwordx4 v216, s[40:43], s54 offen lds\n" \
    "s_add_u32 m0, s51, 131072\n" \
    "s_mov_b32 s54, 3145728\n" \
    "buffer_load_dwordx4 v216, s[40:43], s54 offen lds\n" \
    "s_add_u32 m0, s51, 135168\n" \
    "s_mov_b32 s54, 3276800\n" \
    "buffer_load_dwordx4 v216, s[40:43], s54 offen lds\n" \
    "s_add_u32 m0, s51, 139264\n" \
    "s_mov_b32 s54, 3407872\n" \
    "buffer_load_dwordx4 v216, s[40:43], s54 offen lds\n" \
    "s_add_u32 m0, s51, 143360\n" \
    "s_mov_b32 s54, 3538944\n" \
    "buffer_load_dwordx4 v216, s[40:43], s54 offen lds\n" \
    "s_add_u32 m0, s51, 147456\n" \
    "s_mov_b32 s54, 3670016\n" \
    "buffer_load_dwordx4 v216, s[40:43], s54 offen lds\n" \
    "s_add_u32 m0, s51, 151552\n" \
    "s_mov_b32 s54, 3801088\n" \
    "buffer_load_dwordx4 v216, s[40:43], s54 offen lds\n" \
    "s_add_u32 m0, s51, 155648\n" \
    "s_mov_b32 s54, 3932160\n" \
    "buffer_load_dwordx4 v216, s[40:43], s54 offen lds\n" \
    "s_add_u32 m0, s51, 159744\n" \
    "s_mov_b32 s54, 4063232\n" \
    "buffer_load_dwordx4 v216, s[40:43], s54 offen lds\n" \
    "s_waitcnt vmcnt(16)\n" \
    "s_barrier\n" \
    "v_xor_b32 v223, 0, v217\n" \
    "v_add_u32 v223, 32768, v223\n" \
    "ds_read_b64_tr_b16 v[0:1], v223\n" \
    "ds_read_b64_tr_b16 v[2:3], v223 offset:512\n" \
    "ds_read_b64_tr_b16 v[4:5], v223 offset:8192\n" \
    "ds_read_b64_tr_b16 v[6:7], v223 offset:8704\n" \
    "v_xor_b32 v223, 16384, v217\n" \
    "v_add_u32 v223, 32768, v223\n" \
    "ds_read_b64_tr_b16 v[8:9], v223\n" \
    "ds_read_b64_tr_b16 v[10:11], v223 offset:512\n" \
    "ds_read_b64_tr_b16 v[12:13], v223 offset:8192\n" \
    "ds_read_b64_tr_b16 v[14:15], v223 offset:8704\n" \
    "v_xor_b32 v223, 32768, v217\n" \
    "v_add_u32 v223, 32768, v223\n" \
    "ds_read_b64_tr_b16 v[16:17], v223\n" \
    "ds_read_b64_tr_b16 v[18:19], v223 offset:512\n" \
    "ds_read_b64_tr_b16 v[20:21], v223 offset:8192\n" \
    "ds_read_b64_tr_b16 v[22:23], v223 offset:8704\n" \
    "v_xor_b32 v223, 49152, v217\n" \
    "v_add_u32 v223, 32768, v223\n" \
    "ds_read_b64_tr_b16 v[24:25], v223\n" \
    "ds_read_b64_tr_b16 v[26:27], v223 offset:512\n" \
    "ds_read_b64_tr_b16 v[28:29], v223 offset:8192\n" \
    "ds_read_b64_tr_b16 v[30:31], v223 offset:8704\n" \
    "v_xor_b32 v223, 0, v218\n" \
    "v_add_u32 v223, 32768, v223\n" \
    "ds_read_b64_tr_b16 v[64:65], v223\n" \
    "ds_read_b64_tr_b16 v[66:67], v223 offset:512\n" \
    "ds_read_b64_tr_b16 v[68:69], v223 offset:8192\n" \
    "ds_read_b64_tr_b16 v[70:71], v223 offset:8704\n" \
    "v_xor_b32 v223, 16384, v218\n" \
    "v_add_u32 v223, 32768, v223\n" \
    "ds_read_b64_tr_b16 v[72:73], v223\n" \
    "ds_read_b64_tr_b16 v[74:75], v223 offset:512\n" \
    "ds_read_b64_tr_b16 v[76:77], v223 offset:8192\n" \
    "ds_read_b64_tr_b16 v[78:79], v223 offset:8704\n" \
    "v_xor_b32 v223, 32768, v218\n" \
    "v_add_u32 v223, 32768, v223\n" \
    "ds_read_b64_tr_b16 v[80:81], v223\n" \
    "ds_read_b64_tr_b16 v[82:83], v223 offset:512\n" \
    "ds_read_b64_tr_b16 v[84:85], v223 offset:8192\n" \
    "ds_read_b64_tr_b16 v[86:87], v223 offset:8704\n" \
    "v_xor_b32 v223, 49152, v218\n" \
    "v_add_u32 v223, 32768, v223\n" \
    "ds_read_b64_tr_b16 v[88:89], v223\n" \
    "ds_read_b64_tr_b16 v[90:91], v223 offset:512\n" \
    "ds_read_b64_tr_b16 v[92:93], v223 offset:8192\n" \
    "ds_read_b64_tr_b16 v[94:95], v223 offset:8704\n" \
    "s_waitcnt lgkmcnt(0)\n" \
    "s_barrier\n" \
    "s_mov_b32 s40, s66\n" \
    "s_and_b32 s41, s67, 0xffff\n" \
    "s_add_u32 m0, s51, 32768\n" \
    "s_mov_b32 s54, 0\n" \
    "buffer_load_dwordx4 v219, s[40:43], s54 offen lds\n" \
    "s_add_u32 m0, s51, 36864\n" \
    "s_mov_b32 s54, 8192\n" \
    "buffer_load_dwordx4 v219, s[40:43], s54 offen lds\n" \
    "s_add_u32 m0, s51, 40960\n" \
    "s_mov_b32 s54, 16384\n" \
    "buffer_load_dwordx4 v222, s[40:43], s54 offen lds\n" \
    "s_add_u32 m0, s51, 45056\n" \
    "s_mov_b32 s54, 24576\n" \
    "buffer_load_dwordx4 v222, s[40:43], s54 offen lds\n" \
    "s_add_u32 m0, s51, 49152\n" \
    "s_mov_b32 s54, 32768\n" \
    "buffer_load_dwordx4 v219, s[40:43], s54 offen lds\n" \
    "s_add_u32 m0, s51, 53248\n" \
    "s_mov_b32 s54, 40960\n" \
    "buffer_load_dwordx4 v219, s[40:43], s54 offen lds\n" \
    "s_add_u32 m0, s51, 57344\n" \
    "s_mov_b32 s54, 49152\n" \
    "buffer_load_dwordx4 v222, s[40:43], s54 offen lds\n" \
    "s_add_u32 m0, s51, 61440\n" \
    "s_mov_b32 s54, 57344\n" \
    "buffer_load_dwordx4 v222, s[40:43], s54 offen lds\n" \
    "s_add_u32 m0, s51, 65536\n" \
    "s_mov_b32 s54, 65536\n" \
    "buffer_load_dwordx4 v219, s[40:43], s54 offen lds\n" \
    "s_add_u32 m0, s51, 69632\n" \
    "s_mov_b32 s54, 73728\n" \
    "buffer_load_dwordx4 v219, s[40:43], s54 offen lds\n" \
    "s_add_u32 m0, s51, 73728\n" \
    "s_mov_b32 s54, 81920\n" \
    "buffer_load_dwordx4 v222, s[40:43], s54 offen lds\n" \
    "s_add_u32 m0, s51, 77824\n" \
    "s_mov_b32 s54, 90112\n" \
    "buffer_load_dwordx4 v222, s[40:43], s54 offen lds\n" \
    "s_add_u32 m0, s51, 81920\n" \
    "s_mov_b32 s54, 98304\n" \
    "buffer_load_dwordx4 v219, s[40:43], s54 offen lds\n" \
    "s_add_u32 m0, s51, 86016\n" \
    "s_mov_b32 s54, 106496\n" \
    "buffer_load_dwordx4 v219, s[40:43], s54 offen lds\n" \
    "s_add_u32 m0, s51, 90112\n" \
    "s_mov_b32 s54, 114688\n" \
    "buffer_load_dwordx4 v222, s[40:43], s54 offen lds\n" \
    "s_add_u32 m0, s51, 94208\n" \
    "s_mov_b32 s54, 122880\n" \
    "buffer_load_dwordx4 v222, s[40:43], s54 offen lds\n" \
    "s_waitcnt vmcnt(16)\n" \
    "s_barrier\n" \
    "v_xor_b32 v223, 0, v217\n" \
    "v_add_u32 v223, 98304, v223\n" \
    "ds_read_b64_tr_b16 v[32:33], v223\n" \
    "ds_read_b64_tr_b16 v[34:35], v223 offset:512\n" \
    "ds_read_b64_tr_b16 v[36:37], v223 offset:8192\n" \
    "ds_read_b64_tr_b16 v[38:39], v223 offset:8704\n" \
    "v_xor_b32 v223, 16384, v217\n" \
    "v_add_u32 v223, 98304, v223\n" \
    "ds_read_b64_tr_b16 v[40:41], v223\n" \
    "ds_read_b64_tr_b16 v[42:43], v223 offset:512\n" \
    "ds_read_b64_tr_b16 v[44:45], v223 offset:8192\n" \
    "ds_read_b64_tr_b16 v[46:47], v223 offset:8704\n" \
    "v_xor_b32 v223, 32768, v217\n" \
    "v_add_u32 v223, 98304, v223\n" \
    "ds_read_b64_tr_b16 v[48:49], v223\n" \
    "ds_read_b64_tr_b16 v[50:51], v223 offset:512\n" \
    "ds_read_b64_tr_b16 v[52:53], v223 offset:8192\n" \
    "ds_read_b64_tr_b16 v[54:55], v223 offset:8704\n" \
    "v_xor_b32 v223, 49152, v217\n" \
    "v_add_u32 v223, 98304, v223\n" \
    "ds_read_b64_tr_b16 v[56:57], v223\n" \
    "ds_read_b64_tr_b16 v[58:59], v223 offset:512\n" \
    "ds_read_b64_tr_b16 v[60:61], v223 offset:8192\n" \
    "ds_read_b64_tr_b16 v[62:63], v223 offset:8704\n" \
    "v_xor_b32 v223, 0, v218\n" \
    "v_add_u32 v223, 98304, v223\n" \
    "ds_read_b64_tr_b16 v[96:97], v223\n" \
    "ds_read_b64_tr_b16 v[98:99], v223 offset:512\n" \
    "ds_read_b64_tr_b16 v[100:101], v223 offset:8192\n" \
    "ds_read_b64_tr_b16 v[102:103], v223 offset:8704\n" \
    "v_xor_b32 v223, 16384, v218\n" \
    "v_add_u32 v223, 98304, v223\n" \
    "ds_read_b64_tr_b16 v[104:105], v223\n" \
    "ds_read_b64_tr_b16 v[106:107], v223 offset:512\n" \
    "ds_read_b64_tr_b16 v[108:109], v223 offset:8192\n" \
    "ds_read_b64_tr_b16 v[110:111], v223 offset:8704\n" \
    "v_xor_b32 v223, 32768, v218\n" \
    "v_add_u32 v223, 98304, v223\n" \
    "ds_read_b64_tr_b16 v[112:113], v223\n" \
    "ds_read_b64_tr_b16 v[114:115], v223 offset:512\n" \
    "ds_read_b64_tr_b16 v[116:117], v223 offset:8192\n" \
    "ds_read_b64_tr_b16 v[118:119], v223 offset:8704\n" \
    "v_xor_b32 v223, 49152, v218\n" \
    "v_add_u32 v223, 98304, v223\n" \
    "ds_read_b64_tr_b16 v[120:121], v223\n" \
    "ds_read_b64_tr_b16 v[122:123], v223 offset:512\n" \
    "ds_read_b64_tr_b16 v[124:125], v223 offset:8192\n" \
    "ds_read_b64_tr_b16 v[126:127], v223 offset:8704\n" \
    "s_waitcnt lgkmcnt(0)\n" \
    "s_barrier\n" \
    "s_mov_b32 s40, s60\n" \
    "s_and_b32 s41, s61, 0xffff\n" \
    "s_add_u32 m0, s51, 98304\n" \
    "s_mov_b32 s54, 0\n" \
    "buffer_load_dwordx4 v216, s[40:43], s54 offen lds\n" \
    "s_add_u32 m0, s51, 102400\n" \
    "s_mov_b32 s54, 131072\n" \
    "buffer_load_dwordx4 v216, s[40:43], s54 offen lds\n" \
    "s_add_u32 m0, s51, 106496\n" \
    "s_mov_b32 s54, 262144\n" \
    "buffer_load_dwordx4 v216, s[40:43], s54 offen lds\n" \
    "s_add_u32 m0, s51, 110592\n" \
    "s_mov_b32 s54, 393216\n" \
    "buffer_load_dwordx4 v216, s[40:43], s54 offen lds\n" \
    "s_add_u32 m0, s51, 114688\n" \
    "s_mov_b32 s54, 524288\n" \
    "buffer_load_dwordx4 v216, s[40:43], s54 offen lds\n" \
    "s_add_u32 m0, s51, 118784\n" \
    "s_mov_b32 s54, 655360\n" \
    "buffer_load_dwordx4 v216, s[40:43], s54 offen lds\n" \
    "s_add_u32 m0, s51, 122880\n" \
    "s_mov_b32 s54, 786432\n" \
    "buffer_load_dwordx4 v216, s[40:43], s54 offen lds\n" \
    "s_add_u32 m0, s51, 126976\n" \
    "s_mov_b32 s54, 917504\n" \
    "buffer_load_dwordx4 v216, s[40:43], s54 offen lds\n" \
    "s_add_u32 m0, s51, 131072\n" \
    "s_mov_b32 s54, 1048576\n" \
    "buffer_load_dwordx4 v216, s[40:43], s54 offen lds\n" \
    "s_add_u32 m0, s51, 135168\n" \
    "s_mov_b32 s54, 1179648\n" \
    "buffer_load_dwordx4 v216, s[40:43], s54 offen lds\n" \
    "s_add_u32 m0, s51, 139264\n" \
    "s_mov_b32 s54, 1310720\n" \
    "buffer_load_dwordx4 v216, s[40:43], s54 offen lds\n" \
    "s_add_u32 m0, s51, 143360\n" \
    "s_mov_b32 s54, 1441792\n" \
    "buffer_load_dwordx4 v216, s[40:43], s54 offen lds\n" \
    "s_add_u32 m0, s51, 147456\n" \
    "s_mov_b32 s54, 1572864\n" \
    "buffer_load_dwordx4 v216, s[40:43], s54 offen lds\n" \
    "s_add_u32 m0, s51, 151552\n" \
    "s_mov_b32 s54, 1703936\n" \
    "buffer_load_dwordx4 v216, s[40:43], s54 offen lds\n" \
    "s_add_u32 m0, s51, 155648\n" \
    "s_mov_b32 s54, 1835008\n" \
    "buffer_load_dwordx4 v216, s[40:43], s54 offen lds\n" \
    "s_add_u32 m0, s51, 159744\n" \
    "s_mov_b32 s54, 1966080\n" \
    "buffer_load_dwordx4 v216, s[40:43], s54 offen lds\n" \
    "s_waitcnt vmcnt(16)\n" \
    "s_barrier\n" \
    "v_xor_b32 v223, 0, v220\n" \
    "ds_read_b64_tr_b16 v[136:137], v223\n" \
    "ds_read_b64_tr_b16 v[138:139], v223 offset:1024\n" \
    "ds_read_b64_tr_b16 v[140:141], v223 offset:16384\n" \
    "ds_read_b64_tr_b16 v[142:143], v223 offset:17408\n" \
    "ds_read_b64_tr_b16 v[144:145], v223 offset:32768\n" \
    "ds_read_b64_tr_b16 v[146:147], v223 offset:33792\n" \
    "ds_read_b64_tr_b16 v[148:149], v223 offset:49152\n" \
    "ds_read_b64_tr_b16 v[150:151], v223 offset:50176\n" \
    "v_xor_b32 v223, 32, v220\n" \
    "ds_read_b64_tr_b16 v[152:153], v223\n" \
    "ds_read_b64_tr_b16 v[154:155], v223 offset:1024\n" \
    "ds_read_b64_tr_b16 v[156:157], v223 offset:16384\n" \
    "ds_read_b64_tr_b16 v[158:159], v223 offset:17408\n" \
    "ds_read_b64_tr_b16 v[160:161], v223 offset:32768\n" \
    "ds_read_b64_tr_b16 v[162:163], v223 offset:33792\n" \
    "ds_read_b64_tr_b16 v[164:165], v223 offset:49152\n" \
    "ds_read_b64_tr_b16 v[166:167], v223 offset:50176\n" \
    "v_xor_b32 v223, 64, v220\n" \
    "ds_read_b64_tr_b16 v[168:169], v223\n" \
    "ds_read_b64_tr_b16 v[170:171], v223 offset:1024\n" \
    "ds_read_b64_tr_b16 v[172:173], v223 offset:16384\n" \
    "ds_read_b64_tr_b16 v[174:175], v223 offset:17408\n" \
    "ds_read_b64_tr_b16 v[176:177], v223 offset:32768\n" \
    "ds_read_b64_tr_b16 v[178:179], v223 offset:33792\n" \
    "ds_read_b64_tr_b16 v[180:181], v223 offset:49152\n" \
    "ds_read_b64_tr_b16 v[182:183], v223 offset:50176\n" \
    "v_xor_b32 v223, 96, v220\n" \
    "ds_read_b64_tr_b16 v[184:185], v223\n" \
    "ds_read_b64_tr_b16 v[186:187], v223 offset:1024\n" \
    "ds_read_b64_tr_b16 v[188:189], v223 offset:16384\n" \
    "ds_read_b64_tr_b16 v[190:191], v223 offset:17408\n" \
    "ds_read_b64_tr_b16 v[192:193], v223 offset:32768\n" \
    "ds_read_b64_tr_b16 v[194:195], v223 offset:33792\n" \
    "ds_read_b64_tr_b16 v[196:197], v223 offset:49152\n" \
    "ds_read_b64_tr_b16 v[198:199], v223 offset:50176\n" \
    "s_waitcnt lgkmcnt(0)\n" \
    "s_barrier\n" \
    "v_lshlrev_b32 v206, 3, v253\n" \
    "s_mov_b32 s54, 2163712\n" \
    "buffer_load_dwordx2 v[204:205], v206, s[36:39], s54 offen sc1\n" \
    "s_add_u32 m0, s51, 32768\n" \
    "s_mov_b32 s54, 2097152\n" \
    "buffer_load_dwordx4 v216, s[40:43], s54 offen lds\n" \
    "s_add_u32 m0, s51, 36864\n" \
    "s_mov_b32 s54, 2228224\n" \
    "buffer_load_dwordx4 v216, s[40:43], s54 offen lds\n" \
    "s_add_u32 m0, s51, 40960\n" \
    "s_mov_b32 s54, 2359296\n" \
    "buffer_load_dwordx4 v216, s[40:43], s54 offen lds\n" \
    "s_add_u32 m0, s51, 45056\n" \
    "s_mov_b32 s54, 2490368\n" \
    "buffer_load_dwordx4 v216, s[40:43], s54 offen lds\n" \
    "s_add_u32 m0, s51, 49152\n" \
    "s_mov_b32 s54, 2621440\n" \
    "buffer_load_dwordx4 v216, s[40:43], s54 offen lds\n" \
    "s_add_u32 m0, s51, 53248\n" \
    "s_mov_b32 s54, 2752512\n" \
    "buffer_load_dwordx4 v216, s[40:43], s54 offen lds\n" \
    "s_add_u32 m0, s51, 57344\n" \
    "s_mov_b32 s54, 2883584\n" \
    "buffer_load_dwordx4 v216, s[40:43], s54 offen lds\n" \
    "s_add_u32 m0, s51, 61440\n" \
    "s_mov_b32 s54, 3014656\n" \
    "buffer_load_dwordx4 v216, s[40:43], s54 offen lds\n" \
    "s_add_u32 m0, s51, 65536\n" \
    "s_mov_b32 s54, 3145728\n" \
    "buffer_load_dwordx4 v216, s[40:43], s54 offen lds\n" \
    "s_add_u32 m0, s51, 69632\n" \
    "s_mov_b32 s54, 3276800\n" \
    "buffer_load_dwordx4 v216, s[40:43], s54 offen lds\n" \
    "s_add_u32 m0, s51, 73728\n" \
    "s_mov_b32 s54, 3407872\n" \
    "buffer_load_dwordx4 v216, s[40:43], s54 offen lds\n" \
    "s_add_u32 m0, s51, 77824\n" \
    "s_mov_b32 s54, 3538944\n" \
    "buffer_load_dwordx4 v216, s[40:43], s54 offen lds\n" \
    "s_add_u32 m0, s51, 81920\n" \
    "s_mov_b32 s54, 3670016\n" \
    "buffer_load_dwordx4 v216, s[40:43], s54 offen lds\n" \
    "s_add_u32 m0, s51, 86016\n" \
    "s_mov_b32 s54, 3801088\n" \
    "buffer_load_dwordx4 v216, s[40:43], s54 offen lds\n" \
    "s_add_u32 m0, s51, 90112\n" \
    "s_mov_b32 s54, 3932160\n" \
    "buffer_load_dwordx4 v216, s[40:43], s54 offen lds\n" \
    "s_add_u32 m0, s51, 94208\n" \
    "s_mov_b32 s54, 4063232\n" \
    "buffer_load_dwordx4 v216, s[40:43], s54 offen lds\n" \
    "s_waitcnt vmcnt(16)\n" \
    "s_barrier\n" \
    "v_xor_b32 v223, 0, v217\n" \
    "v_add_u32 v223, 98304, v223\n" \
    "ds_read_b64_tr_b16 a[0:1], v223\n" \
    "ds_read_b64_tr_b16 a[2:3], v223 offset:512\n" \
    "ds_read_b64_tr_b16 a[4:5], v223 offset:8192\n" \
    "ds_read_b64_tr_b16 a[6:7], v223 offset:8704\n" \
    "v_xor_b32 v223, 16384, v217\n" \
    "v_add_u32 v223, 98304, v223\n" \
    "ds_read_b64_tr_b16 a[8:9], v223\n" \
    "ds_read_b64_tr_b16 a[10:11], v223 offset:512\n" \
    "ds_read_b64_tr_b16 a[12:13], v223 offset:8192\n" \
    "ds_read_b64_tr_b16 a[14:15], v223 offset:8704\n" \
    "v_xor_b32 v223, 32768, v217\n" \
    "v_add_u32 v223, 98304, v223\n" \
    "ds_read_b64_tr_b16 a[16:17], v223\n" \
    "ds_read_b64_tr_b16 a[18:19], v223 offset:512\n" \
    "ds_read_b64_tr_b16 a[20:21], v223 offset:8192\n" \
    "ds_read_b64_tr_b16 a[22:23], v223 offset:8704\n" \
    "v_xor_b32 v223, 49152, v217\n" \
    "v_add_u32 v223, 98304, v223\n" \
    "ds_read_b64_tr_b16 a[24:25], v223\n" \
    "ds_read_b64_tr_b16 a[26:27], v223 offset:512\n" \
    "ds_read_b64_tr_b16 a[28:29], v223 offset:8192\n" \
    "ds_read_b64_tr_b16 a[30:31], v223 offset:8704\n" \
    "v_xor_b32 v223, 0, v218\n" \
    "v_add_u32 v223, 98304, v223\n" \
    "ds_read_b64_tr_b16 a[64:65], v223\n" \
    "ds_read_b64_tr_b16 a[66:67], v223 offset:512\n" \
    "ds_read_b64_tr_b16 a[68:69], v223 offset:8192\n" \
    "ds_read_b64_tr_b16 a[70:71], v223 offset:8704\n" \
    "v_xor_b32 v223, 16384, v218\n" \
    "v_add_u32 v223, 98304, v223\n" \
    "ds_read_b64_tr_b16 a[72:73], v223\n" \
    "ds_read_b64_tr_b16 a[74:75], v223 offset:512\n" \
    "ds_read_b64_tr_b16 a[76:77], v223 offset:8192\n" \
    "ds_read_b64_tr_b16 a[78:79], v223 offset:8704\n" \
    "v_xor_b32 v223, 32768, v218\n" \
    "v_add_u32 v223, 98304, v223\n" \
    "ds_read_b64_tr_b16 a[80:81], v223\n" \
    "ds_read_b64_tr_b16 a[82:83], v223 offset:512\n" \
    "ds_read_b64_tr_b16 a[84:85], v223 offset:8192\n" \
    "ds_read_b64_tr_b16 a[86:87], v223 offset:8704\n" \
    "v_xor_b32 v223, 49152, v218\n" \
    "v_add_u32 v223, 98304, v223\n" \
    "ds_read_b64_tr_b16 a[88:89], v223\n" \
    "ds_read_b64_tr_b16 a[90:91], v223 offset:512\n" \
    "ds_read_b64_tr_b16 a[92:93], v223 offset:8192\n" \
    "ds_read_b64_tr_b16 a[94:95], v223 offset:8704\n" \
    "s_waitcnt lgkmcnt(0)\n" \
    "s_barrier\n" \
    "v_add_u32 v234, 1, v234\n" \
    "v_add_u32 v235, 8, v235\n" \
    "v_or_b32 v235, 7, v235\n" \
    "s_mov_b32 s54, 2163712\n" \
    "WIa1024%=:\n" \
    "v_or_b32 v205, 7, v205\n" \
    "v_cmp_ne_u64 vcc, v[234:235], v[204:205]\n" \
    "s_cbranch_vccz WIb1024%=\n" \
    "s_sleep 2\n" \
    "buffer_load_dwordx2 v[204:205], v206, s[36:39], s54 offen sc1\n" \
    "s_waitcnt vmcnt(0)\n" \
    "s_add_u32 s57, s57, 1\n" \
    "s_cmp_lt_u32 s57, 262144\n" \
    "s_cbranch_scc1 WIa1024%=\n" \
    "WIb1024%=:\n" \
    "buffer_load_dwordx2 v[208:209], v241, s[36:39], s68 offen sc1\n" \
    "v_lshlrev_b32 v206, 3, v253\n" \
    "s_mov_b32 s54, 2179072\n" \
    "buffer_load_dwordx2 v[204:205], v206, s[36:39], s54 offen sc1\n" \
    "ds_write_b128 v221, v[136:139]\n" \
    "ds_write_b128 v221, v[140:143] offset:4096\n" \
    "ds_write_b128 v221, v[144:147] offset:8192\n" \
    "ds_write_b128 v221, v[148:151] offset:12288\n" \
    "ds_write_b128 v221, v[152:155] offset:64\n" \
    "ds_write_b128 v221, v[156:159] offset:4160\n" \
    "ds_write_b128 v221, v[160:163] offset:8256\n" \
    "ds_write_b128 v221, v[164:167] offset:12352\n" \
    "ds_write_b128 v221, v[168:171] offset:128\n" \
    "ds_write_b128 v221, v[172:175] offset:4224\n" \
    "ds_write_b128 v221, v[176:179] offset:8320\n" \
    "ds_write_b128 v221, v[180:183] offset:12416\n" \
    "ds_write_b128 v221, v[184:187] offset:192\n" \
    "ds_write_b128 v221, v[188:191] offset:4288\n" \
    "ds_write_b128 v221, v[192:195] offset:8384\n" \
    "ds_write_b128 v221, v[196:199] offset:12480\n" \
    "s_waitcnt lgkmcnt(0)\n" \
    "ds_read_b128 v[224:227], v247 offset:57344\n" \
    "ds_read_b128 v[228:231], v247 offset:61440\n" \
    "v_min_u32 v128, -2, v128\n" \
    "v_min_u32 v129, -2, v129\n" \
    "v_min_u32 v130, -2, v130\n" \
    "v_min_u32 v131, -2, v131\n" \
    "v_min_u32 v132, -2, v132\n" \
    "v_min_u32 v133, -2, v133\n" \
    "v_min_u32 v134, -2, v134\n" \
    "v_min_u32 v135, -2, v135\n" \
    "s_mov_b32 s24, s22\n" \
    "s_mov_b32 s25, s23\n" \
    "s_memtime s[20:21]\n" \
    "s_memrealtime s[4:5]\n" \
    "s_nop 2\n" \
    "s_memrealtime s[6:7]\n" \
    "s_nop 2\n" \
    "s_memrealtime s[8:9]\n" \
    "s_nop 2\n" \
    "s_memrealtime s[10:11]\n" \
    "s_nop 2\n" \
    "s_memrealtime s[12:13]\n" \
    "s_nop 2\n" \
    "s_memrealtime s[14:15]\n" \
    "s_nop 2\n" \
    "s_memrealtime s[16:17]\n" \
    "s_nop 2\n" \
    "s_memrealtime s[18:19]\n" \
    "s_mov_b32 s29, 0\n" \
    "s_waitcnt lgkmcnt(0)\n" \
    "s_cmp_eq_u32 s6, s4\n" \
    "s_addc_u32 s29, s29, 0\n" \
    "s_cmp_eq_u32 s8, s4\n" \
    "s_addc_u32 s29, s29, 0\n" \
    "s_cmp_eq_u32 s10, s4\n" \
    "s_addc_u32 s29, s29, 0\n" \
    "s_cmp_eq_u32 s12, s4\n" \
    "s_addc_u32 s29, s29, 0\n" \
    "s_cmp_eq_u32 s14, s4\n" \
    "s_addc_u32 s29, s29, 0\n" \
    "s_cmp_eq_u32 s16, s4\n" \
    "s_addc_u32 s29, s29, 0\n" \
    "s_cmp_eq_u32 s18, s4\n" \
    "s_addc_u32 s29, s29, 0\n" \
    "s_lshl_b32 s29, s29, 4\n" \
    "s_add_u32 s23, s20, s29\n" \
    "s_lshl_b32 s22, s4, 2\n" \
    "s_lshl_b32 s28, s22, 6\n" \
    "s_sub_u32 s29, s23, s25\n" \
    "s_sub_u32 s30, s22, s24\n" \
    "v_cvt_f32_u32 v232, s29\n" \
    "v_cvt_f32_u32 v233, s30\n" \
    "v_rcp_f32 v233, v233\n" \
    "v_or_b32 v243, v243, v243\n" \
    "v_mul_f32 v232, v232, v233\n" \
    "v_rcp_f32 v233, v232\n" \
    "s_nop 0\n" \
    "v_readfirstlane_b32 s26, v232\n" \
    "v_readfirstlane_b32 s27, v233\n" \
    "s_nop 1\n" \
    "s_waitcnt vmcnt(0)\n" \
    "s_barrier\n" \
    "s_mov_b32 s52, 8192\n" \
    "s_mov_b32 s48, 8192\n" \
    "s_mov_b32 s31, 0\n" \
    "KVa%=:\n" \
    "v_and_b32 v210, v208, v209\n" \
    "v_cmp_ne_u32 vcc, 1, v210\n" \
    "s_cbranch_vccnz KVc%=\n" \
    "s_add_u32 m0, s52, 4096\n" \
    "s_add_u32 s55, s49, 4096\n" \
    "buffer_load_dwordx4 v253, s[36:39], s55 offen sc1 lds\n" \
    "s_add_u32 m0, s52, 5120\n" \
    "s_add_u32 s55, s49, 5120\n" \
    "buffer_load_dwordx4 v253, s[36:39], s55 offen sc1 lds\n" \
    "s_add_u32 m0, s52, 6144\n" \
    "s_add_u32 s55, s49, 6144\n" \
    "buffer_load_dwordx4 v253, s[36:39], s55 offen sc1 lds\n" \
    "s_add_u32 m0, s52, 7168\n" \
    "s_add_u32 s55, s49, 7168\n" \
    "buffer_load_dwordx4 v253, s[36:39], s55 offen sc1 lds\n" \
    "s_mov_b32 s31, 1\n" \
    "KVc%=:\n" \
    "v_xor_b32 v223, 0, v217\n" \
    "v_add_u32 v223, 32768, v223\n" \
    "ds_read_b64_tr_b16 a[32:33], v223\n" \
    "ds_read_b64_tr_b16 a[34:35], v223 offset:512\n" \
    "ds_read_b64_tr_b16 a[36:37], v223 offset:8192\n" \
    "ds_read_b64_tr_b16 a[38:39], v223 offset:8704\n" \
    "v_xor_b32 v223, 16384, v217\n" \
    "v_add_u32 v223, 32768, v223\n" \
    "ds_read_b64_tr_b16 a[40:41], v223\n" \
    "ds_read_b64_tr_b16 a[42:43], v223 offset:512\n" \
    "ds_read_b64_tr_b16 a[44:45], v223 offset:8192\n" \
    "ds_read_b64_tr_b16 a[46:47], v223 offset:8704\n" \
    "v_xor_b32 v223, 32768, v217\n" \
    "v_add_u32 v223, 32768, v223\n" \
    "ds_read_b64_tr_b16 a[48:49], v223\n" \
    "ds_read_b64_tr_b16 a[50:51], v223 offset:512\n" \
    "ds_read_b64_tr_b16 a[52:53], v223 offset:8192\n" \
    "ds_read_b64_tr_b16 a[54:55], v223 offset:8704\n" \
    "v_xor_b32 v223, 49152, v217\n" \
    "v_add_u32 v223, 32768, v223\n" \
    "ds_read_b64_tr_b16 a[56:57], v223\n" \
    "ds_read_b64_tr_b16 a[58:59], v223 offset:512\n" \
    "ds_read_b64_tr_b16 a[60:61], v223 offset:8192\n" \
    "ds_read_b64_tr_b16 a[62:63], v223 offset:8704\n" \
    "v_xor_b32 v223, 0, v218\n" \
    "v_add_u32 v223, 32768, v223\n" \
    "ds_read_b64_tr_b16 a[96:97], v223\n" \
    "ds_read_b64_tr_b16 a[98:99], v223 offset:512\n" \
    "ds_read_b64_tr_b16 a[100:101], v223 offset:8192\n" \
    "ds_read_b64_tr_b16 a[102:103], v223 offset:8704\n" \
    "v_xor_b32 v223, 16384, v218\n" \
    "v_add_u32 v223, 32768, v223\n" \
    "ds_read_b64_tr_b16 a[104:105], v223\n" \
    "ds_read_b64_tr_b16 a[106:107], v223 offset:512\n" \
    "ds_read_b64_tr_b16 a[108:109], v223 offset:8192\n" \
    "ds_read_b64_tr_b16 a[110:111], v223 offset:8704\n" \
    "v_xor_b32 v223, 32768, v218\n" \
    "v_add_u32 v223, 32768, v223\n" \
    "ds_read_b64_tr_b16 a[112:113], v223\n" \
    "ds_read_b64_tr_b16 a[114:115], v223 offset:512\n" \
    "ds_read_b64_tr_b16 a[116:117], v223 offset:8192\n" \
    "ds_read_b64_tr_b16 a[118:119], v223 offset:8704\n" \
    "v_xor_b32 v223, 49152, v218\n" \
    "v_add_u32 v223, 32768, v223\n" \
    "ds_read_b64_tr_b16 a[120:121], v223\n" \
    "ds_read_b64_tr_b16 a[122:123], v223 offset:512\n" \
    "ds_read_b64_tr_b16 a[124:125], v223 offset:8192\n" \
    "ds_read_b64_tr_b16 a[126:127], v223 offset:8704\n" \
    "s_waitcnt lgkmcnt(0)\n" \
    "s_barrier\n" \
    "s_cmp_eq_u32 s31, 1\n" \
    "s_cbranch_scc1 KWc%=\n" \
    "KWa%=:\n" \
    "v_and_b32 v210, v208, v209\n" \
    "v_cmp_ne_u32 vcc, 1, v210\n" \
    "s_cbranch_vccz KWb%=\n" \
    "s_sleep 4\n" \
    "buffer_load_dwordx2 v[208:209], v241, s[36:39], s68 offen sc1\n" \
    "s_waitcnt vmcnt(0)\n" \
    "s_add_u32 s57, s57, 1\n" \
    "s_cmp_lt_u32 s57, 262144\n" \
    "s_cbranch_scc1 KWa%=\n" \
    "KWb%=:\n" \
    "s_add_u32 m0, s52, 4096\n" \
    "s_add_u32 s55, s49, 4096\n" \
    "buffer_load_dwordx4 v253, s[36:39], s55 offen sc1 lds\n" \
    "s_add_u32 m0, s52, 5120\n" \
    "s_add_u32 s55, s49, 5120\n" \
    "buffer_load_dwordx4 v253, s[36:39], s55 offen sc1 lds\n" \
    "s_add_u32 m0, s52, 6144\n" \
    "s_add_u32 s55, s49, 6144\n" \
    "buffer_load_dwordx4 v253, s[36:39], s55 offen sc1 lds\n" \
    "s_add_u32 m0, s52, 7168\n" \
    "s_add_u32 s55, s49, 7168\n" \
    "buffer_load_dwordx4 v253, s[36:39], s55 offen sc1 lds\n" \
    "s_mov_b32 s31, 1\n" \
    "KWc%=:\n" \
    "s_mov_b32 s54, 2179072\n" \
    "WNa%=:\n" \
    "v_mov_b32 v207, v205\n" \
    "v_or_b32 v205, 7, v205\n" \
    "v_cmp_ne_u64 vcc, v[234:235], v[204:205]\n" \
    "s_cbranch_vccz WNb%=\n" \
    "s_sleep 2\n" \
    "buffer_load_dwordx2 v[204:205], v206, s[36:39], s54 offen sc1\n" \
    "s_waitcnt vmcnt(0)\n" \
    "s_add_u32 s57, s57, 1\n" \
    "s_cmp_lt_u32 s57, 262144\n" \
    "s_cbranch_scc1 WNa%=\n" \
    "WNb%=:\n" \
    "s_mov_b32 s32, 0\n" \
    "v_and_b32 v204, 1, v207\n" \
    "v_cmp_ne_u32 vcc, 0, v204\n" \
    "s_bcnt1_i32_b64 s54, vcc\n" \
    "s_cmp_gt_u32 s54, 32\n" \
    "s_cselect_b32 s54, 4096, 0\n" \
    "s_or_b32 s32, s32, s54\n" \
    "v_and_b32 v204, 2, v207\n" \
    "v_cmp_ne_u32 vcc, 0, v204\n" \
    "s_bcnt1_i32_b64 s54, vcc\n" \
    "s_cmp_gt_u32 s54, 32\n" \
    "s_cselect_b32 s54, 8192, 0\n" \
    "s_or_b32 s32, s32, s54\n" \
    "v_and_b32 v204, 4, v207\n" \
    "v_cmp_ne_u32 vcc, 0, v204\n" \
    "s_bcnt1_i32_b64 s54, vcc\n" \
    "s_cmp_gt_u32 s54, 32\n" \
    "s_cselect_b32 s54, 16384, 0\n" \
    "s_or_b32 s32, s32, s54\n" \
    "s_xor_b32 s45, s45, s32\n" \
    "s_xor_b32 s46, s46, s32\n" \
    "s_mov_b32 exec_hi, 0\n" \
    "v_xor_b32 v245, s32, v245\n" \
    "s_mov_b32 exec_hi, -1\n" \
    "s_mov_b32 s40, s36\n" \
    "s_mov_b32 s41, s37\n" \
    "s_mov_b32 s43, s39\n" \
    "s_mov_b32 s60, s36\n" \
    "s_mov_b32 s61, s37\n" \
    "s_mov_b32 s63, s39\n" \
    "s_cmp_eq_u32 s58, 0\n" \
    "s_cselect_b32 s42, s38, 0\n" \
    "s_cselect_b32 s62, 0, s38\n" \
    "s_mov_b32 s33, 1750\n" \
    "s_mov_b32 s31, 179\n" \
    "s_mov_b32 s65, 0\n" \
    "v_mov_b64 v[160:161], 0\n" \
    "v_mov_b64 v[162:163], 0\n" \
    "v_mov_b64 v[164:165], 0\n" \
    "v_mov_b64 v[166:167], 0\n" \
    "v_mov_b64 v[168:169], 0\n" \
    "v_mov_b64 v[170:171], 0\n" \
    "v_mov_b64 v[172:173], 0\n" \
    "v_mov_b64 v[174:175], 0\n" \
    "v_mov_b64 v[176:177], 0\n" \
    "v_mov_b64 v[178:179], 0\n" \
    "v_mov_b64 v[180:181], 0\n" \
    "v_mov_b64 v[182:183], 0\n" \
    "v_xor_b32 v136, 2048, v255\n" \
    "v_xor_b32 v140, 3072, v255\n" \
    "v_xor_b32 v144, 4096, v255\n" \
    "v_xor_b32 v148, 5120, v255\n" \
    "v_xor_b32 v152, 6144, v255\n" \
    "v_xor_b32 v156, 7168, v255\n" \
    "v_add_u32 v216, s48, v253\n" \
    "v_add_u32 v220, s48, v255\n" \
    "s_xor_b32 s52, s48, 32768\n" \
    "s_add_u32 s49, s49, 16384\n" \
    "s_add_u32 m0, s52, 4096\n" \
    "s_add_u32 s55, s49, 4096\n" \
    "buffer_load_dwordx4 v253, s[36:39], s55 offen sc1 lds\n" \
    "s_add_u32 m0, s52, 5120\n" \
    "s_add_u32 s55, s49, 5120\n" \
    "buffer_load_dwordx4 v253, s[36:39], s55 offen sc1 lds\n" \
    "s_add_u32 m0, s52, 6144\n" \
    "s_add_u32 s55, s49, 6144\n" \
    "buffer_load_dwordx4 v253, s[36:39], s55 offen sc1 lds\n" \
    "s_add_u32 m0, s52, 7168\n" \
    "s_add_u32 s55, s49, 7168\n" \
    "buffer_load_dwordx4 v253, s[36:39], s55 offen sc1 lds\n" \
    "HEAD0%=:\n" \
    "s_waitcnt vmcnt(5)\n" \
    "ds_write_b128 v255, v[128:131]\n" \
    "v_smfmac_f32_16x16x64_bf16 v[160:163], v[128:131], a[0:7], v254\n" \
    "s_waitcnt vmcnt(4)\n" \
    "ds_write_b128 v255, v[132:135] offset:1024\n" \
    "v_max3_u32 v232, v128, v129, v130\n" \
    "v_smfmac_f32_16x16x64_bf16 v[168:171], v[128:131], a[128:135], v254\n" \
    "v_max3_u32 v240, v132, v133, v134\n" \
    "v_smfmac_f32_16x16x64_bf16 v[164:167], v[128:131], a[64:71], v254\n" \
    "v_max3_u32 v232, v232, v131, v135\n" \
    "v_smfmac_f32_16x16x64_bf16 v[172:175], v[128:131], a[192:199], v254\n" \
    "v_max_u32 v232, v232, v240\n" \
    "v_smfmac_f32_16x16x64_bf16 v[160:163], v[132:135], a[8:15], v254\n" \
    "v_cmp_eq_u32 vcc, -1, v232\n" \
    "v_smfmac_f32_16x16x64_bf16 v[168:171], v[132:135], a[136:143], v254\n" \
    "s_cbranch_vccnz RETRY2%=\n" \
    "OK2%=:\n" \
    "s_waitcnt lgkmcnt(0)\n" \
    "s_barrier\n" \
    "ds_read_b128 v[136:139], v136\n" \
    "ds_read_b128 v[140:143], v140\n" \
    "v_smfmac_f32_16x16x64_bf16 v[164:167], v[132:135], a[72:79], v254\n" \
    "ds_read_b128 v[144:147], v144\n" \
    "v_smfmac_f32_16x16x64_bf16 v[172:175], v[132:135], a[200:207], v254\n" \
    "ds_read_b128 v[148:151], v148\n" \
    "v_smfmac_f32_16x16x64_bf16 v[176:179], v[128:131], v[0:7], v254\n" \
    "ds_read_b128 v[152:155], v152\n" \
    "v_smfmac_f32_16x16x64_bf16 v[180:183], v[128:131], v[64:71], v254\n" \
    "ds_read_b128 v[156:159], v156\n" \
    "v_smfmac_f32_16x16x64_bf16 v[176:179], v[132:135], v[8:15], v254\n" \
    "v_smfmac_f32_16x16x64_bf16 v[180:183], v[132:135], v[72:79], v254\n" \
    "CONT%=:\n" \
    "s_waitcnt lgkmcnt(5)\n" \
    "v_smfmac_f32_16x16x64_bf16 v[160:163], v[136:139], a[16:23], v254\n" \
    "s_waitcnt lgkmcnt(4)\n" \
    "v_smfmac_f32_16x16x64_bf16 v[160:163], v[140:143], a[24:31], v254\n" \
    "s_waitcnt lgkmcnt(3)\n" \
    "v_smfmac_f32_16x16x64_bf16 v[160:163], v[144:147], a[32:39], v254\n" \
    "s_waitcnt lgkmcnt(2)\n" \
    "v_smfmac_f32_16x16x64_bf16 v[160:163], v[148:151], a[40:47], v254\n" \
    "s_waitcnt lgkmcnt(1)\n" \
    "v_smfmac_f32_16x16x64_bf16 v[160:163], v[152:155], a[48:55], v254\n" \
    "s_waitcnt lgkmcnt(0)\n" \
    "v_smfmac_f32_16x16x64_bf16 v[160:163], v[156:159], a[56:63], v254\n" \
    "v_smfmac_f32_16x16x64_bf16 v[168:171], v[136:139], a[144:151], v254\n" \
    "v_smfmac_f32_16x16x64_bf16 v[168:171], v[140:143], a[152:159], v254\n" \
    "ds_read_b128 v[184:187], v216\n" \
    "v_or_b32 v243, v243, v243\n" \
    "v_smfmac_f32_16x16x64_bf16 v[168:171], v[144:147], a[160:167], v254\n" \
    "ds_read_b128 v[188:191], v216 offset:1024\n" \
    "v_or_b32 v243, v243, v243\n" \
    "v_smfmac_f32_16x16x64_bf16 v[168:171], v[148:151], a[168:175], v254\n" \
    "ds_read_b128 v[192:195], v216 offset:2048\n" \
    "v_add_f32 v160, v160, v161\n" \
    "v_smfmac_f32_16x16x64_bf16 v[168:171], v[152:155], a[176:183], v254\n" \
    "ds_read_b128 v[196:199], v216 offset:3072\n" \
    "v_add_f32 v162, v162, v163\n" \
    "v_smfmac_f32_16x16x64_bf16 v[168:171], v[156:159], a[184:191], v254\n" \
    "ds_read_b128 v[200:203], v216 offset:4096\n" \
    "v_cvt_pk_bf16_f32 v161, v160, v162\n" \
    "v_smfmac_f32_16x16x64_bf16 v[164:167], v[136:139], a[80:87], v254\n" \
    "ds_read_b128 v[204:207], v216 offset:5120\n" \
    "v_or_b32 v243, v243, v243\n" \
    "v_smfmac_f32_16x16x64_bf16 v[164:167], v[140:143], a[88:95], v254\n" \
    "ds_read_b128 v[208:211], v216 offset:6144\n" \
    "v_or_b32 v243, v243, v243\n" \
    "v_smfmac_f32_16x16x64_bf16 v[164:167], v[144:147], a[96:103], v254\n" \
    "ds_read_b128 v[212:215], v216 offset:7168\n" \
    "v_or_b32 v243, v243, v243\n" \
    "v_smfmac_f32_16x16x64_bf16 v[164:167], v[148:151], a[104:111], v254\n" \
    "ds_read_b128 v[216:219], v220 offset:8192\n" \
    "v_add_f32 v168, v168, v169\n" \
    "v_smfmac_f32_16x16x64_bf16 v[164:167], v[152:155], a[112:119], v254\n" \
    "ds_read_b128 v[220:223], v220 offset:9216\n" \
    "v_add_f32 v170, v170, v171\n" \
    "v_smfmac_f32_16x16x64_bf16 v[164:167], v[156:159], a[120:127], v254\n" \
    "v_cvt_pk_bf16_f32 v160, v168, v170\n" \
    "v_smfmac_f32_16x16x64_bf16 v[172:175], v[136:139], a[208:215], v254\n" \
    "ds_write_b64 v251, v[160:161]\n" \
    "v_smfmac_f32_16x16x64_bf16 v[172:175], v[140:143], a[216:223], v254\n" \
    "ds_read_b128 v[128:131], v247\n" \
    "v_or_b32 v243, v243, v243\n" \
    "v_smfmac_f32_16x16x64_bf16 v[172:175], v[144:147], a[224:231], v254\n" \
    "ds_read_b128 v[132:135], v247 offset:4096\n" \
    "v_or_b32 v243, v243, v243\n" \
    "v_smfmac_f32_16x16x64_bf16 v[172:175], v[148:151], a[232:239], v254\n" \
    "v_add_f32 v164, v164, v165\n" \
    "v_smfmac_f32_16x16x64_bf16 v[172:175], v[152:155], a[240:247], v254\n" \
    "v_add_f32 v166, v166, v167\n" \
    "v_smfmac_f32_16x16x64_bf16 v[172:175], v[156:159], a[248:255], v254\n" \
    "v_cvt_pk_bf16_f32 v165, v164, v166\n" \
    "v_smfmac_f32_16x16x64_bf16 v[176:179], v[136:139], v[16:23], v254\n" \
    "s_nop 1\n" \
    "v_or_b32 v243, v243, v243\n" \
    "v_smfmac_f32_16x16x64_bf16 v[180:183], v[136:139], v[80:87], v254\n" \
    "s_nop 1\n" \
    "v_add_f32 v172, v172, v173\n" \
    "v_add_f32 v174, v174, v175\n" \
    "v_cvt_pk_bf16_f32 v164, v172, v174\n" \
    "v_smfmac_f32_16x16x64_bf16 v[176:179], v[140:143], v[24:31], v254\n" \
    "ds_write_b64 v251, v[164:165] offset:512\n" \
    "s_cmp_eq_u32 s44, 63\n" \
    "v_smfmac_f32_16x16x64_bf16 v[180:183], v[140:143], v[88:95], v254\n" \
    "v_or_b32 v243, v243, v243\n" \
    "v_smfmac_f32_16x16x64_bf16 v[176:179], v[144:147], v[32:39], v254\n" \
    "v_or_b32 v243, v243, v243\n" \
    "v_smfmac_f32_16x16x64_bf16 v[180:183], v[144:147], v[96:103], v254\n" \
    "v_or_b32 v243, v243, v243\n" \
    "v_smfmac_f32_16x16x64_bf16 v[176:179], v[148:151], v[40:47], v254\n" \
    "v_or_b32 v243, v243, v243\n" \
    "v_smfmac_f32_16x16x64_bf16 v[180:183], v[148:151], v[104:111], v254\n" \
    "s_waitcnt lgkmcnt(0)\n" \
    "s_barrier\n" \
    "ds_read_b64_tr_b16 v[160:161], v250\n" \
    "ds_read_b64_tr_b16 v[162:163], v250 offset:512\n" \
    "s_cbranch_scc1 TAIL%=\n" \
    "v_smfmac_f32_16x16x64_bf16 v[176:179], v[152:155], v[48:55], v254\n" \
    "ds_read_b64_tr_b16 v[164:165], v250 offset:1024\n" \
    "ds_read_b64_tr_b16 v[166:167], v250 offset:1536\n" \
    "v_smfmac_f32_16x16x64_bf16 v[180:183], v[152:155], v[112:119], v254\n" \
    "ds_read_b64_tr_b16 v[168:169], v250 offset:2048\n" \
    "ds_read_b64_tr_b16 v[170:171], v250 offset:2560\n" \
    "v_smfmac_f32_16x16x64_bf16 v[176:179], v[156:159], v[56:63], v254\n" \
    "ds_read_b64_tr_b16 v[172:173], v250 offset:3072\n" \
    "ds_read_b64_tr_b16 v[174:175], v250 offset:3584\n" \
    "v_smfmac_f32_16x16x64_bf16 v[180:183], v[156:159], v[120:127], v254\n" \
    "v_or_b32 v243, v243, v243\n" \
    "s_waitcnt lgkmcnt(6)\n" \
    "v_mfma_f32_16x16x32_bf16 v[184:187], v[184:187], v[160:163], 0\n" \
    "ds_read_b128 v[136:139], v247 offset:8192\n" \
    "v_or_b32 v243, v243, v243\n" \
    "v_mfma_f32_16x16x32_bf16 v[200:203], v[200:203], v[160:163], 0\n" \
    "ds_read_b128 v[140:143], v247 offset:12288\n" \
    "v_or_b32 v243, v243, v243\n" \
    "s_waitcnt lgkmcnt(6)\n" \
    "v_mfma_f32_16x16x32_bf16 v[184:187], v[188:191], v[164:167], v[184:187]\n" \
    "ds_read_b128 v[144:147], v247 offset:16384\n" \
    "v_or_b32 v243, v243, v243\n" \
    "v_mfma_f32_16x16x32_bf16 v[200:203], v[204:207], v[164:167], v[200:203]\n" \
    "ds_read_b128 v[148:151], v247 offset:20480\n" \
    "v_add_f32 v176, v176, v177\n" \
    "s_waitcnt lgkmcnt(6)\n" \
    "v_mfma_f32_16x16x32_bf16 v[184:187], v[192:195], v[168:171], v[184:187]\n" \
    "ds_read_b128 v[152:155], v247 offset:24576\n" \
    "v_add_f32 v178, v178, v179\n" \
    "v_mfma_f32_16x16x32_bf16 v[200:203], v[208:211], v[168:171], v[200:203]\n" \
    "ds_read_b128 v[156:159], v247 offset:28672\n" \
    "v_add_f32 v180, v180, v181\n" \
    "s_waitcnt lgkmcnt(6)\n" \
    "v_mfma_f32_16x16x32_bf16 v[184:187], v[196:199], v[172:175], v[184:187]\n" \
    "v_add_f32 v182, v182, v183\n" \
    "v_cvt_pk_bf16_f32 v176, v176, v178\n" \
    "v_mfma_f32_16x16x32_bf16 v[200:203], v[212:215], v[172:175], v[200:203]\n" \
    "buffer_store_dwordx4 v[164:167], v253, s[40:43], s50 offen sc1\n" \
    "v_cvt_pk_bf16_f32 v177, v180, v182\n" \
    "v_mfma_f32_16x16x32_bf16 v[160:163], v[160:163], v[160:163], 0\n" \
    "ds_read_b128 v[188:191], v247 offset:32768\n" \
    "v_cvt_pk_bf16_f32 v178, v180, v182\n" \
    "v_mfma_f32_16x16x32_bf16 v[160:163], v[164:167], v[164:167], v[160:163]\n" \
    "ds_read_b128 v[192:195], v247 offset:36864\n" \
    "v_mov_b32 v179, 0\n" \
    "v_mfma_f32_16x16x32_bf16 v[160:163], v[168:171], v[168:171], v[160:163]\n" \
    "ds_read_b128 v[204:207], v247 offset:40960\n" \
    "v_max3_f32 v232, v184, v185, v186\n" \
    "v_mfma_f32_16x16x32_bf16 v[160:163], v[172:175], v[172:175], v[160:163]\n" \
    "ds_read_b128 v[208:211], v247 offset:45056\n" \
    "ds_read_b128 v[164:167], v247 offset:49152\n" \
    "ds_read_b128 v[168:171], v247 offset:53248\n" \
    "v_max3_f32 v233, v200, v201, v202\n" \
    "v_max3_f32 v232, v232, v187, v203\n" \
    "v_max_f32 v232, v232, v233\n" \
    "v_mov_b32 v172, 0\n" \
    "v_mov_b32 v173, 0\n" \
    "v_max3_f32 v234, v232, v160, v161\n" \
    "v_max3_f32 v235, v232, v160, v161\n" \
    "v_mov_b32 v174, 0\n" \
    "v_mov_b32 v175, 0\n" \
    "v_permlane32_swap_b32 v234, v235\n" \
    "v_max_f32 v232, v234, v235\n" \
    "v_max_f32 v233, v234, v235\n" \
    "v_or_b32 v243, v243, v243\n" \
    "v_or_b32 v243, v243, v243\n" \
    "v_permlane16_swap_b32 v232, v233\n" \
    "v_max_f32 v234, v232, v233\n" \
    "v_mul_f32 v234, s53, v234\n" \
    "v_pk_fma_f32 v[160:161], v[160:161], s[52:53], v[234:235] op_sel:[0,1,0] op_sel_hi:[1,1,0] neg_lo:[0,0,1] neg_hi:[0,0,1]\n" \
    "v_pk_fma_f32 v[184:185], v[184:185], s[52:53], v[234:235] op_sel:[0,1,0] op_sel_hi:[1,1,0] neg_lo:[0,0,1] neg_hi:[0,0,1]\n" \
    "v_pk_fma_f32 v[186:187], v[186:187], s[52:53], v[234:235] op_sel:[0,1,0] op_sel_hi:[1,1,0] neg_lo:[0,0,1] neg_hi:[0,0,1]\n" \
    "v_pk_fma_f32 v[200:201], v[200:201], s[52:53], v[234:235] op_sel:[0,1,0] op_sel_hi:[1,1,0] neg_lo:[0,0,1] neg_hi:[0,0,1]\n" \
    "v_pk_fma_f32 v[202:203], v[202:203], s[52:53], v[234:235] op_sel:[0,1,0] op_sel_hi:[1,1,0] neg_lo:[0,0,1] neg_hi:[0,0,1]\n" \
    "v_exp_f32 v160, v160\n" \
    "v_exp_f32 v161, v161\n" \
    "v_exp_f32 v184, v184\n" \
    "v_cvt_pk_bf16_f32 v240, v160, v161\n" \
    "v_exp_f32 v185, v185\n" \
    "v_exp_f32 v186, v186\n" \
    "v_mfma_f32_16x16x16_bf16 v[212:215], v[236:237], v[240:241], 0\n" \
    "v_exp_f32 v187, v187\n" \
    "v_mfma_f32_16x16x16_bf16 v[180:183], v[176:177], v[240:241], 0\n" \
    "v_exp_f32 v200, v200\n" \
    "v_mfma_f32_16x16x16_bf16 v[232:235], v[178:179], v[240:241], 0\n" \
    "v_exp_f32 v201, v201\n" \
    "v_exp_f32 v202, v202\n" \
    "v_exp_f32 v203, v203\n" \
    "v_cvt_pk_bf16_f32 v196, v184, v185\n" \
    "v_cvt_pk_bf16_f32 v197, v186, v187\n" \
    "v_cvt_pk_bf16_f32 v198, v200, v201\n" \
    "v_cvt_pk_bf16_f32 v199, v202, v203\n" \
    "v_mov_b32 v184, 0\n" \
    "v_mov_b32 v185, 0\n" \
    "v_mfma_f32_16x16x32_bf16 v[212:215], v[236:239], v[196:199], v[212:215]\n" \
    "buffer_load_dword v242, v245, s[36:39], s59 offen sc1\n" \
    "v_mov_b32 v186, 0\n" \
    "v_mov_b32 v187, 0\n" \
    "v_mfma_f32_16x16x32_bf16 v[180:183], v[216:219], v[196:199], v[180:183]\n" \
    "v_mov_b32 v200, 0\n" \
    "v_mov_b32 v201, 0\n" \
    "v_mfma_f32_16x16x32_bf16 v[232:235], v[220:223], v[196:199], v[232:235]\n" \
    "v_mov_b32 v202, 0\n" \
    "v_mov_b32 v203, 0\n" \
    "v_rcp_f32 v240, v212\n" \
    "v_mov_b32 v160, 0\n" \
    "v_mov_b32 v161, 0\n" \
    "v_mov_b32 v162, 0\n" \
    "v_mov_b32 v163, 0\n" \
    "v_pk_mul_f32 v[180:181], v[180:181], v[240:241] op_sel_hi:[1,0]\n" \
    "v_pk_mul_f32 v[182:183], v[182:183], v[240:241] op_sel_hi:[1,0]\n" \
    "v_pk_mul_f32 v[232:233], v[232:233], v[240:241] op_sel_hi:[1,0]\n" \
    "v_pk_mul_f32 v[234:235], v[234:235], v[240:241] op_sel_hi:[1,0]\n" \
    "v_cvt_pk_bf16_f32 v196, v180, v181\n" \
    "v_cvt_pk_bf16_f32 v197, v182, v183\n" \
    "v_cvt_pk_bf16_f32 v198, v232, v233\n" \
    "v_cvt_pk_bf16_f32 v199, v234, v235\n" \
    "ds_write_b128 v249, v[196:199]\n" \
    "v_lshrrev_b32 v240, 1, v253\n" \
    "buffer_store_dwordx2 v[176:177], v240, s[60:63], s50 offen sc1\n" \
    "s_waitcnt lgkmcnt(0)\n" \
    "s_barrier\n" \
    "ds_read_b128 v[216:219], v253 offset:28672\n" \
    "ds_read_b128 v[220:223], v253 offset:29696\n" \
    "s_waitcnt lgkmcnt(1)\n" \
    "v_smfmac_f32_16x16x64_bf16 v[184:187], v[216:219], v[128:135], v254\n" \
    "s_waitcnt lgkmcnt(0)\n" \
    "s_nop 0\n" \
    "v_smfmac_f32_16x16x64_bf16 v[184:187], v[220:223], v[136:143], v254\n" \
    "s_nop 1\n" \
    "s_nop 0\n" \
    "v_smfmac_f32_16x16x64_bf16 v[200:203], v[216:219], v[144:151], v254\n" \
    "s_nop 1\n" \
    "s_nop 0\n" \
    "v_smfmac_f32_16x16x64_bf16 v[200:203], v[220:223], v[152:159], v254\n" \
    "v_add_f32 v184, v184, v185\n" \
    "v_smfmac_f32_16x16x64_bf16 v[160:163], v[216:219], v[188:195], v254\n" \
    "s_nop 1\n" \
    "v_add_f32 v186, v186, v187\n" \
    "v_smfmac_f32_16x16x64_bf16 v[160:163], v[220:223], v[204:211], v254\n" \
    "s_nop 0\n" \
    "v_cvt_pk_bf16_f32 v184, v184, v186\n" \
    "v_smfmac_f32_16x16x64_bf16 v[172:175], v[216:219], v[164:171], v254\n" \
    "v_add_f32 v200, v200, v201\n" \
    "v_smfmac_f32_16x16x64_bf16 v[172:175], v[220:223], v[224:231], v254\n" \
    "v_add_f32 v202, v202, v203\n" \
    "v_min_u32 v212, -2, v184\n" \
    "v_cvt_pk_bf16_f32 v200, v200, v202\n" \
    "v_add_f32 v160, v160, v161\n" \
    "v_add_f32 v162, v162, v163\n" \
    "v_min_u32 v213, -2, v200\n" \
    "v_cvt_pk_bf16_f32 v160, v160, v162\n" \
    "v_min_u32 v214, -2, v160\n" \
    "v_add_f32 v172, v172, v173\n" \
    "v_add_f32 v174, v174, v175\n" \
    "v_cvt_pk_bf16_f32 v172, v172, v174\n" \
    "v_min_u32 v215, -2, v172\n" \
    "buffer_store_dwordx4 v[212:215], v252, s[36:39], s45 offen sc1\n" \
    "s_add_u32 s46, s46, 131072\n" \
    "s_and_b32 s46, s46, 0x7ffff\n" \
    "s_add_u32 s44, s44, 1\n" \
    "s_xor_b32 s45, s45, 0x84000\n" \
    "s_xor_b32 s48, s48, 32768\n" \
    "s_add_u32 s50, s50, 8192\n" \
    "v_mov_b64 v[160:161], 0\n" \
    "v_mov_b64 v[162:163], 0\n" \
    "v_mov_b64 v[164:165], 0\n" \
    "v_mov_b64 v[166:167], 0\n" \
    "v_mov_b64 v[168:169], 0\n" \
    "v_mov_b64 v[170:171], 0\n" \
    "v_mov_b64 v[172:173], 0\n" \
    "v_mov_b64 v[174:175], 0\n" \
    "v_mov_b64 v[176:177], 0\n" \
    "v_mov_b64 v[178:179], 0\n" \
    "v_mov_b64 v[180:181], 0\n" \
    "v_mov_b64 v[182:183], 0\n" \
    "v_xor_b32 v136, 2048, v255\n" \
    "v_xor_b32 v140, 3072, v255\n" \
    "v_xor_b32 v144, 4096, v255\n" \
    "v_xor_b32 v148, 5120, v255\n" \
    "v_xor_b32 v152, 6144, v255\n" \
    "v_xor_b32 v156, 7168, v255\n" \
    "v_add_u32 v216, s48, v253\n" \
    "v_add_u32 v220, s48, v255\n" \
    "s_xor_b32 s52, s48, 32768\n" \
    "s_add_u32 s49, s49, 16384\n" \
    "s_mov_b32 s24, s22\n" \
    "s_mov_b32 s25, s23\n" \
    "s_memtime s[20:21]\n" \
    "s_memrealtime s[4:5]\n" \
    "s_nop 2\n" \
    "s_memrealtime s[6:7]\n" \
    "s_nop 2\n" \
    "s_memrealtime s[8:9]\n" \
    "s_nop 2\n" \
    "s_memrealtime s[10:11]\n" \
    "s_nop 2\n" \
    "s_memrealtime s[12:13]\n" \
    "s_nop 2\n" \
    "s_memrealtime s[14:15]\n" \
    "s_nop 2\n" \
    "s_memrealtime s[16:17]\n" \
    "s_nop 2\n" \
    "s_memrealtime s[18:19]\n" \
    "s_mov_b32 s29, 0\n" \
    "s_waitcnt lgkmcnt(0)\n" \
    "s_cmp_eq_u32 s6, s4\n" \
    "s_addc_u32 s29, s29, 0\n" \
    "s_cmp_eq_u32 s8, s4\n" \
    "s_addc_u32 s29, s29, 0\n" \
    "s_cmp_eq_u32 s10, s4\n" \
    "s_addc_u32 s29, s29, 0\n" \
    "s_cmp_eq_u32 s12, s4\n" \
    "s_addc_u32 s29, s29, 0\n" \
    "s_cmp_eq_u32 s14, s4\n" \
    "s_addc_u32 s29, s29, 0\n" \
    "s_cmp_eq_u32 s16, s4\n" \
    "s_addc_u32 s29, s29, 0\n" \
    "s_cmp_eq_u32 s18, s4\n" \
    "s_addc_u32 s29, s29, 0\n" \
    "s_lshl_b32 s29, s29, 4\n" \
    "s_add_u32 s23, s20, s29\n" \
    "s_lshl_b32 s22, s4, 2\n" \
    "s_lshl_b32 s28, s22, 6\n" \
    "s_sub_u32 s29, s23, s25\n" \
    "s_sub_u32 s30, s22, s24\n" \
    "v_cvt_f32_u32 v234, s29\n" \
    "v_cvt_f32_u32 v235, s30\n" \
    "v_rcp_f32 v235, v235\n" \
    "v_or_b32 v243, v243, v243\n" \
    "v_mul_f32 v234, v234, v235\n" \
    "v_add_f32 v234, s26, v234\n" \
    "v_mul_f32 v234, 0.5, v234\n" \
    "v_rcp_f32 v235, v234\n" \
    "s_nop 0\n" \
    "v_readfirstlane_b32 s26, v234\n" \
    "v_readfirstlane_b32 s27, v235\n" \
    "s_nop 1\n" \
    "s_waitcnt vmcnt(2)\n" \
    "SP%=:\n" \
    "v_sub_u32 v232, v242, v244\n" \
    "v_and_b32 v232, 63, v232\n" \
    "v_cmp_ne_u32 vcc, 0, v232\n" \
    "s_cbranch_vccz SG%=\n" \
    "v_cmp_lt_u32 vcc, 1, v232\n" \
    "s_cbranch_vccz VE%=\n" \
    "buffer_load_dword v242, v245, s[36:39], s59 offen sc1\n" \
    "s_waitcnt vmcnt(0)\n" \
    "s_add_u32 s57, s57, 1\n" \
    "s_cmp_lt_u32 s57, 1048576\n" \
    "s_cbranch_scc1 SP%=\n" \
    "SG%=:\n" \
    "v_subrev_u32 v242, s28, v242\n" \
    "v_or_b32 v243, v243, v243\n" \
    "v_or_b32 v243, v243, v243\n" \
    "v_max_i32_dpp v242, v242, v242 row_ror:8 row_mask:0xf bank_mask:0xf\n" \
    "v_or_b32 v243, v243, v243\n" \
    "v_or_b32 v243, v243, v243\n" \
    "v_max_i32_dpp v242, v242, v242 row_ror:4 row_mask:0xf bank_mask:0xf\n" \
    "v_or_b32 v243, v243, v243\n" \
    "v_or_b32 v243, v243, v243\n" \
    "v_max_i32_dpp v242, v242, v242 row_ror:2 row_mask:0xf bank_mask:0xf\n" \
    "v_or_b32 v243, v243, v243\n" \
    "v_or_b32 v243, v243, v243\n" \
    "v_max_i32_dpp v242, v242, v242 row_ror:1 row_mask:0xf bank_mask:0xf\n" \
    "v_mov_b32 v232, v242\n" \
    "v_or_b32 v243, v243, v243\n" \
    "v_or_b32 v243, v243, v243\n" \
    "v_permlane16_swap_b32 v242, v232\n" \
    "v_max_i32 v242, v242, v232\n" \
    "v_ashrrev_i32 v242, 6, v242\n" \
    "v_add_u32 v242, s31, v242\n" \
    "v_cvt_f32_i32 v242, v242\n" \
    "v_mul_f32 v242, s26, v242\n" \
    "v_cvt_i32_f32 v242, v242\n" \
    "s_memtime s[20:21]\n" \
    "v_readfirstlane_b32 s30, v242\n" \
    "s_waitcnt lgkmcnt(0)\n" \
    "s_add_u32 s30, s30, s23\n" \
    "s_sub_u32 s29, s30, s20\n" \
    "s_min_i32 s29, s29, 4096\n" \
    "s_add_u32 s30, s20, s29\n" \
    "VL%=:\n" \
    "s_memtime s[20:21]\n" \
    "s_waitcnt lgkmcnt(0)\n" \
    "s_sub_u32 s29, s30, s20\n" \
    "s_cmp_gt_i32 s29, 192\n" \
    "s_cbranch_scc1 VL%=\n" \
    "s_max_i32 s29, s29, 0\n" \
    "s_lshr_b32 s29, s29, 3\n" \
    "s_lshl_b32 s64, s29, 2\n" \
    "s_getpc_b64 s[66:67]\n" \
    "VD%=:\n" \
    "s_sub_u32 s34, VE%=-VD%=, s64\n" \
    "s_add_u32 s66, s66, s34\n" \
    "s_addc_u32 s67, s67, 0\n" \
    "s_setpc_b64 s[66:67]\n" \
    "s_nop 1\n" \
    "s_nop 1\n" \
    "s_nop 1\n" \
    "s_nop 1\n" \
    "s_nop 1\n" \
    "s_nop 1\n" \
    "s_nop 1\n" \
    "s_nop 1\n" \
    "s_nop 1\n" \
    "s_nop 1\n" \
    "s_nop 1\n" \
    "s_nop 1\n" \
    "s_nop 1\n" \
    "s_nop 1\n" \
    "s_nop 1\n" \
    "s_nop 1\n" \
    "s_nop 1\n" \
    "s_nop 1\n" \
    "s_nop 1\n" \
    "s_nop 1\n" \
    "s_nop 1\n" \
    "s_nop 1\n" \
    "s_nop 1\n" \
    "s_nop 1\n" \
    "VE%=:\n" \
    "buffer_load_dwordx4 v[128:131], v248, s[36:39], s46 offen sc1\n" \
    "buffer_load_dwordx4 v[132:135], v248, s[36:39], s46 offen offset:1024 sc1\n" \
    "s_mov_b32 exec_hi, 0\n" \
    "v_add_u32 v244, 1, v244\n" \
    "s_mov_b32 exec_hi, -1\n" \
    "s_mov_b32 exec_lo, 0\n" \
    "v_add_u32 v245, 4, v245\n" \
    "s_mov_b32 exec_lo, -1\n" \
    "s_cmp_gt_u32 s44, 2\n" \
    "s_cselect_b32 s65, 4, 0\n" \
    "s_nop 15\n" \
    "s_nop 8\n" \
    "s_add_u32 m0, s52, 4096\n" \
    "s_add_u32 s55, s49, 4096\n" \
    "buffer_load_dwordx4 v253, s[36:39], s55 offen sc1 lds\n" \
    "s_add_u32 m0, s52, 5120\n" \
    "s_add_u32 s55, s49, 5120\n" \
    "buffer_load_dwordx4 v253, s[36:39], s55 offen sc1 lds\n" \
    "s_add_u32 m0, s52, 6144\n" \
    "s_add_u32 s55, s49, 6144\n" \
    "buffer_load_dwordx4 v253, s[36:39], s55 offen sc1 lds\n" \
    "s_add_u32 m0, s52, 7168\n" \
    "s_add_u32 s55, s49, 7168\n" \
    "buffer_load_dwordx4 v253, s[36:39], s55 offen sc1 lds\n" \
    "s_branch HEAD0%=\n" \
    "RETRY2%=:\n" \
    "s_add_u32 s31, s31, s65\n" \
    "s_min_u32 s31, s31, 239\n" \
    "s_mov_b32 s65, 0\n" \
    "s_add_u32 s56, s56, 1\n" \
    "s_cmp_gt_u32 s56, 262144\n" \
    "s_cbranch_scc1 OK2%=\n" \
    "s_waitcnt lgkmcnt(0)\n" \
    "s_add_u32 s54, s46, 65536\n" \
    "s_nop 15\n" \
    "s_nop 8\n" \
    "buffer_load_dwordx4 v[128:131], v248, s[36:39], s54 offen sc1\n" \
    "buffer_load_dwordx4 v[132:135], v248, s[36:39], s54 offen offset:1024 sc1\n" \
    "s_nop 7\n" \
    "v_mov_b64 v[160:161], 0\n" \
    "v_mov_b64 v[162:163], 0\n" \
    "v_mov_b64 v[164:165], 0\n" \
    "v_mov_b64 v[166:167], 0\n" \
    "v_mov_b64 v[168:169], 0\n" \
    "v_mov_b64 v[170:171], 0\n" \
    "v_mov_b64 v[172:173], 0\n" \
    "v_mov_b64 v[174:175], 0\n" \
    "v_mov_b64 v[176:177], 0\n" \
    "v_mov_b64 v[178:179], 0\n" \
    "v_mov_b64 v[180:181], 0\n" \
    "v_mov_b64 v[182:183], 0\n" \
    "s_waitcnt vmcnt(0)\n" \
    "s_branch HEAD0%=\n" \
    "TAIL%=:\n" \
    "v_smfmac_f32_16x16x64_bf16 v[176:179], v[152:155], v[48:55], v254\n" \
    "v_smfmac_f32_16x16x64_bf16 v[180:183], v[152:155], v[112:119], v254\n" \
    "v_smfmac_f32_16x16x64_bf16 v[176:179], v[156:159], v[56:63], v254\n" \
    "v_smfmac_f32_16x16x64_bf16 v[180:183], v[156:159], v[120:127], v254\n" \
    "s_nop 7\n" \
    "v_add_f32 v176, v176, v177\n" \
    "v_add_f32 v178, v178, v179\n" \
    "v_add_f32 v180, v180, v181\n" \
    "v_add_f32 v182, v182, v183\n" \
    "v_cvt_pk_bf16_f32 v176, v176, v178\n" \
    "v_cvt_pk_bf16_f32 v177, v180, v182\n" \
    "v_cvt_pk_bf16_f32 v178, v180, v182\n" \
    "v_mov_b32 v179, 0\n" \
    "s_waitcnt lgkmcnt(0)\n" \
    "s_cmp_eq_u32 s58, 0\n" \
    "s_cbranch_scc1 PK%=_1\n" \
    "v_lshrrev_b32 v240, 1, v253\n" \
    "buffer_store_dwordx2 v[176:177], v240, s[36:39], s50 offen sc1\n" \
    "s_branch PE%=_1\n" \
    "PK%=_1:\n" \
    "v_add_u32 v240, s51, v250\n" \
    "ds_read_b64_tr_b16 v[192:193], v240\n" \
    "ds_read_b64_tr_b16 v[194:195], v240 offset:512\n" \
    "s_waitcnt lgkmcnt(0)\n" \
    "buffer_store_dwordx4 v[192:195], v253, s[36:39], s50 offen sc1\n" \
    "PE%=_1:\n" \
    "s_nop 1\n" \
    "v_add_u32 v244, 1, v244\n" \
    "s_waitcnt vmcnt(0)\n" \
    "s_waitcnt lgkmcnt(0)\n" \
    "s_barrier\n" \
    "s_endpgm\n" \
    "RE_w1:\n"

#define CHAIN_W2 \
    "RS_w2:\n" \
    "s_getpc_b64 s[2:3]\n" \
    "v_lshlrev_b32 v0, 3, v253\n" \
    "s_mov_b32 s0, RE_w2-RS_w2-8\n" \
    "v_add_u32 v1, 0, v0\n" \
    "v_min_u32 v1, s0, v1\n" \
    "global_load_dword v2, v1, s[2:3]\n" \
    "v_add_u32 v1, 8192, v0\n" \
    "v_min_u32 v1, s0, v1\n" \
    "global_load_dword v3, v1, s[2:3]\n" \
    "global_load_dwordx4 v[128:131], v[232:233], off\n" \
    "global_load_dwordx4 v[132:135], v[232:233], off offset:128\n" \
    "v_mov_b32 v242, 0\n" \
    "s_mov_b32 s54, 2162688\n" \
    "buffer_load_dwordx2 v[234:235], v242, s[36:39], s54 offen sc1\n" \
    "s_mov_b32 s42, 0x400000\n" \
    "s_mov_b32 s43, 0x20000\n" \
    "v_xor_b32 v222, 128, v219\n" \
    "s_mov_b32 s40, s62\n" \
    "s_and_b32 s41, s63, 0xffff\n" \
    "s_add_u32 m0, s51, 32768\n" \
    "s_mov_b32 s54, 0\n" \
    "buffer_load_dwordx4 v216, s[40:43], s54 offen lds\n" \
    "s_add_u32 m0, s51, 36864\n" \
    "s_mov_b32 s54, 131072\n" \
    "buffer_load_dwordx4 v216, s[40:43], s54 offen lds\n" \
    "s_add_u32 m0, s51, 40960\n" \
    "s_mov_b32 s54, 262144\n" \
    "buffer_load_dwordx4 v216, s[40:43], s54 offen lds\n" \
    "s_add_u32 m0, s51, 45056\n" \
    "s_mov_b32 s54, 393216\n" \
    "buffer_load_dwordx4 v216, s[40:43], s54 offen lds\n" \
    "s_add_u32 m0, s51, 49152\n" \
    "s_mov_b32 s54, 524288\n" \
    "buffer_load_dwordx4 v216, s[40:43], s54 offen lds\n" \
    "s_add_u32 m0, s51, 53248\n" \
    "s_mov_b32 s54, 655360\n" \
    "buffer_load_dwordx4 v216, s[40:43], s54 offen lds\n" \
    "s_add_u32 m0, s51, 57344\n" \
    "s_mov_b32 s54, 786432\n" \
    "buffer_load_dwordx4 v216, s[40:43], s54 offen lds\n" \
    "s_add_u32 m0, s51, 61440\n" \
    "s_mov_b32 s54, 917504\n" \
    "buffer_load_dwordx4 v216, s[40:43], s54 offen lds\n" \
    "s_add_u32 m0, s51, 65536\n" \
    "s_mov_b32 s54, 1048576\n" \
    "buffer_load_dwordx4 v216, s[40:43], s54 offen lds\n" \
    "s_add_u32 m0, s51, 69632\n" \
    "s_mov_b32 s54, 1179648\n" \
    "buffer_load_dwordx4 v216, s[40:43], s54 offen lds\n" \
    "s_add_u32 m0, s51, 73728\n" \
    "s_mov_b32 s54, 1310720\n" \
    "buffer_load_dwordx4 v216, s[40:43], s54 offen lds\n" \
    "s_add_u32 m0, s51, 77824\n" \
    "s_mov_b32 s54, 1441792\n" \
    "buffer_load_dwordx4 v216, s[40:43], s54 offen lds\n" \
    "s_add_u32 m0, s51, 81920\n" \
    "s_mov_b32 s54, 1572864\n" \
    "buffer_load_dwordx4 v216, s[40:43], s54 offen lds\n" \
    "s_add_u32 m0, s51, 86016\n" \
    "s_mov_b32 s54, 1703936\n" \
    "buffer_load_dwordx4 v216, s[40:43], s54 offen lds\n" \
    "s_add_u32 m0, s51, 90112\n" \
    "s_mov_b32 s54, 1835008\n" \
    "buffer_load_dwordx4 v216, s[40:43], s54 offen lds\n" \
    "s_add_u32 m0, s51, 94208\n" \
    "s_mov_b32 s54, 1966080\n" \
    "buffer_load_dwordx4 v216, s[40:43], s54 offen lds\n" \
    "s_add_u32 m0, s51, 98304\n" \
    "s_mov_b32 s54, 2097152\n" \
    "buffer_load_dwordx4 v216, s[40:43], s54 offen lds\n" \
    "s_add_u32 m0, s51, 102400\n" \
    "s_mov_b32 s54, 2228224\n" \
    "buffer_load_dwordx4 v216, s[40:43], s54 offen lds\n" \
    "s_add_u32 m0, s51, 106496\n" \
    "s_mov_b32 s54, 2359296\n" \
    "buffer_load_dwordx4 v216, s[40:43], s54 offen lds\n" \
    "s_add_u32 m0, s51, 110592\n" \
    "s_mov_b32 s54, 2490368\n" \
    "buffer_load_dwordx4 v216, s[40:43], s54 offen lds\n" \
    "s_add_u32 m0, s51, 114688\n" \
    "s_mov_b32 s54, 2621440\n" \
    "buffer_load_dwordx4 v216, s[40:43], s54 offen lds\n" \
    "s_add_u32 m0, s51, 118784\n" \
    "s_mov_b32 s54, 2752512\n" \
    "buffer_load_dwordx4 v216, s[40:43], s54 offen lds\n" \
    "s_add_u32 m0, s51, 122880\n" \
    "s_mov_b32 s54, 2883584\n" \
    "buffer_load_dwordx4 v216, s[40:43], s54 offen lds\n" \
    "s_add_u32 m0, s51, 126976\n" \
    "s_mov_b32 s54, 3014656\n" \
    "buffer_load_dwordx4 v216, s[40:43], s54 offen lds\n" \
    "s_add_u32 m0, s51, 131072\n" \
    "s_mov_b32 s54, 3145728\n" \
    "buffer_load_dwordx4 v216, s[40:43], s54 offen lds\n" \
    "s_add_u32 m0, s51, 135168\n" \
    "s_mov_b32 s54, 3276800\n" \
    "buffer_load_dwordx4 v216, s[40:43], s54 offen lds\n" \
    "s_add_u32 m0, s51, 139264\n" \
    "s_mov_b32 s54, 3407872\n" \
    "buffer_load_dwordx4 v216, s[40:43], s54 offen lds\n" \
    "s_add_u32 m0, s51, 143360\n" \
    "s_mov_b32 s54, 3538944\n" \
    "buffer_load_dwordx4 v216, s[40:43], s54 offen lds\n" \
    "s_add_u32 m0, s51, 147456\n" \
    "s_mov_b32 s54, 3670016\n" \
    "buffer_load_dwordx4 v216, s[40:43], s54 offen lds\n" \
    "s_add_u32 m0, s51, 151552\n" \
    "s_mov_b32 s54, 3801088\n" \
    "buffer_load_dwordx4 v216, s[40:43], s54 offen lds\n" \
    "s_add_u32 m0, s51, 155648\n" \
    "s_mov_b32 s54, 3932160\n" \
    "buffer_load_dwordx4 v216, s[40:43], s54 offen lds\n" \
    "s_add_u32 m0, s51, 159744\n" \
    "s_mov_b32 s54, 4063232\n" \
    "buffer_load_dwordx4 v216, s[40:43], s54 offen lds\n" \
    "s_mov_b32 s24, s22\n" \
    "s_mov_b32 s25, s23\n" \
    "s_memtime s[20:21]\n" \
    "s_memrealtime s[4:5]\n" \
    "s_nop 2\n" \
    "s_memrealtime s[6:7]\n" \
    "s_nop 2\n" \
    "s_memrealtime s[8:9]\n" \
    "s_nop 2\n" \
    "s_memrealtime s[10:11]\n" \
    "s_nop 2\n" \
    "s_memrealtime s[12:13]\n" \
    "s_nop 2\n" \
    "s_memrealtime s[14:15]\n" \
    "s_nop 2\n" \
    "s_memrealtime s[16:17]\n" \
    "s_nop 2\n" \
    "s_memrealtime s[18:19]\n" \
    "s_mov_b32 s29, 0\n" \
    "s_waitcnt lgkmcnt(0)\n" \
    "s_cmp_eq_u32 s6, s4\n" \
    "s_addc_u32 s29, s29, 0\n" \
    "s_cmp_eq_u32 s8, s4\n" \
    "s_addc_u32 s29, s29, 0\n" \
    "s_cmp_eq_u32 s10, s4\n" \
    "s_addc_u32 s29, s29, 0\n" \
    "s_cmp_eq_u32 s12, s4\n" \
    "s_addc_u32 s29, s29, 0\n" \
    "s_cmp_eq_u32 s14, s4\n" \
    "s_addc_u32 s29, s29, 0\n" \
    "s_cmp_eq_u32 s16, s4\n" \
    "s_addc_u32 s29, s29, 0\n" \
    "s_cmp_eq_u32 s18, s4\n" \
    "s_addc_u32 s29, s29, 0\n" \
    "s_lshl_b32 s29, s29, 4\n" \
    "s_add_u32 s23, s20, s29\n" \
    "s_lshl_b32 s22, s4, 2\n" \
    "s_lshl_b32 s28, s22, 6\n" \
    "s_waitcnt vmcnt(16)\n" \
    "s_barrier\n" \
    "v_xor_b32 v223, 0, v217\n" \
    "v_add_u32 v223, 32768, v223\n" \
    "ds_read_b64_tr_b16 a[160:161], v223\n" \
    "ds_read_b64_tr_b16 a[162:163], v223 offset:512\n" \
    "ds_read_b64_tr_b16 a[164:165], v223 offset:8192\n" \
    "ds_read_b64_tr_b16 a[166:167], v223 offset:8704\n" \
    "v_xor_b32 v223, 16384, v217\n" \
    "v_add_u32 v223, 32768, v223\n" \
    "ds_read_b64_tr_b16 a[168:169], v223\n" \
    "ds_read_b64_tr_b16 a[170:171], v223 offset:512\n" \
    "ds_read_b64_tr_b16 a[172:173], v223 offset:8192\n" \
    "ds_read_b64_tr_b16 a[174:175], v223 offset:8704\n" \
    "v_xor_b32 v223, 32768, v217\n" \
    "v_add_u32 v223, 32768, v223\n" \
    "ds_read_b64_tr_b16 a[176:177], v223\n" \
    "ds_read_b64_tr_b16 a[178:179], v223 offset:512\n" \
    "ds_read_b64_tr_b16 a[180:181], v223 offset:8192\n" \
    "ds_read_b64_tr_b16 a[182:183], v223 offset:8704\n" \
    "v_xor_b32 v223, 49152, v217\n" \
    "v_add_u32 v223, 32768, v223\n" \
    "ds_read_b64_tr_b16 a[184:185], v223\n" \
    "ds_read_b64_tr_b16 a[186:187], v223 offset:512\n" \
    "ds_read_b64_tr_b16 a[188:189], v223 offset:8192\n" \
    "ds_read_b64_tr_b16 a[190:191], v223 offset:8704\n" \
    "v_xor_b32 v223, 0, v218\n" \
    "v_add_u32 v223, 32768, v223\n" \
    "ds_read_b64_tr_b16 a[224:225], v223\n" \
    "ds_read_b64_tr_b16 a[226:227], v223 offset:512\n" \
    "ds_read_b64_tr_b16 a[228:229], v223 offset:8192\n" \
    "ds_read_b64_tr_b16 a[230:231], v223 offset:8704\n" \
    "v_xor_b32 v223, 16384, v218\n" \
    "v_add_u32 v223, 32768, v223\n" \
    "ds_read_b64_tr_b16 a[232:233], v223\n" \
    "ds_read_b64_tr_b16 a[234:235], v223 offset:512\n" \
    "ds_read_b64_tr_b16 a[236:237], v223 offset:8192\n" \
    "ds_read_b64_tr_b16 a[238:239], v223 offset:8704\n" \
    "v_xor_b32 v223, 32768, v218\n" \
    "v_add_u32 v223, 32768, v223\n" \
    "ds_read_b64_tr_b16 a[240:241], v223\n" \
    "ds_read_b64_tr_b16 a[242:243], v223 offset:512\n" \
    "ds_read_b64_tr_b16 a[244:245], v223 offset:8192\n" \
    "ds_read_b64_tr_b16 a[246:247], v223 offset:8704\n" \
    "v_xor_b32 v223, 49152, v218\n" \
    "v_add_u32 v223, 32768, v223\n" \
    "ds_read_b64_tr_b16 a[248:249], v223\n" \
    "ds_read_b64_tr_b16 a[250:251], v223 offset:512\n" \
    "ds_read_b64_tr_b16 a[252:253], v223 offset:8192\n" \
    "ds_read_b64_tr_b16 a[254:255], v223 offset:8704\n" \
    "s_waitcnt lgkmcnt(0)\n" \
    "s_barrier\n" \
    "s_mov_b32 s40, s64\n" \
    "s_and_b32 s41, s65, 0xffff\n" \
    "s_add_u32 m0, s51, 32768\n" \
    "s_mov_b32 s54, 0\n" \
    "buffer_load_dwordx4 v216, s[40:43], s54 offen lds\n" \
    "s_add_u32 m0, s51, 36864\n" \
    "s_mov_b32 s54, 131072\n" \
    "buffer_load_dwordx4 v216, s[40:43], s54 offen lds\n" \
    "s_add_u32 m0, s51, 40960\n" \
    "s_mov_b32 s54, 262144\n" \
    "buffer_load_dwordx4 v216, s[40:43], s54 offen lds\n" \
    "s_add_u32 m0, s51, 45056\n" \
    "s_mov_b32 s54, 393216\n" \
    "buffer_load_dwordx4 v216, s[40:43], s54 offen lds\n" \
    "s_add_u32 m0, s51, 49152\n" \
    "s_mov_b32 s54, 524288\n" \
    "buffer_load_dwordx4 v216, s[40:43], s54 offen lds\n" \
    "s_add_u32 m0, s51, 53248\n" \
    "s_mov_b32 s54, 655360\n" \
    "buffer_load_dwordx4 v216, s[40:43], s54 offen lds\n" \
    "s_add_u32 m0, s51, 57344\n" \
    "s_mov_b32 s54, 786432\n" \
    "buffer_load_dwordx4 v216, s[40:43], s54 offen lds\n" \
    "s_add_u32 m0, s51, 61440\n" \
    "s_mov_b32 s54, 917504\n" \
    "buffer_load_dwordx4 v216, s[40:43], s54 offen lds\n" \
    "s_add_u32 m0, s51, 65536\n" \
    "s_mov_b32 s54, 1048576\n" \
    "buffer_load_dwordx4 v216, s[40:43], s54 offen lds\n" \
    "s_add_u32 m0, s51, 69632\n" \
    "s_mov_b32 s54, 1179648\n" \
    "buffer_load_dwordx4 v216, s[40:43], s54 offen lds\n" \
    "s_add_u32 m0, s51, 73728\n" \
    "s_mov_b32 s54, 1310720\n" \
    "buffer_load_dwordx4 v216, s[40:43], s54 offen lds\n" \
    "s_add_u32 m0, s51, 77824\n" \
    "s_mov_b32 s54, 1441792\n" \
    "buffer_load_dwordx4 v216, s[40:43], s54 offen lds\n" \
    "s_add_u32 m0, s51, 81920\n" \
    "s_mov_b32 s54, 1572864\n" \
    "buffer_load_dwordx4 v216, s[40:43], s54 offen lds\n" \
    "s_add_u32 m0, s51, 86016\n" \
    "s_mov_b32 s54, 1703936\n" \
    "buffer_load_dwordx4 v216, s[40:43], s54 offen lds\n" \
    "s_add_u32 m0, s51, 90112\n" \
    "s_mov_b32 s54, 1835008\n" \
    "buffer_load_dwordx4 v216, s[40:43], s54 offen lds\n" \
    "s_add_u32 m0, s51, 94208\n" \
    "s_mov_b32 s54, 1966080\n" \
    "buffer_load_dwordx4 v216, s[40:43], s54 offen lds\n" \
    "s_waitcnt vmcnt(16)\n" \
    "s_barrier\n" \
    "v_xor_b32 v223, 0, v217\n" \
    "v_add_u32 v223, 98304, v223\n" \
    "ds_read_b64_tr_b16 a[128:129], v223\n" \
    "ds_read_b64_tr_b16 a[130:131], v223 offset:512\n" \
    "ds_read_b64_tr_b16 a[132:133], v223 offset:8192\n" \
    "ds_read_b64_tr_b16 a[134:135], v223 offset:8704\n" \
    "v_xor_b32 v223, 16384, v217\n" \
    "v_add_u32 v223, 98304, v223\n" \
    "ds_read_b64_tr_b16 a[136:137], v223\n" \
    "ds_read_b64_tr_b16 a[138:139], v223 offset:512\n" \
    "ds_read_b64_tr_b16 a[140:141], v223 offset:8192\n" \
    "ds_read_b64_tr_b16 a[142:143], v223 offset:8704\n" \
    "v_xor_b32 v223, 32768, v217\n" \
    "v_add_u32 v223, 98304, v223\n" \
    "ds_read_b64_tr_b16 a[144:145], v223\n" \
    "ds_read_b64_tr_b16 a[146:147], v223 offset:512\n" \
    "ds_read_b64_tr_b16 a[148:149], v223 offset:8192\n" \
    "ds_read_b64_tr_b16 a[150:151], v223 offset:8704\n" \
    "v_xor_b32 v223, 49152, v217\n" \
    "v_add_u32 v223, 98304, v223\n" \
    "ds_read_b64_tr_b16 a[152:153], v223\n" \
    "ds_read_b64_tr_b16 a[154:155], v223 offset:512\n" \
    "ds_read_b64_tr_b16 a[156:157], v223 offset:8192\n" \
    "ds_read_b64_tr_b16 a[158:159], v223 offset:8704\n" \
    "v_xor_b32 v223, 0, v218\n" \
    "v_add_u32 v223, 98304, v223\n" \
    "ds_read_b64_tr_b16 a[192:193], v223\n" \
    "ds_read_b64_tr_b16 a[194:195], v223 offset:512\n" \
    "ds_read_b64_tr_b16 a[196:197], v223 offset:8192\n" \
    "ds_read_b64_tr_b16 a[198:199], v223 offset:8704\n" \
    "v_xor_b32 v223, 16384, v218\n" \
    "v_add_u32 v223, 98304, v223\n" \
    "ds_read_b64_tr_b16 a[200:201], v223\n" \
    "ds_read_b64_tr_b16 a[202:203], v223 offset:512\n" \
    "ds_read_b64_tr_b16 a[204:205], v223 offset:8192\n" \
    "ds_read_b64_tr_b16 a[206:207], v223 offset:8704\n" \
    "v_xor_b32 v223, 32768, v218\n" \
    "v_add_u32 v223, 98304, v223\n" \
    "ds_read_b64_tr_b16 a[208:209], v223\n" \
    "ds_read_b64_tr_b16 a[210:211], v223 offset:512\n" \
    "ds_read_b64_tr_b16 a[212:213], v223 offset:8192\n" \
    "ds_read_b64_tr_b16 a[214:215], v223 offset:8704\n" \
    "v_xor_b32 v223, 49152, v218\n" \
    "v_add_u32 v223, 98304, v223\n" \
    "ds_read_b64_tr_b16 a[216:217], v223\n" \
    "ds_read_b64_tr_b16 a[218:219], v223 offset:512\n" \
    "ds_read_b64_tr_b16 a[220:221], v223 offset:8192\n" \
    "ds_read_b64_tr_b16 a[222:223], v223 offset:8704\n" \
    "s_waitcnt lgkmcnt(0)\n" \
    "s_barrier\n" \
    "s_add_u32 m0, s51, 98304\n" \
    "s_mov_b32 s54, 2097152\n" \
    "buffer_load_dwordx4 v216, s[40:43], s54 offen lds\n" \
    "s_add_u32 m0, s51, 102400\n" \
    "s_mov_b32 s54, 2228224\n" \
    "buffer_load_dwordx4 v216, s[40:43], s54 offen lds\n" \
    "s_add_u32 m0, s51, 106496\n" \
    "s_mov_b32 s54, 2359296\n" \
    "buffer_load_dwordx4 v216, s[40:43], s54 offen lds\n" \
    "s_add_u32 m0, s51, 110592\n" \
    "s_mov_b32 s54, 2490368\n" \
    "buffer_load_dwordx4 v216, s[40:43], s54 offen lds\n" \
    "s_add_u32 m0, s51, 114688\n" \
    "s_mov_b32 s54, 2621440\n" \
    "buffer_load_dwordx4 v216, s[40:43], s54 offen lds\n" \
    "s_add_u32 m0, s51, 118784\n" \
    "s_mov_b32 s54, 2752512\n" \
    "buffer_load_dwordx4 v216, s[40:43], s54 offen lds\n" \
    "s_add_u32 m0, s51, 122880\n" \
    "s_mov_b32 s54, 2883584\n" \
    "buffer_load_dwordx4 v216, s[40:43], s54 offen lds\n" \
    "s_add_u32 m0, s51, 126976\n" \
    "s_mov_b32 s54, 3014656\n" \
    "buffer_load_dwordx4 v216, s[40:43], s54 offen lds\n" \
    "s_add_u32 m0, s51, 131072\n" \
    "s_mov_b32 s54, 3145728\n" \
    "buffer_load_dwordx4 v216, s[40:43], s54 offen lds\n" \
    "s_add_u32 m0, s51, 135168\n" \
    "s_mov_b32 s54, 3276800\n" \
    "buffer_load_dwordx4 v216, s[40:43], s54 offen lds\n" \
    "s_add_u32 m0, s51, 139264\n" \
    "s_mov_b32 s54, 3407872\n" \
    "buffer_load_dwordx4 v216, s[40:43], s54 offen lds\n" \
    "s_add_u32 m0, s51, 143360\n" \
    "s_mov_b32 s54, 3538944\n" \
    "buffer_load_dwordx4 v216, s[40:43], s54 offen lds\n" \
    "s_add_u32 m0, s51, 147456\n" \
    "s_mov_b32 s54, 3670016\n" \
    "buffer_load_dwordx4 v216, s[40:43], s54 offen lds\n" \
    "s_add_u32 m0, s51, 151552\n" \
    "s_mov_b32 s54, 3801088\n" \
    "buffer_load_dwordx4 v216, s[40:43], s54 offen lds\n" \
    "s_add_u32 m0, s51, 155648\n" \
    "s_mov_b32 s54, 3932160\n" \
    "buffer_load_dwordx4 v216, s[40:43], s54 offen lds\n" \
    "s_add_u32 m0, s51, 159744\n" \
    "s_mov_b32 s54, 4063232\n" \
    "buffer_load_dwordx4 v216, s[40:43], s54 offen lds\n" \
    "s_waitcnt vmcnt(16)\n" \
    "s_barrier\n" \
    "v_xor_b32 v223, 0, v217\n" \
    "v_add_u32 v223, 32768, v223\n" \
    "ds_read_b64_tr_b16 v[32:33], v223\n" \
    "ds_read_b64_tr_b16 v[34:35], v223 offset:512\n" \
    "ds_read_b64_tr_b16 v[36:37], v223 offset:8192\n" \
    "ds_read_b64_tr_b16 v[38:39], v223 offset:8704\n" \
    "v_xor_b32 v223, 16384, v217\n" \
    "v_add_u32 v223, 32768, v223\n" \
    "ds_read_b64_tr_b16 v[40:41], v223\n" \
    "ds_read_b64_tr_b16 v[42:43], v223 offset:512\n" \
    "ds_read_b64_tr_b16 v[44:45], v223 offset:8192\n" \
    "ds_read_b64_tr_b16 v[46:47], v223 offset:8704\n" \
    "v_xor_b32 v223, 32768, v217\n" \
    "v_add_u32 v223, 32768, v223\n" \
    "ds_read_b64_tr_b16 v[48:49], v223\n" \
    "ds_read_b64_tr_b16 v[50:51], v223 offset:512\n" \
    "ds_read_b64_tr_b16 v[52:53], v223 offset:8192\n" \
    "ds_read_b64_tr_b16 v[54:55], v223 offset:8704\n" \
    "v_xor_b32 v223, 49152, v217\n" \
    "v_add_u32 v223, 32768, v223\n" \
    "ds_read_b64_tr_b16 v[56:57], v223\n" \
    "ds_read_b64_tr_b16 v[58:59], v223 offset:512\n" \
    "ds_read_b64_tr_b16 v[60:61], v223 offset:8192\n" \
    "ds_read_b64_tr_b16 v[62:63], v223 offset:8704\n" \
    "v_xor_b32 v223, 0, v218\n" \
    "v_add_u32 v223, 32768, v223\n" \
    "ds_read_b64_tr_b16 v[96:97], v223\n" \
    "ds_read_b64_tr_b16 v[98:99], v223 offset:512\n" \
    "ds_read_b64_tr_b16 v[100:101], v223 offset:8192\n" \
    "ds_read_b64_tr_b16 v[102:103], v223 offset:8704\n" \
    "v_xor_b32 v223, 16384, v218\n" \
    "v_add_u32 v223, 32768, v223\n" \
    "ds_read_b64_tr_b16 v[104:105], v223\n" \
    "ds_read_b64_tr_b16 v[106:107], v223 offset:512\n" \
    "ds_read_b64_tr_b16 v[108:109], v223 offset:8192\n" \
    "ds_read_b64_tr_b16 v[110:111], v223 offset:8704\n" \
    "v_xor_b32 v223, 32768, v218\n" \
    "v_add_u32 v223, 32768, v223\n" \
    "ds_read_b64_tr_b16 v[112:113], v223\n" \
    "ds_read_b64_tr_b16 v[114:115], v223 offset:512\n" \
    "ds_read_b64_tr_b16 v[116:117], v223 offset:8192\n" \
    "ds_read_b64_tr_b16 v[118:119], v223 offset:8704\n" \
    "v_xor_b32 v223, 49152, v218\n" \
    "v_add_u32 v223, 32768, v223\n" \
    "ds_read_b64_tr_b16 v[120:121], v223\n" \
    "ds_read_b64_tr_b16 v[122:123], v223 offset:512\n" \
    "ds_read_b64_tr_b16 v[124:125], v223 offset:8192\n" \
    "ds_read_b64_tr_b16 v[126:127], v223 offset:8704\n" \
    "s_waitcnt lgkmcnt(0)\n" \
    "s_barrier\n" \
    "s_mov_b32 s40, s66\n" \
    "s_and_b32 s41, s67, 0xffff\n" \
    "s_add_u32 m0, s51, 32768\n" \
    "s_mov_b32 s54, 0\n" \
    "buffer_load_dwordx4 v219, s[40:43], s54 offen lds\n" \
    "s_add_u32 m0, s51, 36864\n" \
    "s_mov_b32 s54, 8192\n" \
    "buffer_load_dwordx4 v219, s[40:43], s54 offen lds\n" \
    "s_add_u32 m0, s51, 40960\n" \
    "s_mov_b32 s54, 16384\n" \
    "buffer_load_dwordx4 v222, s[40:43], s54 offen lds\n" \
    "s_add_u32 m0, s51, 45056\n" \
    "s_mov_b32 s54, 24576\n" \
    "buffer_load_dwordx4 v222, s[40:43], s54 offen lds\n" \
    "s_add_u32 m0, s51, 49152\n" \
    "s_mov_b32 s54, 32768\n" \
    "buffer_load_dwordx4 v219, s[40:43], s54 offen lds\n" \
    "s_add_u32 m0, s51, 53248\n" \
    "s_mov_b32 s54, 40960\n" \
    "buffer_load_dwordx4 v219, s[40:43], s54 offen lds\n" \
    "s_add_u32 m0, s51, 57344\n" \
    "s_mov_b32 s54, 49152\n" \
    "buffer_load_dwordx4 v222, s[40:43], s54 offen lds\n" \
    "s_add_u32 m0, s51, 61440\n" \
    "s_mov_b32 s54, 57344\n" \
    "buffer_load_dwordx4 v222, s[40:43], s54 offen lds\n" \
    "s_add_u32 m0, s51, 65536\n" \
    "s_mov_b32 s54, 65536\n" \
    "buffer_load_dwordx4 v219, s[40:43], s54 offen lds\n" \
    "s_add_u32 m0, s51, 69632\n" \
    "s_mov_b32 s54, 73728\n" \
    "buffer_load_dwordx4 v219, s[40:43], s54 offen lds\n" \
    "s_add_u32 m0, s51, 73728\n" \
    "s_mov_b32 s54, 81920\n" \
    "buffer_load_dwordx4 v222, s[40:43], s54 offen lds\n" \
    "s_add_u32 m0, s51, 77824\n" \
    "s_mov_b32 s54, 90112\n" \
    "buffer_load_dwordx4 v222, s[40:43], s54 offen lds\n" \
    "s_add_u32 m0, s51, 81920\n" \
    "s_mov_b32 s54, 98304\n" \
    "buffer_load_dwordx4 v219, s[40:43], s54 offen lds\n" \
    "s_add_u32 m0, s51, 86016\n" \
    "s_mov_b32 s54, 106496\n" \
    "buffer_load_dwordx4 v219, s[40:43], s54 offen lds\n" \
    "s_add_u32 m0, s51, 90112\n" \
    "s_mov_b32 s54, 114688\n" \
    "buffer_load_dwordx4 v222, s[40:43], s54 offen lds\n" \
    "s_add_u32 m0, s51, 94208\n" \
    "s_mov_b32 s54, 122880\n" \
    "buffer_load_dwordx4 v222, s[40:43], s54 offen lds\n" \
    "s_waitcnt vmcnt(16)\n" \
    "s_barrier\n" \
    "v_xor_b32 v223, 0, v217\n" \
    "v_add_u32 v223, 98304, v223\n" \
    "ds_read_b64_tr_b16 v[0:1], v223\n" \
    "ds_read_b64_tr_b16 v[2:3], v223 offset:512\n" \
    "ds_read_b64_tr_b16 v[4:5], v223 offset:8192\n" \
    "ds_read_b64_tr_b16 v[6:7], v223 offset:8704\n" \
    "v_xor_b32 v223, 16384, v217\n" \
    "v_add_u32 v223, 98304, v223\n" \
    "ds_read_b64_tr_b16 v[8:9], v223\n" \
    "ds_read_b64_tr_b16 v[10:11], v223 offset:512\n" \
    "ds_read_b64_tr_b16 v[12:13], v223 offset:8192\n" \
    "ds_read_b64_tr_b16 v[14:15], v223 offset:8704\n" \
    "v_xor_b32 v223, 32768, v217\n" \
    "v_add_u32 v223, 98304, v223\n" \
    "ds_read_b64_tr_b16 v[16:17], v223\n" \
    "ds_read_b64_tr_b16 v[18:19], v223 offset:512\n" \
    "ds_read_b64_tr_b16 v[20:21], v223 offset:8192\n" \
    "ds_read_b64_tr_b16 v[22:23], v223 offset:8704\n" \
    "v_xor_b32 v223, 49152, v217\n" \
    "v_add_u32 v223, 98304, v223\n" \
    "ds_read_b64_tr_b16 v[24:25], v223\n" \
    "ds_read_b64_tr_b16 v[26:27], v223 offset:512\n" \
    "ds_read_b64_tr_b16 v[28:29], v223 offset:8192\n" \
    "ds_read_b64_tr_b16 v[30:31], v223 offset:8704\n" \
    "v_xor_b32 v223, 0, v218\n" \
    "v_add_u32 v223, 98304, v223\n" \
    "ds_read_b64_tr_b16 v[64:65], v223\n" \
    "ds_read_b64_tr_b16 v[66:67], v223 offset:512\n" \
    "ds_read_b64_tr_b16 v[68:69], v223 offset:8192\n" \
    "ds_read_b64_tr_b16 v[70:71], v223 offset:8704\n" \
    "v_xor_b32 v223, 16384, v218\n" \
    "v_add_u32 v223, 98304, v223\n" \
    "ds_read_b64_tr_b16 v[72:73], v223\n" \
    "ds_read_b64_tr_b16 v[74:75], v223 offset:512\n" \
    "ds_read_b64_tr_b16 v[76:77], v223 offset:8192\n" \
    "ds_read_b64_tr_b16 v[78:79], v223 offset:8704\n" \
    "v_xor_b32 v223, 32768, v218\n" \
    "v_add_u32 v223, 98304, v223\n" \
    "ds_read_b64_tr_b16 v[80:81], v223\n" \
    "ds_read_b64_tr_b16 v[82:83], v223 offset:512\n" \
    "ds_read_b64_tr_b16 v[84:85], v223 offset:8192\n" \
    "ds_read_b64_tr_b16 v[86:87], v223 offset:8704\n" \
    "v_xor_b32 v223, 49152, v218\n" \
    "v_add_u32 v223, 98304, v223\n" \
    "ds_read_b64_tr_b16 v[88:89], v223\n" \
    "ds_read_b64_tr_b16 v[90:91], v223 offset:512\n" \
    "ds_read_b64_tr_b16 v[92:93], v223 offset:8192\n" \
    "ds_read_b64_tr_b16 v[94:95], v223 offset:8704\n" \
    "s_waitcnt lgkmcnt(0)\n" \
    "s_barrier\n" \
    "s_mov_b32 s40, s60\n" \
    "s_and_b32 s41, s61, 0xffff\n" \
    "s_add_u32 m0, s51, 98304\n" \
    "s_mov_b32 s54, 0\n" \
    "buffer_load_dwordx4 v216, s[40:43], s54 offen lds\n" \
    "s_add_u32 m0, s51, 102400\n" \
    "s_mov_b32 s54, 131072\n" \
    "buffer_load_dwordx4 v216, s[40:43], s54 offen lds\n" \
    "s_add_u32 m0, s51, 106496\n" \
    "s_mov_b32 s54, 262144\n" \
    "buffer_load_dwordx4 v216, s[40:43], s54 offen lds\n" \
    "s_add_u32 m0, s51, 110592\n" \
    "s_mov_b32 s54, 393216\n" \
    "buffer_load_dwordx4 v216, s[40:43], s54 offen lds\n" \
    "s_add_u32 m0, s51, 114688\n" \
    "s_mov_b32 s54, 524288\n" \
    "buffer_load_dwordx4 v216, s[40:43], s54 offen lds\n" \
    "s_add_u32 m0, s51, 118784\n" \
    "s_mov_b32 s54, 655360\n" \
    "buffer_load_dwordx4 v216, s[40:43], s54 offen lds\n" \
    "s_add_u32 m0, s51, 122880\n" \
    "s_mov_b32 s54, 786432\n" \
    "buffer_load_dwordx4 v216, s[40:43], s54 offen lds\n" \
    "s_add_u32 m0, s51, 126976\n" \
    "s_mov_b32 s54, 917504\n" \
    "buffer_load_dwordx4 v216, s[40:43], s54 offen lds\n" \
    "s_add_u32 m0, s51, 131072\n" \
    "s_mov_b32 s54, 1048576\n" \
    "buffer_load_dwordx4 v216, s[40:43], s54 offen lds\n" \
    "s_add_u32 m0, s51, 135168\n" \
    "s_mov_b32 s54, 1179648\n" \
    "buffer_load_dwordx4 v216, s[40:43], s54 offen lds\n" \
    "s_add_u32 m0, s51, 139264\n" \
    "s_mov_b32 s54, 1310720\n" \
    "buffer_load_dwordx4 v216, s[40:43], s54 offen lds\n" \
    "s_add_u32 m0, s51, 143360\n" \
    "s_mov_b32 s54, 1441792\n" \
    "buffer_load_dwordx4 v216, s[40:43], s54 offen lds\n" \
    "s_add_u32 m0, s51, 147456\n" \
    "s_mov_b32 s54, 1572864\n" \
    "buffer_load_dwordx4 v216, s[40:43], s54 offen lds\n" \
    "s_add_u32 m0, s51, 151552\n" \
    "s_mov_b32 s54, 1703936\n" \
    "buffer_load_dwordx4 v216, s[40:43], s54 offen lds\n" \
    "s_add_u32 m0, s51, 155648\n" \
    "s_mov_b32 s54, 1835008\n" \
    "buffer_load_dwordx4 v216, s[40:43], s54 offen lds\n" \
    "s_add_u32 m0, s51, 159744\n" \
    "s_mov_b32 s54, 1966080\n" \
    "buffer_load_dwordx4 v216, s[40:43], s54 offen lds\n" \
    "s_waitcnt vmcnt(16)\n" \
    "s_barrier\n" \
    "v_xor_b32 v223, 0, v220\n" \
    "ds_read_b64_tr_b16 v[136:137], v223\n" \
    "ds_read_b64_tr_b16 v[138:139], v223 offset:1024\n" \
    "ds_read_b64_tr_b16 v[140:141], v223 offset:16384\n" \
    "ds_read_b64_tr_b16 v[142:143], v223 offset:17408\n" \
    "ds_read_b64_tr_b16 v[144:145], v223 offset:32768\n" \
    "ds_read_b64_tr_b16 v[146:147], v223 offset:33792\n" \
    "ds_read_b64_tr_b16 v[148:149], v223 offset:49152\n" \
    "ds_read_b64_tr_b16 v[150:151], v223 offset:50176\n" \
    "v_xor_b32 v223, 32, v220\n" \
    "ds_read_b64_tr_b16 v[152:153], v223\n" \
    "ds_read_b64_tr_b16 v[154:155], v223 offset:1024\n" \
    "ds_read_b64_tr_b16 v[156:157], v223 offset:16384\n" \
    "ds_read_b64_tr_b16 v[158:159], v223 offset:17408\n" \
    "ds_read_b64_tr_b16 v[160:161], v223 offset:32768\n" \
    "ds_read_b64_tr_b16 v[162:163], v223 offset:33792\n" \
    "ds_read_b64_tr_b16 v[164:165], v223 offset:49152\n" \
    "ds_read_b64_tr_b16 v[166:167], v223 offset:50176\n" \
    "v_xor_b32 v223, 64, v220\n" \
    "ds_read_b64_tr_b16 v[168:169], v223\n" \
    "ds_read_b64_tr_b16 v[170:171], v223 offset:1024\n" \
    "ds_read_b64_tr_b16 v[172:173], v223 offset:16384\n" \
    "ds_read_b64_tr_b16 v[174:175], v223 offset:17408\n" \
    "ds_read_b64_tr_b16 v[176:177], v223 offset:32768\n" \
    "ds_read_b64_tr_b16 v[178:179], v223 offset:33792\n" \
    "ds_read_b64_tr_b16 v[180:181], v223 offset:49152\n" \
    "ds_read_b64_tr_b16 v[182:183], v223 offset:50176\n" \
    "v_xor_b32 v223, 96, v220\n" \
    "ds_read_b64_tr_b16 v[184:185], v223\n" \
    "ds_read_b64_tr_b16 v[186:187], v223 offset:1024\n" \
    "ds_read_b64_tr_b16 v[188:189], v223 offset:16384\n" \
    "ds_read_b64_tr_b16 v[190:191], v223 offset:17408\n" \
    "ds_read_b64_tr_b16 v[192:193], v223 offset:32768\n" \
    "ds_read_b64_tr_b16 v[194:195], v223 offset:33792\n" \
    "ds_read_b64_tr_b16 v[196:197], v223 offset:49152\n" \
    "ds_read_b64_tr_b16 v[198:199], v223 offset:50176\n" \
    "s_waitcnt lgkmcnt(0)\n" \
    "s_barrier\n" \
    "v_lshlrev_b32 v206, 3, v253\n" \
    "s_mov_b32 s54, 2163712\n" \
    "buffer_load_dwordx2 v[204:205], v206, s[36:39], s54 offen sc1\n" \
    "s_add_u32 m0, s51, 32768\n" \
    "s_mov_b32 s54, 2097152\n" \
    "buffer_load_dwordx4 v216, s[40:43], s54 offen lds\n" \
    "s_add_u32 m0, s51, 36864\n" \
    "s_mov_b32 s54, 2228224\n" \
    "buffer_load_dwordx4 v216, s[40:43], s54 offen lds\n" \
    "s_add_u32 m0, s51, 40960\n" \
    "s_mov_b32 s54, 2359296\n" \
    "buffer_load_dwordx4 v216, s[40:43], s54 offen lds\n" \
    "s_add_u32 m0, s51, 45056\n" \
    "s_mov_b32 s54, 2490368\n" \
    "buffer_load_dwordx4 v216, s[40:43], s54 offen lds\n" \
    "s_add_u32 m0, s51, 49152\n" \
    "s_mov_b32 s54, 2621440\n" \
    "buffer_load_dwordx4 v216, s[40:43], s54 offen lds\n" \
    "s_add_u32 m0, s51, 53248\n" \
    "s_mov_b32 s54, 2752512\n" \
    "buffer_load_dwordx4 v216, s[40:43], s54 offen lds\n" \
    "s_add_u32 m0, s51, 57344\n" \
    "s_mov_b32 s54, 2883584\n" \
    "buffer_load_dwordx4 v216, s[40:43], s54 offen lds\n" \
    "s_add_u32 m0, s51, 61440\n" \
    "s_mov_b32 s54, 3014656\n" \
    "buffer_load_dwordx4 v216, s[40:43], s54 offen lds\n" \
    "s_add_u32 m0, s51, 65536\n" \
    "s_mov_b32 s54, 3145728\n" \
    "buffer_load_dwordx4 v216, s[40:43], s54 offen lds\n" \
    "s_add_u32 m0, s51, 69632\n" \
    "s_mov_b32 s54, 3276800\n" \
    "buffer_load_dwordx4 v216, s[40:43], s54 offen lds\n" \
    "s_add_u32 m0, s51, 73728\n" \
    "s_mov_b32 s54, 3407872\n" \
    "buffer_load_dwordx4 v216, s[40:43], s54 offen lds\n" \
    "s_add_u32 m0, s51, 77824\n" \
    "s_mov_b32 s54, 3538944\n" \
    "buffer_load_dwordx4 v216, s[40:43], s54 offen lds\n" \
    "s_add_u32 m0, s51, 81920\n" \
    "s_mov_b32 s54, 3670016\n" \
    "buffer_load_dwordx4 v216, s[40:43], s54 offen lds\n" \
    "s_add_u32 m0, s51, 86016\n" \
    "s_mov_b32 s54, 3801088\n" \
    "buffer_load_dwordx4 v216, s[40:43], s54 offen lds\n" \
    "s_add_u32 m0, s51, 90112\n" \
    "s_mov_b32 s54, 3932160\n" \
    "buffer_load_dwordx4 v216, s[40:43], s54 offen lds\n" \
    "s_add_u32 m0, s51, 94208\n" \
    "s_mov_b32 s54, 4063232\n" \
    "buffer_load_dwordx4 v216, s[40:43], s54 offen lds\n" \
    "s_waitcnt vmcnt(16)\n" \
    "s_barrier\n" \
    "v_xor_b32 v223, 0, v217\n" \
    "v_add_u32 v223, 98304, v223\n" \
    "ds_read_b64_tr_b16 a[32:33], v223\n" \
    "ds_read_b64_tr_b16 a[34:35], v223 offset:512\n" \
    "ds_read_b64_tr_b16 a[36:37], v223 offset:8192\n" \
    "ds_read_b64_tr_b16 a[38:39], v223 offset:8704\n" \
    "v_xor_b32 v223, 16384, v217\n" \
    "v_add_u32 v223, 98304, v223\n" \
    "ds_read_b64_tr_b16 a[40:41], v223\n" \
    "ds_read_b64_tr_b16 a[42:43], v223 offset:512\n" \
    "ds_read_b64_tr_b16 a[44:45], v223 offset:8192\n" \
    "ds_read_b64_tr_b16 a[46:47], v223 offset:8704\n" \
    "v_xor_b32 v223, 32768, v217\n" \
    "v_add_u32 v223, 98304, v223\n" \
    "ds_read_b64_tr_b16 a[48:49], v223\n" \
    "ds_read_b64_tr_b16 a[50:51], v223 offset:512\n" \
    "ds_read_b64_tr_b16 a[52:53], v223 offset:8192\n" \
    "ds_read_b64_tr_b16 a[54:55], v223 offset:8704\n" \
    "v_xor_b32 v223, 49152, v217\n" \
    "v_add_u32 v223, 98304, v223\n" \
    "ds_read_b64_tr_b16 a[56:57], v223\n" \
    "ds_read_b64_tr_b16 a[58:59], v223 offset:512\n" \
    "ds_read_b64_tr_b16 a[60:61], v223 offset:8192\n" \
    "ds_read_b64_tr_b16 a[62:63], v223 offset:8704\n" \
    "v_xor_b32 v223, 0, v218\n" \
    "v_add_u32 v223, 98304, v223\n" \
    "ds_read_b64_tr_b16 a[96:97], v223\n" \
    "ds_read_b64_tr_b16 a[98:99], v223 offset:512\n" \
    "ds_read_b64_tr_b16 a[100:101], v223 offset:8192\n" \
    "ds_read_b64_tr_b16 a[102:103], v223 offset:8704\n" \
    "v_xor_b32 v223, 16384, v218\n" \
    "v_add_u32 v223, 98304, v223\n" \
    "ds_read_b64_tr_b16 a[104:105], v223\n" \
    "ds_read_b64_tr_b16 a[106:107], v223 offset:512\n" \
    "ds_read_b64_tr_b16 a[108:109], v223 offset:8192\n" \
    "ds_read_b64_tr_b16 a[110:111], v223 offset:8704\n" \
    "v_xor_b32 v223, 32768, v218\n" \
    "v_add_u32 v223, 98304, v223\n" \
    "ds_read_b64_tr_b16 a[112:113], v223\n" \
    "ds_read_b64_tr_b16 a[114:115], v223 offset:512\n" \
    "ds_read_b64_tr_b16 a[116:117], v223 offset:8192\n" \
    "ds_read_b64_tr_b16 a[118:119], v223 offset:8704\n" \
    "v_xor_b32 v223, 49152, v218\n" \
    "v_add_u32 v223, 98304, v223\n" \
    "ds_read_b64_tr_b16 a[120:121], v223\n" \
    "ds_read_b64_tr_b16 a[122:123], v223 offset:512\n" \
    "ds_read_b64_tr_b16 a[124:125], v223 offset:8192\n" \
    "ds_read_b64_tr_b16 a[126:127], v223 offset:8704\n" \
    "s_waitcnt lgkmcnt(0)\n" \
    "s_barrier\n" \
    "v_add_u32 v234, 1, v234\n" \
    "v_add_u32 v235, 8, v235\n" \
    "v_or_b32 v235, 7, v235\n" \
    "s_mov_b32 s54, 2163712\n" \
    "WIa1024%=:\n" \
    "v_or_b32 v205, 7, v205\n" \
    "v_cmp_ne_u64 vcc, v[234:235], v[204:205]\n" \
    "s_cbranch_vccz WIb1024%=\n" \
    "s_sleep 2\n" \
    "buffer_load_dwordx2 v[204:205], v206, s[36:39], s54 offen sc1\n" \
    "s_waitcnt vmcnt(0)\n" \
    "s_add_u32 s57, s57, 1\n" \
    "s_cmp_lt_u32 s57, 262144\n" \
    "s_cbranch_scc1 WIa1024%=\n" \
    "WIb1024%=:\n" \
    "buffer_load_dwordx2 v[208:209], v241, s[36:39], s68 offen sc1\n" \
    "v_lshlrev_b32 v206, 3, v253\n" \
    "s_mov_b32 s54, 2179072\n" \
    "buffer_load_dwordx2 v[204:205], v206, s[36:39], s54 offen sc1\n" \
    "ds_write_b128 v221, v[136:139]\n" \
    "ds_write_b128 v221, v[140:143] offset:4096\n" \
    "ds_write_b128 v221, v[144:147] offset:8192\n" \
    "ds_write_b128 v221, v[148:151] offset:12288\n" \
    "ds_write_b128 v221, v[152:155] offset:64\n" \
    "ds_write_b128 v221, v[156:159] offset:4160\n" \
    "ds_write_b128 v221, v[160:163] offset:8256\n" \
    "ds_write_b128 v221, v[164:167] offset:12352\n" \
    "ds_write_b128 v221, v[168:171] offset:128\n" \
    "ds_write_b128 v221, v[172:175] offset:4224\n" \
    "ds_write_b128 v221, v[176:179] offset:8320\n" \
    "ds_write_b128 v221, v[180:183] offset:12416\n" \
    "ds_write_b128 v221, v[184:187] offset:192\n" \
    "ds_write_b128 v221, v[188:191] offset:4288\n" \
    "ds_write_b128 v221, v[192:195] offset:8384\n" \
    "ds_write_b128 v221, v[196:199] offset:12480\n" \
    "s_waitcnt lgkmcnt(0)\n" \
    "ds_read_b128 v[224:227], v247 offset:57344\n" \
    "ds_read_b128 v[228:231], v247 offset:61440\n" \
    "v_min_u32 v128, -2, v128\n" \
    "v_min_u32 v129, -2, v129\n" \
    "v_min_u32 v130, -2, v130\n" \
    "v_min_u32 v131, -2, v131\n" \
    "v_min_u32 v132, -2, v132\n" \
    "v_min_u32 v133, -2, v133\n" \
    "v_min_u32 v134, -2, v134\n" \
    "v_min_u32 v135, -2, v135\n" \
    "s_mov_b32 s24, s22\n" \
    "s_mov_b32 s25, s23\n" \
    "s_memtime s[20:21]\n" \
    "s_memrealtime s[4:5]\n" \
    "s_nop 2\n" \
    "s_memrealtime s[6:7]\n" \
    "s_nop 2\n" \
    "s_memrealtime s[8:9]\n" \
    "s_nop 2\n" \
    "s_memrealtime s[10:11]\n" \
    "s_nop 2\n" \
    "s_memrealtime s[12:13]\n" \
    "s_nop 2\n" \
    "s_memrealtime s[14:15]\n" \
    "s_nop 2\n" \
    "s_memrealtime s[16:17]\n" \
    "s_nop 2\n" \
    "s_memrealtime s[18:19]\n" \
    "s_mov_b32 s29, 0\n" \
    "s_waitcnt lgkmcnt(0)\n" \
    "s_cmp_eq_u32 s6, s4\n" \
    "s_addc_u32 s29, s29, 0\n" \
    "s_cmp_eq_u32 s8, s4\n" \
    "s_addc_u32 s29, s29, 0\n" \
    "s_cmp_eq_u32 s10, s4\n" \
    "s_addc_u32 s29, s29, 0\n" \
    "s_cmp_eq_u32 s12, s4\n" \
    "s_addc_u32 s29, s29, 0\n" \
    "s_cmp_eq_u32 s14, s4\n" \
    "s_addc_u32 s29, s29, 0\n" \
    "s_cmp_eq_u32 s16, s4\n" \
    "s_addc_u32 s29, s29, 0\n" \
    "s_cmp_eq_u32 s18, s4\n" \
    "s_addc_u32 s29, s29, 0\n" \
    "s_lshl_b32 s29, s29, 4\n" \
    "s_add_u32 s23, s20, s29\n" \
    "s_lshl_b32 s22, s4, 2\n" \
    "s_lshl_b32 s28, s22, 6\n" \
    "s_sub_u32 s29, s23, s25\n" \
    "s_sub_u32 s30, s22, s24\n" \
    "v_cvt_f32_u32 v232, s29\n" \
    "v_cvt_f32_u32 v233, s30\n" \
    "v_rcp_f32 v233, v233\n" \
    "v_or_b32 v243, v243, v243\n" \
    "v_mul_f32 v232, v232, v233\n" \
    "v_rcp_f32 v233, v232\n" \
    "s_nop 0\n" \
    "v_readfirstlane_b32 s26, v232\n" \
    "v_readfirstlane_b32 s27, v233\n" \
    "s_nop 1\n" \
    "s_waitcnt vmcnt(0)\n" \
    "s_barrier\n" \
    "s_mov_b32 s52, 8192\n" \
    "s_mov_b32 s48, 8192\n" \
    "s_mov_b32 s31, 0\n" \
    "KVa%=:\n" \
    "v_and_b32 v210, v208, v209\n" \
    "v_cmp_ne_u32 vcc, 1, v210\n" \
    "s_cbranch_vccnz KVc%=\n" \
    "s_add_u32 m0, s52, 8192\n" \
    "s_add_u32 s55, s49, 8192\n" \
    "buffer_load_dwordx4 v253, s[36:39], s55 offen sc1 lds\n" \
    "s_add_u32 m0, s52, 9216\n" \
    "s_add_u32 s55, s49, 9216\n" \
    "buffer_load_dwordx4 v253, s[36:39], s55 offen sc1 lds\n" \
    "s_add_u32 m0, s52, 10240\n" \
    "s_add_u32 s55, s49, 10240\n" \
    "buffer_load_dwordx4 v253, s[36:39], s55 offen sc1 lds\n" \
    "s_add_u32 m0, s52, 11264\n" \
    "s_add_u32 s55, s49, 11264\n" \
    "buffer_load_dwordx4 v253, s[36:39], s55 offen sc1 lds\n" \
    "s_mov_b32 s31, 1\n" \
    "KVc%=:\n" \
    "v_xor_b32 v223, 0, v217\n" \
    "v_add_u32 v223, 32768, v223\n" \
    "ds_read_b64_tr_b16 a[0:1], v223\n" \
    "ds_read_b64_tr_b16 a[2:3], v223 offset:512\n" \
    "ds_read_b64_tr_b16 a[4:5], v223 offset:8192\n" \
    "ds_read_b64_tr_b16 a[6:7], v223 offset:8704\n" \
    "v_xor_b32 v223, 16384, v217\n" \
    "v_add_u32 v223, 32768, v223\n" \
    "ds_read_b64_tr_b16 a[8:9], v223\n" \
    "ds_read_b64_tr_b16 a[10:11], v223 offset:512\n" \
    "ds_read_b64_tr_b16 a[12:13], v223 offset:8192\n" \
    "ds_read_b64_tr_b16 a[14:15], v223 offset:8704\n" \
    "v_xor_b32 v223, 32768, v217\n" \
    "v_add_u32 v223, 32768, v223\n" \
    "ds_read_b64_tr_b16 a[16:17], v223\n" \
    "ds_read_b64_tr_b16 a[18:19], v223 offset:512\n" \
    "ds_read_b64_tr_b16 a[20:21], v223 offset:8192\n" \
    "ds_read_b64_tr_b16 a[22:23], v223 offset:8704\n" \
    "v_xor_b32 v223, 49152, v217\n" \
    "v_add_u32 v223, 32768, v223\n" \
    "ds_read_b64_tr_b16 a[24:25], v223\n" \
    "ds_read_b64_tr_b16 a[26:27], v223 offset:512\n" \
    "ds_read_b64_tr_b16 a[28:29], v223 offset:8192\n" \
    "ds_read_b64_tr_b16 a[30:31], v223 offset:8704\n" \
    "v_xor_b32 v223, 0, v218\n" \
    "v_add_u32 v223, 32768, v223\n" \
    "ds_read_b64_tr_b16 a[64:65], v223\n" \
    "ds_read_b64_tr_b16 a[66:67], v223 offset:512\n" \
    "ds_read_b64_tr_b16 a[68:69], v223 offset:8192\n" \
    "ds_read_b64_tr_b16 a[70:71], v223 offset:8704\n" \
    "v_xor_b32 v223, 16384, v218\n" \
    "v_add_u32 v223, 32768, v223\n" \
    "ds_read_b64_tr_b16 a[72:73], v223\n" \
    "ds_read_b64_tr_b16 a[74:75], v223 offset:512\n" \
    "ds_read_b64_tr_b16 a[76:77], v223 offset:8192\n" \
    "ds_read_b64_tr_b16 a[78:79], v223 offset:8704\n" \
    "v_xor_b32 v223, 32768, v218\n" \
    "v_add_u32 v223, 32768, v223\n" \
    "ds_read_b64_tr_b16 a[80:81], v223\n" \
    "ds_read_b64_tr_b16 a[82:83], v223 offset:512\n" \
    "ds_read_b64_tr_b16 a[84:85], v223 offset:8192\n" \
    "ds_read_b64_tr_b16 a[86:87], v223 offset:8704\n" \
    "v_xor_b32 v223, 49152, v218\n" \
    "v_add_u32 v223, 32768, v223\n" \
    "ds_read_b64_tr_b16 a[88:89], v223\n" \
    "ds_read_b64_tr_b16 a[90:91], v223 offset:512\n" \
    "ds_read_b64_tr_b16 a[92:93], v223 offset:8192\n" \
    "ds_read_b64_tr_b16 a[94:95], v223 offset:8704\n" \
    "s_waitcnt lgkmcnt(0)\n" \
    "s_barrier\n" \
    "s_cmp_eq_u32 s31, 1\n" \
    "s_cbranch_scc1 KWc%=\n" \
    "KWa%=:\n" \
    "v_and_b32 v210, v208, v209\n" \
    "v_cmp_ne_u32 vcc, 1, v210\n" \
    "s_cbranch_vccz KWb%=\n" \
    "s_sleep 4\n" \
    "buffer_load_dwordx2 v[208:209], v241, s[36:39], s68 offen sc1\n" \
    "s_waitcnt vmcnt(0)\n" \
    "s_add_u32 s57, s57, 1\n" \
    "s_cmp_lt_u32 s57, 262144\n" \
    "s_cbranch_scc1 KWa%=\n" \
    "KWb%=:\n" \
    "s_add_u32 m0, s52, 8192\n" \
    "s_add_u32 s55, s49, 8192\n" \
    "buffer_load_dwordx4 v253, s[36:39], s55 offen sc1 lds\n" \
    "s_add_u32 m0, s52, 9216\n" \
    "s_add_u32 s55, s49, 9216\n" \
    "buffer_load_dwordx4 v253, s[36:39], s55 offen sc1 lds\n" \
    "s_add_u32 m0, s52, 10240\n" \
    "s_add_u32 s55, s49, 10240\n" \
    "buffer_load_dwordx4 v253, s[36:39], s55 offen sc1 lds\n" \
    "s_add_u32 m0, s52, 11264\n" \
    "s_add_u32 s55, s49, 11264\n" \
    "buffer_load_dwordx4 v253, s[36:39], s55 offen sc1 lds\n" \
    "s_mov_b32 s31, 1\n" \
    "KWc%=:\n" \
    "s_mov_b32 s54, 2179072\n" \
    "WNa%=:\n" \
    "v_mov_b32 v207, v205\n" \
    "v_or_b32 v205, 7, v205\n" \
    "v_cmp_ne_u64 vcc, v[234:235], v[204:205]\n" \
    "s_cbranch_vccz WNb%=\n" \
    "s_sleep 2\n" \
    "buffer_load_dwordx2 v[204:205], v206, s[36:39], s54 offen sc1\n" \
    "s_waitcnt vmcnt(0)\n" \
    "s_add_u32 s57, s57, 1\n" \
    "s_cmp_lt_u32 s57, 262144\n" \
    "s_cbranch_scc1 WNa%=\n" \
    "WNb%=:\n" \
    "s_mov_b32 s32, 0\n" \
    "v_and_b32 v204, 1, v207\n" \
    "v_cmp_ne_u32 vcc, 0, v204\n" \
    "s_bcnt1_i32_b64 s54, vcc\n" \
    "s_cmp_gt_u32 s54, 32\n" \
    "s_cselect_b32 s54, 4096, 0\n" \
    "s_or_b32 s32, s32, s54\n" \
    "v_and_b32 v204, 2, v207\n" \
    "v_cmp_ne_u32 vcc, 0, v204\n" \
    "s_bcnt1_i32_b64 s54, vcc\n" \
    "s_cmp_gt_u32 s54, 32\n" \
    "s_cselect_b32 s54, 8192, 0\n" \
    "s_or_b32 s32, s32, s54\n" \
    "v_and_b32 v204, 4, v207\n" \
    "v_cmp_ne_u32 vcc, 0, v204\n" \
    "s_bcnt1_i32_b64 s54, vcc\n" \
    "s_cmp_gt_u32 s54, 32\n" \
    "s_cselect_b32 s54, 16384, 0\n" \
    "s_or_b32 s32, s32, s54\n" \
    "s_xor_b32 s45, s45, s32\n" \
    "s_xor_b32 s46, s46, s32\n" \
    "s_mov_b32 exec_hi, 0\n" \
    "v_xor_b32 v245, s32, v245\n" \
    "s_mov_b32 exec_hi, -1\n" \
    "s_mov_b32 s40, s36\n" \
    "s_mov_b32 s41, s37\n" \
    "s_mov_b32 s43, s39\n" \
    "s_mov_b32 s60, s36\n" \
    "s_mov_b32 s61, s37\n" \
    "s_mov_b32 s63, s39\n" \
    "s_cmp_eq_u32 s58, 0\n" \
    "s_cselect_b32 s42, s38, 0\n" \
    "s_cselect_b32 s62, 0, s38\n" \
    "s_mov_b32 s33, 1750\n" \
    "s_mov_b32 s31, 179\n" \
    "s_mov_b32 s65, 0\n" \
    "v_mov_b64 v[160:161], 0\n" \
    "v_mov_b64 v[162:163], 0\n" \
    "v_mov_b64 v[164:165], 0\n" \
    "v_mov_b64 v[166:167], 0\n" \
    "v_mov_b64 v[168:169], 0\n" \
    "v_mov_b64 v[170:171], 0\n" \
    "v_mov_b64 v[172:173], 0\n" \
    "v_mov_b64 v[174:175], 0\n" \
    "v_mov_b64 v[176:177], 0\n" \
    "v_mov_b64 v[178:179], 0\n" \
    "v_mov_b64 v[180:181], 0\n" \
    "v_mov_b64 v[182:183], 0\n" \
    "v_xor_b32 v136, 2048, v255\n" \
    "v_xor_b32 v140, 3072, v255\n" \
    "v_xor_b32 v144, 4096, v255\n" \
    "v_xor_b32 v148, 5120, v255\n" \
    "v_xor_b32 v152, 6144, v255\n" \
    "v_xor_b32 v156, 7168, v255\n" \
    "v_add_u32 v216, s48, v253\n" \
    "v_add_u32 v220, s48, v255\n" \
    "s_xor_b32 s52, s48, 32768\n" \
    "s_add_u32 s49, s49, 16384\n" \
    "s_add_u32 m0, s52, 8192\n" \
    "s_add_u32 s55, s49, 8192\n" \
    "buffer_load_dwordx4 v253, s[36:39], s55 offen sc1 lds\n" \
    "s_add_u32 m0, s52, 9216\n" \
    "s_add_u32 s55, s49, 9216\n" \
    "buffer_load_dwordx4 v253, s[36:39], s55 offen sc1 lds\n" \
    "s_add_u32 m0, s52, 10240\n" \
    "s_add_u32 s55, s49, 10240\n" \
    "buffer_load_dwordx4 v253, s[36:39], s55 offen sc1 lds\n" \
    "s_add_u32 m0, s52, 11264\n" \
    "s_add_u32 s55, s49, 11264\n" \
    "buffer_load_dwordx4 v253, s[36:39], s55 offen sc1 lds\n" \
    "HEAD0%=:\n" \
    "s_waitcnt vmcnt(5)\n" \
    "ds_write_b128 v255, v[128:131]\n" \
    "v_smfmac_f32_16x16x64_bf16 v[160:163], v[128:131], a[0:7], v254\n" \
    "s_waitcnt vmcnt(4)\n" \
    "ds_write_b128 v255, v[132:135] offset:1024\n" \
    "v_max3_u32 v232, v128, v129, v130\n" \
    "v_smfmac_f32_16x16x64_bf16 v[168:171], v[128:131], a[128:135], v254\n" \
    "v_max3_u32 v240, v132, v133, v134\n" \
    "v_smfmac_f32_16x16x64_bf16 v[164:167], v[128:131], a[64:71], v254\n" \
    "v_max3_u32 v232, v232, v131, v135\n" \
    "v_smfmac_f32_16x16x64_bf16 v[172:175], v[128:131], a[192:199], v254\n" \
    "v_max_u32 v232, v232, v240\n" \
    "v_smfmac_f32_16x16x64_bf16 v[160:163], v[132:135], a[8:15], v254\n" \
    "v_cmp_eq_u32 vcc, -1, v232\n" \
    "v_smfmac_f32_16x16x64_bf16 v[168:171], v[132:135], a[136:143], v254\n" \
    "s_cbranch_vccnz RETRY2%=\n" \
    "OK2%=:\n" \
    "s_waitcnt lgkmcnt(0)\n" \
    "s_barrier\n" \
    "ds_read_b128 v[136:139], v136\n" \
    "ds_read_b128 v[140:143], v140\n" \
    "v_smfmac_f32_16x16x64_bf16 v[164:167], v[132:135], a[72:79], v254\n" \
    "ds_read_b128 v[144:147], v144\n" \
    "v_smfmac_f32_16x16x64_bf16 v[172:175], v[132:135], a[200:207], v254\n" \
    "ds_read_b128 v[148:151], v148\n" \
    "v_smfmac_f32_16x16x64_bf16 v[176:179], v[128:131], v[0:7], v254\n" \
    "ds_read_b128 v[152:155], v152\n" \
    "v_smfmac_f32_16x16x64_bf16 v[180:183], v[128:131], v[64:71], v254\n" \
    "ds_read_b128 v[156:159], v156\n" \
    "v_smfmac_f32_16x16x64_bf16 v[176:179], v[132:135], v[8:15], v254\n" \
    "v_smfmac_f32_16x16x64_bf16 v[180:183], v[132:135], v[72:79], v254\n" \
    "CONT%=:\n" \
    "s_waitcnt lgkmcnt(5)\n" \
    "v_smfmac_f32_16x16x64_bf16 v[160:163], v[136:139], a[16:23], v254\n" \
    "s_waitcnt lgkmcnt(4)\n" \
    "v_smfmac_f32_16x16x64_bf16 v[160:163], v[140:143], a[24:31], v254\n" \
    "s_waitcnt lgkmcnt(3)\n" \
    "v_smfmac_f32_16x16x64_bf16 v[160:163], v[144:147], a[32:39], v254\n" \
    "s_waitcnt lgkmcnt(2)\n" \
    "v_smfmac_f32_16x16x64_bf16 v[160:163], v[148:151], a[40:47], v254\n" \
    "s_waitcnt lgkmcnt(1)\n" \
    "v_smfmac_f32_16x16x64_bf16 v[160:163], v[152:155], a[48:55], v254\n" \
    "s_waitcnt lgkmcnt(0)\n" \
    "v_smfmac_f32_16x16x64_bf16 v[160:163], v[156:159], a[56:63], v254\n" \
    "v_smfmac_f32_16x16x64_bf16 v[168:171], v[136:139], a[144:151], v254\n" \
    "v_smfmac_f32_16x16x64_bf16 v[168:171], v[140:143], a[152:159], v254\n" \
    "ds_read_b128 v[184:187], v216\n" \
    "v_or_b32 v243, v243, v243\n" \
    "v_smfmac_f32_16x16x64_bf16 v[168:171], v[144:147], a[160:167], v254\n" \
    "ds_read_b128 v[188:191], v216 offset:1024\n" \
    "v_or_b32 v243, v243, v243\n" \
    "v_smfmac_f32_16x16x64_bf16 v[168:171], v[148:151], a[168:175], v254\n" \
    "ds_read_b128 v[192:195], v216 offset:2048\n" \
    "v_add_f32 v160, v160, v161\n" \
    "v_smfmac_f32_16x16x64_bf16 v[168:171], v[152:155], a[176:183], v254\n" \
    "ds_read_b128 v[196:199], v216 offset:3072\n" \
    "v_add_f32 v162, v162, v163\n" \
    "v_smfmac_f32_16x16x64_bf16 v[168:171], v[156:159], a[184:191], v254\n" \
    "ds_read_b128 v[200:203], v216 offset:4096\n" \
    "v_cvt_pk_bf16_f32 v161, v160, v162\n" \
    "v_smfmac_f32_16x16x64_bf16 v[164:167], v[136:139], a[80:87], v254\n" \
    "ds_read_b128 v[204:207], v216 offset:5120\n" \
    "v_or_b32 v243, v243, v243\n" \
    "v_smfmac_f32_16x16x64_bf16 v[164:167], v[140:143], a[88:95], v254\n" \
    "ds_read_b128 v[208:211], v216 offset:6144\n" \
    "v_or_b32 v243, v243, v243\n" \
    "v_smfmac_f32_16x16x64_bf16 v[164:167], v[144:147], a[96:103], v254\n" \
    "ds_read_b128 v[212:215], v216 offset:7168\n" \
    "v_or_b32 v243, v243, v243\n" \
    "v_smfmac_f32_16x16x64_bf16 v[164:167], v[148:151], a[104:111], v254\n" \
    "ds_read_b128 v[216:219], v220 offset:8192\n" \
    "v_add_f32 v168, v168, v169\n" \
    "v_smfmac_f32_16x16x64_bf16 v[164:167], v[152:155], a[112:119], v254\n" \
    "ds_read_b128 v[220:223], v220 offset:9216\n" \
    "v_add_f32 v170, v170, v171\n" \
    "v_smfmac_f32_16x16x64_bf16 v[164:167], v[156:159], a[120:127], v254\n" \
    "v_cvt_pk_bf16_f32 v160, v168, v170\n" \
    "v_smfmac_f32_16x16x64_bf16 v[172:175], v[136:139], a[208:215], v254\n" \
    "ds_write_b64 v251, v[160:161]\n" \
    "v_smfmac_f32_16x16x64_bf16 v[172:175], v[140:143], a[216:223], v254\n" \
    "ds_read_b128 v[128:131], v247\n" \
    "v_or_b32 v243, v243, v243\n" \
    "v_smfmac_f32_16x16x64_bf16 v[172:175], v[144:147], a[224:231], v254\n" \
    "ds_read_b128 v[132:135], v247 offset:4096\n" \
    "v_or_b32 v243, v243, v243\n" \
    "v_smfmac_f32_16x16x64_bf16 v[172:175], v[148:151], a[232:239], v254\n" \
    "v_add_f32 v164, v164, v165\n" \
    "v_smfmac_f32_16x16x64_bf16 v[172:175], v[152:155], a[240:247], v254\n" \
    "v_add_f32 v166, v166, v167\n" \
    "v_smfmac_f32_16x16x64_bf16 v[172:175], v[156:159], a[248:255], v254\n" \
    "v_cvt_pk_bf16_f32 v165, v164, v166\n" \
    "v_smfmac_f32_16x16x64_bf16 v[176:179], v[136:139], v[16:23], v254\n" \
    "s_nop 1\n" \
    "v_or_b32 v243, v243, v243\n" \
    "v_smfmac_f32_16x16x64_bf16 v[180:183], v[136:139], v[80:87], v254\n" \
    "s_nop 1\n" \
    "v_add_f32 v172, v172, v173\n" \
    "v_add_f32 v174, v174, v175\n" \
    "v_cvt_pk_bf16_f32 v164, v172, v174\n" \
    "v_smfmac_f32_16x16x64_bf16 v[176:179], v[140:143], v[24:31], v254\n" \
    "ds_write_b64 v251, v[164:165] offset:512\n" \
    "s_cmp_eq_u32 s44, 63\n" \
    "v_smfmac_f32_16x16x64_bf16 v[180:183], v[140:143], v[88:95], v254\n" \
    "v_or_b32 v243, v243, v243\n" \
    "v_smfmac_f32_16x16x64_bf16 v[176:179], v[144:147], v[32:39], v254\n" \
    "v_or_b32 v243, v243, v243\n" \
    "v_smfmac_f32_16x16x64_bf16 v[180:183], v[144:147], v[96:103], v254\n" \
    "v_or_b32 v243, v243, v243\n" \
    "v_smfmac_f32_16x16x64_bf16 v[176:179], v[148:151], v[40:47], v254\n" \
    "v_or_b32 v243, v243, v243\n" \
    "v_smfmac_f32_16x16x64_bf16 v[180:183], v[148:151], v[104:111], v254\n" \
    "s_waitcnt lgkmcnt(0)\n" \
    "s_barrier\n" \
    "ds_read_b64_tr_b16 v[160:161], v250\n" \
    "ds_read_b64_tr_b16 v[162:163], v250 offset:512\n" \
    "s_cbranch_scc1 TAIL%=\n" \
    "v_smfmac_f32_16x16x64_bf16 v[176:179], v[152:155], v[48:55], v254\n" \
    "ds_read_b64_tr_b16 v[164:165], v250 offset:1024\n" \
    "ds_read_b64_tr_b16 v[166:167], v250 offset:1536\n" \
    "v_smfmac_f32_16x16x64_bf16 v[180:183], v[152:155], v[112:119], v254\n" \
    "ds_read_b64_tr_b16 v[168:169], v250 offset:2048\n" \
    "ds_read_b64_tr_b16 v[170:171], v250 offset:2560\n" \
    "v_smfmac_f32_16x16x64_bf16 v[176:179], v[156:159], v[56:63], v254\n" \
    "ds_read_b64_tr_b16 v[172:173], v250 offset:3072\n" \
    "ds_read_b64_tr_b16 v[174:175], v250 offset:3584\n" \
    "v_smfmac_f32_16x16x64_bf16 v[180:183], v[156:159], v[120:127], v254\n" \
    "v_or_b32 v243, v243, v243\n" \
    "s_waitcnt lgkmcnt(6)\n" \
    "v_mfma_f32_16x16x32_bf16 v[184:187], v[184:187], v[160:163], 0\n" \
    "ds_read_b128 v[136:139], v247 offset:8192\n" \
    "v_or_b32 v243, v243, v243\n" \
    "v_mfma_f32_16x16x32_bf16 v[200:203], v[200:203], v[160:163], 0\n" \
    "ds_read_b128 v[140:143], v247 offset:12288\n" \
    "v_or_b32 v243, v243, v243\n" \
    "s_waitcnt lgkmcnt(6)\n" \
    "v_mfma_f32_16x16x32_bf16 v[184:187], v[188:191], v[164:167], v[184:187]\n" \
    "ds_read_b128 v[144:147], v247 offset:16384\n" \
    "v_or_b32 v243, v243, v243\n" \
    "v_mfma_f32_16x16x32_bf16 v[200:203], v[204:207], v[164:167], v[200:203]\n" \
    "ds_read_b128 v[148:151], v247 offset:20480\n" \
    "v_add_f32 v176, v176, v177\n" \
    "s_waitcnt lgkmcnt(6)\n" \
    "v_mfma_f32_16x16x32_bf16 v[184:187], v[192:195], v[168:171], v[184:187]\n" \
    "ds_read_b128 v[152:155], v247 offset:24576\n" \
    "v_add_f32 v178, v178, v179\n" \
    "v_mfma_f32_16x16x32_bf16 v[200:203], v[208:211], v[168:171], v[200:203]\n" \
    "ds_read_b128 v[156:159], v247 offset:28672\n" \
    "v_add_f32 v180, v180, v181\n" \
    "s_waitcnt lgkmcnt(6)\n" \
    "v_mfma_f32_16x16x32_bf16 v[184:187], v[196:199], v[172:175], v[184:187]\n" \
    "v_add_f32 v182, v182, v183\n" \
    "v_cvt_pk_bf16_f32 v176, v176, v178\n" \
    "v_mfma_f32_16x16x32_bf16 v[200:203], v[212:215], v[172:175], v[200:203]\n" \
    "buffer_store_dwordx4 v[168:171], v253, s[40:43], s50 offen sc1\n" \
    "v_cvt_pk_bf16_f32 v177, v180, v182\n" \
    "v_mfma_f32_16x16x32_bf16 v[160:163], v[160:163], v[160:163], 0\n" \
    "ds_read_b128 v[188:191], v247 offset:32768\n" \
    "v_cvt_pk_bf16_f32 v178, v180, v182\n" \
    "v_mfma_f32_16x16x32_bf16 v[160:163], v[164:167], v[164:167], v[160:163]\n" \
    "ds_read_b128 v[192:195], v247 offset:36864\n" \
    "v_mov_b32 v179, 0\n" \
    "v_mfma_f32_16x16x32_bf16 v[160:163], v[168:171], v[168:171], v[160:163]\n" \
    "ds_read_b128 v[204:207], v247 offset:40960\n" \
    "v_max3_f32 v232, v184, v185, v186\n" \
    "v_mfma_f32_16x16x32_bf16 v[160:163], v[172:175], v[172:175], v[160:163]\n" \
    "ds_read_b128 v[208:211], v247 offset:45056\n" \
    "ds_read_b128 v[164:167], v247 offset:49152\n" \
    "ds_read_b128 v[168:171], v247 offset:53248\n" \
    "v_max3_f32 v233, v200, v201, v202\n" \
    "v_max3_f32 v232, v232, v187, v203\n" \
    "v_max_f32 v232, v232, v233\n" \
    "v_mov_b32 v172, 0\n" \
    "v_mov_b32 v173, 0\n" \
    "v_max3_f32 v234, v232, v160, v161\n" \
    "v_max3_f32 v235, v232, v160, v161\n" \
    "v_mov_b32 v174, 0\n" \
    "v_mov_b32 v175, 0\n" \
    "v_permlane32_swap_b32 v234, v235\n" \
    "v_max_f32 v232, v234, v235\n" \
    "v_max_f32 v233, v234, v235\n" \
    "v_or_b32 v243, v243, v243\n" \
    "v_or_b32 v243, v243, v243\n" \
    "v_permlane16_swap_b32 v232, v233\n" \
    "v_max_f32 v234, v232, v233\n" \
    "v_mul_f32 v234, s53, v234\n" \
    "v_pk_fma_f32 v[160:161], v[160:161], s[52:53], v[234:235] op_sel:[0,1,0] op_sel_hi:[1,1,0] neg_lo:[0,0,1] neg_hi:[0,0,1]\n" \
    "v_pk_fma_f32 v[184:185], v[184:185], s[52:53], v[234:235] op_sel:[0,1,0] op_sel_hi:[1,1,0] neg_lo:[0,0,1] neg_hi:[0,0,1]\n" \
    "v_pk_fma_f32 v[186:187], v[186:187], s[52:53], v[234:235] op_sel:[0,1,0] op_sel_hi:[1,1,0] neg_lo:[0,0,1] neg_hi:[0,0,1]\n" \
    "v_pk_fma_f32 v[200:201], v[200:201], s[52:53], v[234:235] op_sel:[0,1,0] op_sel_hi:[1,1,0] neg_lo:[0,0,1] neg_hi:[0,0,1]\n" \
    "v_pk_fma_f32 v[202:203], v[202:203], s[52:53], v[234:235] op_sel:[0,1,0] op_sel_hi:[1,1,0] neg_lo:[0,0,1] neg_hi:[0,0,1]\n" \
    "v_exp_f32 v160, v160\n" \
    "v_exp_f32 v161, v161\n" \
    "v_exp_f32 v184, v184\n" \
    "v_cvt_pk_bf16_f32 v240, v160, v161\n" \
    "v_exp_f32 v185, v185\n" \
    "v_exp_f32 v186, v186\n" \
    "v_mfma_f32_16x16x16_bf16 v[212:215], v[236:237], v[240:241], 0\n" \
    "v_exp_f32 v187, v187\n" \
    "v_mfma_f32_16x16x16_bf16 v[180:183], v[176:177], v[240:241], 0\n" \
    "v_exp_f32 v200, v200\n" \
    "v_mfma_f32_16x16x16_bf16 v[232:235], v[178:179], v[240:241], 0\n" \
    "v_exp_f32 v201, v201\n" \
    "v_exp_f32 v202, v202\n" \
    "v_exp_f32 v203, v203\n" \
    "v_cvt_pk_bf16_f32 v196, v184, v185\n" \
    "v_cvt_pk_bf16_f32 v197, v186, v187\n" \
    "v_cvt_pk_bf16_f32 v198, v200, v201\n" \
    "v_cvt_pk_bf16_f32 v199, v202, v203\n" \
    "v_mov_b32 v184, 0\n" \
    "v_mov_b32 v185, 0\n" \
    "v_mfma_f32_16x16x32_bf16 v[212:215], v[236:239], v[196:199], v[212:215]\n" \
    "buffer_load_dword v242, v245, s[36:39], s59 offen sc1\n" \
    "v_mov_b32 v186, 0\n" \
    "v_mov_b32 v187, 0\n" \
    "v_mfma_f32_16x16x32_bf16 v[180:183], v[216:219], v[196:199], v[180:183]\n" \
    "v_mov_b32 v200, 0\n" \
    "v_mov_b32 v201, 0\n" \
    "v_mfma_f32_16x16x32_bf16 v[232:235], v[220:223], v[196:199], v[232:235]\n" \
    "v_mov_b32 v202, 0\n" \
    "v_mov_b32 v203, 0\n" \
    "v_rcp_f32 v240, v212\n" \
    "v_mov_b32 v160, 0\n" \
    "v_mov_b32 v161, 0\n" \
    "v_mov_b32 v162, 0\n" \
    "v_mov_b32 v163, 0\n" \
    "v_pk_mul_f32 v[180:181], v[180:181], v[240:241] op_sel_hi:[1,0]\n" \
    "v_pk_mul_f32 v[182:183], v[182:183], v[240:241] op_sel_hi:[1,0]\n" \
    "v_pk_mul_f32 v[232:233], v[232:233], v[240:241] op_sel_hi:[1,0]\n" \
    "v_pk_mul_f32 v[234:235], v[234:235], v[240:241] op_sel_hi:[1,0]\n" \
    "v_cvt_pk_bf16_f32 v196, v180, v181\n" \
    "v_cvt_pk_bf16_f32 v197, v182, v183\n" \
    "v_cvt_pk_bf16_f32 v198, v232, v233\n" \
    "v_cvt_pk_bf16_f32 v199, v234, v235\n" \
    "ds_write_b128 v249, v[196:199]\n" \
    "v_lshrrev_b32 v240, 1, v253\n" \
    "buffer_store_dwordx2 v[176:177], v240, s[60:63], s50 offen sc1\n" \
    "s_waitcnt lgkmcnt(0)\n" \
    "s_barrier\n" \
    "ds_read_b128 v[216:219], v253 offset:28672\n" \
    "ds_read_b128 v[220:223], v253 offset:29696\n" \
    "s_waitcnt lgkmcnt(1)\n" \
    "v_smfmac_f32_16x16x64_bf16 v[184:187], v[216:219], v[128:135], v254\n" \
    "s_waitcnt lgkmcnt(0)\n" \
    "s_nop 0\n" \
    "v_smfmac_f32_16x16x64_bf16 v[184:187], v[220:223], v[136:143], v254\n" \
    "s_nop 1\n" \
    "s_nop 0\n" \
    "v_smfmac_f32_16x16x64_bf16 v[200:203], v[216:219], v[144:151], v254\n" \
    "s_nop 1\n" \
    "s_nop 0\n" \
    "v_smfmac_f32_16x16x64_bf16 v[200:203], v[220:223], v[152:159], v254\n" \
    "v_add_f32 v184, v184, v185\n" \
    "v_smfmac_f32_16x16x64_bf16 v[160:163], v[216:219], v[188:195], v254\n" \
    "s_nop 1\n" \
    "v_add_f32 v186, v186, v187\n" \
    "v_smfmac_f32_16x16x64_bf16 v[160:163], v[220:223], v[204:211], v254\n" \
    "s_nop 0\n" \
    "v_cvt_pk_bf16_f32 v184, v184, v186\n" \
    "v_smfmac_f32_16x16x64_bf16 v[172:175], v[216:219], v[164:171], v254\n" \
    "v_add_f32 v200, v200, v201\n" \
    "v_smfmac_f32_16x16x64_bf16 v[172:175], v[220:223], v[224:231], v254\n" \
    "v_add_f32 v202, v202, v203\n" \
    "v_min_u32 v212, -2, v184\n" \
    "v_cvt_pk_bf16_f32 v200, v200, v202\n" \
    "v_add_f32 v160, v160, v161\n" \
    "v_add_f32 v162, v162, v163\n" \
    "v_min_u32 v213, -2, v200\n" \
    "v_cvt_pk_bf16_f32 v160, v160, v162\n" \
    "v_min_u32 v214, -2, v160\n" \
    "v_add_f32 v172, v172, v173\n" \
    "v_add_f32 v174, v174, v175\n" \
    "v_cvt_pk_bf16_f32 v172, v172, v174\n" \
    "v_min_u32 v215, -2, v172\n" \
    "buffer_store_dwordx4 v[212:215], v252, s[36:39], s45 offen sc1\n" \
    "s_add_u32 s46, s46, 131072\n" \
    "s_and_b32 s46, s46, 0x7ffff\n" \
    "s_add_u32 s44, s44, 1\n" \
    "s_xor_b32 s45, s45, 0x84000\n" \
    "s_xor_b32 s48, s48, 32768\n" \
    "s_add_u32 s50, s50, 8192\n" \
    "v_mov_b64 v[160:161], 0\n" \
    "v_mov_b64 v[162:163], 0\n" \
    "v_mov_b64 v[164:165], 0\n" \
    "v_mov_b64 v[166:167], 0\n" \
    "v_mov_b64 v[168:169], 0\n" \
    "v_mov_b64 v[170:171], 0\n" \
    "v_mov_b64 v[172:173], 0\n" \
    "v_mov_b64 v[174:175], 0\n" \
    "v_mov_b64 v[176:177], 0\n" \
    "v_mov_b64 v[178:179], 0\n" \
    "v_mov_b64 v[180:181], 0\n" \
    "v_mov_b64 v[182:183], 0\n" \
    "v_xor_b32 v136, 2048, v255\n" \
    "v_xor_b32 v140, 3072, v255\n" \
    "v_xor_b32 v144, 4096, v255\n" \
    "v_xor_b32 v148, 5120, v255\n" \
    "v_xor_b32 v152, 6144, v255\n" \
    "v_xor_b32 v156, 7168, v255\n" \
    "v_add_u32 v216, s48, v253\n" \
    "v_add_u32 v220, s48, v255\n" \
    "s_xor_b32 s52, s48, 32768\n" \
    "s_add_u32 s49, s49, 16384\n" \
    "s_mov_b32 s24, s22\n" \
    "s_mov_b32 s25, s23\n" \
    "s_memtime s[20:21]\n" \
    "s_memrealtime s[4:5]\n" \
    "s_nop 2\n" \
    "s_memrealtime s[6:7]\n" \
    "s_nop 2\n" \
    "s_memrealtime s[8:9]\n" \
    "s_nop 2\n" \
    "s_memrealtime s[10:11]\n" \
    "s_nop 2\n" \
    "s_memrealtime s[12:13]\n" \
    "s_nop 2\n" \
    "s_memrealtime s[14:15]\n" \
    "s_nop 2\n" \
    "s_memrealtime s[16:17]\n" \
    "s_nop 2\n" \
    "s_memrealtime s[18:19]\n" \
    "s_mov_b32 s29, 0\n" \
    "s_waitcnt lgkmcnt(0)\n" \
    "s_cmp_eq_u32 s6, s4\n" \
    "s_addc_u32 s29, s29, 0\n" \
    "s_cmp_eq_u32 s8, s4\n" \
    "s_addc_u32 s29, s29, 0\n" \
    "s_cmp_eq_u32 s10, s4\n" \
    "s_addc_u32 s29, s29, 0\n" \
    "s_cmp_eq_u32 s12, s4\n" \
    "s_addc_u32 s29, s29, 0\n" \
    "s_cmp_eq_u32 s14, s4\n" \
    "s_addc_u32 s29, s29, 0\n" \
    "s_cmp_eq_u32 s16, s4\n" \
    "s_addc_u32 s29, s29, 0\n" \
    "s_cmp_eq_u32 s18, s4\n" \
    "s_addc_u32 s29, s29, 0\n" \
    "s_lshl_b32 s29, s29, 4\n" \
    "s_add_u32 s23, s20, s29\n" \
    "s_lshl_b32 s22, s4, 2\n" \
    "s_lshl_b32 s28, s22, 6\n" \
    "s_sub_u32 s29, s23, s25\n" \
    "s_sub_u32 s30, s22, s24\n" \
    "v_cvt_f32_u32 v234, s29\n" \
    "v_cvt_f32_u32 v235, s30\n" \
    "v_rcp_f32 v235, v235\n" \
    "v_or_b32 v243, v243, v243\n" \
    "v_mul_f32 v234, v234, v235\n" \
    "v_add_f32 v234, s26, v234\n" \
    "v_mul_f32 v234, 0.5, v234\n" \
    "v_rcp_f32 v235, v234\n" \
    "s_nop 0\n" \
    "v_readfirstlane_b32 s26, v234\n" \
    "v_readfirstlane_b32 s27, v235\n" \
    "s_nop 1\n" \
    "s_waitcnt vmcnt(2)\n" \
    "SP%=:\n" \
    "v_sub_u32 v232, v242, v244\n" \
    "v_and_b32 v232, 63, v232\n" \
    "v_cmp_ne_u32 vcc, 0, v232\n" \
    "s_cbranch_vccz SG%=\n" \
    "v_cmp_lt_u32 vcc, 1, v232\n" \
    "s_cbranch_vccz VE%=\n" \
    "buffer_load_dword v242, v245, s[36:39], s59 offen sc1\n" \
    "s_waitcnt vmcnt(0)\n" \
    "s_add_u32 s57, s57, 1\n" \
    "s_cmp_lt_u32 s57, 1048576\n" \
    "s_cbranch_scc1 SP%=\n" \
    "SG%=:\n" \
    "v_subrev_u32 v242, s28, v242\n" \
    "v_or_b32 v243, v243, v243\n" \
    "v_or_b32 v243, v243, v243\n" \
    "v_max_i32_dpp v242, v242, v242 row_ror:8 row_mask:0xf bank_mask:0xf\n" \
    "v_or_b32 v243, v243, v243\n" \
    "v_or_b32 v243, v243, v243\n" \
    "v_max_i32_dpp v242, v242, v242 row_ror:4 row_mask:0xf bank_mask:0xf\n" \
    "v_or_b32 v243, v243, v243\n" \
    "v_or_b32 v243, v243, v243\n" \
    "v_max_i32_dpp v242, v242, v242 row_ror:2 row_mask:0xf bank_mask:0xf\n" \
    "v_or_b32 v243, v243, v243\n" \
    "v_or_b32 v243, v243, v243\n" \
    "v_max_i32_dpp v242, v242, v242 row_ror:1 row_mask:0xf bank_mask:0xf\n" \
    "v_mov_b32 v232, v242\n" \
    "v_or_b32 v243, v243, v243\n" \
    "v_or_b32 v243, v243, v243\n" \
    "v_permlane16_swap_b32 v242, v232\n" \
    "v_max_i32 v242, v242, v232\n" \
    "v_ashrrev_i32 v242, 6, v242\n" \
    "v_add_u32 v242, s31, v242\n" \
    "v_cvt_f32_i32 v242, v242\n" \
    "v_mul_f32 v242, s26, v242\n" \
    "v_cvt_i32_f32 v242, v242\n" \
    "s_memtime s[20:21]\n" \
    "v_readfirstlane_b32 s30, v242\n" \
    "s_waitcnt lgkmcnt(0)\n" \
    "s_add_u32 s30, s30, s23\n" \
    "s_sub_u32 s29, s30, s20\n" \
    "s_min_i32 s29, s29, 4096\n" \
    "s_add_u32 s30, s20, s29\n" \
    "VL%=:\n" \
    "s_memtime s[20:21]\n" \
    "s_waitcnt lgkmcnt(0)\n" \
    "s_sub_u32 s29, s30, s20\n" \
    "s_cmp_gt_i32 s29, 192\n" \
    "s_cbranch_scc1 VL%=\n" \
    "s_max_i32 s29, s29, 0\n" \
    "s_lshr_b32 s29, s29, 3\n" \
    "s_lshl_b32 s64, s29, 2\n" \
    "s_getpc_b64 s[66:67]\n" \
    "VD%=:\n" \
    "s_sub_u32 s34, VE%=-VD%=, s64\n" \
    "s_add_u32 s66, s66, s34\n" \
    "s_addc_u32 s67, s67, 0\n" \
    "s_setpc_b64 s[66:67]\n" \
    "s_nop 1\n" \
    "s_nop 1\n" \
    "s_nop 1\n" \
    "s_nop 1\n" \
    "s_nop 1\n" \
    "s_nop 1\n" \
    "s_nop 1\n" \
    "s_nop 1\n" \
    "s_nop 1\n" \
    "s_nop 1\n" \
    "s_nop 1\n" \
    "s_nop 1\n" \
    "s_nop 1\n" \
    "s_nop 1\n" \
    "s_nop 1\n" \
    "s_nop 1\n" \
    "s_nop 1\n" \
    "s_nop 1\n" \
    "s_nop 1\n" \
    "s_nop 1\n" \
    "s_nop 1\n" \
    "s_nop 1\n" \
    "s_nop 1\n" \
    "s_nop 1\n" \
    "VE%=:\n" \
    "buffer_load_dwordx4 v[128:131], v248, s[36:39], s46 offen sc1\n" \
    "buffer_load_dwordx4 v[132:135], v248, s[36:39], s46 offen offset:1024 sc1\n" \
    "s_mov_b32 exec_hi, 0\n" \
    "v_add_u32 v244, 1, v244\n" \
    "s_mov_b32 exec_hi, -1\n" \
    "s_mov_b32 exec_lo, 0\n" \
    "v_add_u32 v245, 4, v245\n" \
    "s_mov_b32 exec_lo, -1\n" \
    "s_cmp_gt_u32 s44, 2\n" \
    "s_cselect_b32 s65, 4, 0\n" \
    "s_nop 15\n" \
    "s_nop 8\n" \
    "s_add_u32 m0, s52, 8192\n" \
    "s_add_u32 s55, s49, 8192\n" \
    "buffer_load_dwordx4 v253, s[36:39], s55 offen sc1 lds\n" \
    "s_add_u32 m0, s52, 9216\n" \
    "s_add_u32 s55, s49, 9216\n" \
    "buffer_load_dwordx4 v253, s[36:39], s55 offen sc1 lds\n" \
    "s_add_u32 m0, s52, 10240\n" \
    "s_add_u32 s55, s49, 10240\n" \
    "buffer_load_dwordx4 v253, s[36:39], s55 offen sc1 lds\n" \
    "s_add_u32 m0, s52, 11264\n" \
    "s_add_u32 s55, s49, 11264\n" \
    "buffer_load_dwordx4 v253, s[36:39], s55 offen sc1 lds\n" \
    "s_branch HEAD0%=\n" \
    "RETRY2%=:\n" \
    "s_add_u32 s31, s31, s65\n" \
    "s_min_u32 s31, s31, 239\n" \
    "s_mov_b32 s65, 0\n" \
    "s_add_u32 s56, s56, 1\n" \
    "s_cmp_gt_u32 s56, 262144\n" \
    "s_cbranch_scc1 OK2%=\n" \
    "s_waitcnt lgkmcnt(0)\n" \
    "s_add_u32 s54, s46, 65536\n" \
    "s_nop 15\n" \
    "s_nop 8\n" \
    "buffer_load_dwordx4 v[128:131], v248, s[36:39], s54 offen sc1\n" \
    "buffer_load_dwordx4 v[132:135], v248, s[36:39], s54 offen offset:1024 sc1\n" \
    "s_nop 7\n" \
    "v_mov_b64 v[160:161], 0\n" \
    "v_mov_b64 v[162:163], 0\n" \
    "v_mov_b64 v[164:165], 0\n" \
    "v_mov_b64 v[166:167], 0\n" \
    "v_mov_b64 v[168:169], 0\n" \
    "v_mov_b64 v[170:171], 0\n" \
    "v_mov_b64 v[172:173], 0\n" \
    "v_mov_b64 v[174:175], 0\n" \
    "v_mov_b64 v[176:177], 0\n" \
    "v_mov_b64 v[178:179], 0\n" \
    "v_mov_b64 v[180:181], 0\n" \
    "v_mov_b64 v[182:183], 0\n" \
    "s_waitcnt vmcnt(0)\n" \
    "s_branch HEAD0%=\n" \
    "TAIL%=:\n" \
    "v_smfmac_f32_16x16x64_bf16 v[176:179], v[152:155], v[48:55], v254\n" \
    "v_smfmac_f32_16x16x64_bf16 v[180:183], v[152:155], v[112:119], v254\n" \
    "v_smfmac_f32_16x16x64_bf16 v[176:179], v[156:159], v[56:63], v254\n" \
    "v_smfmac_f32_16x16x64_bf16 v[180:183], v[156:159], v[120:127], v254\n" \
    "s_nop 7\n" \
    "v_add_f32 v176, v176, v177\n" \
    "v_add_f32 v178, v178, v179\n" \
    "v_add_f32 v180, v180, v181\n" \
    "v_add_f32 v182, v182, v183\n" \
    "v_cvt_pk_bf16_f32 v176, v176, v178\n" \
    "v_cvt_pk_bf16_f32 v177, v180, v182\n" \
    "v_cvt_pk_bf16_f32 v178, v180, v182\n" \
    "v_mov_b32 v179, 0\n" \
    "s_waitcnt lgkmcnt(0)\n" \
    "s_cmp_eq_u32 s58, 0\n" \
    "s_cbranch_scc1 PK%=_1\n" \
    "v_lshrrev_b32 v240, 1, v253\n" \
    "buffer_store_dwordx2 v[176:177], v240, s[36:39], s50 offen sc1\n" \
    "s_branch PE%=_1\n" \
    "PK%=_1:\n" \
    "v_add_u32 v240, s51, v250\n" \
    "ds_read_b64_tr_b16 v[192:193], v240\n" \
    "ds_read_b64_tr_b16 v[194:195], v240 offset:512\n" \
    "s_waitcnt lgkmcnt(0)\n" \
    "buffer_store_dwordx4 v[192:195], v253, s[36:39], s50 offen sc1\n" \
    "PE%=_1:\n" \
    "s_nop 1\n" \
    "v_add_u32 v244, 1, v244\n" \
    "s_waitcnt vmcnt(0)\n" \
    "s_waitcnt lgkmcnt(0)\n" \
    "s_barrier\n" \
    "s_endpgm\n" \
    "RE_w2:\n"

#define CHAIN_W3 \
    "RS_w3:\n" \
    "s_getpc_b64 s[2:3]\n" \
    "v_lshlrev_b32 v0, 3, v253\n" \
    "s_mov_b32 s0, RE_w3-RS_w3-8\n" \
    "v_add_u32 v1, 0, v0\n" \
    "v_min_u32 v1, s0, v1\n" \
    "global_load_dword v2, v1, s[2:3]\n" \
    "v_add_u32 v1, 8192, v0\n" \
    "v_min_u32 v1, s0, v1\n" \
    "global_load_dword v3, v1, s[2:3]\n" \
    "global_load_dwordx4 v[128:131], v[232:233], off\n" \
    "global_load_dwordx4 v[132:135], v[232:233], off offset:128\n" \
    "v_mov_b32 v242, 0\n" \
    "s_mov_b32 s54, 2162688\n" \
    "buffer_load_dwordx2 v[234:235], v242, s[36:39], s54 offen sc1\n" \
    "s_mov_b32 s42, 0x400000\n" \
    "s_mov_b32 s43, 0x20000\n" \
    "v_xor_b32 v222, 128, v219\n" \
    "s_mov_b32 s40, s62\n" \
    "s_and_b32 s41, s63, 0xffff\n" \
    "s_add_u32 m0, s51, 32768\n" \
    "s_mov_b32 s54, 0\n" \
    "buffer_load_dwordx4 v216, s[40:43], s54 offen lds\n" \
    "s_add_u32 m0, s51, 36864\n" \
    "s_mov_b32 s54, 131072\n" \
    "buffer_load_dwordx4 v216, s[40:43], s54 offen lds\n" \
    "s_add_u32 m0, s51, 40960\n" \
    "s_mov_b32 s54, 262144\n" \
    "buffer_load_dwordx4 v216, s[40:43], s54 offen lds\n" \
    "s_add_u32 m0, s51, 45056\n" \
    "s_mov_b32 s54, 393216\n" \
    "buffer_load_dwordx4 v216, s[40:43], s54 offen lds\n" \
    "s_add_u32 m0, s51, 49152\n" \
    "s_mov_b32 s54, 524288\n" \
    "buffer_load_dwordx4 v216, s[40:43], s54 offen lds\n" \
    "s_add_u32 m0, s51, 53248\n" \
    "s_mov_b32 s54, 655360\n" \
    "buffer_load_dwordx4 v216, s[40:43], s54 offen lds\n" \
    "s_add_u32 m0, s51, 57344\n" \
    "s_mov_b32 s54, 786432\n" \
    "buffer_load_dwordx4 v216, s[40:43], s54 offen lds\n" \
    "s_add_u32 m0, s51, 61440\n" \
    "s_mov_b32 s54, 917504\n" \
    "buffer_load_dwordx4 v216, s[40:43], s54 offen lds\n" \
    "s_add_u32 m0, s51, 65536\n" \
    "s_mov_b32 s54, 1048576\n" \
    "buffer_load_dwordx4 v216, s[40:43], s54 offen lds\n" \
    "s_add_u32 m0, s51, 69632\n" \
    "s_mov_b32 s54, 1179648\n" \
    "buffer_load_dwordx4 v216, s[40:43], s54 offen lds\n" \
    "s_add_u32 m0, s51, 73728\n" \
    "s_mov_b32 s54, 1310720\n" \
    "buffer_load_dwordx4 v216, s[40:43], s54 offen lds\n" \
    "s_add_u32 m0, s51, 77824\n" \
    "s_mov_b32 s54, 1441792\n" \
    "buffer_load_dwordx4 v216, s[40:43], s54 offen lds\n" \
    "s_add_u32 m0, s51, 81920\n" \
    "s_mov_b32 s54, 1572864\n" \
    "buffer_load_dwordx4 v216, s[40:43], s54 offen lds\n" \
    "s_add_u32 m0, s51, 86016\n" \
    "s_mov_b32 s54, 1703936\n" \
    "buffer_load_dwordx4 v216, s[40:43], s54 offen lds\n" \
    "s_add_u32 m0, s51, 90112\n" \
    "s_mov_b32 s54, 1835008\n" \
    "buffer_load_dwordx4 v216, s[40:43], s54 offen lds\n" \
    "s_add_u32 m0, s51, 94208\n" \
    "s_mov_b32 s54, 1966080\n" \
    "buffer_load_dwordx4 v216, s[40:43], s54 offen lds\n" \
    "s_add_u32 m0, s51, 98304\n" \
    "s_mov_b32 s54, 2097152\n" \
    "buffer_load_dwordx4 v216, s[40:43], s54 offen lds\n" \
    "s_add_u32 m0, s51, 102400\n" \
    "s_mov_b32 s54, 2228224\n" \
    "buffer_load_dwordx4 v216, s[40:43], s54 offen lds\n" \
    "s_add_u32 m0, s51, 106496\n" \
    "s_mov_b32 s54, 2359296\n" \
    "buffer_load_dwordx4 v216, s[40:43], s54 offen lds\n" \
    "s_add_u32 m0, s51, 110592\n" \
    "s_mov_b32 s54, 2490368\n" \
    "buffer_load_dwordx4 v216, s[40:43], s54 offen lds\n" \
    "s_add_u32 m0, s51, 114688\n" \
    "s_mov_b32 s54, 2621440\n" \
    "buffer_load_dwordx4 v216, s[40:43], s54 offen lds\n" \
    "s_add_u32 m0, s51, 118784\n" \
    "s_mov_b32 s54, 2752512\n" \
    "buffer_load_dwordx4 v216, s[40:43], s54 offen lds\n" \
    "s_add_u32 m0, s51, 122880\n" \
    "s_mov_b32 s54, 2883584\n" \
    "buffer_load_dwordx4 v216, s[40:43], s54 offen lds\n" \
    "s_add_u32 m0, s51, 126976\n" \
    "s_mov_b32 s54, 3014656\n" \
    "buffer_load_dwordx4 v216, s[40:43], s54 offen lds\n" \
    "s_add_u32 m0, s51, 131072\n" \
    "s_mov_b32 s54, 3145728\n" \
    "buffer_load_dwordx4 v216, s[40:43], s54 offen lds\n" \
    "s_add_u32 m0, s51, 135168\n" \
    "s_mov_b32 s54, 3276800\n" \
    "buffer_load_dwordx4 v216, s[40:43], s54 offen lds\n" \
    "s_add_u32 m0, s51, 139264\n" \
    "s_mov_b32 s54, 3407872\n" \
    "buffer_load_dwordx4 v216, s[40:43], s54 offen lds\n" \
    "s_add_u32 m0, s51, 143360\n" \
    "s_mov_b32 s54, 3538944\n" \
    "buffer_load_dwordx4 v216, s[40:43], s54 offen lds\n" \
    "s_add_u32 m0, s51, 147456\n" \
    "s_mov_b32 s54, 3670016\n" \
    "buffer_load_dwordx4 v216, s[40:43], s54 offen lds\n" \
    "s_add_u32 m0, s51, 151552\n" \
    "s_mov_b32 s54, 3801088\n" \
    "buffer_load_dwordx4 v216, s[40:43], s54 offen lds\n" \
    "s_add_u32 m0, s51, 155648\n" \
    "s_mov_b32 s54, 3932160\n" \
    "buffer_load_dwordx4 v216, s[40:43], s54 offen lds\n" \
    "s_add_u32 m0, s51, 159744\n" \
    "s_mov_b32 s54, 4063232\n" \
    "buffer_load_dwordx4 v216, s[40:43], s54 offen lds\n" \
    "s_mov_b32 s24, s22\n" \
    "s_mov_b32 s25, s23\n" \
    "s_memtime s[20:21]\n" \
    "s_memrealtime s[4:5]\n" \
    "s_nop 2\n" \
    "s_memrealtime s[6:7]\n" \
    "s_nop 2\n" \
    "s_memrealtime s[8:9]\n" \
    "s_nop 2\n" \
    "s_memrealtime s[10:11]\n" \
    "s_nop 2\n" \
    "s_memrealtime s[12:13]\n" \
    "s_nop 2\n" \
    "s_memrealtime s[14:15]\n" \
    "s_nop 2\n" \
    "s_memrealtime s[16:17]\n" \
    "s_nop 2\n" \
    "s_memrealtime s[18:19]\n" \
    "s_mov_b32 s29, 0\n" \
    "s_waitcnt lgkmcnt(0)\n" \
    "s_cmp_eq_u32 s6, s4\n" \
    "s_addc_u32 s29, s29, 0\n" \
    "s_cmp_eq_u32 s8, s4\n" \
    "s_addc_u32 s29, s29, 0\n" \
    "s_cmp_eq_u32 s10, s4\n" \
    "s_addc_u32 s29, s29, 0\n" \
    "s_cmp_eq_u32 s12, s4\n" \
    "s_addc_u32 s29, s29, 0\n" \
    "s_cmp_eq_u32 s14, s4\n" \
    "s_addc_u32 s29, s29, 0\n" \
    "s_cmp_eq_u32 s16, s4\n" \
    "s_addc_u32 s29, s29, 0\n" \
    "s_cmp_eq_u32 s18, s4\n" \
    "s_addc_u32 s29, s29, 0\n" \
    "s_lshl_b32 s29, s29, 4\n" \
    "s_add_u32 s23, s20, s29\n" \
    "s_lshl_b32 s22, s4, 2\n" \
    "s_lshl_b32 s28, s22, 6\n" \
    "s_waitcnt vmcnt(16)\n" \
    "s_barrier\n" \
    "v_xor_b32 v223, 0, v217\n" \
    "v_add_u32 v223, 32768, v223\n" \
    "ds_read_b64_tr_b16 a[160:161], v223\n" \
    "ds_read_b64_tr_b16 a[162:163], v223 offset:512\n" \
    "ds_read_b64_tr_b16 a[164:165], v223 offset:8192\n" \
    "ds_read_b64_tr_b16 a[166:167], v223 offset:8704\n" \
    "v_xor_b32 v223, 16384, v217\n" \
    "v_add_u32 v223, 32768, v223\n" \
    "ds_read_b64_tr_b16 a[168:169], v223\n" \
    "ds_read_b64_tr_b16 a[170:171], v223 offset:512\n" \
    "ds_read_b64_tr_b16 a[172:173], v223 offset:8192\n" \
    "ds_read_b64_tr_b16 a[174:175], v223 offset:8704\n" \
    "v_xor_b32 v223, 32768, v217\n" \
    "v_add_u32 v223, 32768, v223\n" \
    "ds_read_b64_tr_b16 a[176:177], v223\n" \
    "ds_read_b64_tr_b16 a[178:179], v223 offset:512\n" \
    "ds_read_b64_tr_b16 a[180:181], v223 offset:8192\n" \
    "ds_read_b64_tr_b16 a[182:183], v223 offset:8704\n" \
    "v_xor_b32 v223, 49152, v217\n" \
    "v_add_u32 v223, 32768, v223\n" \
    "ds_read_b64_tr_b16 a[184:185], v223\n" \
    "ds_read_b64_tr_b16 a[186:187], v223 offset:512\n" \
    "ds_read_b64_tr_b16 a[188:189], v223 offset:8192\n" \
    "ds_read_b64_tr_b16 a[190:191], v223 offset:8704\n" \
    "v_xor_b32 v223, 0, v218\n" \
    "v_add_u32 v223, 32768, v223\n" \
    "ds_read_b64_tr_b16 a[224:225], v223\n" \
    "ds_read_b64_tr_b16 a[226:227], v223 offset:512\n" \
    "ds_read_b64_tr_b16 a[228:229], v223 offset:8192\n" \
    "ds_read_b64_tr_b16 a[230:231], v223 offset:8704\n" \
    "v_xor_b32 v223, 16384, v218\n" \
    "v_add_u32 v223, 32768, v223\n" \
    "ds_read_b64_tr_b16 a[232:233], v223\n" \
    "ds_read_b64_tr_b16 a[234:235], v223 offset:512\n" \
    "ds_read_b64_tr_b16 a[236:237], v223 offset:8192\n" \
    "ds_read_b64_tr_b16 a[238:239], v223 offset:8704\n" \
    "v_xor_b32 v223, 32768, v218\n" \
    "v_add_u32 v223, 32768, v223\n" \
    "ds_read_b64_tr_b16 a[240:241], v223\n" \
    "ds_read_b64_tr_b16 a[242:243], v223 offset:512\n" \
    "ds_read_b64_tr_b16 a[244:245], v223 offset:8192\n" \
    "ds_read_b64_tr_b16 a[246:247], v223 offset:8704\n" \
    "v_xor_b32 v223, 49152, v218\n" \
    "v_add_u32 v223, 32768, v223\n" \
    "ds_read_b64_tr_b16 a[248:249], v223\n" \
    "ds_read_b64_tr_b16 a[250:251], v223 offset:512\n" \
    "ds_read_b64_tr_b16 a[252:253], v223 offset:8192\n" \
    "ds_read_b64_tr_b16 a[254:255], v223 offset:8704\n" \
    "s_waitcnt lgkmcnt(0)\n" \
    "s_barrier\n" \
    "s_mov_b32 s40, s64\n" \
    "s_and_b32 s41, s65, 0xffff\n" \
    "s_add_u32 m0, s51, 32768\n" \
    "s_mov_b32 s54, 0\n" \
    "buffer_load_dwordx4 v216, s[40:43], s54 offen lds\n" \
    "s_add_u32 m0, s51, 36864\n" \
    "s_mov_b32 s54, 131072\n" \
    "buffer_load_dwordx4 v216, s[40:43], s54 offen lds\n" \
    "s_add_u32 m0, s51, 40960\n" \
    "s_mov_b32 s54, 262144\n" \
    "buffer_load_dwordx4 v216, s[40:43], s54 offen lds\n" \
    "s_add_u32 m0, s51, 45056\n" \
    "s_mov_b32 s54, 393216\n" \
    "buffer_load_dwordx4 v216, s[40:43], s54 offen lds\n" \
    "s_add_u32 m0, s51, 49152\n" \
    "s_mov_b32 s54, 524288\n" \
    "buffer_load_dwordx4 v216, s[40:43], s54 offen lds\n" \
    "s_add_u32 m0, s51, 53248\n" \
    "s_mov_b32 s54, 655360\n" \
    "buffer_load_dwordx4 v216, s[40:43], s54 offen lds\n" \
    "s_add_u32 m0, s51, 57344\n" \
    "s_mov_b32 s54, 786432\n" \
    "buffer_load_dwordx4 v216, s[40:43], s54 offen lds\n" \
    "s_add_u32 m0, s51, 61440\n" \
    "s_mov_b32 s54, 917504\n" \
    "buffer_load_dwordx4 v216, s[40:43], s54 offen lds\n" \
    "s_add_u32 m0, s51, 65536\n" \
    "s_mov_b32 s54, 1048576\n" \
    "buffer_load_dwordx4 v216, s[40:43], s54 offen lds\n" \
    "s_add_u32 m0, s51, 69632\n" \
    "s_mov_b32 s54, 1179648\n" \
    "buffer_load_dwordx4 v216, s[40:43], s54 offen lds\n" \
    "s_add_u32 m0, s51, 73728\n" \
    "s_mov_b32 s54, 1310720\n" \
    "buffer_load_dwordx4 v216, s[40:43], s54 offen lds\n" \
    "s_add_u32 m0, s51, 77824\n" \
    "s_mov_b32 s54, 1441792\n" \
    "buffer_load_dwordx4 v216, s[40:43], s54 offen lds\n" \
    "s_add_u32 m0, s51, 81920\n" \
    "s_mov_b32 s54, 1572864\n" \
    "buffer_load_dwordx4 v216, s[40:43], s54 offen lds\n" \
    "s_add_u32 m0, s51, 86016\n" \
    "s_mov_b32 s54, 1703936\n" \
    "buffer_load_dwordx4 v216, s[40:43], s54 offen lds\n" \
    "s_add_u32 m0, s51, 90112\n" \
    "s_mov_b32 s54, 1835008\n" \
    "buffer_load_dwordx4 v216, s[40:43], s54 offen lds\n" \
    "s_add_u32 m0, s51, 94208\n" \
    "s_mov_b32 s54, 1966080\n" \
    "buffer_load_dwordx4 v216, s[40:43], s54 offen lds\n" \
    "s_waitcnt vmcnt(16)\n" \
    "s_barrier\n" \
    "v_xor_b32 v223, 0, v217\n" \
    "v_add_u32 v223, 98304, v223\n" \
    "ds_read_b64_tr_b16 a[128:129], v223\n" \
    "ds_read_b64_tr_b16 a[130:131], v223 offset:512\n" \
    "ds_read_b64_tr_b16 a[132:133], v223 offset:8192\n" \
    "ds_read_b64_tr_b16 a[134:135], v223 offset:8704\n" \
    "v_xor_b32 v223, 16384, v217\n" \
    "v_add_u32 v223, 98304, v223\n" \
    "ds_read_b64_tr_b16 a[136:137], v223\n" \
    "ds_read_b64_tr_b16 a[138:139], v223 offset:512\n" \
    "ds_read_b64_tr_b16 a[140:141], v223 offset:8192\n" \
    "ds_read_b64_tr_b16 a[142:143], v223 offset:8704\n" \
    "v_xor_b32 v223, 32768, v217\n" \
    "v_add_u32 v223, 98304, v223\n" \
    "ds_read_b64_tr_b16 a[144:145], v223\n" \
    "ds_read_b64_tr_b16 a[146:147], v223 offset:512\n" \
    "ds_read_b64_tr_b16 a[148:149], v223 offset:8192\n" \
    "ds_read_b64_tr_b16 a[150:151], v223 offset:8704\n" \
    "v_xor_b32 v223, 49152, v217\n" \
    "v_add_u32 v223, 98304, v223\n" \
    "ds_read_b64_tr_b16 a[152:153], v223\n" \
    "ds_read_b64_tr_b16 a[154:155], v223 offset:512\n" \
    "ds_read_b64_tr_b16 a[156:157], v223 offset:8192\n" \
    "ds_read_b64_tr_b16 a[158:159], v223 offset:8704\n" \
    "v_xor_b32 v223, 0, v218\n" \
    "v_add_u32 v223, 98304, v223\n" \
    "ds_read_b64_tr_b16 a[192:193], v223\n" \
    "ds_read_b64_tr_b16 a[194:195], v223 offset:512\n" \
    "ds_read_b64_tr_b16 a[196:197], v223 offset:8192\n" \
    "ds_read_b64_tr_b16 a[198:199], v223 offset:8704\n" \
    "v_xor_b32 v223, 16384, v218\n" \
    "v_add_u32 v223, 98304, v223\n" \
    "ds_read_b64_tr_b16 a[200:201], v223\n" \
    "ds_read_b64_tr_b16 a[202:203], v223 offset:512\n" \
    "ds_read_b64_tr_b16 a[204:205], v223 offset:8192\n" \
    "ds_read_b64_tr_b16 a[206:207], v223 offset:8704\n" \
    "v_xor_b32 v223, 32768, v218\n" \
    "v_add_u32 v223, 98304, v223\n" \
    "ds_read_b64_tr_b16 a[208:209], v223\n" \
    "ds_read_b64_tr_b16 a[210:211], v223 offset:512\n" \
    "ds_read_b64_tr_b16 a[212:213], v223 offset:8192\n" \
    "ds_read_b64_tr_b16 a[214:215], v223 offset:8704\n" \
    "v_xor_b32 v223, 49152, v218\n" \
    "v_add_u32 v223, 98304, v223\n" \
    "ds_read_b64_tr_b16 a[216:217], v223\n" \
    "ds_read_b64_tr_b16 a[218:219], v223 offset:512\n" \
    "ds_read_b64_tr_b16 a[220:221], v223 offset:8192\n" \
    "ds_read_b64_tr_b16 a[222:223], v223 offset:8704\n" \
    "s_waitcnt lgkmcnt(0)\n" \
    "s_barrier\n" \
    "s_add_u32 m0, s51, 98304\n" \
    "s_mov_b32 s54, 2097152\n" \
    "buffer_load_dwordx4 v216, s[40:43], s54 offen lds\n" \
    "s_add_u32 m0, s51, 102400\n" \
    "s_mov_b32 s54, 2228224\n" \
    "buffer_load_dwordx4 v216, s[40:43], s54 offen lds\n" \
    "s_add_u32 m0, s51, 106496\n" \
    "s_mov_b32 s54, 2359296\n" \
    "buffer_load_dwordx4 v216, s[40:43], s54 offen lds\n" \
    "s_add_u32 m0, s51, 110592\n" \
    "s_mov_b32 s54, 2490368\n" \
    "buffer_load_dwordx4 v216, s[40:43], s54 offen lds\n" \
    "s_add_u32 m0, s51, 114688\n" \
    "s_mov_b32 s54, 2621440\n" \
    "buffer_load_dwordx4 v216, s[40:43], s54 offen lds\n" \
    "s_add_u32 m0, s51, 118784\n" \
    "s_mov_b32 s54, 2752512\n" \
    "buffer_load_dwordx4 v216, s[40:43], s54 offen lds\n" \
    "s_add_u32 m0, s51, 122880\n" \
    "s_mov_b32 s54, 2883584\n" \
    "buffer_load_dwordx4 v216, s[40:43], s54 offen lds\n" \
    "s_add_u32 m0, s51, 126976\n" \
    "s_mov_b32 s54, 3014656\n" \
    "buffer_load_dwordx4 v216, s[40:43], s54 offen lds\n" \
    "s_add_u32 m0, s51, 131072\n" \
    "s_mov_b32 s54, 3145728\n" \
    "buffer_load_dwordx4 v216, s[40:43], s54 offen lds\n" \
    "s_add_u32 m0, s51, 135168\n" \
    "s_mov_b32 s54, 3276800\n" \
    "buffer_load_dwordx4 v216, s[40:43], s54 offen lds\n" \
    "s_add_u32 m0, s51, 139264\n" \
    "s_mov_b32 s54, 3407872\n" \
    "buffer_load_dwordx4 v216, s[40:43], s54 offen lds\n" \
    "s_add_u32 m0, s51, 143360\n" \
    "s_mov_b32 s54, 3538944\n" \
    "buffer_load_dwordx4 v216, s[40:43], s54 offen lds\n" \
    "s_add_u32 m0, s51, 147456\n" \
    "s_mov_b32 s54, 3670016\n" \
    "buffer_load_dwordx4 v216, s[40:43], s54 offen lds\n" \
    "s_add_u32 m0, s51, 151552\n" \
    "s_mov_b32 s54, 3801088\n" \
    "buffer_load_dwordx4 v216, s[40:43], s54 offen lds\n" \
    "s_add_u32 m0, s51, 155648\n" \
    "s_mov_b32 s54, 3932160\n" \
    "buffer_load_dwordx4 v216, s[40:43], s54 offen lds\n" \
    "s_add_u32 m0, s51, 159744\n" \
    "s_mov_b32 s54, 4063232\n" \
    "buffer_load_dwordx4 v216, s[40:43], s54 offen lds\n" \
    "s_waitcnt vmcnt(16)\n" \
    "s_barrier\n" \
    "v_xor_b32 v223, 0, v217\n" \
    "v_add_u32 v223, 32768, v223\n" \
    "ds_read_b64_tr_b16 v[32:33], v223\n" \
    "ds_read_b64_tr_b16 v[34:35], v223 offset:512\n" \
    "ds_read_b64_tr_b16 v[36:37], v223 offset:8192\n" \
    "ds_read_b64_tr_b16 v[38:39], v223 offset:8704\n" \
    "v_xor_b32 v223, 16384, v217\n" \
    "v_add_u32 v223, 32768, v223\n" \
    "ds_read_b64_tr_b16 v[40:41], v223\n" \
    "ds_read_b64_tr_b16 v[42:43], v223 offset:512\n" \
    "ds_read_b64_tr_b16 v[44:45], v223 offset:8192\n" \
    "ds_read_b64_tr_b16 v[46:47], v223 offset:8704\n" \
    "v_xor_b32 v223, 32768, v217\n" \
    "v_add_u32 v223, 32768, v223\n" \
    "ds_read_b64_tr_b16 v[48:49], v223\n" \
    "ds_read_b64_tr_b16 v[50:51], v223 offset:512\n" \
    "ds_read_b64_tr_b16 v[52:53], v223 offset:8192\n" \
    "ds_read_b64_tr_b16 v[54:55], v223 offset:8704\n" \
    "v_xor_b32 v223, 49152, v217\n" \
    "v_add_u32 v223, 32768, v223\n" \
    "ds_read_b64_tr_b16 v[56:57], v223\n" \
    "ds_read_b64_tr_b16 v[58:59], v223 offset:512\n" \
    "ds_read_b64_tr_b16 v[60:61], v223 offset:8192\n" \
    "ds_read_b64_tr_b16 v[62:63], v223 offset:8704\n" \
    "v_xor_b32 v223, 0, v218\n" \
    "v_add_u32 v223, 32768, v223\n" \
    "ds_read_b64_tr_b16 v[96:97], v223\n" \
    "ds_read_b64_tr_b16 v[98:99], v223 offset:512\n" \
    "ds_read_b64_tr_b16 v[100:101], v223 offset:8192\n" \
    "ds_read_b64_tr_b16 v[102:103], v223 offset:8704\n" \
    "v_xor_b32 v223, 16384, v218\n" \
    "v_add_u32 v223, 32768, v223\n" \
    "ds_read_b64_tr_b16 v[104:105], v223\n" \
    "ds_read_b64_tr_b16 v[106:107], v223 offset:512\n" \
    "ds_read_b64_tr_b16 v[108:109], v223 offset:8192\n" \
    "ds_read_b64_tr_b16 v[110:111], v223 offset:8704\n" \
    "v_xor_b32 v223, 32768, v218\n" \
    "v_add_u32 v223, 32768, v223\n" \
    "ds_read_b64_tr_b16 v[112:113], v223\n" \
    "ds_read_b64_tr_b16 v[114:115], v223 offset:512\n" \
    "ds_read_b64_tr_b16 v[116:117], v223 offset:8192\n" \
    "ds_read_b64_tr_b16 v[118:119], v223 offset:8704\n" \
    "v_xor_b32 v223, 49152, v218\n" \
    "v_add_u32 v223, 32768, v223\n" \
    "ds_read_b64_tr_b16 v[120:121], v223\n" \
    "ds_read_b64_tr_b16 v[122:123], v223 offset:512\n" \
    "ds_read_b64_tr_b16 v[124:125], v223 offset:8192\n" \
    "ds_read_b64_tr_b16 v[126:127], v223 offset:8704\n" \
    "s_waitcnt lgkmcnt(0)\n" \
    "s_barrier\n" \
    "s_mov_b32 s40, s66\n" \
    "s_and_b32 s41, s67, 0xffff\n" \
    "s_add_u32 m0, s51, 32768\n" \
    "s_mov_b32 s54, 0\n" \
    "buffer_load_dwordx4 v219, s[40:43], s54 offen lds\n" \
    "s_add_u32 m0, s51, 36864\n" \
    "s_mov_b32 s54, 8192\n" \
    "buffer_load_dwordx4 v219, s[40:43], s54 offen lds\n" \
    "s_add_u32 m0, s51, 40960\n" \
    "s_mov_b32 s54, 16384\n" \
    "buffer_load_dwordx4 v222, s[40:43], s54 offen lds\n" \
    "s_add_u32 m0, s51, 45056\n" \
    "s_mov_b32 s54, 24576\n" \
    "buffer_load_dwordx4 v222, s[40:43], s54 offen lds\n" \
    "s_add_u32 m0, s51, 49152\n" \
    "s_mov_b32 s54, 32768\n" \
    "buffer_load_dwordx4 v219, s[40:43], s54 offen lds\n" \
    "s_add_u32 m0, s51, 53248\n" \
    "s_mov_b32 s54, 40960\n" \
    "buffer_load_dwordx4 v219, s[40:43], s54 offen lds\n" \
    "s_add_u32 m0, s51, 57344\n" \
    "s_mov_b32 s54, 49152\n" \
    "buffer_load_dwordx4 v222, s[40:43], s54 offen lds\n" \
    "s_add_u32 m0, s51, 61440\n" \
    "s_mov_b32 s54, 57344\n" \
    "buffer_load_dwordx4 v222, s[40:43], s54 offen lds\n" \
    "s_add_u32 m0, s51, 65536\n" \
    "s_mov_b32 s54, 65536\n" \
    "buffer_load_dwordx4 v219, s[40:43], s54 offen lds\n" \
    "s_add_u32 m0, s51, 69632\n" \
    "s_mov_b32 s54, 73728\n" \
    "buffer_load_dwordx4 v219, s[40:43], s54 offen lds\n" \
    "s_add_u32 m0, s51, 73728\n" \
    "s_mov_b32 s54, 81920\n" \
    "buffer_load_dwordx4 v222, s[40:43], s54 offen lds\n" \
    "s_add_u32 m0, s51, 77824\n" \
    "s_mov_b32 s54, 90112\n" \
    "buffer_load_dwordx4 v222, s[40:43], s54 offen lds\n" \
    "s_add_u32 m0, s51, 81920\n" \
    "s_mov_b32 s54, 98304\n" \
    "buffer_load_dwordx4 v219, s[40:43], s54 offen lds\n" \
    "s_add_u32 m0, s51, 86016\n" \
    "s_mov_b32 s54, 106496\n" \
    "buffer_load_dwordx4 v219, s[40:43], s54 offen lds\n" \
    "s_add_u32 m0, s51, 90112\n" \
    "s_mov_b32 s54, 114688\n" \
    "buffer_load_dwordx4 v222, s[40:43], s54 offen lds\n" \
    "s_add_u32 m0, s51, 94208\n" \
    "s_mov_b32 s54, 122880\n" \
    "buffer_load_dwordx4 v222, s[40:43], s54 offen lds\n" \
    "s_waitcnt vmcnt(16)\n" \
    "s_barrier\n" \
    "v_xor_b32 v223, 0, v217\n" \
    "v_add_u32 v223, 98304, v223\n" \
    "ds_read_b64_tr_b16 v[0:1], v223\n" \
    "ds_read_b64_tr_b16 v[2:3], v223 offset:512\n" \
    "ds_read_b64_tr_b16 v[4:5], v223 offset:8192\n" \
    "ds_read_b64_tr_b16 v[6:7], v223 offset:8704\n" \
    "v_xor_b32 v223, 16384, v217\n" \
    "v_add_u32 v223, 98304, v223\n" \
    "ds_read_b64_tr_b16 v[8:9], v223\n" \
    "ds_read_b64_tr_b16 v[10:11], v223 offset:512\n" \
    "ds_read_b64_tr_b16 v[12:13], v223 offset:8192\n" \
    "ds_read_b64_tr_b16 v[14:15], v223 offset:8704\n" \
    "v_xor_b32 v223, 32768, v217\n" \
    "v_add_u32 v223, 98304, v223\n" \
    "ds_read_b64_tr_b16 v[16:17], v223\n" \
    "ds_read_b64_tr_b16 v[18:19], v223 offset:512\n" \
    "ds_read_b64_tr_b16 v[20:21], v223 offset:8192\n" \
    "ds_read_b64_tr_b16 v[22:23], v223 offset:8704\n" \
    "v_xor_b32 v223, 49152, v217\n" \
    "v_add_u32 v223, 98304, v223\n" \
    "ds_read_b64_tr_b16 v[24:25], v223\n" \
    "ds_read_b64_tr_b16 v[26:27], v223 offset:512\n" \
    "ds_read_b64_tr_b16 v[28:29], v223 offset:8192\n" \
    "ds_read_b64_tr_b16 v[30:31], v223 offset:8704\n" \
    "v_xor_b32 v223, 0, v218\n" \
    "v_add_u32 v223, 98304, v223\n" \
    "ds_read_b64_tr_b16 v[64:65], v223\n" \
    "ds_read_b64_tr_b16 v[66:67], v223 offset:512\n" \
    "ds_read_b64_tr_b16 v[68:69], v223 offset:8192\n" \
    "ds_read_b64_tr_b16 v[70:71], v223 offset:8704\n" \
    "v_xor_b32 v223, 16384, v218\n" \
    "v_add_u32 v223, 98304, v223\n" \
    "ds_read_b64_tr_b16 v[72:73], v223\n" \
    "ds_read_b64_tr_b16 v[74:75], v223 offset:512\n" \
    "ds_read_b64_tr_b16 v[76:77], v223 offset:8192\n" \
    "ds_read_b64_tr_b16 v[78:79], v223 offset:8704\n" \
    "v_xor_b32 v223, 32768, v218\n" \
    "v_add_u32 v223, 98304, v223\n" \
    "ds_read_b64_tr_b16 v[80:81], v223\n" \
    "ds_read_b64_tr_b16 v[82:83], v223 offset:512\n" \
    "ds_read_b64_tr_b16 v[84:85], v223 offset:8192\n" \
    "ds_read_b64_tr_b16 v[86:87], v223 offset:8704\n" \
    "v_xor_b32 v223, 49152, v218\n" \
    "v_add_u32 v223, 98304, v223\n" \
    "ds_read_b64_tr_b16 v[88:89], v223\n" \
    "ds_read_b64_tr_b16 v[90:91], v223 offset:512\n" \
    "ds_read_b64_tr_b16 v[92:93], v223 offset:8192\n" \
    "ds_read_b64_tr_b16 v[94:95], v223 offset:8704\n" \
    "s_waitcnt lgkmcnt(0)\n" \
    "s_barrier\n" \
    "s_mov_b32 s40, s60\n" \
    "s_and_b32 s41, s61, 0xffff\n" \
    "s_add_u32 m0, s51, 98304\n" \
    "s_mov_b32 s54, 0\n" \
    "buffer_load_dwordx4 v216, s[40:43], s54 offen lds\n" \
    "s_add_u32 m0, s51, 102400\n" \
    "s_mov_b32 s54, 131072\n" \
    "buffer_load_dwordx4 v216, s[40:43], s54 offen lds\n" \
    "s_add_u32 m0, s51, 106496\n" \
    "s_mov_b32 s54, 262144\n" \
    "buffer_load_dwordx4 v216, s[40:43], s54 offen lds\n" \
    "s_add_u32 m0, s51, 110592\n" \
    "s_mov_b32 s54, 393216\n" \
    "buffer_load_dwordx4 v216, s[40:43], s54 offen lds\n" \
    "s_add_u32 m0, s51, 114688\n" \
    "s_mov_b32 s54, 524288\n" \
    "buffer_load_dwordx4 v216, s[40:43], s54 offen lds\n" \
    "s_add_u32 m0, s51, 118784\n" \
    "s_mov_b32 s54, 655360\n" \
    "buffer_load_dwordx4 v216, s[40:43], s54 offen lds\n" \
    "s_add_u32 m0, s51, 122880\n" \
    "s_mov_b32 s54, 786432\n" \
    "buffer_load_dwordx4 v216, s[40:43], s54 offen lds\n" \
    "s_add_u32 m0, s51, 126976\n" \
    "s_mov_b32 s54, 917504\n" \
    "buffer_load_dwordx4 v216, s[40:43], s54 offen lds\n" \
    "s_add_u32 m0, s51, 131072\n" \
    "s_mov_b32 s54, 1048576\n" \
    "buffer_load_dwordx4 v216, s[40:43], s54 offen lds\n" \
    "s_add_u32 m0, s51, 135168\n" \
    "s_mov_b32 s54, 1179648\n" \
    "buffer_load_dwordx4 v216, s[40:43], s54 offen lds\n" \
    "s_add_u32 m0, s51, 139264\n" \
    "s_mov_b32 s54, 1310720\n" \
    "buffer_load_dwordx4 v216, s[40:43], s54 offen lds\n" \
    "s_add_u32 m0, s51, 143360\n" \
    "s_mov_b32 s54, 1441792\n" \
    "buffer_load_dwordx4 v216, s[40:43], s54 offen lds\n" \
    "s_add_u32 m0, s51, 147456\n" \
    "s_mov_b32 s54, 1572864\n" \
    "buffer_load_dwordx4 v216, s[40:43], s54 offen lds\n" \
    "s_add_u32 m0, s51, 151552\n" \
    "s_mov_b32 s54, 1703936\n" \
    "buffer_load_dwordx4 v216, s[40:43], s54 offen lds\n" \
    "s_add_u32 m0, s51, 155648\n" \
    "s_mov_b32 s54, 1835008\n" \
    "buffer_load_dwordx4 v216, s[40:43], s54 offen lds\n" \
    "s_add_u32 m0, s51, 159744\n" \
    "s_mov_b32 s54, 1966080\n" \
    "buffer_load_dwordx4 v216, s[40:43], s54 offen lds\n" \
    "s_waitcnt vmcnt(16)\n" \
    "s_barrier\n" \
    "v_xor_b32 v223, 0, v220\n" \
    "ds_read_b64_tr_b16 v[136:137], v223\n" \
    "ds_read_b64_tr_b16 v[138:139], v223 offset:1024\n" \
    "ds_read_b64_tr_b16 v[140:141], v223 offset:16384\n" \
    "ds_read_b64_tr_b16 v[142:143], v223 offset:17408\n" \
    "ds_read_b64_tr_b16 v[144:145], v223 offset:32768\n" \
    "ds_read_b64_tr_b16 v[146:147], v223 offset:33792\n" \
    "ds_read_b64_tr_b16 v[148:149], v223 offset:49152\n" \
    "ds_read_b64_tr_b16 v[150:151], v223 offset:50176\n" \
    "v_xor_b32 v223, 32, v220\n" \
    "ds_read_b64_tr_b16 v[152:153], v223\n" \
    "ds_read_b64_tr_b16 v[154:155], v223 offset:1024\n" \
    "ds_read_b64_tr_b16 v[156:157], v223 offset:16384\n" \
    "ds_read_b64_tr_b16 v[158:159], v223 offset:17408\n" \
    "ds_read_b64_tr_b16 v[160:161], v223 offset:32768\n" \
    "ds_read_b64_tr_b16 v[162:163], v223 offset:33792\n" \
    "ds_read_b64_tr_b16 v[164:165], v223 offset:49152\n" \
    "ds_read_b64_tr_b16 v[166:167], v223 offset:50176\n" \
    "v_xor_b32 v223, 64, v220\n" \
    "ds_read_b64_tr_b16 v[168:169], v223\n" \
    "ds_read_b64_tr_b16 v[170:171], v223 offset:1024\n" \
    "ds_read_b64_tr_b16 v[172:173], v223 offset:16384\n" \
    "ds_read_b64_tr_b16 v[174:175], v223 offset:17408\n" \
    "ds_read_b64_tr_b16 v[176:177], v223 offset:32768\n" \
    "ds_read_b64_tr_b16 v[178:179], v223 offset:33792\n" \
    "ds_read_b64_tr_b16 v[180:181], v223 offset:49152\n" \
    "ds_read_b64_tr_b16 v[182:183], v223 offset:50176\n" \
    "v_xor_b32 v223, 96, v220\n" \
    "ds_read_b64_tr_b16 v[184:185], v223\n" \
    "ds_read_b64_tr_b16 v[186:187], v223 offset:1024\n" \
    "ds_read_b64_tr_b16 v[188:189], v223 offset:16384\n" \
    "ds_read_b64_tr_b16 v[190:191], v223 offset:17408\n" \
    "ds_read_b64_tr_b16 v[192:193], v223 offset:32768\n" \
    "ds_read_b64_tr_b16 v[194:195], v223 offset:33792\n" \
    "ds_read_b64_tr_b16 v[196:197], v223 offset:49152\n" \
    "ds_read_b64_tr_b16 v[198:199], v223 offset:50176\n" \
    "s_waitcnt lgkmcnt(0)\n" \
    "s_barrier\n" \
    "v_lshlrev_b32 v206, 3, v253\n" \
    "s_mov_b32 s54, 2163712\n" \
    "buffer_load_dwordx2 v[204:205], v206, s[36:39], s54 offen sc1\n" \
    "s_add_u32 m0, s51, 32768\n" \
    "s_mov_b32 s54, 2097152\n" \
    "buffer_load_dwordx4 v216, s[40:43], s54 offen lds\n" \
    "s_add_u32 m0, s51, 36864\n" \
    "s_mov_b32 s54, 2228224\n" \
    "buffer_load_dwordx4 v216, s[40:43], s54 offen lds\n" \
    "s_add_u32 m0, s51, 40960\n" \
    "s_mov_b32 s54, 2359296\n" \
    "buffer_load_dwordx4 v216, s[40:43], s54 offen lds\n" \
    "s_add_u32 m0, s51, 45056\n" \
    "s_mov_b32 s54, 2490368\n" \
    "buffer_load_dwordx4 v216, s[40:43], s54 offen lds\n" \
    "s_add_u32 m0, s51, 49152\n" \
    "s_mov_b32 s54, 2621440\n" \
    "buffer_load_dwordx4 v216, s[40:43], s54 offen lds\n" \
    "s_add_u32 m0, s51, 53248\n" \
    "s_mov_b32 s54, 2752512\n" \
    "buffer_load_dwordx4 v216, s[40:43], s54 offen lds\n" \
    "s_add_u32 m0, s51, 57344\n" \
    "s_mov_b32 s54, 2883584\n" \
    "buffer_load_dwordx4 v216, s[40:43], s54 offen lds\n" \
    "s_add_u32 m0, s51, 61440\n" \
    "s_mov_b32 s54, 3014656\n" \
    "buffer_load_dwordx4 v216, s[40:43], s54 offen lds\n" \
    "s_add_u32 m0, s51, 65536\n" \
    "s_mov_b32 s54, 3145728\n" \
    "buffer_load_dwordx4 v216, s[40:43], s54 offen lds\n" \
    "s_add_u32 m0, s51, 69632\n" \
    "s_mov_b32 s54, 3276800\n" \
    "buffer_load_dwordx4 v216, s[40:43], s54 offen lds\n" \
    "s_add_u32 m0, s51, 73728\n" \
    "s_mov_b32 s54, 3407872\n" \
    "buffer_load_dwordx4 v216, s[40:43], s54 offen lds\n" \
    "s_add_u32 m0, s51, 77824\n" \
    "s_mov_b32 s54, 3538944\n" \
    "buffer_load_dwordx4 v216, s[40:43], s54 offen lds\n" \
    "s_add_u32 m0, s51, 81920\n" \
    "s_mov_b32 s54, 3670016\n" \
    "buffer_load_dwordx4 v216, s[40:43], s54 offen lds\n" \
    "s_add_u32 m0, s51, 86016\n" \
    "s_mov_b32 s54, 3801088\n" \
    "buffer_load_dwordx4 v216, s[40:43], s54 offen lds\n" \
    "s_add_u32 m0, s51, 90112\n" \
    "s_mov_b32 s54, 3932160\n" \
    "buffer_load_dwordx4 v216, s[40:43], s54 offen lds\n" \
    "s_add_u32 m0, s51, 94208\n" \
    "s_mov_b32 s54, 4063232\n" \
    "buffer_load_dwordx4 v216, s[40:43], s54 offen lds\n" \
    "s_waitcnt vmcnt(16)\n" \
    "s_barrier\n" \
    "v_xor_b32 v223, 0, v217\n" \
    "v_add_u32 v223, 98304, v223\n" \
    "ds_read_b64_tr_b16 a[32:33], v223\n" \
    "ds_read_b64_tr_b16 a[34:35], v223 offset:512\n" \
    "ds_read_b64_tr_b16 a[36:37], v223 offset:8192\n" \
    "ds_read_b64_tr_b16 a[38:39], v223 offset:8704\n" \
    "v_xor_b32 v223, 16384, v217\n" \
    "v_add_u32 v223, 98304, v223\n" \
    "ds_read_b64_tr_b16 a[40:41], v223\n" \
    "ds_read_b64_tr_b16 a[42:43], v223 offset:512\n" \
    "ds_read_b64_tr_b16 a[44:45], v223 offset:8192\n" \
    "ds_read_b64_tr_b16 a[46:47], v223 offset:8704\n" \
    "v_xor_b32 v223, 32768, v217\n" \
    "v_add_u32 v223, 98304, v223\n" \
    "ds_read_b64_tr_b16 a[48:49], v223\n" \
    "ds_read_b64_tr_b16 a[50:51], v223 offset:512\n" \
    "ds_read_b64_tr_b16 a[52:53], v223 offset:8192\n" \
    "ds_read_b64_tr_b16 a[54:55], v223 offset:8704\n" \
    "v_xor_b32 v223, 49152, v217\n" \
    "v_add_u32 v223, 98304, v223\n" \
    "ds_read_b64_tr_b16 a[56:57], v223\n" \
    "ds_read_b64_tr_b16 a[58:59], v223 offset:512\n" \
    "ds_read_b64_tr_b16 a[60:61], v223 offset:8192\n" \
    "ds_read_b64_tr_b16 a[62:63], v223 offset:8704\n" \
    "v_xor_b32 v223, 0, v218\n" \
    "v_add_u32 v223, 98304, v223\n" \
    "ds_read_b64_tr_b16 a[96:97], v223\n" \
    "ds_read_b64_tr_b16 a[98:99], v223 offset:512\n" \
    "ds_read_b64_tr_b16 a[100:101], v223 offset:8192\n" \
    "ds_read_b64_tr_b16 a[102:103], v223 offset:8704\n" \
    "v_xor_b32 v223, 16384, v218\n" \
    "v_add_u32 v223, 98304, v223\n" \
    "ds_read_b64_tr_b16 a[104:105], v223\n" \
    "ds_read_b64_tr_b16 a[106:107], v223 offset:512\n" \
    "ds_read_b64_tr_b16 a[108:109], v223 offset:8192\n" \
    "ds_read_b64_tr_b16 a[110:111], v223 offset:8704\n" \
    "v_xor_b32 v223, 32768, v218\n" \
    "v_add_u32 v223, 98304, v223\n" \
    "ds_read_b64_tr_b16 a[112:113], v223\n" \
    "ds_read_b64_tr_b16 a[114:115], v223 offset:512\n" \
    "ds_read_b64_tr_b16 a[116:117], v223 offset:8192\n" \
    "ds_read_b64_tr_b16 a[118:119], v223 offset:8704\n" \
    "v_xor_b32 v223, 49152, v218\n" \
    "v_add_u32 v223, 98304, v223\n" \
    "ds_read_b64_tr_b16 a[120:121], v223\n" \
    "ds_read_b64_tr_b16 a[122:123], v223 offset:512\n" \
    "ds_read_b64_tr_b16 a[124:125], v223 offset:8192\n" \
    "ds_read_b64_tr_b16 a[126:127], v223 offset:8704\n" \
    "s_waitcnt lgkmcnt(0)\n" \
    "s_barrier\n" \
    "v_add_u32 v234, 1, v234\n" \
    "v_add_u32 v235, 8, v235\n" \
    "v_or_b32 v235, 7, v235\n" \
    "s_mov_b32 s54, 2163712\n" \
    "WIa1024%=:\n" \
    "v_or_b32 v205, 7, v205\n" \
    "v_cmp_ne_u64 vcc, v[234:235], v[204:205]\n" \
    "s_cbranch_vccz WIb1024%=\n" \
    "s_sleep 2\n" \
    "buffer_load_dwordx2 v[204:205], v206, s[36:39], s54 offen sc1\n" \
    "s_waitcnt vmcnt(0)\n" \
    "s_add_u32 s57, s57, 1\n" \
    "s_cmp_lt_u32 s57, 262144\n" \
    "s_cbranch_scc1 WIa1024%=\n" \
    "WIb1024%=:\n" \
    "buffer_load_dwordx2 v[208:209], v241, s[36:39], s68 offen sc1\n" \
    "v_lshlrev_b32 v206, 3, v253\n" \
    "s_mov_b32 s54, 2179072\n" \
    "buffer_load_dwordx2 v[204:205], v206, s[36:39], s54 offen sc1\n" \
    "ds_write_b128 v221, v[136:139]\n" \
    "ds_write_b128 v221, v[140:143] offset:4096\n" \
    "ds_write_b128 v221, v[144:147] offset:8192\n" \
    "ds_write_b128 v221, v[148:151] offset:12288\n" \
    "ds_write_b128 v221, v[152:155] offset:64\n" \
    "ds_write_b128 v221, v[156:159] offset:4160\n" \
    "ds_write_b128 v221, v[160:163] offset:8256\n" \
    "ds_write_b128 v221, v[164:167] offset:12352\n" \
    "ds_write_b128 v221, v[168:171] offset:128\n" \
    "ds_write_b128 v221, v[172:175] offset:4224\n" \
    "ds_write_b128 v221, v[176:179] offset:8320\n" \
    "ds_write_b128 v221, v[180:183] offset:12416\n" \
    "ds_write_b128 v221, v[184:187] offset:192\n" \
    "ds_write_b128 v221, v[188:191] offset:4288\n" \
    "ds_write_b128 v221, v[192:195] offset:8384\n" \
    "ds_write_b128 v221, v[196:199] offset:12480\n" \
    "s_waitcnt lgkmcnt(0)\n" \
    "ds_read_b128 v[224:227], v247 offset:57344\n" \
    "ds_read_b128 v[228:231], v247 offset:61440\n" \
    "v_min_u32 v128, -2, v128\n" \
    "v_min_u32 v129, -2, v129\n" \
    "v_min_u32 v130, -2, v130\n" \
    "v_min_u32 v131, -2, v131\n" \
    "v_min_u32 v132, -2, v132\n" \
    "v_min_u32 v133, -2, v133\n" \
    "v_min_u32 v134, -2, v134\n" \
    "v_min_u32 v135, -2, v135\n" \
    "s_mov_b32 s24, s22\n" \
    "s_mov_b32 s25, s23\n" \
    "s_memtime s[20:21]\n" \
    "s_memrealtime s[4:5]\n" \
    "s_nop 2\n" \
    "s_memrealtime s[6:7]\n" \
    "s_nop 2\n" \
    "s_memrealtime s[8:9]\n" \
    "s_nop 2\n" \
    "s_memrealtime s[10:11]\n" \
    "s_nop 2\n" \
    "s_memrealtime s[12:13]\n" \
    "s_nop 2\n" \
    "s_memrealtime s[14:15]\n" \
    "s_nop 2\n" \
    "s_memrealtime s[16:17]\n" \
    "s_nop 2\n" \
    "s_memrealtime s[18:19]\n" \
    "s_mov_b32 s29, 0\n" \
    "s_waitcnt lgkmcnt(0)\n" \
    "s_cmp_eq_u32 s6, s4\n" \
    "s_addc_u32 s29, s29, 0\n" \
    "s_cmp_eq_u32 s8, s4\n" \
    "s_addc_u32 s29, s29, 0\n" \
    "s_cmp_eq_u32 s10, s4\n" \
    "s_addc_u32 s29, s29, 0\n" \
    "s_cmp_eq_u32 s12, s4\n" \
    "s_addc_u32 s29, s29, 0\n" \
    "s_cmp_eq_u32 s14, s4\n" \
    "s_addc_u32 s29, s29, 0\n" \
    "s_cmp_eq_u32 s16, s4\n" \
    "s_addc_u32 s29, s29, 0\n" \
    "s_cmp_eq_u32 s18, s4\n" \
    "s_addc_u32 s29, s29, 0\n" \
    "s_lshl_b32 s29, s29, 4\n" \
    "s_add_u32 s23, s20, s29\n" \
    "s_lshl_b32 s22, s4, 2\n" \
    "s_lshl_b32 s28, s22, 6\n" \
    "s_sub_u32 s29, s23, s25\n" \
    "s_sub_u32 s30, s22, s24\n" \
    "v_cvt_f32_u32 v232, s29\n" \
    "v_cvt_f32_u32 v233, s30\n" \
    "v_rcp_f32 v233, v233\n" \
    "v_or_b32 v243, v243, v243\n" \
    "v_mul_f32 v232, v232, v233\n" \
    "v_rcp_f32 v233, v232\n" \
    "s_nop 0\n" \
    "v_readfirstlane_b32 s26, v232\n" \
    "v_readfirstlane_b32 s27, v233\n" \
    "s_nop 1\n" \
    "s_waitcnt vmcnt(0)\n" \
    "s_barrier\n" \
    "s_mov_b32 s52, 8192\n" \
    "s_mov_b32 s48, 8192\n" \
    "s_mov_b32 s31, 0\n" \
    "KVa%=:\n" \
    "v_and_b32 v210, v208, v209\n" \
    "v_cmp_ne_u32 vcc, 1, v210\n" \
    "s_cbranch_vccnz KVc%=\n" \
    "s_add_u32 m0, s52, 12288\n" \
    "s_add_u32 s55, s49, 12288\n" \
    "buffer_load_dwordx4 v253, s[36:39], s55 offen sc1 lds\n" \
    "s_add_u32 m0, s52, 13312\n" \
    "s_add_u32 s55, s49, 13312\n" \
    "buffer_load_dwordx4 v253, s[36:39], s55 offen sc1 lds\n" \
    "s_add_u32 m0, s52, 14336\n" \
    "s_add_u32 s55, s49, 14336\n" \
    "buffer_load_dwordx4 v253, s[36:39], s55 offen sc1 lds\n" \
    "s_add_u32 m0, s52, 15360\n" \
    "s_add_u32 s55, s49, 15360\n" \
    "buffer_load_dwordx4 v253, s[36:39], s55 offen sc1 lds\n" \
    "s_mov_b32 s31, 1\n" \
    "KVc%=:\n" \
    "v_xor_b32 v223, 0, v217\n" \
    "v_add_u32 v223, 32768, v223\n" \
    "ds_read_b64_tr_b16 a[0:1], v223\n" \
    "ds_read_b64_tr_b16 a[2:3], v223 offset:512\n" \
    "ds_read_b64_tr_b16 a[4:5], v223 offset:8192\n" \
    "ds_read_b64_tr_b16 a[6:7], v223 offset:8704\n" \
    "v_xor_b32 v223, 16384, v217\n" \
    "v_add_u32 v223, 32768, v223\n" \
    "ds_read_b64_tr_b16 a[8:9], v223\n" \
    "ds_read_b64_tr_b16 a[10:11], v223 offset:512\n" \
    "ds_read_b64_tr_b16 a[12:13], v223 offset:8192\n" \
    "ds_read_b64_tr_b16 a[14:15], v223 offset:8704\n" \
    "v_xor_b32 v223, 32768, v217\n" \
    "v_add_u32 v223, 32768, v223\n" \
    "ds_read_b64_tr_b16 a[16:17], v223\n" \
    "ds_read_b64_tr_b16 a[18:19], v223 offset:512\n" \
    "ds_read_b64_tr_b16 a[20:21], v223 offset:8192\n" \
    "ds_read_b64_tr_b16 a[22:23], v223 offset:8704\n" \
    "v_xor_b32 v223, 49152, v217\n" \
    "v_add_u32 v223, 32768, v223\n" \
    "ds_read_b64_tr_b16 a[24:25], v223\n" \
    "ds_read_b64_tr_b16 a[26:27], v223 offset:512\n" \
    "ds_read_b64_tr_b16 a[28:29], v223 offset:8192\n" \
    "ds_read_b64_tr_b16 a[30:31], v223 offset:8704\n" \
    "v_xor_b32 v223, 0, v218\n" \
    "v_add_u32 v223, 32768, v223\n" \
    "ds_read_b64_tr_b16 a[64:65], v223\n" \
    "ds_read_b64_tr_b16 a[66:67], v223 offset:512\n" \
    "ds_read_b64_tr_b16 a[68:69], v223 offset:8192\n" \
    "ds_read_b64_tr_b16 a[70:71], v223 offset:8704\n" \
    "v_xor_b32 v223, 16384, v218\n" \
    "v_add_u32 v223, 32768, v223\n" \
    "ds_read_b64_tr_b16 a[72:73], v223\n" \
    "ds_read_b64_tr_b16 a[74:75], v223 offset:512\n" \
    "ds_read_b64_tr_b16 a[76:77], v223 offset:8192\n" \
    "ds_read_b64_tr_b16 a[78:79], v223 offset:8704\n" \
    "v_xor_b32 v223, 32768, v218\n" \
    "v_add_u32 v223, 32768, v223\n" \
    "ds_read_b64_tr_b16 a[80:81], v223\n" \
    "ds_read_b64_tr_b16 a[82:83], v223 offset:512\n" \
    "ds_read_b64_tr_b16 a[84:85], v223 offset:8192\n" \
    "ds_read_b64_tr_b16 a[86:87], v223 offset:8704\n" \
    "v_xor_b32 v223, 49152, v218\n" \
    "v_add_u32 v223, 32768, v223\n" \
    "ds_read_b64_tr_b16 a[88:89], v223\n" \
    "ds_read_b64_tr_b16 a[90:91], v223 offset:512\n" \
    "ds_read_b64_tr_b16 a[92:93], v223 offset:8192\n" \
    "ds_read_b64_tr_b16 a[94:95], v223 offset:8704\n" \
    "s_waitcnt lgkmcnt(0)\n" \
    "s_barrier\n" \
    "s_cmp_eq_u32 s31, 1\n" \
    "s_cbranch_scc1 KWc%=\n" \
    "KWa%=:\n" \
    "v_and_b32 v210, v208, v209\n" \
    "v_cmp_ne_u32 vcc, 1, v210\n" \
    "s_cbranch_vccz KWb%=\n" \
    "s_sleep 4\n" \
    "buffer_load_dwordx2 v[208:209], v241, s[36:39], s68 offen sc1\n" \
    "s_waitcnt vmcnt(0)\n" \
    "s_add_u32 s57, s57, 1\n" \
    "s_cmp_lt_u32 s57, 262144\n" \
    "s_cbranch_scc1 KWa%=\n" \
    "KWb%=:\n" \
    "s_add_u32 m0, s52, 12288\n" \
    "s_add_u32 s55, s49, 12288\n" \
    "buffer_load_dwordx4 v253, s[36:39], s55 offen sc1 lds\n" \
    "s_add_u32 m0, s52, 13312\n" \
    "s_add_u32 s55, s49, 13312\n" \
    "buffer_load_dwordx4 v253, s[36:39], s55 offen sc1 lds\n" \
    "s_add_u32 m0, s52, 14336\n" \
    "s_add_u32 s55, s49, 14336\n" \
    "buffer_load_dwordx4 v253, s[36:39], s55 offen sc1 lds\n" \
    "s_add_u32 m0, s52, 15360\n" \
    "s_add_u32 s55, s49, 15360\n" \
    "buffer_load_dwordx4 v253, s[36:39], s55 offen sc1 lds\n" \
    "s_mov_b32 s31, 1\n" \
    "KWc%=:\n" \
    "s_mov_b32 s54, 2179072\n" \
    "WNa%=:\n" \
    "v_mov_b32 v207, v205\n" \
    "v_or_b32 v205, 7, v205\n" \
    "v_cmp_ne_u64 vcc, v[234:235], v[204:205]\n" \
    "s_cbranch_vccz WNb%=\n" \
    "s_sleep 2\n" \
    "buffer_load_dwordx2 v[204:205], v206, s[36:39], s54 offen sc1\n" \
    "s_waitcnt vmcnt(0)\n" \
    "s_add_u32 s57, s57, 1\n" \
    "s_cmp_lt_u32 s57, 262144\n" \
    "s_cbranch_scc1 WNa%=\n" \
    "WNb%=:\n" \
    "s_mov_b32 s32, 0\n" \
    "v_and_b32 v204, 1, v207\n" \
    "v_cmp_ne_u32 vcc, 0, v204\n" \
    "s_bcnt1_i32_b64 s54, vcc\n" \
    "s_cmp_gt_u32 s54, 32\n" \
    "s_cselect_b32 s54, 4096, 0\n" \
    "s_or_b32 s32, s32, s54\n" \
    "v_and_b32 v204, 2, v207\n" \
    "v_cmp_ne_u32 vcc, 0, v204\n" \
    "s_bcnt1_i32_b64 s54, vcc\n" \
    "s_cmp_gt_u32 s54, 32\n" \
    "s_cselect_b32 s54, 8192, 0\n" \
    "s_or_b32 s32, s32, s54\n" \
    "v_and_b32 v204, 4, v207\n" \
    "v_cmp_ne_u32 vcc, 0, v204\n" \
    "s_bcnt1_i32_b64 s54, vcc\n" \
    "s_cmp_gt_u32 s54, 32\n" \
    "s_cselect_b32 s54, 16384, 0\n" \
    "s_or_b32 s32, s32, s54\n" \
    "s_xor_b32 s45, s45, s32\n" \
    "s_xor_b32 s46, s46, s32\n" \
    "s_mov_b32 exec_hi, 0\n" \
    "v_xor_b32 v245, s32, v245\n" \
    "s_mov_b32 exec_hi, -1\n" \
    "v_xor_b32 v246, s32, v246\n" \
    "s_mov_b32 s40, s36\n" \
    "s_mov_b32 s41, s37\n" \
    "s_mov_b32 s43, s39\n" \
    "s_mov_b32 s60, s36\n" \
    "s_mov_b32 s61, s37\n" \
    "s_mov_b32 s63, s39\n" \
    "s_cmp_eq_u32 s58, 0\n" \
    "s_cselect_b32 s42, s38, 0\n" \
    "s_cselect_b32 s62, 0, s38\n" \
    "s_mov_b32 s33, 1750\n" \
    "s_mov_b32 s31, 179\n" \
    "s_mov_b32 s65, 0\n" \
    "v_mov_b64 v[160:161], 0\n" \
    "v_mov_b64 v[162:163], 0\n" \
    "v_mov_b64 v[164:165], 0\n" \
    "v_mov_b64 v[166:167], 0\n" \
    "v_mov_b64 v[168:169], 0\n" \
    "v_mov_b64 v[170:171], 0\n" \
    "v_mov_b64 v[172:173], 0\n" \
    "v_mov_b64 v[174:175], 0\n" \
    "v_mov_b64 v[176:177], 0\n" \
    "v_mov_b64 v[178:179], 0\n" \
    "v_mov_b64 v[180:181], 0\n" \
    "v_mov_b64 v[182:183], 0\n" \
    "v_xor_b32 v136, 2048, v255\n" \
    "v_xor_b32 v140, 3072, v255\n" \
    "v_xor_b32 v144, 4096, v255\n" \
    "v_xor_b32 v148, 5120, v255\n" \
    "v_xor_b32 v152, 6144, v255\n" \
    "v_xor_b32 v156, 7168, v255\n" \
    "v_add_u32 v216, s48, v253\n" \
    "v_add_u32 v220, s48, v255\n" \
    "s_xor_b32 s52, s48, 32768\n" \
    "s_add_u32 s49, s49, 16384\n" \
    "s_add_u32 m0, s52, 12288\n" \
    "s_add_u32 s55, s49, 12288\n" \
    "buffer_load_dwordx4 v253, s[36:39], s55 offen sc1 lds\n" \
    "s_add_u32 m0, s52, 13312\n" \
    "s_add_u32 s55, s49, 13312\n" \
    "buffer_load_dwordx4 v253, s[36:39], s55 offen sc1 lds\n" \
    "s_add_u32 m0, s52, 14336\n" \
    "s_add_u32 s55, s49, 14336\n" \
    "buffer_load_dwordx4 v253, s[36:39], s55 offen sc1 lds\n" \
    "s_add_u32 m0, s52, 15360\n" \
    "s_add_u32 s55, s49, 15360\n" \
    "buffer_load_dwordx4 v253, s[36:39], s55 offen sc1 lds\n" \
    "HEAD0%=:\n" \
    "s_waitcnt vmcnt(5)\n" \
    "ds_write_b128 v255, v[128:131]\n" \
    "v_smfmac_f32_16x16x64_bf16 v[160:163], v[128:131], a[0:7], v254\n" \
    "s_waitcnt vmcnt(4)\n" \
    "ds_write_b128 v255, v[132:135] offset:1024\n" \
    "v_max3_u32 v232, v128, v129, v130\n" \
    "v_smfmac_f32_16x16x64_bf16 v[168:171], v[128:131], a[128:135], v254\n" \
    "v_max3_u32 v240, v132, v133, v134\n" \
    "v_smfmac_f32_16x16x64_bf16 v[164:167], v[128:131], a[64:71], v254\n" \
    "v_max3_u32 v232, v232, v131, v135\n" \
    "v_smfmac_f32_16x16x64_bf16 v[172:175], v[128:131], a[192:199], v254\n" \
    "v_max_u32 v232, v232, v240\n" \
    "v_smfmac_f32_16x16x64_bf16 v[160:163], v[132:135], a[8:15], v254\n" \
    "v_cmp_eq_u32 vcc, -1, v232\n" \
    "v_smfmac_f32_16x16x64_bf16 v[168:171], v[132:135], a[136:143], v254\n" \
    "s_cbranch_vccnz RETRY2%=\n" \
    "OK2%=:\n" \
    "s_waitcnt lgkmcnt(0)\n" \
    "s_barrier\n" \
    "ds_read_b128 v[136:139], v136\n" \
    "ds_read_b128 v[140:143], v140\n" \
    "s_memtime s[20:21]\n" \
    "v_smfmac_f32_16x16x64_bf16 v[164:167], v[132:135], a[72:79], v254\n" \
    "ds_read_b128 v[144:147], v144\n" \
    "v_smfmac_f32_16x16x64_bf16 v[172:175], v[132:135], a[200:207], v254\n" \
    "ds_read_b128 v[148:151], v148\n" \
    "v_smfmac_f32_16x16x64_bf16 v[176:179], v[128:131], v[0:7], v254\n" \
    "ds_read_b128 v[152:155], v152\n" \
    "v_smfmac_f32_16x16x64_bf16 v[180:183], v[128:131], v[64:71], v254\n" \
    "ds_read_b128 v[156:159], v156\n" \
    "v_smfmac_f32_16x16x64_bf16 v[176:179], v[132:135], v[8:15], v254\n" \
    "v_smfmac_f32_16x16x64_bf16 v[180:183], v[132:135], v[72:79], v254\n" \
    "CONT%=:\n" \
    "s_waitcnt lgkmcnt(5)\n" \
    "v_smfmac_f32_16x16x64_bf16 v[160:163], v[136:139], a[16:23], v254\n" \
    "s_waitcnt lgkmcnt(4)\n" \
    "v_smfmac_f32_16x16x64_bf16 v[160:163], v[140:143], a[24:31], v254\n" \
    "s_waitcnt lgkmcnt(3)\n" \
    "v_smfmac_f32_16x16x64_bf16 v[160:163], v[144:147], a[32:39], v254\n" \
    "s_waitcnt lgkmcnt(2)\n" \
    "v_smfmac_f32_16x16x64_bf16 v[160:163], v[148:151], a[40:47], v254\n" \
    "s_waitcnt lgkmcnt(1)\n" \
    "v_smfmac_f32_16x16x64_bf16 v[160:163], v[152:155], a[48:55], v254\n" \
    "s_waitcnt lgkmcnt(0)\n" \
    "v_smfmac_f32_16x16x64_bf16 v[160:163], v[156:159], a[56:63], v254\n" \
    "s_sub_u32 s29, s20, s23\n" \
    "s_add_u32 s29, s29, s33\n" \
    "v_smfmac_f32_16x16x64_bf16 v[168:171], v[136:139], a[144:151], v254\n" \
    "v_cvt_f32_u32 v232, s29\n" \
    "v_smfmac_f32_16x16x64_bf16 v[168:171], v[140:143], a[152:159], v254\n" \
    "ds_read_b128 v[184:187], v216\n" \
    "v_mul_f32 v232, s27, v232\n" \
    "v_smfmac_f32_16x16x64_bf16 v[168:171], v[144:147], a[160:167], v254\n" \
    "ds_read_b128 v[188:191], v216 offset:1024\n" \
    "v_cvt_u32_f32 v232, v232\n" \
    "v_smfmac_f32_16x16x64_bf16 v[168:171], v[148:151], a[168:175], v254\n" \
    "ds_read_b128 v[192:195], v216 offset:2048\n" \
    "v_add_f32 v160, v160, v161\n" \
    "v_smfmac_f32_16x16x64_bf16 v[168:171], v[152:155], a[176:183], v254\n" \
    "ds_read_b128 v[196:199], v216 offset:3072\n" \
    "v_add_f32 v162, v162, v163\n" \
    "v_smfmac_f32_16x16x64_bf16 v[168:171], v[156:159], a[184:191], v254\n" \
    "ds_read_b128 v[200:203], v216 offset:4096\n" \
    "v_cvt_pk_bf16_f32 v161, v160, v162\n" \
    "v_smfmac_f32_16x16x64_bf16 v[164:167], v[136:139], a[80:87], v254\n" \
    "ds_read_b128 v[204:207], v216 offset:5120\n" \
    "v_add_u32 v232, s22, v232\n" \
    "v_smfmac_f32_16x16x64_bf16 v[164:167], v[140:143], a[88:95], v254\n" \
    "ds_read_b128 v[208:211], v216 offset:6144\n" \
    "v_lshl_or_b32 v232, v232, 6, v244\n" \
    "v_smfmac_f32_16x16x64_bf16 v[164:167], v[144:147], a[96:103], v254\n" \
    "ds_read_b128 v[212:215], v216 offset:7168\n" \
    "buffer_store_dword v232, v246, s[36:39], s59 offen sc1\n" \
    "v_or_b32 v243, v243, v243\n" \
    "v_smfmac_f32_16x16x64_bf16 v[164:167], v[148:151], a[104:111], v254\n" \
    "ds_read_b128 v[216:219], v220 offset:8192\n" \
    "v_add_f32 v168, v168, v169\n" \
    "v_smfmac_f32_16x16x64_bf16 v[164:167], v[152:155], a[112:119], v254\n" \
    "ds_read_b128 v[220:223], v220 offset:9216\n" \
    "v_add_f32 v170, v170, v171\n" \
    "v_smfmac_f32_16x16x64_bf16 v[164:167], v[156:159], a[120:127], v254\n" \
    "v_cvt_pk_bf16_f32 v160, v168, v170\n" \
    "v_smfmac_f32_16x16x64_bf16 v[172:175], v[136:139], a[208:215], v254\n" \
    "ds_write_b64 v251, v[160:161]\n" \
    "v_smfmac_f32_16x16x64_bf16 v[172:175], v[140:143], a[216:223], v254\n" \
    "ds_read_b128 v[128:131], v247\n" \
    "v_or_b32 v243, v243, v243\n" \
    "v_smfmac_f32_16x16x64_bf16 v[172:175], v[144:147], a[224:231], v254\n" \
    "ds_read_b128 v[132:135], v247 offset:4096\n" \
    "v_or_b32 v243, v243, v243\n" \
    "v_smfmac_f32_16x16x64_bf16 v[172:175], v[148:151], a[232:239], v254\n" \
    "v_add_f32 v164, v164, v165\n" \
    "v_smfmac_f32_16x16x64_bf16 v[172:175], v[152:155], a[240:247], v254\n" \
    "v_add_f32 v166, v166, v167\n" \
    "v_smfmac_f32_16x16x64_bf16 v[172:175], v[156:159], a[248:255], v254\n" \
    "v_cvt_pk_bf16_f32 v165, v164, v166\n" \
    "v_smfmac_f32_16x16x64_bf16 v[176:179], v[136:139], v[16:23], v254\n" \
    "s_nop 1\n" \
    "v_or_b32 v243, v243, v243\n" \
    "v_smfmac_f32_16x16x64_bf16 v[180:183], v[136:139], v[80:87], v254\n" \
    "s_nop 1\n" \
    "v_add_f32 v172, v172, v173\n" \
    "v_add_f32 v174, v174, v175\n" \
    "v_cvt_pk_bf16_f32 v164, v172, v174\n" \
    "v_smfmac_f32_16x16x64_bf16 v[176:179], v[140:143], v[24:31], v254\n" \
    "ds_write_b64 v251, v[164:165] offset:512\n" \
    "s_cmp_eq_u32 s44, 63\n" \
    "v_smfmac_f32_16x16x64_bf16 v[180:183], v[140:143], v[88:95], v254\n" \
    "v_or_b32 v243, v243, v243\n" \
    "v_smfmac_f32_16x16x64_bf16 v[176:179], v[144:147], v[32:39], v254\n" \
    "v_or_b32 v243, v243, v243\n" \
    "v_smfmac_f32_16x16x64_bf16 v[180:183], v[144:147], v[96:103], v254\n" \
    "v_or_b32 v243, v243, v243\n" \
    "v_smfmac_f32_16x16x64_bf16 v[176:179], v[148:151], v[40:47], v254\n" \
    "v_or_b32 v243, v243, v243\n" \
    "v_smfmac_f32_16x16x64_bf16 v[180:183], v[148:151], v[104:111], v254\n" \
    "s_waitcnt lgkmcnt(0)\n" \
    "s_barrier\n" \
    "ds_read_b64_tr_b16 v[160:161], v250\n" \
    "ds_read_b64_tr_b16 v[162:163], v250 offset:512\n" \
    "s_cbranch_scc1 TAIL%=\n" \
    "v_smfmac_f32_16x16x64_bf16 v[176:179], v[152:155], v[48:55], v254\n" \
    "ds_read_b64_tr_b16 v[164:165], v250 offset:1024\n" \
    "ds_read_b64_tr_b16 v[166:167], v250 offset:1536\n" \
    "v_smfmac_f32_16x16x64_bf16 v[180:183], v[152:155], v[112:119], v254\n" \
    "ds_read_b64_tr_b16 v[168:169], v250 offset:2048\n" \
    "ds_read_b64_tr_b16 v[170:171], v250 offset:2560\n" \
    "v_smfmac_f32_16x16x64_bf16 v[176:179], v[156:159], v[56:63], v254\n" \
    "ds_read_b64_tr_b16 v[172:173], v250 offset:3072\n" \
    "ds_read_b64_tr_b16 v[174:175], v250 offset:3584\n" \
    "v_smfmac_f32_16x16x64_bf16 v[180:183], v[156:159], v[120:127], v254\n" \
    "v_or_b32 v243, v243, v243\n" \
    "s_waitcnt lgkmcnt(6)\n" \
    "v_mfma_f32_16x16x32_bf16 v[184:187], v[184:187], v[160:163], 0\n" \
    "ds_read_b128 v[136:139], v247 offset:8192\n" \
    "v_or_b32 v243, v243, v243\n" \
    "v_mfma_f32_16x16x32_bf16 v[200:203], v[200:203], v[160:163], 0\n" \
    "ds_read_b128 v[140:143], v247 offset:12288\n" \
    "v_or_b32 v243, v243, v243\n" \
    "s_waitcnt lgkmcnt(6)\n" \
    "v_mfma_f32_16x16x32_bf16 v[184:187], v[188:191], v[164:167], v[184:187]\n" \
    "ds_read_b128 v[144:147], v247 offset:16384\n" \
    "v_or_b32 v243, v243, v243\n" \
    "v_mfma_f32_16x16x32_bf16 v[200:203], v[204:207], v[164:167], v[200:203]\n" \
    "ds_read_b128 v[148:151], v247 offset:20480\n" \
    "v_add_f32 v176, v176, v177\n" \
    "s_waitcnt lgkmcnt(6)\n" \
    "v_mfma_f32_16x16x32_bf16 v[184:187], v[192:195], v[168:171], v[184:187]\n" \
    "ds_read_b128 v[152:155], v247 offset:24576\n" \
    "v_add_f32 v178, v178, v179\n" \
    "v_mfma_f32_16x16x32_bf16 v[200:203], v[208:211], v[168:171], v[200:203]\n" \
    "ds_read_b128 v[156:159], v247 offset:28672\n" \
    "v_add_f32 v180, v180, v181\n" \
    "s_waitcnt lgkmcnt(6)\n" \
    "v_mfma_f32_16x16x32_bf16 v[184:187], v[196:199], v[172:175], v[184:187]\n" \
    "v_add_f32 v182, v182, v183\n" \
    "v_cvt_pk_bf16_f32 v176, v176, v178\n" \
    "v_mfma_f32_16x16x32_bf16 v[200:203], v[212:215], v[172:175], v[200:203]\n" \
    "buffer_store_dwordx4 v[172:175], v253, s[40:43], s50 offen sc1\n" \
    "v_cvt_pk_bf16_f32 v177, v180, v182\n" \
    "v_mfma_f32_16x16x32_bf16 v[160:163], v[160:163], v[160:163], 0\n" \
    "ds_read_b128 v[188:191], v247 offset:32768\n" \
    "v_cvt_pk_bf16_f32 v178, v180, v182\n" \
    "v_mfma_f32_16x16x32_bf16 v[160:163], v[164:167], v[164:167], v[160:163]\n" \
    "ds_read_b128 v[192:195], v247 offset:36864\n" \
    "v_mov_b32 v179, 0\n" \
    "v_mfma_f32_16x16x32_bf16 v[160:163], v[168:171], v[168:171], v[160:163]\n" \
    "ds_read_b128 v[204:207], v247 offset:40960\n" \
    "v_max3_f32 v232, v184, v185, v186\n" \
    "v_mfma_f32_16x16x32_bf16 v[160:163], v[172:175], v[172:175], v[160:163]\n" \
    "ds_read_b128 v[208:211], v247 offset:45056\n" \
    "ds_read_b128 v[164:167], v247 offset:49152\n" \
    "ds_read_b128 v[168:171], v247 offset:53248\n" \
    "v_max3_f32 v233, v200, v201, v202\n" \
    "v_max3_f32 v232, v232, v187, v203\n" \
    "v_max_f32 v232, v232, v233\n" \
    "v_mov_b32 v172, 0\n" \
    "v_mov_b32 v173, 0\n" \
    "v_max3_f32 v234, v232, v160, v161\n" \
    "v_max3_f32 v235, v232, v160, v161\n" \
    "v_mov_b32 v174, 0\n" \
    "v_mov_b32 v175, 0\n" \
    "v_permlane32_swap_b32 v234, v235\n" \
    "v_max_f32 v232, v234, v235\n" \
    "v_max_f32 v233, v234, v235\n" \
    "v_or_b32 v243, v243, v243\n" \
    "v_or_b32 v243, v243, v243\n" \
    "v_permlane16_swap_b32 v232, v233\n" \
    "v_max_f32 v234, v232, v233\n" \
    "v_mul_f32 v234, s53, v234\n" \
    "v_pk_fma_f32 v[160:161], v[160:161], s[52:53], v[234:235] op_sel:[0,1,0] op_sel_hi:[1,1,0] neg_lo:[0,0,1] neg_hi:[0,0,1]\n" \
    "v_pk_fma_f32 v[184:185], v[184:185], s[52:53], v[234:235] op_sel:[0,1,0] op_sel_hi:[1,1,0] neg_lo:[0,0,1] neg_hi:[0,0,1]\n" \
    "v_pk_fma_f32 v[186:187], v[186:187], s[52:53], v[234:235] op_sel:[0,1,0] op_sel_hi:[1,1,0] neg_lo:[0,0,1] neg_hi:[0,0,1]\n" \
    "v_pk_fma_f32 v[200:201], v[200:201], s[52:53], v[234:235] op_sel:[0,1,0] op_sel_hi:[1,1,0] neg_lo:[0,0,1] neg_hi:[0,0,1]\n" \
    "v_pk_fma_f32 v[202:203], v[202:203], s[52:53], v[234:235] op_sel:[0,1,0] op_sel_hi:[1,1,0] neg_lo:[0,0,1] neg_hi:[0,0,1]\n" \
    "v_exp_f32 v160, v160\n" \
    "v_exp_f32 v161, v161\n" \
    "v_exp_f32 v184, v184\n" \
    "v_cvt_pk_bf16_f32 v240, v160, v161\n" \
    "v_exp_f32 v185, v185\n" \
    "v_exp_f32 v186, v186\n" \
    "v_mfma_f32_16x16x16_bf16 v[212:215], v[236:237], v[240:241], 0\n" \
    "v_exp_f32 v187, v187\n" \
    "v_mfma_f32_16x16x16_bf16 v[180:183], v[176:177], v[240:241], 0\n" \
    "v_exp_f32 v200, v200\n" \
    "v_mfma_f32_16x16x16_bf16 v[232:235], v[178:179], v[240:241], 0\n" \
    "v_exp_f32 v201, v201\n" \
    "v_exp_f32 v202, v202\n" \
    "v_exp_f32 v203, v203\n" \
    "v_cvt_pk_bf16_f32 v196, v184, v185\n" \
    "v_cvt_pk_bf16_f32 v197, v186, v187\n" \
    "v_cvt_pk_bf16_f32 v198, v200, v201\n" \
    "v_cvt_pk_bf16_f32 v199, v202, v203\n" \
    "v_mov_b32 v184, 0\n" \
    "v_mov_b32 v185, 0\n" \
    "v_mfma_f32_16x16x32_bf16 v[212:215], v[236:239], v[196:199], v[212:215]\n" \
    "buffer_load_dword v242, v245, s[36:39], s59 offen sc1\n" \
    "v_mov_b32 v186, 0\n" \
    "v_mov_b32 v187, 0\n" \
    "v_mfma_f32_16x16x32_bf16 v[180:183], v[216:219], v[196:199], v[180:183]\n" \
    "v_mov_b32 v200, 0\n" \
    "v_mov_b32 v201, 0\n" \
    "v_mfma_f32_16x16x32_bf16 v[232:235], v[220:223], v[196:199], v[232:235]\n" \
    "v_mov_b32 v202, 0\n" \
    "v_mov_b32 v203, 0\n" \
    "v_rcp_f32 v240, v212\n" \
    "v_mov_b32 v160, 0\n" \
    "v_mov_b32 v161, 0\n" \
    "v_mov_b32 v162, 0\n" \
    "v_mov_b32 v163, 0\n" \
    "v_pk_mul_f32 v[180:181], v[180:181], v[240:241] op_sel_hi:[1,0]\n" \
    "v_pk_mul_f32 v[182:183], v[182:183], v[240:241] op_sel_hi:[1,0]\n" \
    "v_pk_mul_f32 v[232:233], v[232:233], v[240:241] op_sel_hi:[1,0]\n" \
    "v_pk_mul_f32 v[234:235], v[234:235], v[240:241] op_sel_hi:[1,0]\n" \
    "v_cvt_pk_bf16_f32 v196, v180, v181\n" \
    "v_cvt_pk_bf16_f32 v197, v182, v183\n" \
    "v_cvt_pk_bf16_f32 v198, v232, v233\n" \
    "v_cvt_pk_bf16_f32 v199, v234, v235\n" \
    "ds_write_b128 v249, v[196:199]\n" \
    "v_lshrrev_b32 v240, 1, v253\n" \
    "buffer_store_dwordx2 v[176:177], v240, s[60:63], s50 offen sc1\n" \
    "s_waitcnt lgkmcnt(0)\n" \
    "s_barrier\n" \
    "ds_read_b128 v[216:219], v253 offset:28672\n" \
    "ds_read_b128 v[220:223], v253 offset:29696\n" \
    "s_waitcnt lgkmcnt(1)\n" \
    "v_smfmac_f32_16x16x64_bf16 v[184:187], v[216:219], v[128:135], v254\n" \
    "s_waitcnt lgkmcnt(0)\n" \
    "s_nop 0\n" \
    "v_smfmac_f32_16x16x64_bf16 v[184:187], v[220:223], v[136:143], v254\n" \
    "s_nop 1\n" \
    "s_nop 0\n" \
    "v_smfmac_f32_16x16x64_bf16 v[200:203], v[216:219], v[144:151], v254\n" \
    "s_nop 1\n" \
    "s_nop 0\n" \
    "v_smfmac_f32_16x16x64_bf16 v[200:203], v[220:223], v[152:159], v254\n" \
    "v_add_f32 v184, v184, v185\n" \
    "v_smfmac_f32_16x16x64_bf16 v[160:163], v[216:219], v[188:195], v254\n" \
    "s_nop 1\n" \
    "v_add_f32 v186, v186, v187\n" \
    "v_smfmac_f32_16x16x64_bf16 v[160:163], v[220:223], v[204:211], v254\n" \
    "s_nop 0\n" \
    "v_cvt_pk_bf16_f32 v184, v184, v186\n" \
    "v_smfmac_f32_16x16x64_bf16 v[172:175], v[216:219], v[164:171], v254\n" \
    "v_add_f32 v200, v200, v201\n" \
    "v_smfmac_f32_16x16x64_bf16 v[172:175], v[220:223], v[224:231], v254\n" \
    "v_add_f32 v202, v202, v203\n" \
    "v_min_u32 v212, -2, v184\n" \
    "v_cvt_pk_bf16_f32 v200, v200, v202\n" \
    "v_add_f32 v160, v160, v161\n" \
    "v_add_f32 v162, v162, v163\n" \
    "v_min_u32 v213, -2, v200\n" \
    "v_cvt_pk_bf16_f32 v160, v160, v162\n" \
    "v_min_u32 v214, -2, v160\n" \
    "v_add_f32 v172, v172, v173\n" \
    "v_add_f32 v174, v174, v175\n" \
    "v_cvt_pk_bf16_f32 v172, v172, v174\n" \
    "v_min_u32 v215, -2, v172\n" \
    "buffer_store_dwordx4 v[212:215], v252, s[36:39], s45 offen sc1\n" \
    "s_memtime s[0:1]\n" \
    "s_mov_b32 s2, s20\n" \
    "s_add_u32 s46, s46, 131072\n" \
    "s_and_b32 s46, s46, 0x7ffff\n" \
    "s_add_u32 s44, s44, 1\n" \
    "s_xor_b32 s45, s45, 0x84000\n" \
    "s_xor_b32 s48, s48, 32768\n" \
    "s_add_u32 s50, s50, 8192\n" \
    "v_mov_b64 v[160:161], 0\n" \
    "v_mov_b64 v[162:163], 0\n" \
    "v_mov_b64 v[164:165], 0\n" \
    "v_mov_b64 v[166:167], 0\n" \
    "v_mov_b64 v[168:169], 0\n" \
    "v_mov_b64 v[170:171], 0\n" \
    "v_mov_b64 v[172:173], 0\n" \
    "v_mov_b64 v[174:175], 0\n" \
    "v_mov_b64 v[176:177], 0\n" \
    "v_mov_b64 v[178:179], 0\n" \
    "v_mov_b64 v[180:181], 0\n" \
    "v_mov_b64 v[182:183], 0\n" \
    "v_xor_b32 v136, 2048, v255\n" \
    "v_xor_b32 v140, 3072, v255\n" \
    "v_xor_b32 v144, 4096, v255\n" \
    "v_xor_b32 v148, 5120, v255\n" \
    "v_xor_b32 v152, 6144, v255\n" \
    "v_xor_b32 v156, 7168, v255\n" \
    "v_add_u32 v216, s48, v253\n" \
    "v_add_u32 v220, s48, v255\n" \
    "s_xor_b32 s52, s48, 32768\n" \
    "s_add_u32 s49, s49, 16384\n" \
    "s_mov_b32 s24, s22\n" \
    "s_mov_b32 s25, s23\n" \
    "s_memtime s[20:21]\n" \
    "s_memrealtime s[4:5]\n" \
    "s_nop 2\n" \
    "s_memrealtime s[6:7]\n" \
    "s_nop 2\n" \
    "s_memrealtime s[8:9]\n" \
    "s_nop 2\n" \
    "s_memrealtime s[10:11]\n" \
    "s_nop 2\n" \
    "s_memrealtime s[12:13]\n" \
    "s_nop 2\n" \
    "s_memrealtime s[14:15]\n" \
    "s_nop 2\n" \
    "s_memrealtime s[16:17]\n" \
    "s_nop 2\n" \
    "s_memrealtime s[18:19]\n" \
    "s_mov_b32 s29, 0\n" \
    "s_waitcnt lgkmcnt(0)\n" \
    "s_cmp_eq_u32 s6, s4\n" \
    "s_addc_u32 s29, s29, 0\n" \
    "s_cmp_eq_u32 s8, s4\n" \
    "s_addc_u32 s29, s29, 0\n" \
    "s_cmp_eq_u32 s10, s4\n" \
    "s_addc_u32 s29, s29, 0\n" \
    "s_cmp_eq_u32 s12, s4\n" \
    "s_addc_u32 s29, s29, 0\n" \
    "s_cmp_eq_u32 s14, s4\n" \
    "s_addc_u32 s29, s29, 0\n" \
    "s_cmp_eq_u32 s16, s4\n" \
    "s_addc_u32 s29, s29, 0\n" \
    "s_cmp_eq_u32 s18, s4\n" \
    "s_addc_u32 s29, s29, 0\n" \
    "s_lshl_b32 s29, s29, 4\n" \
    "s_add_u32 s23, s20, s29\n" \
    "s_lshl_b32 s22, s4, 2\n" \
    "s_lshl_b32 s28, s22, 6\n" \
    "s_sub_u32 s29, s23, s25\n" \
    "s_sub_u32 s30, s22, s24\n" \
    "v_cvt_f32_u32 v234, s29\n" \
    "v_cvt_f32_u32 v235, s30\n" \
    "v_rcp_f32 v235, v235\n" \
    "v_or_b32 v243, v243, v243\n" \
    "v_mul_f32 v234, v234, v235\n" \
    "v_add_f32 v234, s26, v234\n" \
    "v_mul_f32 v234, 0.5, v234\n" \
    "v_rcp_f32 v235, v234\n" \
    "s_nop 0\n" \
    "v_readfirstlane_b32 s26, v234\n" \
    "v_readfirstlane_b32 s27, v235\n" \
    "s_nop 1\n" \
    "s_sub_u32 s0, s0, s2\n" \
    "s_min_u32 s0, s0, 1810\n" \
    "s_sub_i32 s0, s0, s33\n" \
    "s_ashr_i32 s0, s0, 2\n" \
    "s_add_i32 s33, s33, s0\n" \
    "s_waitcnt vmcnt(2)\n" \
    "SP%=:\n" \
    "v_sub_u32 v232, v242, v244\n" \
    "v_and_b32 v232, 63, v232\n" \
    "v_cmp_ne_u32 vcc, 0, v232\n" \
    "s_cbranch_vccz SG%=\n" \
    "v_cmp_lt_u32 vcc, 1, v232\n" \
    "s_cbranch_vccz VE%=\n" \
    "buffer_load_dword v242, v245, s[36:39], s59 offen sc1\n" \
    "s_waitcnt vmcnt(0)\n" \
    "s_add_u32 s57, s57, 1\n" \
    "s_cmp_lt_u32 s57, 1048576\n" \
    "s_cbranch_scc1 SP%=\n" \
    "SG%=:\n" \
    "v_subrev_u32 v242, s28, v242\n" \
    "v_or_b32 v243, v243, v243\n" \
    "v_or_b32 v243, v243, v243\n" \
    "v_max_i32_dpp v242, v242, v242 row_ror:8 row_mask:0xf bank_mask:0xf\n" \
    "v_or_b32 v243, v243, v243\n" \
    "v_or_b32 v243, v243, v243\n" \
    "v_max_i32_dpp v242, v242, v242 row_ror:4 row_mask:0xf bank_mask:0xf\n" \
    "v_or_b32 v243, v243, v243\n" \
    "v_or_b32 v243, v243, v243\n" \
    "v_max_i32_dpp v242, v242, v242 row_ror:2 row_mask:0xf bank_mask:0xf\n" \
    "v_or_b32 v243, v243, v243\n" \
    "v_or_b32 v243, v243, v243\n" \
    "v_max_i32_dpp v242, v242, v242 row_ror:1 row_mask:0xf bank_mask:0xf\n" \
    "v_mov_b32 v232, v242\n" \
    "v_or_b32 v243, v243, v243\n" \
    "v_or_b32 v243, v243, v243\n" \
    "v_permlane16_swap_b32 v242, v232\n" \
    "v_max_i32 v242, v242, v232\n" \
    "v_ashrrev_i32 v242, 6, v242\n" \
    "v_add_u32 v242, s31, v242\n" \
    "v_cvt_f32_i32 v242, v242\n" \
    "v_mul_f32 v242, s26, v242\n" \
    "v_cvt_i32_f32 v242, v242\n" \
    "s_memtime s[20:21]\n" \
    "v_readfirstlane_b32 s30, v242\n" \
    "s_waitcnt lgkmcnt(0)\n" \
    "s_add_u32 s30, s30, s23\n" \
    "s_sub_u32 s29, s30, s20\n" \
    "s_min_i32 s29, s29, 4096\n" \
    "s_add_u32 s30, s20, s29\n" \
    "VL%=:\n" \
    "s_memtime s[20:21]\n" \
    "s_waitcnt lgkmcnt(0)\n" \
    "s_sub_u32 s29, s30, s20\n" \
    "s_cmp_gt_i32 s29, 192\n" \
    "s_cbranch_scc1 VL%=\n" \
    "s_max_i32 s29, s29, 0\n" \
    "s_lshr_b32 s29, s29, 3\n" \
    "s_lshl_b32 s64, s29, 2\n" \
    "s_getpc_b64 s[66:67]\n" \
    "VD%=:\n" \
    "s_sub_u32 s34, VE%=-VD%=, s64\n" \
    "s_add_u32 s66, s66, s34\n" \
    "s_addc_u32 s67, s67, 0\n" \
    "s_setpc_b64 s[66:67]\n" \
    "s_nop 1\n" \
    "s_nop 1\n" \
    "s_nop 1\n" \
    "s_nop 1\n" \
    "s_nop 1\n" \
    "s_nop 1\n" \
    "s_nop 1\n" \
    "s_nop 1\n" \
    "s_nop 1\n" \
    "s_nop 1\n" \
    "s_nop 1\n" \
    "s_nop 1\n" \
    "s_nop 1\n" \
    "s_nop 1\n" \
    "s_nop 1\n" \
    "s_nop 1\n" \
    "s_nop 1\n" \
    "s_nop 1\n" \
    "s_nop 1\n" \
    "s_nop 1\n" \
    "s_nop 1\n" \
    "s_nop 1\n" \
    "s_nop 1\n" \
    "s_nop 1\n" \
    "VE%=:\n" \
    "buffer_load_dwordx4 v[128:131], v248, s[36:39], s46 offen sc1\n" \
    "buffer_load_dwordx4 v[132:135], v248, s[36:39], s46 offen offset:1024 sc1\n" \
    "s_mov_b32 exec_hi, 0\n" \
    "v_add_u32 v244, 1, v244\n" \
    "s_mov_b32 exec_hi, -1\n" \
    "s_mov_b32 exec_lo, 0\n" \
    "v_add_u32 v245, 4, v245\n" \
    "s_mov_b32 exec_lo, -1\n" \
    "s_cmp_gt_u32 s44, 2\n" \
    "s_cselect_b32 s65, 4, 0\n" \
    "s_nop 15\n" \
    "s_nop 8\n" \
    "s_add_u32 m0, s52, 12288\n" \
    "s_add_u32 s55, s49, 12288\n" \
    "buffer_load_dwordx4 v253, s[36:39], s55 offen sc1 lds\n" \
    "s_add_u32 m0, s52, 13312\n" \
    "s_add_u32 s55, s49, 13312\n" \
    "buffer_load_dwordx4 v253, s[36:39], s55 offen sc1 lds\n" \
    "s_add_u32 m0, s52, 14336\n" \
    "s_add_u32 s55, s49, 14336\n" \
    "buffer_load_dwordx4 v253, s[36:39], s55 offen sc1 lds\n" \
    "s_add_u32 m0, s52, 15360\n" \
    "s_add_u32 s55, s49, 15360\n" \
    "buffer_load_dwordx4 v253, s[36:39], s55 offen sc1 lds\n" \
    "s_branch HEAD0%=\n" \
    "RETRY2%=:\n" \
    "s_add_u32 s31, s31, s65\n" \
    "s_min_u32 s31, s31, 239\n" \
    "s_mov_b32 s65, 0\n" \
    "s_add_u32 s56, s56, 1\n" \
    "s_cmp_gt_u32 s56, 262144\n" \
    "s_cbranch_scc1 OK2%=\n" \
    "s_waitcnt lgkmcnt(0)\n" \
    "s_add_u32 s54, s46, 65536\n" \
    "s_nop 15\n" \
    "s_nop 8\n" \
    "buffer_load_dwordx4 v[128:131], v248, s[36:39], s54 offen sc1\n" \
    "buffer_load_dwordx4 v[132:135], v248, s[36:39], s54 offen offset:1024 sc1\n" \
    "s_nop 7\n" \
    "v_mov_b64 v[160:161], 0\n" \
    "v_mov_b64 v[162:163], 0\n" \
    "v_mov_b64 v[164:165], 0\n" \
    "v_mov_b64 v[166:167], 0\n" \
    "v_mov_b64 v[168:169], 0\n" \
    "v_mov_b64 v[170:171], 0\n" \
    "v_mov_b64 v[172:173], 0\n" \
    "v_mov_b64 v[174:175], 0\n" \
    "v_mov_b64 v[176:177], 0\n" \
    "v_mov_b64 v[178:179], 0\n" \
    "v_mov_b64 v[180:181], 0\n" \
    "v_mov_b64 v[182:183], 0\n" \
    "s_waitcnt vmcnt(0)\n" \
    "s_branch HEAD0%=\n" \
    "TAIL%=:\n" \
    "v_smfmac_f32_16x16x64_bf16 v[176:179], v[152:155], v[48:55], v254\n" \
    "v_smfmac_f32_16x16x64_bf16 v[180:183], v[152:155], v[112:119], v254\n" \
    "v_smfmac_f32_16x16x64_bf16 v[176:179], v[156:159], v[56:63], v254\n" \
    "v_smfmac_f32_16x16x64_bf16 v[180:183], v[156:159], v[120:127], v254\n" \
    "s_nop 7\n" \
    "v_add_f32 v176, v176, v177\n" \
    "v_add_f32 v178, v178, v179\n" \
    "v_add_f32 v180, v180, v181\n" \
    "v_add_f32 v182, v182, v183\n" \
    "v_cvt_pk_bf16_f32 v176, v176, v178\n" \
    "v_cvt_pk_bf16_f32 v177, v180, v182\n" \
    "v_cvt_pk_bf16_f32 v178, v180, v182\n" \
    "v_mov_b32 v179, 0\n" \
    "s_waitcnt lgkmcnt(0)\n" \
    "s_cmp_eq_u32 s58, 0\n" \
    "s_cbranch_scc1 PK%=_1\n" \
    "v_lshrrev_b32 v240, 1, v253\n" \
    "buffer_store_dwordx2 v[176:177], v240, s[36:39], s50 offen sc1\n" \
    "s_branch PE%=_1\n" \
    "PK%=_1:\n" \
    "v_add_u32 v240, s51, v250\n" \
    "ds_read_b64_tr_b16 v[192:193], v240\n" \
    "ds_read_b64_tr_b16 v[194:195], v240 offset:512\n" \
    "s_waitcnt lgkmcnt(0)\n" \
    "buffer_store_dwordx4 v[192:195], v253, s[36:39], s50 offen sc1\n" \
    "PE%=_1:\n" \
    "s_nop 1\n" \
    "v_add_u32 v244, 1, v244\n" \
    "s_waitcnt vmcnt(0)\n" \
    "s_waitcnt lgkmcnt(0)\n" \
    "s_barrier\n" \
    "s_endpgm\n" \
    "RE_w3:\n"

#define ROLE_H \
    "RS_h:\n" \
    "s_getpc_b64 s[2:3]\n" \
    "v_lshlrev_b32 v0, 3, v253\n" \
    "s_mov_b32 s0, RE_h-RS_h-8\n" \
    "v_add_u32 v1, 0, v0\n" \
    "v_min_u32 v1, s0, v1\n" \
    "global_load_dword v2, v1, s[2:3]\n" \
    "v_add_u32 v1, 8192, v0\n" \
    "v_min_u32 v1, s0, v1\n" \
    "global_load_dword v3, v1, s[2:3]\n" \
    "v_mov_b32 v234, 0\n" \
    "s_mov_b32 s54, 2162688\n" \
    "buffer_load_dwordx2 v[232:233], v234, s[36:39], s54 offen sc1\n" \
    "s_mov_b32 s42, 0x400000\n" \
    "s_mov_b32 s43, 0x20000\n" \
    "s_mov_b32 s54, 2097152\n" \
    "s_mov_b32 s55, 524288\n" \
    "s_mov_b32 s40, s62\n" \
    "s_and_b32 s41, s63, 0xffff\n" \
    "buffer_load_dword v128, v219, s[40:43], 0 offen\n" \
    "buffer_load_dword v129, v219, s[40:43], s54 offen\n" \
    "s_mov_b32 s40, s64\n" \
    "s_and_b32 s41, s65, 0xffff\n" \
    "buffer_load_dword v130, v219, s[40:43], 0 offen\n" \
    "buffer_load_dword v131, v219, s[40:43], s54 offen\n" \
    "s_mov_b32 s40, s66\n" \
    "s_and_b32 s41, s67, 0xffff\n" \
    "buffer_load_dword v132, v220, s[40:43], 0 offen\n" \
    "buffer_load_dword v133, v220, s[40:43], s55 offen\n" \
    "s_mov_b32 s40, s60\n" \
    "s_and_b32 s41, s61, 0xffff\n" \
    "buffer_load_dword v134, v219, s[40:43], 0 offen\n" \
    "buffer_load_dword v135, v219, s[40:43], s54 offen\n" \
    "s_mov_b32 s42, 4194304\n" \
    "s_mov_b32 s43, 0x20000\n" \
    "s_mov_b32 s40, s62\n" \
    "s_and_b32 s41, s63, 0xffff\n" \
    "s_add_u32 m0, s51, 32768\n" \
    "s_mov_b32 s54, 0\n" \
    "buffer_load_dwordx4 v216, s[40:43], s54 offen lds\n" \
    "s_add_u32 m0, s51, 36864\n" \
    "s_mov_b32 s54, 131072\n" \
    "buffer_load_dwordx4 v216, s[40:43], s54 offen lds\n" \
    "s_add_u32 m0, s51, 40960\n" \
    "s_mov_b32 s54, 262144\n" \
    "buffer_load_dwordx4 v216, s[40:43], s54 offen lds\n" \
    "s_add_u32 m0, s51, 45056\n" \
    "s_mov_b32 s54, 393216\n" \
    "buffer_load_dwordx4 v216, s[40:43], s54 offen lds\n" \
    "s_add_u32 m0, s51, 49152\n" \
    "s_mov_b32 s54, 524288\n" \
    "buffer_load_dwordx4 v216, s[40:43], s54 offen lds\n" \
    "s_add_u32 m0, s51, 53248\n" \
    "s_mov_b32 s54, 655360\n" \
    "buffer_load_dwordx4 v216, s[40:43], s54 offen lds\n" \
    "s_add_u32 m0, s51, 57344\n" \
    "s_mov_b32 s54, 786432\n" \
    "buffer_load_dwordx4 v216, s[40:43], s54 offen lds\n" \
    "s_add_u32 m0, s51, 61440\n" \
    "s_mov_b32 s54, 917504\n" \
    "buffer_load_dwordx4 v216, s[40:43], s54 offen lds\n" \
    "s_add_u32 m0, s51, 65536\n" \
    "s_mov_b32 s54, 1048576\n" \
    "buffer_load_dwordx4 v216, s[40:43], s54 offen lds\n" \
    "s_add_u32 m0, s51, 69632\n" \
    "s_mov_b32 s54, 1179648\n" \
    "buffer_load_dwordx4 v216, s[40:43], s54 offen lds\n" \
    "s_add_u32 m0, s51, 73728\n" \
    "s_mov_b32 s54, 1310720\n" \
    "buffer_load_dwordx4 v216, s[40:43], s54 offen lds\n" \
    "s_add_u32 m0, s51, 77824\n" \
    "s_mov_b32 s54, 1441792\n" \
    "buffer_load_dwordx4 v216, s[40:43], s54 offen lds\n" \
    "s_add_u32 m0, s51, 81920\n" \
    "s_mov_b32 s54, 1572864\n" \
    "buffer_load_dwordx4 v216, s[40:43], s54 offen lds\n" \
    "s_add_u32 m0, s51, 86016\n" \
    "s_mov_b32 s54, 1703936\n" \
    "buffer_load_dwordx4 v216, s[40:43], s54 offen lds\n" \
    "s_add_u32 m0, s51, 90112\n" \
    "s_mov_b32 s54, 1835008\n" \
    "buffer_load_dwordx4 v216, s[40:43], s54 offen lds\n" \
    "s_add_u32 m0, s51, 94208\n" \
    "s_mov_b32 s54, 1966080\n" \
    "buffer_load_dwordx4 v216, s[40:43], s54 offen lds\n" \
    "s_add_u32 m0, s51, 98304\n" \
    "s_mov_b32 s54, 2097152\n" \
    "buffer_load_dwordx4 v216, s[40:43], s54 offen lds\n" \
    "s_add_u32 m0, s51, 102400\n" \
    "s_mov_b32 s54, 2228224\n" \
    "buffer_load_dwordx4 v216, s[40:43], s54 offen lds\n" \
    "s_add_u32 m0, s51, 106496\n" \
    "s_mov_b32 s54, 2359296\n" \
    "buffer_load_dwordx4 v216, s[40:43], s54 offen lds\n" \
    "s_add_u32 m0, s51, 110592\n" \
    "s_mov_b32 s54, 2490368\n" \
    "buffer_load_dwordx4 v216, s[40:43], s54 offen lds\n" \
    "s_add_u32 m0, s51, 114688\n" \
    "s_mov_b32 s54, 2621440\n" \
    "buffer_load_dwordx4 v216, s[40:43], s54 offen lds\n" \
    "s_add_u32 m0, s51, 118784\n" \
    "s_mov_b32 s54, 2752512\n" \
    "buffer_load_dwordx4 v216, s[40:43], s54 offen lds\n" \
    "s_add_u32 m0, s51, 122880\n" \
    "s_mov_b32 s54, 2883584\n" \
    "buffer_load_dwordx4 v216, s[40:43], s54 offen lds\n" \
    "s_add_u32 m0, s51, 126976\n" \
    "s_mov_b32 s54, 3014656\n" \
    "buffer_load_dwordx4 v216, s[40:43], s54 offen lds\n" \
    "s_add_u32 m0, s51, 131072\n" \
    "s_mov_b32 s54, 3145728\n" \
    "buffer_load_dwordx4 v216, s[40:43], s54 offen lds\n" \
    "s_add_u32 m0, s51, 135168\n" \
    "s_mov_b32 s54, 3276800\n" \
    "buffer_load_dwordx4 v216, s[40:43], s54 offen lds\n" \
    "s_add_u32 m0, s51, 139264\n" \
    "s_mov_b32 s54, 3407872\n" \
    "buffer_load_dwordx4 v216, s[40:43], s54 offen lds\n" \
    "s_add_u32 m0, s51, 143360\n" \
    "s_mov_b32 s54, 3538944\n" \
    "buffer_load_dwordx4 v216, s[40:43], s54 offen lds\n" \
    "s_add_u32 m0, s51, 147456\n" \
    "s_mov_b32 s54, 3670016\n" \
    "buffer_load_dwordx4 v216, s[40:43], s54 offen lds\n" \
    "s_add_u32 m0, s51, 151552\n" \
    "s_mov_b32 s54, 3801088\n" \
    "buffer_load_dwordx4 v216, s[40:43], s54 offen lds\n" \
    "s_add_u32 m0, s51, 155648\n" \
    "s_mov_b32 s54, 3932160\n" \
    "buffer_load_dwordx4 v216, s[40:43], s54 offen lds\n" \
    "s_add_u32 m0, s51, 159744\n" \
    "s_mov_b32 s54, 4063232\n" \
    "buffer_load_dwordx4 v216, s[40:43], s54 offen lds\n" \
    "s_waitcnt vmcnt(16)\n" \
    "s_barrier\n" \
    "v_add_u32 v223, 32768, v217\n" \
    "ds_read_b64_tr_b16 a[128:129], v223\n" \
    "ds_read_b64_tr_b16 a[130:131], v223 offset:1024\n" \
    "ds_read_b64_tr_b16 a[132:133], v223 offset:8192\n" \
    "ds_read_b64_tr_b16 a[134:135], v223 offset:9216\n" \
    "ds_read_b64_tr_b16 a[136:137], v223 offset:16384\n" \
    "ds_read_b64_tr_b16 a[138:139], v223 offset:17408\n" \
    "ds_read_b64_tr_b16 a[140:141], v223 offset:24576\n" \
    "ds_read_b64_tr_b16 a[142:143], v223 offset:25600\n" \
    "s_waitcnt lgkmcnt(0)\n" \
    "ds_read_b64_tr_b16 a[144:145], v223 offset:32768\n" \
    "ds_read_b64_tr_b16 a[146:147], v223 offset:33792\n" \
    "ds_read_b64_tr_b16 a[148:149], v223 offset:40960\n" \
    "ds_read_b64_tr_b16 a[150:151], v223 offset:41984\n" \
    "ds_read_b64_tr_b16 a[152:153], v223 offset:49152\n" \
    "ds_read_b64_tr_b16 a[154:155], v223 offset:50176\n" \
    "ds_read_b64_tr_b16 a[156:157], v223 offset:57344\n" \
    "ds_read_b64_tr_b16 a[158:159], v223 offset:58368\n" \
    "s_waitcnt lgkmcnt(0)\n" \
    "v_add_u32 v223, 32768, v218\n" \
    "ds_read_b64_tr_b16 a[192:193], v223\n" \
    "ds_read_b64_tr_b16 a[194:195], v223 offset:1024\n" \
    "ds_read_b64_tr_b16 a[196:197], v223 offset:8192\n" \
    "ds_read_b64_tr_b16 a[198:199], v223 offset:9216\n" \
    "ds_read_b64_tr_b16 a[200:201], v223 offset:16384\n" \
    "ds_read_b64_tr_b16 a[202:203], v223 offset:17408\n" \
    "ds_read_b64_tr_b16 a[204:205], v223 offset:24576\n" \
    "ds_read_b64_tr_b16 a[206:207], v223 offset:25600\n" \
    "s_waitcnt lgkmcnt(0)\n" \
    "ds_read_b64_tr_b16 a[208:209], v223 offset:32768\n" \
    "ds_read_b64_tr_b16 a[210:211], v223 offset:33792\n" \
    "ds_read_b64_tr_b16 a[212:213], v223 offset:40960\n" \
    "ds_read_b64_tr_b16 a[214:215], v223 offset:41984\n" \
    "ds_read_b64_tr_b16 a[216:217], v223 offset:49152\n" \
    "ds_read_b64_tr_b16 a[218:219], v223 offset:50176\n" \
    "ds_read_b64_tr_b16 a[220:221], v223 offset:57344\n" \
    "ds_read_b64_tr_b16 a[222:223], v223 offset:58368\n" \
    "s_waitcnt lgkmcnt(0)\n" \
    "s_barrier\n" \
    "v_lshlrev_b32 v230, 3, v253\n" \
    "s_mov_b32 s54, 2163712\n" \
    "buffer_load_dwordx2 v[228:229], v230, s[36:39], s54 offen sc1\n" \
    "s_mov_b32 s40, s64\n" \
    "s_and_b32 s41, s65, 0xffff\n" \
    "s_add_u32 m0, s51, 32768\n" \
    "s_mov_b32 s54, 0\n" \
    "buffer_load_dwordx4 v216, s[40:43], s54 offen lds\n" \
    "s_add_u32 m0, s51, 36864\n" \
    "s_mov_b32 s54, 131072\n" \
    "buffer_load_dwordx4 v216, s[40:43], s54 offen lds\n" \
    "s_add_u32 m0, s51, 40960\n" \
    "s_mov_b32 s54, 262144\n" \
    "buffer_load_dwordx4 v216, s[40:43], s54 offen lds\n" \
    "s_add_u32 m0, s51, 45056\n" \
    "s_mov_b32 s54, 393216\n" \
    "buffer_load_dwordx4 v216, s[40:43], s54 offen lds\n" \
    "s_add_u32 m0, s51, 49152\n" \
    "s_mov_b32 s54, 524288\n" \
    "buffer_load_dwordx4 v216, s[40:43], s54 offen lds\n" \
    "s_add_u32 m0, s51, 53248\n" \
    "s_mov_b32 s54, 655360\n" \
    "buffer_load_dwordx4 v216, s[40:43], s54 offen lds\n" \
    "s_add_u32 m0, s51, 57344\n" \
    "s_mov_b32 s54, 786432\n" \
    "buffer_load_dwordx4 v216, s[40:43], s54 offen lds\n" \
    "s_add_u32 m0, s51, 61440\n" \
    "s_mov_b32 s54, 917504\n" \
    "buffer_load_dwordx4 v216, s[40:43], s54 offen lds\n" \
    "s_add_u32 m0, s51, 65536\n" \
    "s_mov_b32 s54, 1048576\n" \
    "buffer_load_dwordx4 v216, s[40:43], s54 offen lds\n" \
    "s_add_u32 m0, s51, 69632\n" \
    "s_mov_b32 s54, 1179648\n" \
    "buffer_load_dwordx4 v216, s[40:43], s54 offen lds\n" \
    "s_add_u32 m0, s51, 73728\n" \
    "s_mov_b32 s54, 1310720\n" \
    "buffer_load_dwordx4 v216, s[40:43], s54 offen lds\n" \
    "s_add_u32 m0, s51, 77824\n" \
    "s_mov_b32 s54, 1441792\n" \
    "buffer_load_dwordx4 v216, s[40:43], s54 offen lds\n" \
    "s_add_u32 m0, s51, 81920\n" \
    "s_mov_b32 s54, 1572864\n" \
    "buffer_load_dwordx4 v216, s[40:43], s54 offen lds\n" \
    "s_add_u32 m0, s51, 86016\n" \
    "s_mov_b32 s54, 1703936\n" \
    "buffer_load_dwordx4 v216, s[40:43], s54 offen lds\n" \
    "s_add_u32 m0, s51, 90112\n" \
    "s_mov_b32 s54, 1835008\n" \
    "buffer_load_dwordx4 v216, s[40:43], s54 offen lds\n" \
    "s_add_u32 m0, s51, 94208\n" \
    "s_mov_b32 s54, 1966080\n" \
    "buffer_load_dwordx4 v216, s[40:43], s54 offen lds\n" \
    "s_waitcnt vmcnt(16)\n" \
    "s_barrier\n" \
    "v_add_u32 v223, 98304, v217\n" \
    "ds_read_b64_tr_b16 a[160:161], v223\n" \
    "ds_read_b64_tr_b16 a[162:163], v223 offset:1024\n" \
    "ds_read_b64_tr_b16 a[164:165], v223 offset:8192\n" \
    "ds_read_b64_tr_b16 a[166:167], v223 offset:9216\n" \
    "ds_read_b64_tr_b16 a[168:169], v223 offset:16384\n" \
    "ds_read_b64_tr_b16 a[170:171], v223 offset:17408\n" \
    "ds_read_b64_tr_b16 a[172:173], v223 offset:24576\n" \
    "ds_read_b64_tr_b16 a[174:175], v223 offset:25600\n" \
    "s_waitcnt lgkmcnt(0)\n" \
    "ds_read_b64_tr_b16 a[176:177], v223 offset:32768\n" \
    "ds_read_b64_tr_b16 a[178:179], v223 offset:33792\n" \
    "ds_read_b64_tr_b16 a[180:181], v223 offset:40960\n" \
    "ds_read_b64_tr_b16 a[182:183], v223 offset:41984\n" \
    "ds_read_b64_tr_b16 a[184:185], v223 offset:49152\n" \
    "ds_read_b64_tr_b16 a[186:187], v223 offset:50176\n" \
    "ds_read_b64_tr_b16 a[188:189], v223 offset:57344\n" \
    "ds_read_b64_tr_b16 a[190:191], v223 offset:58368\n" \
    "s_waitcnt lgkmcnt(0)\n" \
    "v_add_u32 v223, 98304, v218\n" \
    "ds_read_b64_tr_b16 a[224:225], v223\n" \
    "ds_read_b64_tr_b16 a[226:227], v223 offset:1024\n" \
    "ds_read_b64_tr_b16 a[228:229], v223 offset:8192\n" \
    "ds_read_b64_tr_b16 a[230:231], v223 offset:9216\n" \
    "ds_read_b64_tr_b16 a[232:233], v223 offset:16384\n" \
    "ds_read_b64_tr_b16 a[234:235], v223 offset:17408\n" \
    "ds_read_b64_tr_b16 a[236:237], v223 offset:24576\n" \
    "ds_read_b64_tr_b16 a[238:239], v223 offset:25600\n" \
    "s_waitcnt lgkmcnt(0)\n" \
    "ds_read_b64_tr_b16 a[240:241], v223 offset:32768\n" \
    "ds_read_b64_tr_b16 a[242:243], v223 offset:33792\n" \
    "ds_read_b64_tr_b16 a[244:245], v223 offset:40960\n" \
    "ds_read_b64_tr_b16 a[246:247], v223 offset:41984\n" \
    "ds_read_b64_tr_b16 a[248:249], v223 offset:49152\n" \
    "ds_read_b64_tr_b16 a[250:251], v223 offset:50176\n" \
    "ds_read_b64_tr_b16 a[252:253], v223 offset:57344\n" \
    "ds_read_b64_tr_b16 a[254:255], v223 offset:58368\n" \
    "s_waitcnt lgkmcnt(0)\n" \
    "s_barrier\n" \
    "v_add_u32 v232, 1, v232\n" \
    "v_add_u32 v233, 8, v233\n" \
    "v_or_b32 v233, 7, v233\n" \
    "s_mov_b32 s54, 2163712\n" \
    "WIa1024%=:\n" \
    "v_or_b32 v229, 7, v229\n" \
    "v_cmp_ne_u64 vcc, v[232:233], v[228:229]\n" \
    "s_cbranch_vccz WIb1024%=\n" \
    "s_sleep 2\n" \
    "buffer_load_dwordx2 v[228:229], v230, s[36:39], s54 offen sc1\n" \
    "s_waitcnt vmcnt(0)\n" \
    "s_add_u32 s56, s56, 1\n" \
    "s_cmp_lt_u32 s56, 262144\n" \
    "s_cbranch_scc1 WIa1024%=\n" \
    "WIb1024%=:\n" \
    "s_add_u32 m0, s46, 0\n" \
    "s_add_u32 s54, s45, 0\n" \
    "buffer_load_dwordx4 v253, s[36:39], s54 offen sc1 lds\n" \
    "s_add_u32 m0, s46, 1024\n" \
    "s_add_u32 s54, s45, 1024\n" \
    "buffer_load_dwordx4 v253, s[36:39], s54 offen sc1 lds\n" \
    "s_add_u32 m0, s46, 2048\n" \
    "s_add_u32 s54, s45, 2048\n" \
    "buffer_load_dwordx4 v253, s[36:39], s54 offen sc1 lds\n" \
    "s_add_u32 m0, s46, 3072\n" \
    "s_add_u32 s54, s45, 3072\n" \
    "buffer_load_dwordx4 v253, s[36:39], s54 offen sc1 lds\n" \
    "s_add_u32 m0, s46, 4096\n" \
    "s_add_u32 s54, s45, 4096\n" \
    "buffer_load_dwordx4 v253, s[36:39], s54 offen sc1 lds\n" \
    "s_add_u32 m0, s46, 5120\n" \
    "s_add_u32 s54, s45, 5120\n" \
    "buffer_load_dwordx4 v253, s[36:39], s54 offen sc1 lds\n" \
    "s_add_u32 m0, s46, 6144\n" \
    "s_add_u32 s54, s45, 6144\n" \
    "buffer_load_dwordx4 v253, s[36:39], s54 offen sc1 lds\n" \
    "s_add_u32 m0, s46, 7168\n" \
    "s_add_u32 s54, s45, 7168\n" \
    "buffer_load_dwordx4 v253, s[36:39], s54 offen sc1 lds\n" \
    "s_add_u32 m0, s51, 98304\n" \
    "s_mov_b32 s54, 2097152\n" \
    "buffer_load_dwordx4 v216, s[40:43], s54 offen lds\n" \
    "s_add_u32 m0, s51, 102400\n" \
    "s_mov_b32 s54, 2228224\n" \
    "buffer_load_dwordx4 v216, s[40:43], s54 offen lds\n" \
    "s_add_u32 m0, s51, 106496\n" \
    "s_mov_b32 s54, 2359296\n" \
    "buffer_load_dwordx4 v216, s[40:43], s54 offen lds\n" \
    "s_add_u32 m0, s51, 110592\n" \
    "s_mov_b32 s54, 2490368\n" \
    "buffer_load_dwordx4 v216, s[40:43], s54 offen lds\n" \
    "s_add_u32 m0, s51, 114688\n" \
    "s_mov_b32 s54, 2621440\n" \
    "buffer_load_dwordx4 v216, s[40:43], s54 offen lds\n" \
    "s_add_u32 m0, s51, 118784\n" \
    "s_mov_b32 s54, 2752512\n" \
    "buffer_load_dwordx4 v216, s[40:43], s54 offen lds\n" \
    "s_add_u32 m0, s51, 122880\n" \
    "s_mov_b32 s54, 2883584\n" \
    "buffer_load_dwordx4 v216, s[40:43], s54 offen lds\n" \
    "s_add_u32 m0, s51, 126976\n" \
    "s_mov_b32 s54, 3014656\n" \
    "buffer_load_dwordx4 v216, s[40:43], s54 offen lds\n" \
    "s_add_u32 m0, s51, 131072\n" \
    "s_mov_b32 s54, 3145728\n" \
    "buffer_load_dwordx4 v216, s[40:43], s54 offen lds\n" \
    "s_add_u32 m0, s51, 135168\n" \
    "s_mov_b32 s54, 3276800\n" \
    "buffer_load_dwordx4 v216, s[40:43], s54 offen lds\n" \
    "s_add_u32 m0, s51, 139264\n" \
    "s_mov_b32 s54, 3407872\n" \
    "buffer_load_dwordx4 v216, s[40:43], s54 offen lds\n" \
    "s_add_u32 m0, s51, 143360\n" \
    "s_mov_b32 s54, 3538944\n" \
    "buffer_load_dwordx4 v216, s[40:43], s54 offen lds\n" \
    "s_add_u32 m0, s51, 147456\n" \
    "s_mov_b32 s54, 3670016\n" \
    "buffer_load_dwordx4 v216, s[40:43], s54 offen lds\n" \
    "s_add_u32 m0, s51, 151552\n" \
    "s_mov_b32 s54, 3801088\n" \
    "buffer_load_dwordx4 v216, s[40:43], s54 offen lds\n" \
    "s_add_u32 m0, s51, 155648\n" \
    "s_mov_b32 s54, 3932160\n" \
    "buffer_load_dwordx4 v216, s[40:43], s54 offen lds\n" \
    "s_add_u32 m0, s51, 159744\n" \
    "s_mov_b32 s54, 4063232\n" \
    "buffer_load_dwordx4 v216, s[40:43], s54 offen lds\n" \
    "s_waitcnt vmcnt(16)\n" \
    "s_barrier\n" \
    "v_add_u32 v223, 32768, v217\n" \
    "ds_read_b64_tr_b16 v[0:1], v223\n" \
    "ds_read_b64_tr_b16 v[2:3], v223 offset:1024\n" \
    "ds_read_b64_tr_b16 v[4:5], v223 offset:8192\n" \
    "ds_read_b64_tr_b16 v[6:7], v223 offset:9216\n" \
    "ds_read_b64_tr_b16 v[8:9], v223 offset:16384\n" \
    "ds_read_b64_tr_b16 v[10:11], v223 offset:17408\n" \
    "ds_read_b64_tr_b16 v[12:13], v223 offset:24576\n" \
    "ds_read_b64_tr_b16 v[14:15], v223 offset:25600\n" \
    "s_waitcnt lgkmcnt(0)\n" \
    "ds_read_b64_tr_b16 v[16:17], v223 offset:32768\n" \
    "ds_read_b64_tr_b16 v[18:19], v223 offset:33792\n" \
    "ds_read_b64_tr_b16 v[20:21], v223 offset:40960\n" \
    "ds_read_b64_tr_b16 v[22:23], v223 offset:41984\n" \
    "ds_read_b64_tr_b16 v[24:25], v223 offset:49152\n" \
    "ds_read_b64_tr_b16 v[26:27], v223 offset:50176\n" \
    "ds_read_b64_tr_b16 v[28:29], v223 offset:57344\n" \
    "ds_read_b64_tr_b16 v[30:31], v223 offset:58368\n" \
    "s_waitcnt lgkmcnt(0)\n" \
    "v_add_u32 v223, 32768, v218\n" \
    "ds_read_b64_tr_b16 v[64:65], v223\n" \
    "ds_read_b64_tr_b16 v[66:67], v223 offset:1024\n" \
    "ds_read_b64_tr_b16 v[68:69], v223 offset:8192\n" \
    "ds_read_b64_tr_b16 v[70:71], v223 offset:9216\n" \
    "ds_read_b64_tr_b16 v[72:73], v223 offset:16384\n" \
    "ds_read_b64_tr_b16 v[74:75], v223 offset:17408\n" \
    "ds_read_b64_tr_b16 v[76:77], v223 offset:24576\n" \
    "ds_read_b64_tr_b16 v[78:79], v223 offset:25600\n" \
    "s_waitcnt lgkmcnt(0)\n" \
    "ds_read_b64_tr_b16 v[80:81], v223 offset:32768\n" \
    "ds_read_b64_tr_b16 v[82:83], v223 offset:33792\n" \
    "ds_read_b64_tr_b16 v[84:85], v223 offset:40960\n" \
    "ds_read_b64_tr_b16 v[86:87], v223 offset:41984\n" \
    "ds_read_b64_tr_b16 v[88:89], v223 offset:49152\n" \
    "ds_read_b64_tr_b16 v[90:91], v223 offset:50176\n" \
    "ds_read_b64_tr_b16 v[92:93], v223 offset:57344\n" \
    "ds_read_b64_tr_b16 v[94:95], v223 offset:58368\n" \
    "s_waitcnt lgkmcnt(0)\n" \
    "s_barrier\n" \
    "s_waitcnt vmcnt(0)\n" \
    "s_barrier\n" \
    "v_add_u32 v223, 98304, v217\n" \
    "ds_read_b64_tr_b16 v[32:33], v223\n" \
    "ds_read_b64_tr_b16 v[34:35], v223 offset:1024\n" \
    "ds_read_b64_tr_b16 v[36:37], v223 offset:8192\n" \
    "ds_read_b64_tr_b16 v[38:39], v223 offset:9216\n" \
    "ds_read_b64_tr_b16 v[40:41], v223 offset:16384\n" \
    "ds_read_b64_tr_b16 v[42:43], v223 offset:17408\n" \
    "ds_read_b64_tr_b16 v[44:45], v223 offset:24576\n" \
    "ds_read_b64_tr_b16 v[46:47], v223 offset:25600\n" \
    "s_waitcnt lgkmcnt(0)\n" \
    "ds_read_b64_tr_b16 v[48:49], v223 offset:32768\n" \
    "ds_read_b64_tr_b16 v[50:51], v223 offset:33792\n" \
    "ds_read_b64_tr_b16 v[52:53], v223 offset:40960\n" \
    "ds_read_b64_tr_b16 v[54:55], v223 offset:41984\n" \
    "ds_read_b64_tr_b16 v[56:57], v223 offset:49152\n" \
    "ds_read_b64_tr_b16 v[58:59], v223 offset:50176\n" \
    "ds_read_b64_tr_b16 v[60:61], v223 offset:57344\n" \
    "ds_read_b64_tr_b16 v[62:63], v223 offset:58368\n" \
    "s_waitcnt lgkmcnt(0)\n" \
    "v_add_u32 v223, 98304, v218\n" \
    "ds_read_b64_tr_b16 v[96:97], v223\n" \
    "ds_read_b64_tr_b16 v[98:99], v223 offset:1024\n" \
    "ds_read_b64_tr_b16 v[100:101], v223 offset:8192\n" \
    "ds_read_b64_tr_b16 v[102:103], v223 offset:9216\n" \
    "ds_read_b64_tr_b16 v[104:105], v223 offset:16384\n" \
    "ds_read_b64_tr_b16 v[106:107], v223 offset:17408\n" \
    "ds_read_b64_tr_b16 v[108:109], v223 offset:24576\n" \
    "ds_read_b64_tr_b16 v[110:111], v223 offset:25600\n" \
    "s_waitcnt lgkmcnt(0)\n" \
    "ds_read_b64_tr_b16 v[112:113], v223 offset:32768\n" \
    "ds_read_b64_tr_b16 v[114:115], v223 offset:33792\n" \
    "ds_read_b64_tr_b16 v[116:117], v223 offset:40960\n" \
    "ds_read_b64_tr_b16 v[118:119], v223 offset:41984\n" \
    "ds_read_b64_tr_b16 v[120:121], v223 offset:49152\n" \
    "ds_read_b64_tr_b16 v[122:123], v223 offset:50176\n" \
    "ds_read_b64_tr_b16 v[124:125], v223 offset:57344\n" \
    "ds_read_b64_tr_b16 v[126:127], v223 offset:58368\n" \
    "s_waitcnt lgkmcnt(0)\n" \
    "s_barrier\n" \
    "v_mov_b32 v234, 1\n" \
    "v_lshrrev_b32 v235, 1, v253\n" \
    "v_mov_b32 v233, s52\n" \
    "s_add_u32 s34, s46, 32768\n" \
    "s_add_u32 s45, s45, 65536\n" \
    "s_add_u32 m0, s34, 0\n" \
    "s_add_u32 s54, s45, 0\n" \
    "buffer_load_dwordx4 v253, s[36:39], s54 offen sc1 lds\n" \
    "s_add_u32 m0, s34, 1024\n" \
    "s_add_u32 s54, s45, 1024\n" \
    "buffer_load_dwordx4 v253, s[36:39], s54 offen sc1 lds\n" \
    "s_add_u32 m0, s34, 2048\n" \
    "s_add_u32 s54, s45, 2048\n" \
    "buffer_load_dwordx4 v253, s[36:39], s54 offen sc1 lds\n" \
    "s_add_u32 m0, s34, 3072\n" \
    "s_add_u32 s54, s45, 3072\n" \
    "buffer_load_dwordx4 v253, s[36:39], s54 offen sc1 lds\n" \
    "s_add_u32 m0, s34, 4096\n" \
    "s_add_u32 s54, s45, 4096\n" \
    "buffer_load_dwordx4 v253, s[36:39], s54 offen sc1 lds\n" \
    "s_add_u32 m0, s34, 5120\n" \
    "s_add_u32 s54, s45, 5120\n" \
    "buffer_load_dwordx4 v253, s[36:39], s54 offen sc1 lds\n" \
    "s_add_u32 m0, s34, 6144\n" \
    "s_add_u32 s54, s45, 6144\n" \
    "buffer_load_dwordx4 v253, s[36:39], s54 offen sc1 lds\n" \
    "s_add_u32 m0, s34, 7168\n" \
    "s_add_u32 s54, s45, 7168\n" \
    "buffer_load_dwordx4 v253, s[36:39], s54 offen sc1 lds\n" \
    "s_add_u32 s34, s46, 65536\n" \
    "s_add_u32 s45, s45, 65536\n" \
    "s_add_u32 m0, s34, 0\n" \
    "s_add_u32 s54, s45, 0\n" \
    "buffer_load_dwordx4 v253, s[36:39], s54 offen sc1 lds\n" \
    "s_add_u32 m0, s34, 1024\n" \
    "s_add_u32 s54, s45, 1024\n" \
    "buffer_load_dwordx4 v253, s[36:39], s54 offen sc1 lds\n" \
    "s_add_u32 m0, s34, 2048\n" \
    "s_add_u32 s54, s45, 2048\n" \
    "buffer_load_dwordx4 v253, s[36:39], s54 offen sc1 lds\n" \
    "s_add_u32 m0, s34, 3072\n" \
    "s_add_u32 s54, s45, 3072\n" \
    "buffer_load_dwordx4 v253, s[36:39], s54 offen sc1 lds\n" \
    "s_add_u32 m0, s34, 4096\n" \
    "s_add_u32 s54, s45, 4096\n" \
    "buffer_load_dwordx4 v253, s[36:39], s54 offen sc1 lds\n" \
    "s_add_u32 m0, s34, 5120\n" \
    "s_add_u32 s54, s45, 5120\n" \
    "buffer_load_dwordx4 v253, s[36:39], s54 offen sc1 lds\n" \
    "s_add_u32 m0, s34, 6144\n" \
    "s_add_u32 s54, s45, 6144\n" \
    "buffer_load_dwordx4 v253, s[36:39], s54 offen sc1 lds\n" \
    "s_add_u32 m0, s34, 7168\n" \
    "s_add_u32 s54, s45, 7168\n" \
    "buffer_load_dwordx4 v253, s[36:39], s54 offen sc1 lds\n" \
    "s_mov_b32 s44, 0\n" \
    "HB%=:\n" \
    "v_add_u32 v192, s44, v253\n" \
    "ds_read_b128 v[176:179], v192\n" \
    "ds_read_b128 v[180:183], v192 offset:16384\n" \
    "ds_read_b128 v[184:187], v192 offset:1024\n" \
    "s_waitcnt lgkmcnt(2)\n" \
    "v_mfma_f32_16x16x32_bf16 v[136:139], a[128:131], v[176:179], 0\n" \
    "ds_read_b128 v[188:191], v192 offset:17408\n" \
    "v_mfma_f32_16x16x32_bf16 v[140:143], a[192:195], v[176:179], 0\n" \
    "v_mfma_f32_16x16x32_bf16 v[144:147], v[176:179], v[0:3], 0\n" \
    "v_mfma_f32_16x16x32_bf16 v[148:151], v[176:179], v[64:67], 0\n" \
    "s_waitcnt lgkmcnt(2)\n" \
    "v_mfma_f32_16x16x32_bf16 v[160:163], a[128:131], v[180:183], 0\n" \
    "ds_read_b128 v[176:179], v192 offset:2048\n" \
    "v_mfma_f32_16x16x32_bf16 v[164:167], a[192:195], v[180:183], 0\n" \
    "v_mfma_f32_16x16x32_bf16 v[168:171], v[180:183], v[0:3], 0\n" \
    "v_mfma_f32_16x16x32_bf16 v[172:175], v[180:183], v[64:67], 0\n" \
    "s_waitcnt lgkmcnt(2)\n" \
    "v_mfma_f32_16x16x32_bf16 v[136:139], a[132:135], v[184:187], v[136:139]\n" \
    "ds_read_b128 v[180:183], v192 offset:18432\n" \
    "v_mfma_f32_16x16x32_bf16 v[140:143], a[196:199], v[184:187], v[140:143]\n" \
    "v_mfma_f32_16x16x32_bf16 v[144:147], v[184:187], v[4:7], v[144:147]\n" \
    "v_mfma_f32_16x16x32_bf16 v[148:151], v[184:187], v[68:71], v[148:151]\n" \
    "s_waitcnt lgkmcnt(2)\n" \
    "v_mfma_f32_16x16x32_bf16 v[160:163], a[132:135], v[188:191], v[160:163]\n" \
    "ds_read_b128 v[184:187], v192 offset:3072\n" \
    "v_mfma_f32_16x16x32_bf16 v[164:167], a[196:199], v[188:191], v[164:167]\n" \
    "v_mfma_f32_16x16x32_bf16 v[168:171], v[188:191], v[4:7], v[168:171]\n" \
    "v_mfma_f32_16x16x32_bf16 v[172:175], v[188:191], v[68:71], v[172:175]\n" \
    "s_waitcnt lgkmcnt(2)\n" \
    "v_mfma_f32_16x16x32_bf16 v[136:139], a[136:139], v[176:179], v[136:139]\n" \
    "ds_read_b128 v[188:191], v192 offset:19456\n" \
    "v_mfma_f32_16x16x32_bf16 v[140:143], a[200:203], v[176:179], v[140:143]\n" \
    "v_mfma_f32_16x16x32_bf16 v[144:147], v[176:179], v[8:11], v[144:147]\n" \
    "v_mfma_f32_16x16x32_bf16 v[148:151], v[176:179], v[72:75], v[148:151]\n" \
    "s_waitcnt lgkmcnt(2)\n" \
    "v_mfma_f32_16x16x32_bf16 v[160:163], a[136:139], v[180:183], v[160:163]\n" \
    "ds_read_b128 v[176:179], v192 offset:4096\n" \
    "v_mfma_f32_16x16x32_bf16 v[164:167], a[200:203], v[180:183], v[164:167]\n" \
    "v_mfma_f32_16x16x32_bf16 v[168:171], v[180:183], v[8:11], v[168:171]\n" \
    "v_mfma_f32_16x16x32_bf16 v[172:175], v[180:183], v[72:75], v[172:175]\n" \
    "s_waitcnt lgkmcnt(2)\n" \
    "v_mfma_f32_16x16x32_bf16 v[136:139], a[140:143], v[184:187], v[136:139]\n" \
    "ds_read_b128 v[180:183], v192 offset:20480\n" \
    "v_mfma_f32_16x16x32_bf16 v[140:143], a[204:207], v[184:187], v[140:143]\n" \
    "v_mfma_f32_16x16x32_bf16 v[144:147], v[184:187], v[12:15], v[144:147]\n" \
    "v_mfma_f32_16x16x32_bf16 v[148:151], v[184:187], v[76:79], v[148:151]\n" \
    "s_waitcnt lgkmcnt(2)\n" \
    "v_mfma_f32_16x16x32_bf16 v[160:163], a[140:143], v[188:191], v[160:163]\n" \
    "ds_read_b128 v[184:187], v192 offset:5120\n" \
    "v_mfma_f32_16x16x32_bf16 v[164:167], a[204:207], v[188:191], v[164:167]\n" \
    "v_mfma_f32_16x16x32_bf16 v[168:171], v[188:191], v[12:15], v[168:171]\n" \
    "v_mfma_f32_16x16x32_bf16 v[172:175], v[188:191], v[76:79], v[172:175]\n" \
    "s_waitcnt lgkmcnt(2)\n" \
    "v_mfma_f32_16x16x32_bf16 v[136:139], a[144:147], v[176:179], v[136:139]\n" \
    "ds_read_b128 v[188:191], v192 offset:21504\n" \
    "v_mfma_f32_16x16x32_bf16 v[140:143], a[208:211], v[176:179], v[140:143]\n" \
    "v_mfma_f32_16x16x32_bf16 v[144:147], v[176:179], v[16:19], v[144:147]\n" \
    "v_mfma_f32_16x16x32_bf16 v[148:151], v[176:179], v[80:83], v[148:151]\n" \
    "s_waitcnt lgkmcnt(2)\n" \
    "v_mfma_f32_16x16x32_bf16 v[160:163], a[144:147], v[180:183], v[160:163]\n" \
    "ds_read_b128 v[176:179], v192 offset:6144\n" \
    "v_mfma_f32_16x16x32_bf16 v[164:167], a[208:211], v[180:183], v[164:167]\n" \
    "v_mfma_f32_16x16x32_bf16 v[168:171], v[180:183], v[16:19], v[168:171]\n" \
    "v_mfma_f32_16x16x32_bf16 v[172:175], v[180:183], v[80:83], v[172:175]\n" \
    "s_waitcnt lgkmcnt(2)\n" \
    "v_mfma_f32_16x16x32_bf16 v[136:139], a[148:151], v[184:187], v[136:139]\n" \
    "ds_read_b128 v[180:183], v192 offset:22528\n" \
    "v_mfma_f32_16x16x32_bf16 v[140:143], a[212:215], v[184:187], v[140:143]\n" \
    "v_mfma_f32_16x16x32_bf16 v[144:147], v[184:187], v[20:23], v[144:147]\n" \
    "v_mfma_f32_16x16x32_bf16 v[148:151], v[184:187], v[84:87], v[148:151]\n" \
    "s_waitcnt lgkmcnt(2)\n" \
    "v_mfma_f32_16x16x32_bf16 v[160:163], a[148:151], v[188:191], v[160:163]\n" \
    "ds_read_b128 v[184:187], v192 offset:7168\n" \
    "v_mfma_f32_16x16x32_bf16 v[164:167], a[212:215], v[188:191], v[164:167]\n" \
    "v_mfma_f32_16x16x32_bf16 v[168:171], v[188:191], v[20:23], v[168:171]\n" \
    "v_mfma_f32_16x16x32_bf16 v[172:175], v[188:191], v[84:87], v[172:175]\n" \
    "s_waitcnt lgkmcnt(2)\n" \
    "v_mfma_f32_16x16x32_bf16 v[136:139], a[152:155], v[176:179], v[136:139]\n" \
    "ds_read_b128 v[188:191], v192 offset:23552\n" \
    "v_mfma_f32_16x16x32_bf16 v[140:143], a[216:219], v[176:179], v[140:143]\n" \
    "v_mfma_f32_16x16x32_bf16 v[144:147], v[176:179], v[24:27], v[144:147]\n" \
    "v_mfma_f32_16x16x32_bf16 v[148:151], v[176:179], v[88:91], v[148:151]\n" \
    "s_waitcnt lgkmcnt(2)\n" \
    "v_mfma_f32_16x16x32_bf16 v[160:163], a[152:155], v[180:183], v[160:163]\n" \
    "ds_read_b128 v[176:179], v192 offset:8192\n" \
    "v_mfma_f32_16x16x32_bf16 v[164:167], a[216:219], v[180:183], v[164:167]\n" \
    "v_mfma_f32_16x16x32_bf16 v[168:171], v[180:183], v[24:27], v[168:171]\n" \
    "v_mfma_f32_16x16x32_bf16 v[172:175], v[180:183], v[88:91], v[172:175]\n" \
    "s_waitcnt lgkmcnt(2)\n" \
    "v_mfma_f32_16x16x32_bf16 v[136:139], a[156:159], v[184:187], v[136:139]\n" \
    "ds_read_b128 v[180:183], v192 offset:24576\n" \
    "v_mfma_f32_16x16x32_bf16 v[140:143], a[220:223], v[184:187], v[140:143]\n" \
    "v_mfma_f32_16x16x32_bf16 v[144:147], v[184:187], v[28:31], v[144:147]\n" \
    "v_mfma_f32_16x16x32_bf16 v[148:151], v[184:187], v[92:95], v[148:151]\n" \
    "s_waitcnt lgkmcnt(2)\n" \
    "v_mfma_f32_16x16x32_bf16 v[160:163], a[156:159], v[188:191], v[160:163]\n" \
    "ds_read_b128 v[184:187], v192 offset:9216\n" \
    "v_mfma_f32_16x16x32_bf16 v[164:167], a[220:223], v[188:191], v[164:167]\n" \
    "v_mfma_f32_16x16x32_bf16 v[168:171], v[188:191], v[28:31], v[168:171]\n" \
    "v_mfma_f32_16x16x32_bf16 v[172:175], v[188:191], v[92:95], v[172:175]\n" \
    "s_waitcnt lgkmcnt(2)\n" \
    "v_mfma_f32_16x16x32_bf16 v[136:139], a[160:163], v[176:179], v[136:139]\n" \
    "ds_read_b128 v[188:191], v192 offset:25600\n" \
    "v_mfma_f32_16x16x32_bf16 v[140:143], a[224:227], v[176:179], v[140:143]\n" \
    "v_mfma_f32_16x16x32_bf16 v[144:147], v[176:179], v[32:35], v[144:147]\n" \
    "v_mfma_f32_16x16x32_bf16 v[148:151], v[176:179], v[96:99], v[148:151]\n" \
    "s_waitcnt lgkmcnt(2)\n" \
    "v_mfma_f32_16x16x32_bf16 v[160:163], a[160:163], v[180:183], v[160:163]\n" \
    "ds_read_b128 v[176:179], v192 offset:10240\n" \
    "v_mfma_f32_16x16x32_bf16 v[164:167], a[224:227], v[180:183], v[164:167]\n" \
    "v_mfma_f32_16x16x32_bf16 v[168:171], v[180:183], v[32:35], v[168:171]\n" \
    "v_mfma_f32_16x16x32_bf16 v[172:175], v[180:183], v[96:99], v[172:175]\n" \
    "s_waitcnt lgkmcnt(2)\n" \
    "v_mfma_f32_16x16x32_bf16 v[136:139], a[164:167], v[184:187], v[136:139]\n" \
    "ds_read_b128 v[180:183], v192 offset:26624\n" \
    "v_mfma_f32_16x16x32_bf16 v[140:143], a[228:231], v[184:187], v[140:143]\n" \
    "v_mfma_f32_16x16x32_bf16 v[144:147], v[184:187], v[36:39], v[144:147]\n" \
    "v_mfma_f32_16x16x32_bf16 v[148:151], v[184:187], v[100:103], v[148:151]\n" \
    "s_waitcnt lgkmcnt(2)\n" \
    "v_mfma_f32_16x16x32_bf16 v[160:163], a[164:167], v[188:191], v[160:163]\n" \
    "ds_read_b128 v[184:187], v192 offset:11264\n" \
    "v_mfma_f32_16x16x32_bf16 v[164:167], a[228:231], v[188:191], v[164:167]\n" \
    "v_mfma_f32_16x16x32_bf16 v[168:171], v[188:191], v[36:39], v[168:171]\n" \
    "v_mfma_f32_16x16x32_bf16 v[172:175], v[188:191], v[100:103], v[172:175]\n" \
    "s_waitcnt lgkmcnt(2)\n" \
    "v_mfma_f32_16x16x32_bf16 v[136:139], a[168:171], v[176:179], v[136:139]\n" \
    "ds_read_b128 v[188:191], v192 offset:27648\n" \
    "v_mfma_f32_16x16x32_bf16 v[140:143], a[232:235], v[176:179], v[140:143]\n" \
    "v_mfma_f32_16x16x32_bf16 v[144:147], v[176:179], v[40:43], v[144:147]\n" \
    "v_mfma_f32_16x16x32_bf16 v[148:151], v[176:179], v[104:107], v[148:151]\n" \
    "s_waitcnt lgkmcnt(2)\n" \
    "v_mfma_f32_16x16x32_bf16 v[160:163], a[168:171], v[180:183], v[160:163]\n" \
    "ds_read_b128 v[176:179], v192 offset:12288\n" \
    "v_mfma_f32_16x16x32_bf16 v[164:167], a[232:235], v[180:183], v[164:167]\n" \
    "v_mfma_f32_16x16x32_bf16 v[168:171], v[180:183], v[40:43], v[168:171]\n" \
    "v_mfma_f32_16x16x32_bf16 v[172:175], v[180:183], v[104:107], v[172:175]\n" \
    "s_waitcnt lgkmcnt(2)\n" \
    "v_mfma_f32_16x16x32_bf16 v[136:139], a[172:175], v[184:187], v[136:139]\n" \
    "ds_read_b128 v[180:183], v192 offset:28672\n" \
    "v_mfma_f32_16x16x32_bf16 v[140:143], a[236:239], v[184:187], v[140:143]\n" \
    "v_mfma_f32_16x16x32_bf16 v[144:147], v[184:187], v[44:47], v[144:147]\n" \
    "v_mfma_f32_16x16x32_bf16 v[148:151], v[184:187], v[108:111], v[148:151]\n" \
    "s_waitcnt lgkmcnt(2)\n" \
    "v_mfma_f32_16x16x32_bf16 v[160:163], a[172:175], v[188:191], v[160:163]\n" \
    "ds_read_b128 v[184:187], v192 offset:13312\n" \
    "v_mfma_f32_16x16x32_bf16 v[164:167], a[236:239], v[188:191], v[164:167]\n" \
    "v_mfma_f32_16x16x32_bf16 v[168:171], v[188:191], v[44:47], v[168:171]\n" \
    "v_mfma_f32_16x16x32_bf16 v[172:175], v[188:191], v[108:111], v[172:175]\n" \
    "s_waitcnt lgkmcnt(2)\n" \
    "v_mfma_f32_16x16x32_bf16 v[136:139], a[176:179], v[176:179], v[136:139]\n" \
    "ds_read_b128 v[188:191], v192 offset:29696\n" \
    "v_mfma_f32_16x16x32_bf16 v[140:143], a[240:243], v[176:179], v[140:143]\n" \
    "v_mfma_f32_16x16x32_bf16 v[144:147], v[176:179], v[48:51], v[144:147]\n" \
    "v_mfma_f32_16x16x32_bf16 v[148:151], v[176:179], v[112:115], v[148:151]\n" \
    "s_waitcnt lgkmcnt(2)\n" \
    "v_mfma_f32_16x16x32_bf16 v[160:163], a[176:179], v[180:183], v[160:163]\n" \
    "ds_read_b128 v[176:179], v192 offset:14336\n" \
    "v_mfma_f32_16x16x32_bf16 v[164:167], a[240:243], v[180:183], v[164:167]\n" \
    "v_mfma_f32_16x16x32_bf16 v[168:171], v[180:183], v[48:51], v[168:171]\n" \
    "v_mfma_f32_16x16x32_bf16 v[172:175], v[180:183], v[112:115], v[172:175]\n" \
    "s_waitcnt lgkmcnt(2)\n" \
    "v_mfma_f32_16x16x32_bf16 v[136:139], a[180:183], v[184:187], v[136:139]\n" \
    "ds_read_b128 v[180:183], v192 offset:30720\n" \
    "v_mfma_f32_16x16x32_bf16 v[140:143], a[244:247], v[184:187], v[140:143]\n" \
    "v_mfma_f32_16x16x32_bf16 v[144:147], v[184:187], v[52:55], v[144:147]\n" \
    "v_mfma_f32_16x16x32_bf16 v[148:151], v[184:187], v[116:119], v[148:151]\n" \
    "s_waitcnt lgkmcnt(2)\n" \
    "v_mfma_f32_16x16x32_bf16 v[160:163], a[180:183], v[188:191], v[160:163]\n" \
    "ds_read_b128 v[184:187], v192 offset:15360\n" \
    "v_mfma_f32_16x16x32_bf16 v[164:167], a[244:247], v[188:191], v[164:167]\n" \
    "v_mfma_f32_16x16x32_bf16 v[168:171], v[188:191], v[52:55], v[168:171]\n" \
    "v_mfma_f32_16x16x32_bf16 v[172:175], v[188:191], v[116:119], v[172:175]\n" \
    "s_waitcnt lgkmcnt(2)\n" \
    "v_mfma_f32_16x16x32_bf16 v[136:139], a[184:187], v[176:179], v[136:139]\n" \
    "ds_read_b128 v[188:191], v192 offset:31744\n" \
    "v_mfma_f32_16x16x32_bf16 v[140:143], a[248:251], v[176:179], v[140:143]\n" \
    "v_mfma_f32_16x16x32_bf16 v[144:147], v[176:179], v[56:59], v[144:147]\n" \
    "v_mfma_f32_16x16x32_bf16 v[148:151], v[176:179], v[120:123], v[148:151]\n" \
    "s_waitcnt lgkmcnt(2)\n" \
    "v_mfma_f32_16x16x32_bf16 v[160:163], a[184:187], v[180:183], v[160:163]\n" \
    "v_mfma_f32_16x16x32_bf16 v[164:167], a[248:251], v[180:183], v[164:167]\n" \
    "v_mfma_f32_16x16x32_bf16 v[168:171], v[180:183], v[56:59], v[168:171]\n" \
    "v_mfma_f32_16x16x32_bf16 v[172:175], v[180:183], v[120:123], v[172:175]\n" \
    "s_waitcnt lgkmcnt(1)\n" \
    "v_mfma_f32_16x16x32_bf16 v[136:139], a[188:191], v[184:187], v[136:139]\n" \
    "v_mfma_f32_16x16x32_bf16 v[140:143], a[252:255], v[184:187], v[140:143]\n" \
    "v_mfma_f32_16x16x32_bf16 v[144:147], v[184:187], v[60:63], v[144:147]\n" \
    "v_mfma_f32_16x16x32_bf16 v[148:151], v[184:187], v[124:127], v[148:151]\n" \
    "s_waitcnt lgkmcnt(0)\n" \
    "v_mfma_f32_16x16x32_bf16 v[160:163], a[188:191], v[188:191], v[160:163]\n" \
    "v_mfma_f32_16x16x32_bf16 v[164:167], a[252:255], v[188:191], v[164:167]\n" \
    "v_mfma_f32_16x16x32_bf16 v[168:171], v[188:191], v[60:63], v[168:171]\n" \
    "v_mfma_f32_16x16x32_bf16 v[172:175], v[188:191], v[124:127], v[172:175]\n" \
    "s_add_u32 s54, s47, s51\n" \
    "s_add_u32 s55, s54, s51\n" \
    "s_add_u32 s55, s55, 8192\n" \
    "v_cvt_pk_bf16_f32 v196, v136, v137\n" \
    "v_cvt_pk_bf16_f32 v197, v138, v139\n" \
    "v_cvt_pk_bf16_f32 v198, v140, v141\n" \
    "v_cvt_pk_bf16_f32 v199, v142, v143\n" \
    "buffer_store_dwordx4 v[196:199], v253, s[36:39], s54 offen sc1\n" \
    "s_add_u32 s54, s54, 4096\n" \
    "v_cvt_pk_bf16_f32 v200, v160, v161\n" \
    "v_cvt_pk_bf16_f32 v201, v162, v163\n" \
    "v_cvt_pk_bf16_f32 v202, v164, v165\n" \
    "v_cvt_pk_bf16_f32 v203, v166, v167\n" \
    "buffer_store_dwordx4 v[200:203], v253, s[36:39], s54 offen sc1\n" \
    "v_cvt_pk_bf16_f32 v176, v144, v145\n" \
    "v_cvt_pk_bf16_f32 v177, v146, v147\n" \
    "v_cvt_pk_bf16_f32 v178, v168, v169\n" \
    "v_cvt_pk_bf16_f32 v179, v170, v171\n" \
    "buffer_store_dwordx4 v[176:179], v253, s[36:39], s55 offen sc1\n" \
    "v_cvt_pk_bf16_f32 v180, v148, v149\n" \
    "v_cvt_pk_bf16_f32 v181, v150, v151\n" \
    "v_cvt_pk_bf16_f32 v182, v172, v173\n" \
    "v_cvt_pk_bf16_f32 v183, v174, v175\n" \
    "buffer_store_dwordx4 v[180:183], v253, s[36:39], s55 offen offset:1024 sc1\n" \
    "s_add_u32 s47, s47, 32768\n" \
    "s_waitcnt vmcnt(0)\n" \
    "s_barrier\n" \
    "buffer_store_dword v234, v252, s[36:39], s57 offen sc1\n" \
    "s_add_u32 s57, s57, 8\n" \
    "s_add_u32 s44, s44, 32768\n" \
    "s_cmp_lt_u32 s44, 98304\n" \
    "s_cbranch_scc1 HB%=\n" \
    "s_add_u32 s45, s45, 65536\n" \
    "s_add_u32 m0, s46, 0\n" \
    "s_add_u32 s54, s45, 0\n" \
    "buffer_load_dwordx4 v253, s[36:39], s54 offen sc1 lds\n" \
    "s_add_u32 m0, s46, 1024\n" \
    "s_add_u32 s54, s45, 1024\n" \
    "buffer_load_dwordx4 v253, s[36:39], s54 offen sc1 lds\n" \
    "s_add_u32 m0, s46, 2048\n" \
    "s_add_u32 s54, s45, 2048\n" \
    "buffer_load_dwordx4 v253, s[36:39], s54 offen sc1 lds\n" \
    "s_add_u32 m0, s46, 3072\n" \
    "s_add_u32 s54, s45, 3072\n" \
    "buffer_load_dwordx4 v253, s[36:39], s54 offen sc1 lds\n" \
    "s_add_u32 m0, s46, 4096\n" \
    "s_add_u32 s54, s45, 4096\n" \
    "buffer_load_dwordx4 v253, s[36:39], s54 offen sc1 lds\n" \
    "s_add_u32 m0, s46, 5120\n" \
    "s_add_u32 s54, s45, 5120\n" \
    "buffer_load_dwordx4 v253, s[36:39], s54 offen sc1 lds\n" \
    "s_add_u32 m0, s46, 6144\n" \
    "s_add_u32 s54, s45, 6144\n" \
    "buffer_load_dwordx4 v253, s[36:39], s54 offen sc1 lds\n" \
    "s_add_u32 m0, s46, 7168\n" \
    "s_add_u32 s54, s45, 7168\n" \
    "buffer_load_dwordx4 v253, s[36:39], s54 offen sc1 lds\n" \
    "s_mov_b32 s42, 4194304\n" \
    "s_mov_b32 s43, 0x20000\n" \
    "s_mov_b32 s40, s60\n" \
    "s_and_b32 s41, s61, 0xffff\n" \
    "s_add_u32 m0, s51, 32768\n" \
    "s_mov_b32 s54, 0\n" \
    "buffer_load_dwordx4 v216, s[40:43], s54 offen lds\n" \
    "s_sleep 4\n" \
    "s_add_u32 m0, s51, 36864\n" \
    "s_mov_b32 s54, 131072\n" \
    "buffer_load_dwordx4 v216, s[40:43], s54 offen lds\n" \
    "s_sleep 4\n" \
    "s_add_u32 m0, s51, 40960\n" \
    "s_mov_b32 s54, 262144\n" \
    "buffer_load_dwordx4 v216, s[40:43], s54 offen lds\n" \
    "s_sleep 4\n" \
    "s_add_u32 m0, s51, 45056\n" \
    "s_mov_b32 s54, 393216\n" \
    "buffer_load_dwordx4 v216, s[40:43], s54 offen lds\n" \
    "s_sleep 4\n" \
    "s_add_u32 m0, s51, 49152\n" \
    "s_mov_b32 s54, 524288\n" \
    "buffer_load_dwordx4 v216, s[40:43], s54 offen lds\n" \
    "s_sleep 4\n" \
    "s_add_u32 m0, s51, 53248\n" \
    "s_mov_b32 s54, 655360\n" \
    "buffer_load_dwordx4 v216, s[40:43], s54 offen lds\n" \
    "s_sleep 4\n" \
    "s_add_u32 m0, s51, 57344\n" \
    "s_mov_b32 s54, 786432\n" \
    "buffer_load_dwordx4 v216, s[40:43], s54 offen lds\n" \
    "s_sleep 4\n" \
    "s_add_u32 m0, s51, 61440\n" \
    "s_mov_b32 s54, 917504\n" \
    "buffer_load_dwordx4 v216, s[40:43], s54 offen lds\n" \
    "s_sleep 4\n" \
    "s_add_u32 m0, s51, 65536\n" \
    "s_mov_b32 s54, 1048576\n" \
    "buffer_load_dwordx4 v216, s[40:43], s54 offen lds\n" \
    "s_sleep 4\n" \
    "s_add_u32 m0, s51, 69632\n" \
    "s_mov_b32 s54, 1179648\n" \
    "buffer_load_dwordx4 v216, s[40:43], s54 offen lds\n" \
    "s_sleep 4\n" \
    "s_add_u32 m0, s51, 73728\n" \
    "s_mov_b32 s54, 1310720\n" \
    "buffer_load_dwordx4 v216, s[40:43], s54 offen lds\n" \
    "s_sleep 4\n" \
    "s_add_u32 m0, s51, 77824\n" \
    "s_mov_b32 s54, 1441792\n" \
    "buffer_load_dwordx4 v216, s[40:43], s54 offen lds\n" \
    "s_sleep 4\n" \
    "s_add_u32 m0, s51, 81920\n" \
    "s_mov_b32 s54, 1572864\n" \
    "buffer_load_dwordx4 v216, s[40:43], s54 offen lds\n" \
    "s_sleep 4\n" \
    "s_add_u32 m0, s51, 86016\n" \
    "s_mov_b32 s54, 1703936\n" \
    "buffer_load_dwordx4 v216, s[40:43], s54 offen lds\n" \
    "s_sleep 4\n" \
    "s_add_u32 m0, s51, 90112\n" \
    "s_mov_b32 s54, 1835008\n" \
    "buffer_load_dwordx4 v216, s[40:43], s54 offen lds\n" \
    "s_sleep 4\n" \
    "s_add_u32 m0, s51, 94208\n" \
    "s_mov_b32 s54, 1966080\n" \
    "buffer_load_dwordx4 v216, s[40:43], s54 offen lds\n" \
    "s_sleep 4\n" \
    "s_add_u32 m0, s51, 98304\n" \
    "s_mov_b32 s54, 2097152\n" \
    "buffer_load_dwordx4 v216, s[40:43], s54 offen lds\n" \
    "s_sleep 4\n" \
    "s_add_u32 m0, s51, 102400\n" \
    "s_mov_b32 s54, 2228224\n" \
    "buffer_load_dwordx4 v216, s[40:43], s54 offen lds\n" \
    "s_sleep 4\n" \
    "s_add_u32 m0, s51, 106496\n" \
    "s_mov_b32 s54, 2359296\n" \
    "buffer_load_dwordx4 v216, s[40:43], s54 offen lds\n" \
    "s_sleep 4\n" \
    "s_add_u32 m0, s51, 110592\n" \
    "s_mov_b32 s54, 2490368\n" \
    "buffer_load_dwordx4 v216, s[40:43], s54 offen lds\n" \
    "s_sleep 4\n" \
    "s_add_u32 m0, s51, 114688\n" \
    "s_mov_b32 s54, 2621440\n" \
    "buffer_load_dwordx4 v216, s[40:43], s54 offen lds\n" \
    "s_sleep 4\n" \
    "s_add_u32 m0, s51, 118784\n" \
    "s_mov_b32 s54, 2752512\n" \
    "buffer_load_dwordx4 v216, s[40:43], s54 offen lds\n" \
    "s_sleep 4\n" \
    "s_add_u32 m0, s51, 122880\n" \
    "s_mov_b32 s54, 2883584\n" \
    "buffer_load_dwordx4 v216, s[40:43], s54 offen lds\n" \
    "s_sleep 4\n" \
    "s_add_u32 m0, s51, 126976\n" \
    "s_mov_b32 s54, 3014656\n" \
    "buffer_load_dwordx4 v216, s[40:43], s54 offen lds\n" \
    "s_sleep 4\n" \
    "s_add_u32 m0, s51, 131072\n" \
    "s_mov_b32 s54, 3145728\n" \
    "buffer_load_dwordx4 v216, s[40:43], s54 offen lds\n" \
    "s_sleep 4\n" \
    "s_add_u32 m0, s51, 135168\n" \
    "s_mov_b32 s54, 3276800\n" \
    "buffer_load_dwordx4 v216, s[40:43], s54 offen lds\n" \
    "s_sleep 4\n" \
    "s_add_u32 m0, s51, 139264\n" \
    "s_mov_b32 s54, 3407872\n" \
    "buffer_load_dwordx4 v216, s[40:43], s54 offen lds\n" \
    "s_sleep 4\n" \
    "s_add_u32 m0, s51, 143360\n" \
    "s_mov_b32 s54, 3538944\n" \
    "buffer_load_dwordx4 v216, s[40:43], s54 offen lds\n" \
    "s_sleep 4\n" \
    "s_add_u32 m0, s51, 147456\n" \
    "s_mov_b32 s54, 3670016\n" \
    "buffer_load_dwordx4 v216, s[40:43], s54 offen lds\n" \
    "s_sleep 4\n" \
    "s_add_u32 m0, s51, 151552\n" \
    "s_mov_b32 s54, 3801088\n" \
    "buffer_load_dwordx4 v216, s[40:43], s54 offen lds\n" \
    "s_sleep 4\n" \
    "s_add_u32 m0, s51, 155648\n" \
    "s_mov_b32 s54, 3932160\n" \
    "buffer_load_dwordx4 v216, s[40:43], s54 offen lds\n" \
    "s_sleep 4\n" \
    "s_add_u32 m0, s51, 159744\n" \
    "s_mov_b32 s54, 4063232\n" \
    "buffer_load_dwordx4 v216, s[40:43], s54 offen lds\n" \
    "s_sleep 4\n" \
    "s_waitcnt vmcnt(16)\n" \
    "s_barrier\n" \
    "v_add_u32 v223, 32768, v217\n" \
    "ds_read_b64_tr_b16 a[0:1], v223\n" \
    "ds_read_b64_tr_b16 a[2:3], v223 offset:1024\n" \
    "ds_read_b64_tr_b16 a[4:5], v223 offset:8192\n" \
    "ds_read_b64_tr_b16 a[6:7], v223 offset:9216\n" \
    "ds_read_b64_tr_b16 a[8:9], v223 offset:16384\n" \
    "ds_read_b64_tr_b16 a[10:11], v223 offset:17408\n" \
    "ds_read_b64_tr_b16 a[12:13], v223 offset:24576\n" \
    "ds_read_b64_tr_b16 a[14:15], v223 offset:25600\n" \
    "s_waitcnt lgkmcnt(0)\n" \
    "ds_read_b64_tr_b16 a[16:17], v223 offset:32768\n" \
    "ds_read_b64_tr_b16 a[18:19], v223 offset:33792\n" \
    "ds_read_b64_tr_b16 a[20:21], v223 offset:40960\n" \
    "ds_read_b64_tr_b16 a[22:23], v223 offset:41984\n" \
    "ds_read_b64_tr_b16 a[24:25], v223 offset:49152\n" \
    "ds_read_b64_tr_b16 a[26:27], v223 offset:50176\n" \
    "ds_read_b64_tr_b16 a[28:29], v223 offset:57344\n" \
    "ds_read_b64_tr_b16 a[30:31], v223 offset:58368\n" \
    "s_waitcnt lgkmcnt(0)\n" \
    "v_add_u32 v223, 32768, v218\n" \
    "ds_read_b64_tr_b16 a[64:65], v223\n" \
    "ds_read_b64_tr_b16 a[66:67], v223 offset:1024\n" \
    "ds_read_b64_tr_b16 a[68:69], v223 offset:8192\n" \
    "ds_read_b64_tr_b16 a[70:71], v223 offset:9216\n" \
    "ds_read_b64_tr_b16 a[72:73], v223 offset:16384\n" \
    "ds_read_b64_tr_b16 a[74:75], v223 offset:17408\n" \
    "ds_read_b64_tr_b16 a[76:77], v223 offset:24576\n" \
    "ds_read_b64_tr_b16 a[78:79], v223 offset:25600\n" \
    "s_waitcnt lgkmcnt(0)\n" \
    "ds_read_b64_tr_b16 a[80:81], v223 offset:32768\n" \
    "ds_read_b64_tr_b16 a[82:83], v223 offset:33792\n" \
    "ds_read_b64_tr_b16 a[84:85], v223 offset:40960\n" \
    "ds_read_b64_tr_b16 a[86:87], v223 offset:41984\n" \
    "ds_read_b64_tr_b16 a[88:89], v223 offset:49152\n" \
    "ds_read_b64_tr_b16 a[90:91], v223 offset:50176\n" \
    "ds_read_b64_tr_b16 a[92:93], v223 offset:57344\n" \
    "ds_read_b64_tr_b16 a[94:95], v223 offset:58368\n" \
    "s_waitcnt lgkmcnt(0)\n" \
    "s_barrier\n" \
    "s_waitcnt vmcnt(0)\n" \
    "s_barrier\n" \
    "v_add_u32 v223, 98304, v217\n" \
    "ds_read_b64_tr_b16 a[32:33], v223\n" \
    "ds_read_b64_tr_b16 a[34:35], v223 offset:1024\n" \
    "ds_read_b64_tr_b16 a[36:37], v223 offset:8192\n" \
    "ds_read_b64_tr_b16 a[38:39], v223 offset:9216\n" \
    "ds_read_b64_tr_b16 a[40:41], v223 offset:16384\n" \
    "ds_read_b64_tr_b16 a[42:43], v223 offset:17408\n" \
    "ds_read_b64_tr_b16 a[44:45], v223 offset:24576\n" \
    "ds_read_b64_tr_b16 a[46:47], v223 offset:25600\n" \
    "s_waitcnt lgkmcnt(0)\n" \
    "ds_read_b64_tr_b16 a[48:49], v223 offset:32768\n" \
    "ds_read_b64_tr_b16 a[50:51], v223 offset:33792\n" \
    "ds_read_b64_tr_b16 a[52:53], v223 offset:40960\n" \
    "ds_read_b64_tr_b16 a[54:55], v223 offset:41984\n" \
    "ds_read_b64_tr_b16 a[56:57], v223 offset:49152\n" \
    "ds_read_b64_tr_b16 a[58:59], v223 offset:50176\n" \
    "ds_read_b64_tr_b16 a[60:61], v223 offset:57344\n" \
    "ds_read_b64_tr_b16 a[62:63], v223 offset:58368\n" \
    "s_waitcnt lgkmcnt(0)\n" \
    "v_add_u32 v223, 98304, v218\n" \
    "ds_read_b64_tr_b16 a[96:97], v223\n" \
    "ds_read_b64_tr_b16 a[98:99], v223 offset:1024\n" \
    "ds_read_b64_tr_b16 a[100:101], v223 offset:8192\n" \
    "ds_read_b64_tr_b16 a[102:103], v223 offset:9216\n" \
    "ds_read_b64_tr_b16 a[104:105], v223 offset:16384\n" \
    "ds_read_b64_tr_b16 a[106:107], v223 offset:17408\n" \
    "ds_read_b64_tr_b16 a[108:109], v223 offset:24576\n" \
    "ds_read_b64_tr_b16 a[110:111], v223 offset:25600\n" \
    "s_waitcnt lgkmcnt(0)\n" \
    "ds_read_b64_tr_b16 a[112:113], v223 offset:32768\n" \
    "ds_read_b64_tr_b16 a[114:115], v223 offset:33792\n" \
    "ds_read_b64_tr_b16 a[116:117], v223 offset:40960\n" \
    "ds_read_b64_tr_b16 a[118:119], v223 offset:41984\n" \
    "ds_read_b64_tr_b16 a[120:121], v223 offset:49152\n" \
    "ds_read_b64_tr_b16 a[122:123], v223 offset:50176\n" \
    "ds_read_b64_tr_b16 a[124:125], v223 offset:57344\n" \
    "ds_read_b64_tr_b16 a[126:127], v223 offset:58368\n" \
    "s_waitcnt lgkmcnt(0)\n" \
    "s_barrier\n" \
    "s_add_u32 s34, s46, 32768\n" \
    "v_lshrrev_b32 v192, 4, v254\n" \
    "s_sub_u32 s55, s57, 40\n" \
    "HWa%=:\n" \
    "buffer_load_dword v232, v192, s[36:39], s55 offen sc1\n" \
    "s_waitcnt vmcnt(0)\n" \
    "v_cmp_gt_u32 vcc, v234, v232\n" \
    "s_cbranch_vccz HWb%=\n" \
    "s_sleep 2\n" \
    "s_add_u32 s56, s56, 1\n" \
    "s_cmp_lt_u32 s56, 262144\n" \
    "s_cbranch_scc1 HWa%=\n" \
    "HWb%=:\n" \
    "s_mov_b32 s44, 0\n" \
    "s_sub_u32 s57, s57, 8\n" \
    "s_mov_b32 s28, 81920\n" \
    "s_xor_b32 s55, s28, 16384\n" \
    "s_lshl_b32 s54, s51, 2\n" \
    "s_add_u32 s54, s54, s55\n" \
    "s_add_u32 m0, s54, 0\n" \
    "s_add_u32 s54, s49, 0\n" \
    "buffer_load_dwordx4 v253, s[36:39], s54 offen sc1 lds\n" \
    "s_xor_b32 s55, s28, 16384\n" \
    "s_lshl_b32 s54, s51, 2\n" \
    "s_add_u32 s54, s54, s55\n" \
    "s_add_u32 m0, s54, 1024\n" \
    "s_add_u32 s54, s49, 1024\n" \
    "buffer_load_dwordx4 v253, s[36:39], s54 offen sc1 lds\n" \
    "s_xor_b32 s55, s28, 16384\n" \
    "s_lshl_b32 s54, s51, 2\n" \
    "s_add_u32 s54, s54, s55\n" \
    "s_add_u32 m0, s54, 2048\n" \
    "s_add_u32 s54, s49, 2048\n" \
    "buffer_load_dwordx4 v253, s[36:39], s54 offen sc1 lds\n" \
    "s_xor_b32 s55, s28, 16384\n" \
    "s_lshl_b32 s54, s51, 2\n" \
    "s_add_u32 s54, s54, s55\n" \
    "s_add_u32 m0, s54, 3072\n" \
    "s_add_u32 s54, s49, 3072\n" \
    "buffer_load_dwordx4 v253, s[36:39], s54 offen sc1 lds\n" \
    "s_mov_b32 s29, 32768\n" \
    "s_waitcnt vmcnt(0)\n" \
    "HL%=:\n" \
    "s_mov_b32 s35, 0\n" \
    "s_xor_b32 s28, s28, 16384\n" \
    "s_add_u32 s49, s49, 32768\n" \
    "s_cmp_lt_u32 s44, 27\n" \
    "s_cbranch_scc1 HP%=\n" \
    "s_cmp_eq_u32 s44, 32\n" \
    "s_cbranch_scc1 HE%=\n" \
    "s_waitcnt vmcnt(0)\n" \
    "s_barrier\n" \
    "s_cmp_eq_u32 s44, 0\n" \
    "s_cbranch_scc1 HF%=\n" \
    "v_mov_b32 v240, s44\n" \
    "buffer_store_dword v240, v243, s[36:39], s68 offen sc1\n" \
    "s_cmp_gt_u32 s44, 27\n" \
    "s_cbranch_scc1 HF%=\n" \
    "buffer_store_dword v234, v252, s[36:39], s57 offen sc1\n" \
    "HF%=:\n" \
    "s_add_u32 s57, s57, 8\n" \
    "s_xor_b32 s29, s29, 32768\n" \
    "s_xor_b32 s55, s28, 16384\n" \
    "s_lshl_b32 s54, s51, 2\n" \
    "s_add_u32 s54, s54, s55\n" \
    "s_add_u32 m0, s54, 0\n" \
    "s_add_u32 s54, s49, 0\n" \
    "buffer_load_dwordx4 v253, s[36:39], s54 offen sc1 lds\n" \
    "s_xor_b32 s55, s28, 16384\n" \
    "s_lshl_b32 s54, s51, 2\n" \
    "s_add_u32 s54, s54, s55\n" \
    "s_add_u32 m0, s54, 1024\n" \
    "s_add_u32 s54, s49, 1024\n" \
    "buffer_load_dwordx4 v253, s[36:39], s54 offen sc1 lds\n" \
    "s_xor_b32 s55, s28, 16384\n" \
    "s_lshl_b32 s54, s51, 2\n" \
    "s_add_u32 s54, s54, s55\n" \
    "s_add_u32 m0, s54, 2048\n" \
    "s_add_u32 s54, s49, 2048\n" \
    "buffer_load_dwordx4 v253, s[36:39], s54 offen sc1 lds\n" \
    "s_xor_b32 s55, s28, 16384\n" \
    "s_lshl_b32 s54, s51, 2\n" \
    "s_add_u32 s54, s54, s55\n" \
    "s_add_u32 m0, s54, 3072\n" \
    "s_add_u32 s54, s49, 3072\n" \
    "buffer_load_dwordx4 v253, s[36:39], s54 offen sc1 lds\n" \
    "s_branch HA%=\n" \
    "HP%=:\n" \
    "s_waitcnt vmcnt(2)\n" \
    "s_barrier\n" \
    "ds_read_b128 v[176:179], v253\n" \
    "ds_read_b128 v[180:183], v253 offset:16384\n" \
    "ds_read_b128 v[184:187], v253 offset:1024\n" \
    "s_waitcnt lgkmcnt(2)\n" \
    "v_mfma_f32_16x16x32_bf16 v[136:139], a[128:131], v[176:179], 0\n" \
    "ds_read_b128 v[188:191], v253 offset:17408\n" \
    "v_mfma_f32_16x16x32_bf16 v[140:143], a[192:195], v[176:179], 0\n" \
    "s_add_u32 m0, s34, 0\n" \
    "s_add_u32 s54, s48, 0\n" \
    "buffer_load_dwordx4 v253, s[36:39], s54 offen sc1 lds\n" \
    "v_mfma_f32_16x16x32_bf16 v[144:147], v[176:179], v[0:3], 0\n" \
    "v_mfma_f32_16x16x32_bf16 v[148:151], v[176:179], v[64:67], 0\n" \
    "s_waitcnt lgkmcnt(2)\n" \
    "v_mfma_f32_16x16x32_bf16 v[160:163], a[128:131], v[180:183], 0\n" \
    "ds_read_b128 v[176:179], v253 offset:2048\n" \
    "v_mfma_f32_16x16x32_bf16 v[164:167], a[192:195], v[180:183], 0\n" \
    "v_mfma_f32_16x16x32_bf16 v[168:171], v[180:183], v[0:3], 0\n" \
    "v_mfma_f32_16x16x32_bf16 v[172:175], v[180:183], v[64:67], 0\n" \
    "s_waitcnt lgkmcnt(2)\n" \
    "v_mfma_f32_16x16x32_bf16 v[136:139], a[132:135], v[184:187], v[136:139]\n" \
    "ds_read_b128 v[180:183], v253 offset:18432\n" \
    "v_mfma_f32_16x16x32_bf16 v[140:143], a[196:199], v[184:187], v[140:143]\n" \
    "s_add_u32 m0, s34, 1024\n" \
    "s_add_u32 s54, s48, 1024\n" \
    "buffer_load_dwordx4 v253, s[36:39], s54 offen sc1 lds\n" \
    "v_mfma_f32_16x16x32_bf16 v[144:147], v[184:187], v[4:7], v[144:147]\n" \
    "v_mfma_f32_16x16x32_bf16 v[148:151], v[184:187], v[68:71], v[148:151]\n" \
    "s_waitcnt lgkmcnt(2)\n" \
    "v_mfma_f32_16x16x32_bf16 v[160:163], a[132:135], v[188:191], v[160:163]\n" \
    "ds_read_b128 v[184:187], v253 offset:3072\n" \
    "v_mfma_f32_16x16x32_bf16 v[164:167], a[196:199], v[188:191], v[164:167]\n" \
    "v_mfma_f32_16x16x32_bf16 v[168:171], v[188:191], v[4:7], v[168:171]\n" \
    "v_mfma_f32_16x16x32_bf16 v[172:175], v[188:191], v[68:71], v[172:175]\n" \
    "s_waitcnt lgkmcnt(2)\n" \
    "v_mfma_f32_16x16x32_bf16 v[136:139], a[136:139], v[176:179], v[136:139]\n" \
    "ds_read_b128 v[188:191], v253 offset:19456\n" \
    "v_mfma_f32_16x16x32_bf16 v[140:143], a[200:203], v[176:179], v[140:143]\n" \
    "s_add_u32 m0, s34, 2048\n" \
    "s_add_u32 s54, s48, 2048\n" \
    "buffer_load_dwordx4 v253, s[36:39], s54 offen sc1 lds\n" \
    "v_mfma_f32_16x16x32_bf16 v[144:147], v[176:179], v[8:11], v[144:147]\n" \
    "v_mfma_f32_16x16x32_bf16 v[148:151], v[176:179], v[72:75], v[148:151]\n" \
    "s_waitcnt lgkmcnt(2)\n" \
    "v_mfma_f32_16x16x32_bf16 v[160:163], a[136:139], v[180:183], v[160:163]\n" \
    "ds_read_b128 v[176:179], v253 offset:4096\n" \
    "v_mfma_f32_16x16x32_bf16 v[164:167], a[200:203], v[180:183], v[164:167]\n" \
    "v_mfma_f32_16x16x32_bf16 v[168:171], v[180:183], v[8:11], v[168:171]\n" \
    "v_mfma_f32_16x16x32_bf16 v[172:175], v[180:183], v[72:75], v[172:175]\n" \
    "s_waitcnt lgkmcnt(2)\n" \
    "v_mfma_f32_16x16x32_bf16 v[136:139], a[140:143], v[184:187], v[136:139]\n" \
    "ds_read_b128 v[180:183], v253 offset:20480\n" \
    "v_mfma_f32_16x16x32_bf16 v[140:143], a[204:207], v[184:187], v[140:143]\n" \
    "s_add_u32 m0, s34, 3072\n" \
    "s_add_u32 s54, s48, 3072\n" \
    "buffer_load_dwordx4 v253, s[36:39], s54 offen sc1 lds\n" \
    "v_mfma_f32_16x16x32_bf16 v[144:147], v[184:187], v[12:15], v[144:147]\n" \
    "v_mfma_f32_16x16x32_bf16 v[148:151], v[184:187], v[76:79], v[148:151]\n" \
    "s_waitcnt lgkmcnt(2)\n" \
    "v_mfma_f32_16x16x32_bf16 v[160:163], a[140:143], v[188:191], v[160:163]\n" \
    "ds_read_b128 v[184:187], v253 offset:5120\n" \
    "v_mfma_f32_16x16x32_bf16 v[164:167], a[204:207], v[188:191], v[164:167]\n" \
    "v_mfma_f32_16x16x32_bf16 v[168:171], v[188:191], v[12:15], v[168:171]\n" \
    "v_mfma_f32_16x16x32_bf16 v[172:175], v[188:191], v[76:79], v[172:175]\n" \
    "s_waitcnt lgkmcnt(2)\n" \
    "v_mfma_f32_16x16x32_bf16 v[136:139], a[144:147], v[176:179], v[136:139]\n" \
    "ds_read_b128 v[188:191], v253 offset:21504\n" \
    "v_mfma_f32_16x16x32_bf16 v[140:143], a[208:211], v[176:179], v[140:143]\n" \
    "s_add_u32 m0, s34, 4096\n" \
    "s_add_u32 s54, s48, 4096\n" \
    "buffer_load_dwordx4 v253, s[36:39], s54 offen sc1 lds\n" \
    "v_mfma_f32_16x16x32_bf16 v[144:147], v[176:179], v[16:19], v[144:147]\n" \
    "v_mfma_f32_16x16x32_bf16 v[148:151], v[176:179], v[80:83], v[148:151]\n" \
    "s_waitcnt lgkmcnt(2)\n" \
    "v_mfma_f32_16x16x32_bf16 v[160:163], a[144:147], v[180:183], v[160:163]\n" \
    "ds_read_b128 v[176:179], v253 offset:6144\n" \
    "v_mfma_f32_16x16x32_bf16 v[164:167], a[208:211], v[180:183], v[164:167]\n" \
    "v_mfma_f32_16x16x32_bf16 v[168:171], v[180:183], v[16:19], v[168:171]\n" \
    "v_mfma_f32_16x16x32_bf16 v[172:175], v[180:183], v[80:83], v[172:175]\n" \
    "s_waitcnt lgkmcnt(2)\n" \
    "v_mfma_f32_16x16x32_bf16 v[136:139], a[148:151], v[184:187], v[136:139]\n" \
    "ds_read_b128 v[180:183], v253 offset:22528\n" \
    "v_mfma_f32_16x16x32_bf16 v[140:143], a[212:215], v[184:187], v[140:143]\n" \
    "s_add_u32 m0, s34, 5120\n" \
    "s_add_u32 s54, s48, 5120\n" \
    "buffer_load_dwordx4 v253, s[36:39], s54 offen sc1 lds\n" \
    "v_mfma_f32_16x16x32_bf16 v[144:147], v[184:187], v[20:23], v[144:147]\n" \
    "v_mfma_f32_16x16x32_bf16 v[148:151], v[184:187], v[84:87], v[148:151]\n" \
    "s_waitcnt lgkmcnt(2)\n" \
    "v_mfma_f32_16x16x32_bf16 v[160:163], a[148:151], v[188:191], v[160:163]\n" \
    "ds_read_b128 v[184:187], v253 offset:7168\n" \
    "v_mfma_f32_16x16x32_bf16 v[164:167], a[212:215], v[188:191], v[164:167]\n" \
    "v_mfma_f32_16x16x32_bf16 v[168:171], v[188:191], v[20:23], v[168:171]\n" \
    "v_mfma_f32_16x16x32_bf16 v[172:175], v[188:191], v[84:87], v[172:175]\n" \
    "s_waitcnt lgkmcnt(2)\n" \
    "v_mfma_f32_16x16x32_bf16 v[136:139], a[152:155], v[176:179], v[136:139]\n" \
    "ds_read_b128 v[188:191], v253 offset:23552\n" \
    "v_mfma_f32_16x16x32_bf16 v[140:143], a[216:219], v[176:179], v[140:143]\n" \
    "s_add_u32 m0, s34, 6144\n" \
    "s_add_u32 s54, s48, 6144\n" \
    "buffer_load_dwordx4 v253, s[36:39], s54 offen sc1 lds\n" \
    "v_mfma_f32_16x16x32_bf16 v[144:147], v[176:179], v[24:27], v[144:147]\n" \
    "v_mfma_f32_16x16x32_bf16 v[148:151], v[176:179], v[88:91], v[148:151]\n" \
    "s_waitcnt lgkmcnt(2)\n" \
    "v_mfma_f32_16x16x32_bf16 v[160:163], a[152:155], v[180:183], v[160:163]\n" \
    "ds_read_b128 v[176:179], v253 offset:8192\n" \
    "v_mfma_f32_16x16x32_bf16 v[164:167], a[216:219], v[180:183], v[164:167]\n" \
    "s_waitcnt vmcnt(7)\n" \
    "s_barrier\n" \
    "s_cmp_eq_u32 s44, 0\n" \
    "s_cbranch_scc1 HG%=\n" \
    "v_mov_b32 v240, s44\n" \
    "buffer_store_dword v240, v243, s[36:39], s68 offen sc1\n" \
    "s_cmp_gt_u32 s44, 27\n" \
    "s_cbranch_scc1 HG%=\n" \
    "buffer_store_dword v234, v252, s[36:39], s57 offen sc1\n" \
    "HG%=:\n" \
    "s_add_u32 s57, s57, 8\n" \
    "v_mfma_f32_16x16x32_bf16 v[168:171], v[180:183], v[24:27], v[168:171]\n" \
    "v_mfma_f32_16x16x32_bf16 v[172:175], v[180:183], v[88:91], v[172:175]\n" \
    "s_waitcnt lgkmcnt(2)\n" \
    "v_mfma_f32_16x16x32_bf16 v[136:139], a[156:159], v[184:187], v[136:139]\n" \
    "ds_read_b128 v[180:183], v253 offset:24576\n" \
    "v_mfma_f32_16x16x32_bf16 v[140:143], a[220:223], v[184:187], v[140:143]\n" \
    "s_add_u32 m0, s34, 7168\n" \
    "s_add_u32 s54, s48, 7168\n" \
    "buffer_load_dwordx4 v253, s[36:39], s54 offen sc1 lds\n" \
    "v_mfma_f32_16x16x32_bf16 v[144:147], v[184:187], v[28:31], v[144:147]\n" \
    "v_mfma_f32_16x16x32_bf16 v[148:151], v[184:187], v[92:95], v[148:151]\n" \
    "s_waitcnt lgkmcnt(2)\n" \
    "v_mfma_f32_16x16x32_bf16 v[160:163], a[156:159], v[188:191], v[160:163]\n" \
    "ds_read_b128 v[184:187], v253 offset:9216\n" \
    "v_mfma_f32_16x16x32_bf16 v[164:167], a[220:223], v[188:191], v[164:167]\n" \
    "buffer_load_dword v232, v254, s[36:39], s58 offen sc1\n" \
    "v_mfma_f32_16x16x32_bf16 v[168:171], v[188:191], v[28:31], v[168:171]\n" \
    "v_mfma_f32_16x16x32_bf16 v[172:175], v[188:191], v[92:95], v[172:175]\n" \
    "s_waitcnt lgkmcnt(2)\n" \
    "v_mfma_f32_16x16x32_bf16 v[136:139], a[160:163], v[176:179], v[136:139]\n" \
    "ds_read_b128 v[188:191], v253 offset:25600\n" \
    "v_mfma_f32_16x16x32_bf16 v[140:143], a[224:227], v[176:179], v[140:143]\n" \
    "v_mfma_f32_16x16x32_bf16 v[144:147], v[176:179], v[32:35], v[144:147]\n" \
    "v_mfma_f32_16x16x32_bf16 v[148:151], v[176:179], v[96:99], v[148:151]\n" \
    "s_waitcnt lgkmcnt(2)\n" \
    "v_mfma_f32_16x16x32_bf16 v[160:163], a[160:163], v[180:183], v[160:163]\n" \
    "ds_read_b128 v[176:179], v253 offset:10240\n" \
    "v_mfma_f32_16x16x32_bf16 v[164:167], a[224:227], v[180:183], v[164:167]\n" \
    "s_xor_b32 s55, s28, 16384\n" \
    "s_lshl_b32 s54, s51, 2\n" \
    "s_add_u32 s54, s54, s55\n" \
    "s_add_u32 m0, s54, 0\n" \
    "s_add_u32 s54, s49, 0\n" \
    "buffer_load_dwordx4 v253, s[36:39], s54 offen sc1 lds\n" \
    "v_mfma_f32_16x16x32_bf16 v[168:171], v[180:183], v[32:35], v[168:171]\n" \
    "v_mfma_f32_16x16x32_bf16 v[172:175], v[180:183], v[96:99], v[172:175]\n" \
    "s_waitcnt lgkmcnt(2)\n" \
    "v_mfma_f32_16x16x32_bf16 v[136:139], a[164:167], v[184:187], v[136:139]\n" \
    "ds_read_b128 v[180:183], v253 offset:26624\n" \
    "v_mfma_f32_16x16x32_bf16 v[140:143], a[228:231], v[184:187], v[140:143]\n" \
    "v_mfma_f32_16x16x32_bf16 v[144:147], v[184:187], v[36:39], v[144:147]\n" \
    "v_mfma_f32_16x16x32_bf16 v[148:151], v[184:187], v[100:103], v[148:151]\n" \
    "s_waitcnt lgkmcnt(2)\n" \
    "v_mfma_f32_16x16x32_bf16 v[160:163], a[164:167], v[188:191], v[160:163]\n" \
    "ds_read_b128 v[184:187], v253 offset:11264\n" \
    "v_mfma_f32_16x16x32_bf16 v[164:167], a[228:231], v[188:191], v[164:167]\n" \
    "s_xor_b32 s55, s28, 16384\n" \
    "s_lshl_b32 s54, s51, 2\n" \
    "s_add_u32 s54, s54, s55\n" \
    "s_add_u32 m0, s54, 1024\n" \
    "s_add_u32 s54, s49, 1024\n" \
    "buffer_load_dwordx4 v253, s[36:39], s54 offen sc1 lds\n" \
    "v_mfma_f32_16x16x32_bf16 v[168:171], v[188:191], v[36:39], v[168:171]\n" \
    "v_mfma_f32_16x16x32_bf16 v[172:175], v[188:191], v[100:103], v[172:175]\n" \
    "s_waitcnt lgkmcnt(2)\n" \
    "v_mfma_f32_16x16x32_bf16 v[136:139], a[168:171], v[176:179], v[136:139]\n" \
    "ds_read_b128 v[188:191], v253 offset:27648\n" \
    "v_mfma_f32_16x16x32_bf16 v[140:143], a[232:235], v[176:179], v[140:143]\n" \
    "v_mfma_f32_16x16x32_bf16 v[144:147], v[176:179], v[40:43], v[144:147]\n" \
    "v_mfma_f32_16x16x32_bf16 v[148:151], v[176:179], v[104:107], v[148:151]\n" \
    "s_waitcnt lgkmcnt(2)\n" \
    "v_mfma_f32_16x16x32_bf16 v[160:163], a[168:171], v[180:183], v[160:163]\n" \
    "ds_read_b128 v[176:179], v253 offset:12288\n" \
    "v_mfma_f32_16x16x32_bf16 v[164:167], a[232:235], v[180:183], v[164:167]\n" \
    "s_xor_b32 s55, s28, 16384\n" \
    "s_lshl_b32 s54, s51, 2\n" \
    "s_add_u32 s54, s54, s55\n" \
    "s_add_u32 m0, s54, 2048\n" \
    "s_add_u32 s54, s49, 2048\n" \
    "buffer_load_dwordx4 v253, s[36:39], s54 offen sc1 lds\n" \
    "v_mfma_f32_16x16x32_bf16 v[168:171], v[180:183], v[40:43], v[168:171]\n" \
    "v_mfma_f32_16x16x32_bf16 v[172:175], v[180:183], v[104:107], v[172:175]\n" \
    "s_waitcnt lgkmcnt(2)\n" \
    "v_mfma_f32_16x16x32_bf16 v[136:139], a[172:175], v[184:187], v[136:139]\n" \
    "ds_read_b128 v[180:183], v253 offset:28672\n" \
    "v_mfma_f32_16x16x32_bf16 v[140:143], a[236:239], v[184:187], v[140:143]\n" \
    "v_mfma_f32_16x16x32_bf16 v[144:147], v[184:187], v[44:47], v[144:147]\n" \
    "v_mfma_f32_16x16x32_bf16 v[148:151], v[184:187], v[108:111], v[148:151]\n" \
    "s_waitcnt lgkmcnt(2)\n" \
    "v_mfma_f32_16x16x32_bf16 v[160:163], a[172:175], v[188:191], v[160:163]\n" \
    "ds_read_b128 v[184:187], v253 offset:13312\n" \
    "v_mfma_f32_16x16x32_bf16 v[164:167], a[236:239], v[188:191], v[164:167]\n" \
    "s_xor_b32 s55, s28, 16384\n" \
    "s_lshl_b32 s54, s51, 2\n" \
    "s_add_u32 s54, s54, s55\n" \
    "s_add_u32 m0, s54, 3072\n" \
    "s_add_u32 s54, s49, 3072\n" \
    "buffer_load_dwordx4 v253, s[36:39], s54 offen sc1 lds\n" \
    "v_mfma_f32_16x16x32_bf16 v[168:171], v[188:191], v[44:47], v[168:171]\n" \
    "v_mfma_f32_16x16x32_bf16 v[172:175], v[188:191], v[108:111], v[172:175]\n" \
    "s_waitcnt lgkmcnt(2)\n" \
    "v_mfma_f32_16x16x32_bf16 v[136:139], a[176:179], v[176:179], v[136:139]\n" \
    "ds_read_b128 v[188:191], v253 offset:29696\n" \
    "v_mfma_f32_16x16x32_bf16 v[140:143], a[240:243], v[176:179], v[140:143]\n" \
    "v_mfma_f32_16x16x32_bf16 v[144:147], v[176:179], v[48:51], v[144:147]\n" \
    "v_mfma_f32_16x16x32_bf16 v[148:151], v[176:179], v[112:115], v[148:151]\n" \
    "s_waitcnt lgkmcnt(2)\n" \
    "v_mfma_f32_16x16x32_bf16 v[160:163], a[176:179], v[180:183], v[160:163]\n" \
    "ds_read_b128 v[176:179], v253 offset:14336\n" \
    "v_mfma_f32_16x16x32_bf16 v[164:167], a[240:243], v[180:183], v[164:167]\n" \
    "v_mfma_f32_16x16x32_bf16 v[168:171], v[180:183], v[48:51], v[168:171]\n" \
    "v_mfma_f32_16x16x32_bf16 v[172:175], v[180:183], v[112:115], v[172:175]\n" \
    "s_waitcnt lgkmcnt(2)\n" \
    "v_mfma_f32_16x16x32_bf16 v[136:139], a[180:183], v[184:187], v[136:139]\n" \
    "ds_read_b128 v[180:183], v253 offset:30720\n" \
    "v_mfma_f32_16x16x32_bf16 v[140:143], a[244:247], v[184:187], v[140:143]\n" \
    "s_waitcnt vmcnt(4)\n" \
    "v_cmp_gt_u32 vcc, v233, v232\n" \
    "s_cbranch_vccnz HQ%=\n" \
    "buffer_load_dwordx4 v[204:207], v253, s[36:39], s50 offen sc1\n" \
    "buffer_load_dwordx4 v[208:211], v253, s[36:39], s50 offen offset:1024 sc1\n" \
    "buffer_load_dwordx4 v[212:215], v253, s[36:39], s50 offen offset:2048 sc1\n" \
    "buffer_load_dwordx4 v[216:219], v253, s[36:39], s50 offen offset:3072 sc1\n" \
    "s_and_b32 s54, s51, 1024\n" \
    "s_add_u32 s54, s54, s50\n" \
    "s_add_u32 s54, s54, 4096\n" \
    "buffer_load_dwordx2 v[224:225], v235, s[36:39], s54 offen sc1\n" \
    "buffer_load_dwordx2 v[228:229], v235, s[36:39], s54 offen offset:512 sc1\n" \
    "s_mov_b32 s35, 1\n" \
    "HQ%=:\n" \
    "v_mfma_f32_16x16x32_bf16 v[144:147], v[184:187], v[52:55], v[144:147]\n" \
    "v_mfma_f32_16x16x32_bf16 v[148:151], v[184:187], v[116:119], v[148:151]\n" \
    "s_waitcnt lgkmcnt(2)\n" \
    "v_mfma_f32_16x16x32_bf16 v[160:163], a[180:183], v[188:191], v[160:163]\n" \
    "ds_read_b128 v[184:187], v253 offset:15360\n" \
    "v_mfma_f32_16x16x32_bf16 v[164:167], a[244:247], v[188:191], v[164:167]\n" \
    "v_mfma_f32_16x16x32_bf16 v[168:171], v[188:191], v[52:55], v[168:171]\n" \
    "v_mfma_f32_16x16x32_bf16 v[172:175], v[188:191], v[116:119], v[172:175]\n" \
    "s_waitcnt lgkmcnt(2)\n" \
    "v_mfma_f32_16x16x32_bf16 v[136:139], a[184:187], v[176:179], v[136:139]\n" \
    "ds_read_b128 v[188:191], v253 offset:31744\n" \
    "v_mfma_f32_16x16x32_bf16 v[140:143], a[248:251], v[176:179], v[140:143]\n" \
    "v_mfma_f32_16x16x32_bf16 v[144:147], v[176:179], v[56:59], v[144:147]\n" \
    "v_mfma_f32_16x16x32_bf16 v[148:151], v[176:179], v[120:123], v[148:151]\n" \
    "s_waitcnt lgkmcnt(2)\n" \
    "v_mfma_f32_16x16x32_bf16 v[160:163], a[184:187], v[180:183], v[160:163]\n" \
    "v_mfma_f32_16x16x32_bf16 v[164:167], a[248:251], v[180:183], v[164:167]\n" \
    "v_mfma_f32_16x16x32_bf16 v[168:171], v[180:183], v[56:59], v[168:171]\n" \
    "v_mfma_f32_16x16x32_bf16 v[172:175], v[180:183], v[120:123], v[172:175]\n" \
    "s_waitcnt lgkmcnt(1)\n" \
    "v_mfma_f32_16x16x32_bf16 v[136:139], a[188:191], v[184:187], v[136:139]\n" \
    "v_mfma_f32_16x16x32_bf16 v[140:143], a[252:255], v[184:187], v[140:143]\n" \
    "v_mfma_f32_16x16x32_bf16 v[144:147], v[184:187], v[60:63], v[144:147]\n" \
    "v_mfma_f32_16x16x32_bf16 v[148:151], v[184:187], v[124:127], v[148:151]\n" \
    "s_waitcnt lgkmcnt(0)\n" \
    "v_mfma_f32_16x16x32_bf16 v[160:163], a[188:191], v[188:191], v[160:163]\n" \
    "v_mfma_f32_16x16x32_bf16 v[164:167], a[252:255], v[188:191], v[164:167]\n" \
    "v_mfma_f32_16x16x32_bf16 v[168:171], v[188:191], v[60:63], v[168:171]\n" \
    "v_mfma_f32_16x16x32_bf16 v[172:175], v[188:191], v[124:127], v[172:175]\n" \
    "s_add_u32 s54, s47, s51\n" \
    "s_add_u32 s55, s54, s51\n" \
    "s_add_u32 s55, s55, 8192\n" \
    "v_cvt_pk_bf16_f32 v196, v136, v137\n" \
    "v_cvt_pk_bf16_f32 v197, v138, v139\n" \
    "v_cvt_pk_bf16_f32 v198, v140, v141\n" \
    "v_cvt_pk_bf16_f32 v199, v142, v143\n" \
    "buffer_store_dwordx4 v[196:199], v253, s[36:39], s54 offen sc1\n" \
    "s_add_u32 s54, s54, 4096\n" \
    "v_cvt_pk_bf16_f32 v200, v160, v161\n" \
    "v_cvt_pk_bf16_f32 v201, v162, v163\n" \
    "v_cvt_pk_bf16_f32 v202, v164, v165\n" \
    "v_cvt_pk_bf16_f32 v203, v166, v167\n" \
    "buffer_store_dwordx4 v[200:203], v253, s[36:39], s54 offen sc1\n" \
    "v_cvt_pk_bf16_f32 v176, v144, v145\n" \
    "v_cvt_pk_bf16_f32 v177, v146, v147\n" \
    "v_cvt_pk_bf16_f32 v178, v168, v169\n" \
    "v_cvt_pk_bf16_f32 v179, v170, v171\n" \
    "buffer_store_dwordx4 v[176:179], v253, s[36:39], s55 offen sc1\n" \
    "v_cvt_pk_bf16_f32 v180, v148, v149\n" \
    "v_cvt_pk_bf16_f32 v181, v150, v151\n" \
    "v_cvt_pk_bf16_f32 v182, v172, v173\n" \
    "v_cvt_pk_bf16_f32 v183, v174, v175\n" \
    "buffer_store_dwordx4 v[180:183], v253, s[36:39], s55 offen offset:1024 sc1\n" \
    "s_add_u32 s47, s47, 32768\n" \
    "HA%=:\n" \
    "v_add_u32 v192, s29, v253\n" \
    "s_add_u32 s45, s45, 65536\n" \
    "s_add_u32 s48, s48, 65536\n" \
    "s_cmp_lt_u32 s44, 26\n" \
    "s_cselect_b32 s30, s45, s48\n" \
    "s_xor_b32 s31, s29, 32768\n" \
    "s_add_u32 s31, s31, s46\n" \
    "v_add_u32 v193, 98304, v255\n" \
    "v_add_u32 v194, s28, v253\n" \
    "v_add_u32 v195, s28, v242\n" \
    "ds_read_b128 v[144:147], v194\n" \
    "ds_read_b128 v[148:151], v194 offset:1024\n" \
    "ds_read_b128 v[152:155], v194 offset:2048\n" \
    "ds_read_b128 v[156:159], v194 offset:3072\n" \
    "ds_read_b128 v[160:163], v194 offset:4096\n" \
    "ds_read_b128 v[164:167], v194 offset:5120\n" \
    "ds_read_b128 v[168:171], v194 offset:6144\n" \
    "ds_read_b128 v[172:175], v194 offset:7168\n" \
    "s_barrier\n" \
    "ds_read_b128 v[184:187], v192\n" \
    "ds_read_b128 v[188:191], v192 offset:16384\n" \
    "ds_read_b128 v[196:199], v192 offset:1024\n" \
    "ds_read_b128 v[200:203], v192 offset:17408\n" \
    "ds_read_b128 v[176:179], v192 offset:2048\n" \
    "s_waitcnt lgkmcnt(4)\n" \
    "v_mfma_f32_16x16x32_bf16 v[128:131], a[0:3], v[184:187], 0\n" \
    "ds_read_b128 v[180:183], v192 offset:18432\n" \
    "v_mfma_f32_16x16x32_bf16 v[132:135], a[64:67], v[184:187], 0\n" \
    "s_waitcnt lgkmcnt(4)\n" \
    "v_mfma_f32_16x16x32_bf16 v[136:139], a[0:3], v[188:191], 0\n" \
    "ds_read_b128 v[184:187], v192 offset:3072\n" \
    "v_mfma_f32_16x16x32_bf16 v[140:143], a[64:67], v[188:191], 0\n" \
    "s_waitcnt lgkmcnt(4)\n" \
    "v_mfma_f32_16x16x32_bf16 v[128:131], a[4:7], v[196:199], v[128:131]\n" \
    "ds_read_b128 v[188:191], v192 offset:19456\n" \
    "v_mfma_f32_16x16x32_bf16 v[132:135], a[68:71], v[196:199], v[132:135]\n" \
    "s_waitcnt lgkmcnt(4)\n" \
    "v_mfma_f32_16x16x32_bf16 v[136:139], a[4:7], v[200:203], v[136:139]\n" \
    "ds_read_b128 v[196:199], v192 offset:4096\n" \
    "v_mfma_f32_16x16x32_bf16 v[140:143], a[68:71], v[200:203], v[140:143]\n" \
    "s_waitcnt lgkmcnt(4)\n" \
    "v_mfma_f32_16x16x32_bf16 v[128:131], a[8:11], v[176:179], v[128:131]\n" \
    "ds_read_b128 v[200:203], v192 offset:20480\n" \
    "v_mfma_f32_16x16x32_bf16 v[132:135], a[72:75], v[176:179], v[132:135]\n" \
    "s_add_u32 m0, s31, 0\n" \
    "s_add_u32 s54, s30, 0\n" \
    "buffer_load_dwordx4 v253, s[36:39], s54 offen sc1 lds\n" \
    "s_waitcnt lgkmcnt(4)\n" \
    "v_mfma_f32_16x16x32_bf16 v[136:139], a[8:11], v[180:183], v[136:139]\n" \
    "ds_read_b128 v[176:179], v192 offset:5120\n" \
    "v_mfma_f32_16x16x32_bf16 v[140:143], a[72:75], v[180:183], v[140:143]\n" \
    "s_waitcnt lgkmcnt(4)\n" \
    "v_mfma_f32_16x16x32_bf16 v[128:131], a[12:15], v[184:187], v[128:131]\n" \
    "ds_read_b128 v[180:183], v192 offset:21504\n" \
    "v_mfma_f32_16x16x32_bf16 v[132:135], a[76:79], v[184:187], v[132:135]\n" \
    "s_waitcnt lgkmcnt(4)\n" \
    "v_mfma_f32_16x16x32_bf16 v[136:139], a[12:15], v[188:191], v[136:139]\n" \
    "ds_read_b128 v[184:187], v192 offset:6144\n" \
    "v_mfma_f32_16x16x32_bf16 v[140:143], a[76:79], v[188:191], v[140:143]\n" \
    "s_waitcnt lgkmcnt(4)\n" \
    "v_mfma_f32_16x16x32_bf16 v[128:131], a[16:19], v[196:199], v[128:131]\n" \
    "ds_read_b128 v[188:191], v192 offset:22528\n" \
    "v_mfma_f32_16x16x32_bf16 v[132:135], a[80:83], v[196:199], v[132:135]\n" \
    "s_waitcnt lgkmcnt(4)\n" \
    "v_mfma_f32_16x16x32_bf16 v[136:139], a[16:19], v[200:203], v[136:139]\n" \
    "ds_read_b128 v[196:199], v192 offset:7168\n" \
    "v_mfma_f32_16x16x32_bf16 v[140:143], a[80:83], v[200:203], v[140:143]\n" \
    "s_add_u32 m0, s31, 1024\n" \
    "s_add_u32 s54, s30, 1024\n" \
    "buffer_load_dwordx4 v253, s[36:39], s54 offen sc1 lds\n" \
    "s_waitcnt lgkmcnt(4)\n" \
    "v_mfma_f32_16x16x32_bf16 v[128:131], a[20:23], v[176:179], v[128:131]\n" \
    "ds_read_b128 v[200:203], v192 offset:23552\n" \
    "v_mfma_f32_16x16x32_bf16 v[132:135], a[84:87], v[176:179], v[132:135]\n" \
    "s_waitcnt lgkmcnt(4)\n" \
    "v_mfma_f32_16x16x32_bf16 v[136:139], a[20:23], v[180:183], v[136:139]\n" \
    "ds_read_b128 v[176:179], v192 offset:8192\n" \
    "v_mfma_f32_16x16x32_bf16 v[140:143], a[84:87], v[180:183], v[140:143]\n" \
    "s_waitcnt lgkmcnt(4)\n" \
    "v_mfma_f32_16x16x32_bf16 v[128:131], a[24:27], v[184:187], v[128:131]\n" \
    "ds_read_b128 v[180:183], v192 offset:24576\n" \
    "v_mfma_f32_16x16x32_bf16 v[132:135], a[88:91], v[184:187], v[132:135]\n" \
    "s_waitcnt lgkmcnt(4)\n" \
    "v_mfma_f32_16x16x32_bf16 v[136:139], a[24:27], v[188:191], v[136:139]\n" \
    "ds_read_b128 v[184:187], v192 offset:9216\n" \
    "v_mfma_f32_16x16x32_bf16 v[140:143], a[88:91], v[188:191], v[140:143]\n" \
    "s_waitcnt lgkmcnt(4)\n" \
    "v_mfma_f32_16x16x32_bf16 v[128:131], a[28:31], v[196:199], v[128:131]\n" \
    "ds_read_b128 v[188:191], v192 offset:25600\n" \
    "v_mfma_f32_16x16x32_bf16 v[132:135], a[92:95], v[196:199], v[132:135]\n" \
    "s_add_u32 m0, s31, 2048\n" \
    "s_add_u32 s54, s30, 2048\n" \
    "buffer_load_dwordx4 v253, s[36:39], s54 offen sc1 lds\n" \
    "s_waitcnt lgkmcnt(4)\n" \
    "v_mfma_f32_16x16x32_bf16 v[136:139], a[28:31], v[200:203], v[136:139]\n" \
    "ds_read_b128 v[196:199], v192 offset:10240\n" \
    "v_mfma_f32_16x16x32_bf16 v[140:143], a[92:95], v[200:203], v[140:143]\n" \
    "s_waitcnt lgkmcnt(4)\n" \
    "v_mfma_f32_16x16x32_bf16 v[128:131], a[32:35], v[176:179], v[128:131]\n" \
    "ds_read_b128 v[200:203], v192 offset:26624\n" \
    "v_mfma_f32_16x16x32_bf16 v[132:135], a[96:99], v[176:179], v[132:135]\n" \
    "s_waitcnt lgkmcnt(4)\n" \
    "v_mfma_f32_16x16x32_bf16 v[136:139], a[32:35], v[180:183], v[136:139]\n" \
    "ds_read_b128 v[176:179], v192 offset:11264\n" \
    "v_mfma_f32_16x16x32_bf16 v[140:143], a[96:99], v[180:183], v[140:143]\n" \
    "s_waitcnt lgkmcnt(4)\n" \
    "v_mfma_f32_16x16x32_bf16 v[128:131], a[36:39], v[184:187], v[128:131]\n" \
    "ds_read_b128 v[180:183], v192 offset:27648\n" \
    "v_mfma_f32_16x16x32_bf16 v[132:135], a[100:103], v[184:187], v[132:135]\n" \
    "s_waitcnt lgkmcnt(4)\n" \
    "v_mfma_f32_16x16x32_bf16 v[136:139], a[36:39], v[188:191], v[136:139]\n" \
    "ds_read_b128 v[184:187], v192 offset:12288\n" \
    "v_mfma_f32_16x16x32_bf16 v[140:143], a[100:103], v[188:191], v[140:143]\n" \
    "s_add_u32 m0, s31, 3072\n" \
    "s_add_u32 s54, s30, 3072\n" \
    "buffer_load_dwordx4 v253, s[36:39], s54 offen sc1 lds\n" \
    "s_waitcnt lgkmcnt(4)\n" \
    "v_mfma_f32_16x16x32_bf16 v[128:131], a[40:43], v[196:199], v[128:131]\n" \
    "ds_read_b128 v[188:191], v192 offset:28672\n" \
    "v_mfma_f32_16x16x32_bf16 v[132:135], a[104:107], v[196:199], v[132:135]\n" \
    "s_waitcnt lgkmcnt(4)\n" \
    "v_mfma_f32_16x16x32_bf16 v[136:139], a[40:43], v[200:203], v[136:139]\n" \
    "ds_read_b128 v[196:199], v192 offset:13312\n" \
    "v_mfma_f32_16x16x32_bf16 v[140:143], a[104:107], v[200:203], v[140:143]\n" \
    "s_waitcnt lgkmcnt(4)\n" \
    "v_mfma_f32_16x16x32_bf16 v[128:131], a[44:47], v[176:179], v[128:131]\n" \
    "ds_read_b128 v[200:203], v192 offset:29696\n" \
    "v_mfma_f32_16x16x32_bf16 v[132:135], a[108:111], v[176:179], v[132:135]\n" \
    "s_waitcnt lgkmcnt(4)\n" \
    "v_mfma_f32_16x16x32_bf16 v[136:139], a[44:47], v[180:183], v[136:139]\n" \
    "ds_read_b128 v[176:179], v192 offset:14336\n" \
    "v_mfma_f32_16x16x32_bf16 v[140:143], a[108:111], v[180:183], v[140:143]\n" \
    "s_waitcnt lgkmcnt(4)\n" \
    "v_mfma_f32_16x16x32_bf16 v[128:131], a[48:51], v[184:187], v[128:131]\n" \
    "ds_read_b128 v[180:183], v192 offset:30720\n" \
    "v_mfma_f32_16x16x32_bf16 v[132:135], a[112:115], v[184:187], v[132:135]\n" \
    "s_add_u32 m0, s31, 4096\n" \
    "s_add_u32 s54, s30, 4096\n" \
    "buffer_load_dwordx4 v253, s[36:39], s54 offen sc1 lds\n" \
    "s_waitcnt lgkmcnt(4)\n" \
    "v_mfma_f32_16x16x32_bf16 v[136:139], a[48:51], v[188:191], v[136:139]\n" \
    "ds_read_b128 v[184:187], v192 offset:15360\n" \
    "v_mfma_f32_16x16x32_bf16 v[140:143], a[112:115], v[188:191], v[140:143]\n" \
    "s_waitcnt lgkmcnt(4)\n" \
    "v_mfma_f32_16x16x32_bf16 v[128:131], a[52:55], v[196:199], v[128:131]\n" \
    "ds_read_b128 v[188:191], v192 offset:31744\n" \
    "v_mfma_f32_16x16x32_bf16 v[132:135], a[116:119], v[196:199], v[132:135]\n" \
    "s_waitcnt lgkmcnt(4)\n" \
    "v_mfma_f32_16x16x32_bf16 v[136:139], a[52:55], v[200:203], v[136:139]\n" \
    "v_mfma_f32_16x16x32_bf16 v[140:143], a[116:119], v[200:203], v[140:143]\n" \
    "s_waitcnt lgkmcnt(3)\n" \
    "v_mfma_f32_16x16x32_bf16 v[128:131], a[56:59], v[176:179], v[128:131]\n" \
    "v_mfma_f32_16x16x32_bf16 v[132:135], a[120:123], v[176:179], v[132:135]\n" \
    "s_waitcnt lgkmcnt(2)\n" \
    "v_mfma_f32_16x16x32_bf16 v[136:139], a[56:59], v[180:183], v[136:139]\n" \
    "v_mfma_f32_16x16x32_bf16 v[140:143], a[120:123], v[180:183], v[140:143]\n" \
    "s_add_u32 m0, s31, 5120\n" \
    "s_add_u32 s54, s30, 5120\n" \
    "buffer_load_dwordx4 v253, s[36:39], s54 offen sc1 lds\n" \
    "s_waitcnt lgkmcnt(1)\n" \
    "v_mfma_f32_16x16x32_bf16 v[128:131], a[60:63], v[184:187], v[128:131]\n" \
    "v_mfma_f32_16x16x32_bf16 v[132:135], a[124:127], v[184:187], v[132:135]\n" \
    "s_waitcnt lgkmcnt(0)\n" \
    "v_mfma_f32_16x16x32_bf16 v[136:139], a[60:63], v[188:191], v[136:139]\n" \
    "v_mfma_f32_16x16x32_bf16 v[140:143], a[124:127], v[188:191], v[140:143]\n" \
    "ds_read_b128 v[176:179], v195 offset:8192\n" \
    "ds_read_b128 v[180:183], v195 offset:9216\n" \
    "ds_read_b128 v[184:187], v195 offset:10240\n" \
    "ds_read_b128 v[188:191], v195 offset:11264\n" \
    "v_add_u32 v192, s51, v253\n" \
    "v_add_u32 v192, 98304, v192\n" \
    "v_cvt_pk_bf16_f32 v128, v128, v129\n" \
    "v_cvt_pk_bf16_f32 v129, v130, v131\n" \
    "v_cvt_pk_bf16_f32 v130, v132, v133\n" \
    "v_cvt_pk_bf16_f32 v131, v134, v135\n" \
    "ds_write_b128 v192, v[128:131]\n" \
    "v_cvt_pk_bf16_f32 v136, v136, v137\n" \
    "v_cvt_pk_bf16_f32 v137, v138, v139\n" \
    "v_cvt_pk_bf16_f32 v138, v140, v141\n" \
    "v_cvt_pk_bf16_f32 v139, v142, v143\n" \
    "ds_write_b128 v192, v[136:139] offset:4096\n" \
    "s_waitcnt lgkmcnt(0)\n" \
    "s_barrier\n" \
    "ds_read_b128 v[128:131], v193\n" \
    "ds_read_b128 v[132:135], v193 offset:1024\n" \
    "ds_read_b128 v[136:139], v193 offset:2048\n" \
    "ds_read_b128 v[140:143], v193 offset:3072\n" \
    "s_waitcnt lgkmcnt(0)\n" \
    "v_mfma_f32_16x16x32_bf16 v[144:147], v[144:147], v[128:131], v[244:247]\n" \
    "v_mfma_f32_16x16x32_bf16 v[144:147], v[148:151], v[132:135], v[144:147]\n" \
    "v_mfma_f32_16x16x32_bf16 v[144:147], v[152:155], v[136:139], v[144:147]\n" \
    "v_mfma_f32_16x16x32_bf16 v[144:147], v[156:159], v[140:143], v[144:147]\n" \
    "s_add_u32 m0, s31, 6144\n" \
    "s_add_u32 s54, s30, 6144\n" \
    "buffer_load_dwordx4 v253, s[36:39], s54 offen sc1 lds\n" \
    "v_mfma_f32_16x16x32_bf16 v[160:163], v[160:163], v[128:131], v[248:251]\n" \
    "v_mfma_f32_16x16x32_bf16 v[160:163], v[164:167], v[132:135], v[160:163]\n" \
    "v_mfma_f32_16x16x32_bf16 v[160:163], v[168:171], v[136:139], v[160:163]\n" \
    "v_mfma_f32_16x16x32_bf16 v[160:163], v[172:175], v[140:143], v[160:163]\n" \
    "s_add_u32 m0, s31, 7168\n" \
    "s_add_u32 s54, s30, 7168\n" \
    "buffer_load_dwordx4 v253, s[36:39], s54 offen sc1 lds\n" \
    "s_cmp_eq_u32 s35, 1\n" \
    "s_cbranch_scc1 HR%=\n" \
    "s_cmp_lt_u32 s44, 29\n" \
    "s_cbranch_scc1 HS%=\n" \
    "HT%=:\n" \
    "buffer_load_dwordx4 v[204:207], v253, s[36:39], s50 offen sc1\n" \
    "buffer_load_dwordx4 v[208:211], v253, s[36:39], s50 offen offset:1024 sc1\n" \
    "buffer_load_dwordx4 v[212:215], v253, s[36:39], s50 offen offset:2048 sc1\n" \
    "buffer_load_dwordx4 v[216:219], v253, s[36:39], s50 offen offset:3072 sc1\n" \
    "s_and_b32 s54, s51, 1024\n" \
    "s_add_u32 s54, s54, s50\n" \
    "s_add_u32 s54, s54, 4096\n" \
    "buffer_load_dwordx2 v[224:225], v235, s[36:39], s54 offen sc1\n" \
    "buffer_load_dwordx2 v[228:229], v235, s[36:39], s54 offen offset:512 sc1\n" \
    "buffer_load_dword v232, v254, s[36:39], s58 offen sc1\n" \
    "s_waitcnt vmcnt(0)\n" \
    "s_cmp_eq_u32 s35, 1\n" \
    "s_cbranch_scc1 HR%=\n" \
    "v_max3_u32 v220, v224, v225, v228\n" \
    "v_max3_u32 v220, v220, v229, v204\n" \
    "v_max_u32 v220, v220, v205\n" \
    "v_max3_u32 v220, v220, v206, v207\n" \
    "v_max3_u32 v220, v220, v208, v209\n" \
    "v_max3_u32 v220, v220, v210, v211\n" \
    "v_max3_u32 v220, v220, v212, v213\n" \
    "v_max3_u32 v220, v220, v214, v215\n" \
    "v_max3_u32 v220, v220, v216, v217\n" \
    "v_max3_u32 v220, v220, v218, v219\n" \
    "v_cmp_eq_u32 vcc, -1, v220\n" \
    "s_cbranch_vccz HR%=\n" \
    "v_cmp_gt_u32 vcc, v233, v232\n" \
    "s_cmp_eq_u64 vcc, 0\n" \
    "s_cselect_b32 s35, 1, 0\n" \
    "s_sleep 1\n" \
    "s_add_u32 s56, s56, 1\n" \
    "s_cmp_lt_u32 s56, 262144\n" \
    "s_cbranch_scc1 HT%=\n" \
    "s_branch HR%=\n" \
    "HS%=:\n" \
    "HKa%=:\n" \
    "buffer_load_dword v232, v254, s[36:39], s58 offen sc1\n" \
    "s_waitcnt vmcnt(0)\n" \
    "v_cmp_gt_u32 vcc, v233, v232\n" \
    "s_cbranch_vccz HKb%=\n" \
    "s_sleep 4\n" \
    "s_add_u32 s56, s56, 1\n" \
    "s_cmp_lt_u32 s56, 262144\n" \
    "s_cbranch_scc1 HKa%=\n" \
    "HKb%=:\n" \
    "buffer_load_dwordx4 v[204:207], v253, s[36:39], s50 offen sc1\n" \
    "buffer_load_dwordx4 v[208:211], v253, s[36:39], s50 offen offset:1024 sc1\n" \
    "buffer_load_dwordx4 v[212:215], v253, s[36:39], s50 offen offset:2048 sc1\n" \
    "buffer_load_dwordx4 v[216:219], v253, s[36:39], s50 offen offset:3072 sc1\n" \
    "s_and_b32 s54, s51, 1024\n" \
    "s_add_u32 s54, s54, s50\n" \
    "s_add_u32 s54, s54, 4096\n" \
    "buffer_load_dwordx2 v[224:225], v235, s[36:39], s54 offen sc1\n" \
    "buffer_load_dwordx2 v[228:229], v235, s[36:39], s54 offen offset:512 sc1\n" \
    "s_waitcnt vmcnt(0)\n" \
    "HR%=:\n" \
    "s_waitcnt vmcnt(12)\n" \
    "v_mfma_f32_16x16x32_bf16 v[204:207], v[204:207], v[128:131], 0\n" \
    "v_mfma_f32_16x16x32_bf16 v[204:207], v[208:211], v[132:135], v[204:207]\n" \
    "v_mfma_f32_16x16x32_bf16 v[204:207], v[212:215], v[136:139], v[204:207]\n" \
    "v_mfma_f32_16x16x32_bf16 v[204:207], v[216:219], v[140:143], v[204:207]\n" \
    "s_nop 3\n" \
    "v_max3_f32 v220, v144, v145, v146\n" \
    "v_max3_f32 v221, v160, v161, v162\n" \
    "v_max3_f32 v220, v220, v147, v163\n" \
    "v_max_f32 v220, v220, v221\n" \
    "v_max3_f32 v222, v220, v204, v205\n" \
    "v_max3_f32 v223, v220, v204, v205\n" \
    "v_mov_b32 v226, v225\n" \
    "v_mov_b32 v227, 0\n" \
    "v_permlane32_swap_b32 v222, v223\n" \
    "v_max_f32 v220, v222, v223\n" \
    "v_max_f32 v221, v222, v223\n" \
    "v_mov_b32 v230, v229\n" \
    "v_mov_b32 v231, 0\n" \
    "v_permlane16_swap_b32 v220, v221\n" \
    "v_max_f32 v222, v220, v221\n" \
    "v_mul_f32 v222, s53, v222\n" \
    "v_pk_fma_f32 v[204:205], v[204:205], s[52:53], v[222:223] op_sel:[0,1,0] op_sel_hi:[1,1,0] neg_lo:[0,0,1] neg_hi:[0,0,1]\n" \
    "v_pk_fma_f32 v[144:145], v[144:145], s[52:53], v[222:223] op_sel:[0,1,0] op_sel_hi:[1,1,0] neg_lo:[0,0,1] neg_hi:[0,0,1]\n" \
    "v_pk_fma_f32 v[146:147], v[146:147], s[52:53], v[222:223] op_sel:[0,1,0] op_sel_hi:[1,1,0] neg_lo:[0,0,1] neg_hi:[0,0,1]\n" \
    "v_pk_fma_f32 v[160:161], v[160:161], s[52:53], v[222:223] op_sel:[0,1,0] op_sel_hi:[1,1,0] neg_lo:[0,0,1] neg_hi:[0,0,1]\n" \
    "v_pk_fma_f32 v[162:163], v[162:163], s[52:53], v[222:223] op_sel:[0,1,0] op_sel_hi:[1,1,0] neg_lo:[0,0,1] neg_hi:[0,0,1]\n" \
    "v_exp_f32 v204, v204\n" \
    "v_exp_f32 v205, v205\n" \
    "v_exp_f32 v144, v144\n" \
    "v_exp_f32 v145, v145\n" \
    "v_exp_f32 v146, v146\n" \
    "v_exp_f32 v147, v147\n" \
    "v_exp_f32 v160, v160\n" \
    "v_exp_f32 v161, v161\n" \
    "v_exp_f32 v162, v162\n" \
    "v_exp_f32 v163, v163\n" \
    "v_cvt_pk_bf16_f32 v148, v144, v145\n" \
    "v_cvt_pk_bf16_f32 v149, v146, v147\n" \
    "v_cvt_pk_bf16_f32 v150, v160, v161\n" \
    "v_cvt_pk_bf16_f32 v151, v162, v163\n" \
    "v_cvt_pk_bf16_f32 v240, v204, v205\n" \
    "s_waitcnt lgkmcnt(0)\n" \
    "v_mfma_f32_16x16x32_bf16 v[152:155], v[236:239], v[148:151], 0\n" \
    "v_mfma_f32_16x16x32_bf16 v[176:179], v[176:179], v[148:151], 0\n" \
    "v_mfma_f32_16x16x32_bf16 v[180:183], v[180:183], v[148:151], 0\n" \
    "v_mfma_f32_16x16x32_bf16 v[184:187], v[184:187], v[148:151], 0\n" \
    "v_mfma_f32_16x16x32_bf16 v[188:191], v[188:191], v[148:151], 0\n" \
    "s_nop 1\n" \
    "v_mfma_f32_16x16x16_bf16 v[152:155], v[236:237], v[240:241], v[152:155]\n" \
    "v_mfma_f32_16x16x16_bf16 v[176:179], v[224:225], v[240:241], v[176:179]\n" \
    "v_mfma_f32_16x16x16_bf16 v[180:183], v[226:227], v[240:241], v[180:183]\n" \
    "v_mfma_f32_16x16x16_bf16 v[184:187], v[228:229], v[240:241], v[184:187]\n" \
    "v_mfma_f32_16x16x16_bf16 v[188:191], v[230:231], v[240:241], v[188:191]\n" \
    "s_add_u32 s50, s50, 16384\n" \
    "v_add_u32 v233, 2, v233\n" \
    "v_or_b32 v243, v243, v243\n" \
    "v_or_b32 v243, v243, v243\n" \
    "v_rcp_f32 v220, v152\n" \
    "v_or_b32 v243, v243, v243\n" \
    "v_pk_mul_f32 v[176:177], v[176:177], v[220:221] op_sel_hi:[1,0]\n" \
    "v_pk_mul_f32 v[178:179], v[178:179], v[220:221] op_sel_hi:[1,0]\n" \
    "v_pk_mul_f32 v[180:181], v[180:181], v[220:221] op_sel_hi:[1,0]\n" \
    "v_pk_mul_f32 v[182:183], v[182:183], v[220:221] op_sel_hi:[1,0]\n" \
    "v_pk_mul_f32 v[184:185], v[184:185], v[220:221] op_sel_hi:[1,0]\n" \
    "v_pk_mul_f32 v[186:187], v[186:187], v[220:221] op_sel_hi:[1,0]\n" \
    "v_pk_mul_f32 v[188:189], v[188:189], v[220:221] op_sel_hi:[1,0]\n" \
    "v_pk_mul_f32 v[190:191], v[190:191], v[220:221] op_sel_hi:[1,0]\n" \
    "v_cvt_pk_bf16_f32 v128, v176, v177\n" \
    "v_cvt_pk_bf16_f32 v129, v178, v179\n" \
    "v_cvt_pk_bf16_f32 v130, v180, v181\n" \
    "v_cvt_pk_bf16_f32 v131, v182, v183\n" \
    "v_cvt_pk_bf16_f32 v132, v184, v185\n" \
    "v_cvt_pk_bf16_f32 v133, v186, v187\n" \
    "v_cvt_pk_bf16_f32 v134, v188, v189\n" \
    "v_cvt_pk_bf16_f32 v135, v190, v191\n" \
    "buffer_store_dwordx4 v[128:131], v253, s[36:39], s59 offen sc1\n" \
    "buffer_store_dwordx4 v[132:135], v253, s[36:39], s59 offen offset:1024 sc1\n" \
    "s_add_u32 s59, s59, 524288\n" \
    "s_add_u32 s44, s44, 1\n" \
    "s_branch HL%=\n" \
    "HE%=:\n" \
    "v_mov_b32 v240, 32\n" \
    "buffer_store_dword v240, v243, s[36:39], s68 offen sc1\n" \
    "v_mov_b32 v220, 33\n" \
    "s_waitcnt vmcnt(0)\n" \
    "s_barrier\n" \
    "buffer_store_dword v220, v243, s[36:39], s68 offen sc1\n" \
    "s_waitcnt vmcnt(0)\n" \
    "s_endpgm\n" \
    "RE_h:\n"

#define ROLE_FO \
    "RS_fo:\n" \
    "s_getpc_b64 s[2:3]\n" \
    "v_lshlrev_b32 v0, 3, v253\n" \
    "s_mov_b32 s0, RE_fo-RS_fo-8\n" \
    "v_add_u32 v1, 0, v0\n" \
    "v_min_u32 v1, s0, v1\n" \
    "global_load_dword v2, v1, s[2:3]\n" \
    "v_add_u32 v1, 8192, v0\n" \
    "v_min_u32 v1, s0, v1\n" \
    "global_load_dword v3, v1, s[2:3]\n" \
    "v_add_u32 v1, 16384, v0\n" \
    "v_min_u32 v1, s0, v1\n" \
    "global_load_dword v4, v1, s[2:3]\n" \
    "v_add_u32 v1, 24576, v0\n" \
    "v_min_u32 v1, s0, v1\n" \
    "global_load_dword v5, v1, s[2:3]\n" \
    "v_mov_b32 v251, 1\n" \
    "s_waitcnt vmcnt(0)\n" \
    "v_add_u32 v240, 1, v240\n" \
    "v_add_u32 v241, 8, v241\n" \
    "v_or_b32 v241, 7, v241\n" \
    "v_lshlrev_b32 v230, 3, v253\n" \
    "s_mov_b32 s54, 2163712\n" \
    "buffer_load_dwordx2 v[228:229], v230, s[36:39], s54 offen sc1\n" \
    "s_waitcnt vmcnt(0)\n" \
    "s_mov_b32 s54, 2163712\n" \
    "WIa1024%=:\n" \
    "v_or_b32 v229, 7, v229\n" \
    "v_cmp_ne_u64 vcc, v[240:241], v[228:229]\n" \
    "s_cbranch_vccz WIb1024%=\n" \
    "s_sleep 2\n" \
    "buffer_load_dwordx2 v[228:229], v230, s[36:39], s54 offen sc1\n" \
    "s_waitcnt vmcnt(0)\n" \
    "s_add_u32 s56, s56, 1\n" \
    "s_cmp_lt_u32 s56, 262144\n" \
    "s_cbranch_scc1 WIa1024%=\n" \
    "WIb1024%=:\n" \
    "s_cmp_eq_u32 s49, 0\n" \
    "s_cbranch_scc1 FE%=\n" \
    "s_add_u32 m0, s46, 0\n" \
    "s_add_u32 s54, s45, 0\n" \
    "buffer_load_dwordx4 v253, s[36:39], s54 offen sc1 lds\n" \
    "s_add_u32 m0, s46, 1024\n" \
    "s_add_u32 s54, s45, 1024\n" \
    "buffer_load_dwordx4 v253, s[36:39], s54 offen sc1 lds\n" \
    "s_add_u32 m0, s46, 2048\n" \
    "s_add_u32 s54, s45, 2048\n" \
    "buffer_load_dwordx4 v253, s[36:39], s54 offen sc1 lds\n" \
    "s_add_u32 m0, s46, 3072\n" \
    "s_add_u32 s54, s45, 3072\n" \
    "buffer_load_dwordx4 v253, s[36:39], s54 offen sc1 lds\n" \
    "s_add_u32 m0, s46, 4096\n" \
    "s_add_u32 s54, s45, 4096\n" \
    "buffer_load_dwordx4 v253, s[36:39], s54 offen sc1 lds\n" \
    "s_add_u32 m0, s46, 5120\n" \
    "s_add_u32 s54, s45, 5120\n" \
    "buffer_load_dwordx4 v253, s[36:39], s54 offen sc1 lds\n" \
    "s_add_u32 m0, s46, 6144\n" \
    "s_add_u32 s54, s45, 6144\n" \
    "buffer_load_dwordx4 v253, s[36:39], s54 offen sc1 lds\n" \
    "s_add_u32 m0, s46, 7168\n" \
    "s_add_u32 s54, s45, 7168\n" \
    "buffer_load_dwordx4 v253, s[36:39], s54 offen sc1 lds\n" \
    "s_mov_b32 s42, 1835008\n" \
    "s_mov_b32 s43, 0x20000\n" \
    "s_mov_b32 s40, s60\n" \
    "s_and_b32 s41, s61, 0xffff\n" \
    "s_add_u32 m0, s51, 32768\n" \
    "s_mov_b32 s54, 0\n" \
    "buffer_load_dwordx4 v216, s[40:43], s54 offen lds\n" \
    "s_sleep 6\n" \
    "s_add_u32 m0, s51, 36864\n" \
    "s_mov_b32 s54, 57344\n" \
    "buffer_load_dwordx4 v216, s[40:43], s54 offen lds\n" \
    "s_sleep 6\n" \
    "s_add_u32 m0, s51, 40960\n" \
    "s_mov_b32 s54, 114688\n" \
    "buffer_load_dwordx4 v216, s[40:43], s54 offen lds\n" \
    "s_sleep 6\n" \
    "s_add_u32 m0, s51, 45056\n" \
    "s_mov_b32 s54, 172032\n" \
    "buffer_load_dwordx4 v216, s[40:43], s54 offen lds\n" \
    "s_sleep 6\n" \
    "s_add_u32 m0, s51, 49152\n" \
    "s_mov_b32 s54, 229376\n" \
    "buffer_load_dwordx4 v216, s[40:43], s54 offen lds\n" \
    "s_sleep 6\n" \
    "s_add_u32 m0, s51, 53248\n" \
    "s_mov_b32 s54, 286720\n" \
    "buffer_load_dwordx4 v216, s[40:43], s54 offen lds\n" \
    "s_sleep 6\n" \
    "s_add_u32 m0, s51, 57344\n" \
    "s_mov_b32 s54, 344064\n" \
    "buffer_load_dwordx4 v216, s[40:43], s54 offen lds\n" \
    "s_sleep 6\n" \
    "s_add_u32 m0, s51, 61440\n" \
    "s_mov_b32 s54, 401408\n" \
    "buffer_load_dwordx4 v216, s[40:43], s54 offen lds\n" \
    "s_sleep 6\n" \
    "s_add_u32 m0, s51, 65536\n" \
    "s_mov_b32 s54, 458752\n" \
    "buffer_load_dwordx4 v216, s[40:43], s54 offen lds\n" \
    "s_sleep 6\n" \
    "s_add_u32 m0, s51, 69632\n" \
    "s_mov_b32 s54, 516096\n" \
    "buffer_load_dwordx4 v216, s[40:43], s54 offen lds\n" \
    "s_sleep 6\n" \
    "s_add_u32 m0, s51, 73728\n" \
    "s_mov_b32 s54, 573440\n" \
    "buffer_load_dwordx4 v216, s[40:43], s54 offen lds\n" \
    "s_sleep 6\n" \
    "s_add_u32 m0, s51, 77824\n" \
    "s_mov_b32 s54, 630784\n" \
    "buffer_load_dwordx4 v216, s[40:43], s54 offen lds\n" \
    "s_sleep 6\n" \
    "s_add_u32 m0, s51, 81920\n" \
    "s_mov_b32 s54, 688128\n" \
    "buffer_load_dwordx4 v216, s[40:43], s54 offen lds\n" \
    "s_sleep 6\n" \
    "s_add_u32 m0, s51, 86016\n" \
    "s_mov_b32 s54, 745472\n" \
    "buffer_load_dwordx4 v216, s[40:43], s54 offen lds\n" \
    "s_sleep 6\n" \
    "s_add_u32 m0, s51, 90112\n" \
    "s_mov_b32 s54, 802816\n" \
    "buffer_load_dwordx4 v216, s[40:43], s54 offen lds\n" \
    "s_sleep 6\n" \
    "s_add_u32 m0, s51, 94208\n" \
    "s_mov_b32 s54, 860160\n" \
    "buffer_load_dwordx4 v216, s[40:43], s54 offen lds\n" \
    "s_sleep 6\n" \
    "s_add_u32 m0, s51, 98304\n" \
    "s_mov_b32 s54, 917504\n" \
    "buffer_load_dwordx4 v216, s[40:43], s54 offen lds\n" \
    "s_sleep 6\n" \
    "s_add_u32 m0, s51, 102400\n" \
    "s_mov_b32 s54, 974848\n" \
    "buffer_load_dwordx4 v216, s[40:43], s54 offen lds\n" \
    "s_sleep 6\n" \
    "s_add_u32 m0, s51, 106496\n" \
    "s_mov_b32 s54, 1032192\n" \
    "buffer_load_dwordx4 v216, s[40:43], s54 offen lds\n" \
    "s_sleep 6\n" \
    "s_add_u32 m0, s51, 110592\n" \
    "s_mov_b32 s54, 1089536\n" \
    "buffer_load_dwordx4 v216, s[40:43], s54 offen lds\n" \
    "s_sleep 6\n" \
    "s_add_u32 m0, s51, 114688\n" \
    "s_mov_b32 s54, 1146880\n" \
    "buffer_load_dwordx4 v216, s[40:43], s54 offen lds\n" \
    "s_sleep 6\n" \
    "s_add_u32 m0, s51, 118784\n" \
    "s_mov_b32 s54, 1204224\n" \
    "buffer_load_dwordx4 v216, s[40:43], s54 offen lds\n" \
    "s_sleep 6\n" \
    "s_add_u32 m0, s51, 122880\n" \
    "s_mov_b32 s54, 1261568\n" \
    "buffer_load_dwordx4 v216, s[40:43], s54 offen lds\n" \
    "s_sleep 6\n" \
    "s_add_u32 m0, s51, 126976\n" \
    "s_mov_b32 s54, 1318912\n" \
    "buffer_load_dwordx4 v216, s[40:43], s54 offen lds\n" \
    "s_sleep 6\n" \
    "s_add_u32 m0, s51, 131072\n" \
    "s_mov_b32 s54, 1376256\n" \
    "buffer_load_dwordx4 v216, s[40:43], s54 offen lds\n" \
    "s_sleep 6\n" \
    "s_add_u32 m0, s51, 135168\n" \
    "s_mov_b32 s54, 1433600\n" \
    "buffer_load_dwordx4 v216, s[40:43], s54 offen lds\n" \
    "s_sleep 6\n" \
    "s_add_u32 m0, s51, 139264\n" \
    "s_mov_b32 s54, 1490944\n" \
    "buffer_load_dwordx4 v216, s[40:43], s54 offen lds\n" \
    "s_sleep 6\n" \
    "s_add_u32 m0, s51, 143360\n" \
    "s_mov_b32 s54, 1548288\n" \
    "buffer_load_dwordx4 v216, s[40:43], s54 offen lds\n" \
    "s_sleep 6\n" \
    "s_add_u32 m0, s51, 147456\n" \
    "s_mov_b32 s54, 1605632\n" \
    "buffer_load_dwordx4 v216, s[40:43], s54 offen lds\n" \
    "s_sleep 6\n" \
    "s_add_u32 m0, s51, 151552\n" \
    "s_mov_b32 s54, 1662976\n" \
    "buffer_load_dwordx4 v216, s[40:43], s54 offen lds\n" \
    "s_sleep 6\n" \
    "s_add_u32 m0, s51, 155648\n" \
    "s_mov_b32 s54, 1720320\n" \
    "buffer_load_dwordx4 v216, s[40:43], s54 offen lds\n" \
    "s_sleep 6\n" \
    "s_add_u32 m0, s51, 159744\n" \
    "s_mov_b32 s54, 1777664\n" \
    "buffer_load_dwordx4 v216, s[40:43], s54 offen lds\n" \
    "s_sleep 6\n" \
    "s_waitcnt vmcnt(16)\n" \
    "s_barrier\n" \
    "v_add_u32 v223, 32768, v217\n" \
    "ds_read_b64_tr_b16 a[0:1], v223\n" \
    "ds_read_b64_tr_b16 a[2:3], v223 offset:1024\n" \
    "ds_read_b64_tr_b16 a[4:5], v223 offset:8192\n" \
    "ds_read_b64_tr_b16 a[6:7], v223 offset:9216\n" \
    "ds_read_b64_tr_b16 a[8:9], v223 offset:16384\n" \
    "ds_read_b64_tr_b16 a[10:11], v223 offset:17408\n" \
    "ds_read_b64_tr_b16 a[12:13], v223 offset:24576\n" \
    "ds_read_b64_tr_b16 a[14:15], v223 offset:25600\n" \
    "s_waitcnt lgkmcnt(0)\n" \
    "ds_read_b64_tr_b16 a[16:17], v223 offset:32768\n" \
    "ds_read_b64_tr_b16 a[18:19], v223 offset:33792\n" \
    "ds_read_b64_tr_b16 a[20:21], v223 offset:40960\n" \
    "ds_read_b64_tr_b16 a[22:23], v223 offset:41984\n" \
    "ds_read_b64_tr_b16 a[24:25], v223 offset:49152\n" \
    "ds_read_b64_tr_b16 a[26:27], v223 offset:50176\n" \
    "ds_read_b64_tr_b16 a[28:29], v223 offset:57344\n" \
    "ds_read_b64_tr_b16 a[30:31], v223 offset:58368\n" \
    "s_waitcnt lgkmcnt(0)\n" \
    "v_add_u32 v223, 32768, v218\n" \
    "ds_read_b64_tr_b16 a[64:65], v223\n" \
    "ds_read_b64_tr_b16 a[66:67], v223 offset:1024\n" \
    "ds_read_b64_tr_b16 a[68:69], v223 offset:8192\n" \
    "ds_read_b64_tr_b16 a[70:71], v223 offset:9216\n" \
    "ds_read_b64_tr_b16 a[72:73], v223 offset:16384\n" \
    "ds_read_b64_tr_b16 a[74:75], v223 offset:17408\n" \
    "ds_read_b64_tr_b16 a[76:77], v223 offset:24576\n" \
    "ds_read_b64_tr_b16 a[78:79], v223 offset:25600\n" \
    "s_waitcnt lgkmcnt(0)\n" \
    "ds_read_b64_tr_b16 a[80:81], v223 offset:32768\n" \
    "ds_read_b64_tr_b16 a[82:83], v223 offset:33792\n" \
    "ds_read_b64_tr_b16 a[84:85], v223 offset:40960\n" \
    "ds_read_b64_tr_b16 a[86:87], v223 offset:41984\n" \
    "ds_read_b64_tr_b16 a[88:89], v223 offset:49152\n" \
    "ds_read_b64_tr_b16 a[90:91], v223 offset:50176\n" \
    "ds_read_b64_tr_b16 a[92:93], v223 offset:57344\n" \
    "ds_read_b64_tr_b16 a[94:95], v223 offset:58368\n" \
    "s_waitcnt lgkmcnt(0)\n" \
    "s_barrier\n" \
    "s_mov_b32 s40, s62\n" \
    "s_and_b32 s41, s63, 0xffff\n" \
    "s_add_u32 m0, s51, 32768\n" \
    "s_mov_b32 s54, 0\n" \
    "buffer_load_dwordx4 v216, s[40:43], s54 offen lds\n" \
    "s_sleep 6\n" \
    "s_add_u32 m0, s51, 36864\n" \
    "s_mov_b32 s54, 57344\n" \
    "buffer_load_dwordx4 v216, s[40:43], s54 offen lds\n" \
    "s_sleep 6\n" \
    "s_add_u32 m0, s51, 40960\n" \
    "s_mov_b32 s54, 114688\n" \
    "buffer_load_dwordx4 v216, s[40:43], s54 offen lds\n" \
    "s_sleep 6\n" \
    "s_add_u32 m0, s51, 45056\n" \
    "s_mov_b32 s54, 172032\n" \
    "buffer_load_dwordx4 v216, s[40:43], s54 offen lds\n" \
    "s_sleep 6\n" \
    "s_add_u32 m0, s51, 49152\n" \
    "s_mov_b32 s54, 229376\n" \
    "buffer_load_dwordx4 v216, s[40:43], s54 offen lds\n" \
    "s_sleep 6\n" \
    "s_add_u32 m0, s51, 53248\n" \
    "s_mov_b32 s54, 286720\n" \
    "buffer_load_dwordx4 v216, s[40:43], s54 offen lds\n" \
    "s_sleep 6\n" \
    "s_add_u32 m0, s51, 57344\n" \
    "s_mov_b32 s54, 344064\n" \
    "buffer_load_dwordx4 v216, s[40:43], s54 offen lds\n" \
    "s_sleep 6\n" \
    "s_add_u32 m0, s51, 61440\n" \
    "s_mov_b32 s54, 401408\n" \
    "buffer_load_dwordx4 v216, s[40:43], s54 offen lds\n" \
    "s_sleep 6\n" \
    "s_add_u32 m0, s51, 65536\n" \
    "s_mov_b32 s54, 458752\n" \
    "buffer_load_dwordx4 v216, s[40:43], s54 offen lds\n" \
    "s_sleep 6\n" \
    "s_add_u32 m0, s51, 69632\n" \
    "s_mov_b32 s54, 516096\n" \
    "buffer_load_dwordx4 v216, s[40:43], s54 offen lds\n" \
    "s_sleep 6\n" \
    "s_add_u32 m0, s51, 73728\n" \
    "s_mov_b32 s54, 573440\n" \
    "buffer_load_dwordx4 v216, s[40:43], s54 offen lds\n" \
    "s_sleep 6\n" \
    "s_add_u32 m0, s51, 77824\n" \
    "s_mov_b32 s54, 630784\n" \
    "buffer_load_dwordx4 v216, s[40:43], s54 offen lds\n" \
    "s_sleep 6\n" \
    "s_add_u32 m0, s51, 81920\n" \
    "s_mov_b32 s54, 688128\n" \
    "buffer_load_dwordx4 v216, s[40:43], s54 offen lds\n" \
    "s_sleep 6\n" \
    "s_add_u32 m0, s51, 86016\n" \
    "s_mov_b32 s54, 745472\n" \
    "buffer_load_dwordx4 v216, s[40:43], s54 offen lds\n" \
    "s_sleep 6\n" \
    "s_add_u32 m0, s51, 90112\n" \
    "s_mov_b32 s54, 802816\n" \
    "buffer_load_dwordx4 v216, s[40:43], s54 offen lds\n" \
    "s_sleep 6\n" \
    "s_add_u32 m0, s51, 94208\n" \
    "s_mov_b32 s54, 860160\n" \
    "buffer_load_dwordx4 v216, s[40:43], s54 offen lds\n" \
    "s_sleep 6\n" \
    "s_waitcnt vmcnt(16)\n" \
    "s_barrier\n" \
    "v_add_u32 v223, 98304, v217\n" \
    "ds_read_b64_tr_b16 a[32:33], v223\n" \
    "ds_read_b64_tr_b16 a[34:35], v223 offset:1024\n" \
    "ds_read_b64_tr_b16 a[36:37], v223 offset:8192\n" \
    "ds_read_b64_tr_b16 a[38:39], v223 offset:9216\n" \
    "ds_read_b64_tr_b16 a[40:41], v223 offset:16384\n" \
    "ds_read_b64_tr_b16 a[42:43], v223 offset:17408\n" \
    "ds_read_b64_tr_b16 a[44:45], v223 offset:24576\n" \
    "ds_read_b64_tr_b16 a[46:47], v223 offset:25600\n" \
    "s_waitcnt lgkmcnt(0)\n" \
    "ds_read_b64_tr_b16 a[48:49], v223 offset:32768\n" \
    "ds_read_b64_tr_b16 a[50:51], v223 offset:33792\n" \
    "ds_read_b64_tr_b16 a[52:53], v223 offset:40960\n" \
    "ds_read_b64_tr_b16 a[54:55], v223 offset:41984\n" \
    "ds_read_b64_tr_b16 a[56:57], v223 offset:49152\n" \
    "ds_read_b64_tr_b16 a[58:59], v223 offset:50176\n" \
    "ds_read_b64_tr_b16 a[60:61], v223 offset:57344\n" \
    "ds_read_b64_tr_b16 a[62:63], v223 offset:58368\n" \
    "s_waitcnt lgkmcnt(0)\n" \
    "v_add_u32 v223, 98304, v218\n" \
    "ds_read_b64_tr_b16 a[96:97], v223\n" \
    "ds_read_b64_tr_b16 a[98:99], v223 offset:1024\n" \
    "ds_read_b64_tr_b16 a[100:101], v223 offset:8192\n" \
    "ds_read_b64_tr_b16 a[102:103], v223 offset:9216\n" \
    "ds_read_b64_tr_b16 a[104:105], v223 offset:16384\n" \
    "ds_read_b64_tr_b16 a[106:107], v223 offset:17408\n" \
    "ds_read_b64_tr_b16 a[108:109], v223 offset:24576\n" \
    "ds_read_b64_tr_b16 a[110:111], v223 offset:25600\n" \
    "s_waitcnt lgkmcnt(0)\n" \
    "ds_read_b64_tr_b16 a[112:113], v223 offset:32768\n" \
    "ds_read_b64_tr_b16 a[114:115], v223 offset:33792\n" \
    "ds_read_b64_tr_b16 a[116:117], v223 offset:40960\n" \
    "ds_read_b64_tr_b16 a[118:119], v223 offset:41984\n" \
    "ds_read_b64_tr_b16 a[120:121], v223 offset:49152\n" \
    "ds_read_b64_tr_b16 a[122:123], v223 offset:50176\n" \
    "ds_read_b64_tr_b16 a[124:125], v223 offset:57344\n" \
    "ds_read_b64_tr_b16 a[126:127], v223 offset:58368\n" \
    "s_waitcnt lgkmcnt(0)\n" \
    "s_barrier\n" \
    "s_add_u32 m0, s51, 98304\n" \
    "s_mov_b32 s54, 917504\n" \
    "buffer_load_dwordx4 v216, s[40:43], s54 offen lds\n" \
    "s_sleep 6\n" \
    "s_add_u32 m0, s51, 102400\n" \
    "s_mov_b32 s54, 974848\n" \
    "buffer_load_dwordx4 v216, s[40:43], s54 offen lds\n" \
    "s_sleep 6\n" \
    "s_add_u32 m0, s51, 106496\n" \
    "s_mov_b32 s54, 1032192\n" \
    "buffer_load_dwordx4 v216, s[40:43], s54 offen lds\n" \
    "s_sleep 6\n" \
    "s_add_u32 m0, s51, 110592\n" \
    "s_mov_b32 s54, 1089536\n" \
    "buffer_load_dwordx4 v216, s[40:43], s54 offen lds\n" \
    "s_sleep 6\n" \
    "s_add_u32 m0, s51, 114688\n" \
    "s_mov_b32 s54, 1146880\n" \
    "buffer_load_dwordx4 v216, s[40:43], s54 offen lds\n" \
    "s_sleep 6\n" \
    "s_add_u32 m0, s51, 118784\n" \
    "s_mov_b32 s54, 1204224\n" \
    "buffer_load_dwordx4 v216, s[40:43], s54 offen lds\n" \
    "s_sleep 6\n" \
    "s_add_u32 m0, s51, 122880\n" \
    "s_mov_b32 s54, 1261568\n" \
    "buffer_load_dwordx4 v216, s[40:43], s54 offen lds\n" \
    "s_sleep 6\n" \
    "s_add_u32 m0, s51, 126976\n" \
    "s_mov_b32 s54, 1318912\n" \
    "buffer_load_dwordx4 v216, s[40:43], s54 offen lds\n" \
    "s_sleep 6\n" \
    "s_add_u32 m0, s51, 131072\n" \
    "s_mov_b32 s54, 1376256\n" \
    "buffer_load_dwordx4 v216, s[40:43], s54 offen lds\n" \
    "s_sleep 6\n" \
    "s_add_u32 m0, s51, 135168\n" \
    "s_mov_b32 s54, 1433600\n" \
    "buffer_load_dwordx4 v216, s[40:43], s54 offen lds\n" \
    "s_sleep 6\n" \
    "s_add_u32 m0, s51, 139264\n" \
    "s_mov_b32 s54, 1490944\n" \
    "buffer_load_dwordx4 v216, s[40:43], s54 offen lds\n" \
    "s_sleep 6\n" \
    "s_add_u32 m0, s51, 143360\n" \
    "s_mov_b32 s54, 1548288\n" \
    "buffer_load_dwordx4 v216, s[40:43], s54 offen lds\n" \
    "s_sleep 6\n" \
    "s_add_u32 m0, s51, 147456\n" \
    "s_mov_b32 s54, 1605632\n" \
    "buffer_load_dwordx4 v216, s[40:43], s54 offen lds\n" \
    "s_sleep 6\n" \
    "s_add_u32 m0, s51, 151552\n" \
    "s_mov_b32 s54, 1662976\n" \
    "buffer_load_dwordx4 v216, s[40:43], s54 offen lds\n" \
    "s_sleep 6\n" \
    "s_add_u32 m0, s51, 155648\n" \
    "s_mov_b32 s54, 1720320\n" \
    "buffer_load_dwordx4 v216, s[40:43], s54 offen lds\n" \
    "s_sleep 6\n" \
    "s_add_u32 m0, s51, 159744\n" \
    "s_mov_b32 s54, 1777664\n" \
    "buffer_load_dwordx4 v216, s[40:43], s54 offen lds\n" \
    "s_sleep 6\n" \
    "s_waitcnt vmcnt(16)\n" \
    "s_barrier\n" \
    "v_add_u32 v223, 32768, v217\n" \
    "ds_read_b64_tr_b16 a[128:129], v223\n" \
    "ds_read_b64_tr_b16 a[130:131], v223 offset:1024\n" \
    "ds_read_b64_tr_b16 a[132:133], v223 offset:8192\n" \
    "ds_read_b64_tr_b16 a[134:135], v223 offset:9216\n" \
    "ds_read_b64_tr_b16 a[136:137], v223 offset:16384\n" \
    "ds_read_b64_tr_b16 a[138:139], v223 offset:17408\n" \
    "ds_read_b64_tr_b16 a[140:141], v223 offset:24576\n" \
    "ds_read_b64_tr_b16 a[142:143], v223 offset:25600\n" \
    "s_waitcnt lgkmcnt(0)\n" \
    "ds_read_b64_tr_b16 a[144:145], v223 offset:32768\n" \
    "ds_read_b64_tr_b16 a[146:147], v223 offset:33792\n" \
    "ds_read_b64_tr_b16 a[148:149], v223 offset:40960\n" \
    "ds_read_b64_tr_b16 a[150:151], v223 offset:41984\n" \
    "ds_read_b64_tr_b16 a[152:153], v223 offset:49152\n" \
    "ds_read_b64_tr_b16 a[154:155], v223 offset:50176\n" \
    "ds_read_b64_tr_b16 a[156:157], v223 offset:57344\n" \
    "ds_read_b64_tr_b16 a[158:159], v223 offset:58368\n" \
    "s_waitcnt lgkmcnt(0)\n" \
    "v_add_u32 v223, 32768, v218\n" \
    "ds_read_b64_tr_b16 a[192:193], v223\n" \
    "ds_read_b64_tr_b16 a[194:195], v223 offset:1024\n" \
    "ds_read_b64_tr_b16 a[196:197], v223 offset:8192\n" \
    "ds_read_b64_tr_b16 a[198:199], v223 offset:9216\n" \
    "ds_read_b64_tr_b16 a[200:201], v223 offset:16384\n" \
    "ds_read_b64_tr_b16 a[202:203], v223 offset:17408\n" \
    "ds_read_b64_tr_b16 a[204:205], v223 offset:24576\n" \
    "ds_read_b64_tr_b16 a[206:207], v223 offset:25600\n" \
    "s_waitcnt lgkmcnt(0)\n" \
    "ds_read_b64_tr_b16 a[208:209], v223 offset:32768\n" \
    "ds_read_b64_tr_b16 a[210:211], v223 offset:33792\n" \
    "ds_read_b64_tr_b16 a[212:213], v223 offset:40960\n" \
    "ds_read_b64_tr_b16 a[214:215], v223 offset:41984\n" \
    "ds_read_b64_tr_b16 a[216:217], v223 offset:49152\n" \
    "ds_read_b64_tr_b16 a[218:219], v223 offset:50176\n" \
    "ds_read_b64_tr_b16 a[220:221], v223 offset:57344\n" \
    "ds_read_b64_tr_b16 a[222:223], v223 offset:58368\n" \
    "s_waitcnt lgkmcnt(0)\n" \
    "s_barrier\n" \
    "s_waitcnt vmcnt(0)\n" \
    "s_barrier\n" \
    "v_add_u32 v223, 98304, v217\n" \
    "ds_read_b64_tr_b16 a[160:161], v223\n" \
    "ds_read_b64_tr_b16 a[162:163], v223 offset:1024\n" \
    "ds_read_b64_tr_b16 a[164:165], v223 offset:8192\n" \
    "ds_read_b64_tr_b16 a[166:167], v223 offset:9216\n" \
    "ds_read_b64_tr_b16 a[168:169], v223 offset:16384\n" \
    "ds_read_b64_tr_b16 a[170:171], v223 offset:17408\n" \
    "ds_read_b64_tr_b16 a[172:173], v223 offset:24576\n" \
    "ds_read_b64_tr_b16 a[174:175], v223 offset:25600\n" \
    "s_waitcnt lgkmcnt(0)\n" \
    "ds_read_b64_tr_b16 a[176:177], v223 offset:32768\n" \
    "ds_read_b64_tr_b16 a[178:179], v223 offset:33792\n" \
    "ds_read_b64_tr_b16 a[180:181], v223 offset:40960\n" \
    "ds_read_b64_tr_b16 a[182:183], v223 offset:41984\n" \
    "ds_read_b64_tr_b16 a[184:185], v223 offset:49152\n" \
    "ds_read_b64_tr_b16 a[186:187], v223 offset:50176\n" \
    "ds_read_b64_tr_b16 a[188:189], v223 offset:57344\n" \
    "ds_read_b64_tr_b16 a[190:191], v223 offset:58368\n" \
    "s_waitcnt lgkmcnt(0)\n" \
    "v_add_u32 v223, 98304, v218\n" \
    "ds_read_b64_tr_b16 a[224:225], v223\n" \
    "ds_read_b64_tr_b16 a[226:227], v223 offset:1024\n" \
    "ds_read_b64_tr_b16 a[228:229], v223 offset:8192\n" \
    "ds_read_b64_tr_b16 a[230:231], v223 offset:9216\n" \
    "ds_read_b64_tr_b16 a[232:233], v223 offset:16384\n" \
    "ds_read_b64_tr_b16 a[234:235], v223 offset:17408\n" \
    "ds_read_b64_tr_b16 a[236:237], v223 offset:24576\n" \
    "ds_read_b64_tr_b16 a[238:239], v223 offset:25600\n" \
    "s_waitcnt lgkmcnt(0)\n" \
    "ds_read_b64_tr_b16 a[240:241], v223 offset:32768\n" \
    "ds_read_b64_tr_b16 a[242:243], v223 offset:33792\n" \
    "ds_read_b64_tr_b16 a[244:245], v223 offset:40960\n" \
    "ds_read_b64_tr_b16 a[246:247], v223 offset:41984\n" \
    "ds_read_b64_tr_b16 a[248:249], v223 offset:49152\n" \
    "ds_read_b64_tr_b16 a[250:251], v223 offset:50176\n" \
    "ds_read_b64_tr_b16 a[252:253], v223 offset:57344\n" \
    "ds_read_b64_tr_b16 a[254:255], v223 offset:58368\n" \
    "s_waitcnt lgkmcnt(0)\n" \
    "s_barrier\n" \
    "s_and_b32 s55, s46, 32768\n" \
    "v_add_u32 v208, s55, v253\n" \
    "s_xor_b32 s46, s46, 32768\n" \
    "s_add_u32 s45, s45, 131072\n" \
    "s_barrier\n" \
    "ds_read_b128 v[192:195], v208\n" \
    "ds_read_b128 v[196:199], v208 offset:16384\n" \
    "ds_read_b128 v[200:203], v208 offset:1024\n" \
    "s_waitcnt lgkmcnt(2)\n" \
    "v_mfma_f32_16x16x32_bf16 v[128:131], a[0:3], v[192:195], 0\n" \
    "ds_read_b128 v[204:207], v208 offset:17408\n" \
    "v_mfma_f32_16x16x32_bf16 v[132:135], a[64:67], v[192:195], 0\n" \
    "v_mfma_f32_16x16x32_bf16 v[136:139], a[128:131], v[192:195], 0\n" \
    "v_mfma_f32_16x16x32_bf16 v[140:143], a[192:195], v[192:195], 0\n" \
    "s_waitcnt lgkmcnt(2)\n" \
    "v_mfma_f32_16x16x32_bf16 v[144:147], a[0:3], v[196:199], 0\n" \
    "ds_read_b128 v[192:195], v208 offset:2048\n" \
    "v_mfma_f32_16x16x32_bf16 v[148:151], a[64:67], v[196:199], 0\n" \
    "v_mfma_f32_16x16x32_bf16 v[152:155], a[128:131], v[196:199], 0\n" \
    "v_mfma_f32_16x16x32_bf16 v[156:159], a[192:195], v[196:199], 0\n" \
    "s_waitcnt lgkmcnt(2)\n" \
    "v_mfma_f32_16x16x32_bf16 v[128:131], a[4:7], v[200:203], v[128:131]\n" \
    "ds_read_b128 v[196:199], v208 offset:18432\n" \
    "v_mfma_f32_16x16x32_bf16 v[132:135], a[68:71], v[200:203], v[132:135]\n" \
    "v_mfma_f32_16x16x32_bf16 v[136:139], a[132:135], v[200:203], v[136:139]\n" \
    "v_mfma_f32_16x16x32_bf16 v[140:143], a[196:199], v[200:203], v[140:143]\n" \
    "s_waitcnt lgkmcnt(2)\n" \
    "v_mfma_f32_16x16x32_bf16 v[144:147], a[4:7], v[204:207], v[144:147]\n" \
    "ds_read_b128 v[200:203], v208 offset:3072\n" \
    "v_mfma_f32_16x16x32_bf16 v[148:151], a[68:71], v[204:207], v[148:151]\n" \
    "v_mfma_f32_16x16x32_bf16 v[152:155], a[132:135], v[204:207], v[152:155]\n" \
    "v_mfma_f32_16x16x32_bf16 v[156:159], a[196:199], v[204:207], v[156:159]\n" \
    "s_waitcnt lgkmcnt(2)\n" \
    "v_mfma_f32_16x16x32_bf16 v[128:131], a[8:11], v[192:195], v[128:131]\n" \
    "ds_read_b128 v[204:207], v208 offset:19456\n" \
    "v_mfma_f32_16x16x32_bf16 v[132:135], a[72:75], v[192:195], v[132:135]\n" \
    "v_mfma_f32_16x16x32_bf16 v[136:139], a[136:139], v[192:195], v[136:139]\n" \
    "v_mfma_f32_16x16x32_bf16 v[140:143], a[200:203], v[192:195], v[140:143]\n" \
    "s_waitcnt lgkmcnt(2)\n" \
    "v_mfma_f32_16x16x32_bf16 v[144:147], a[8:11], v[196:199], v[144:147]\n" \
    "ds_read_b128 v[192:195], v208 offset:4096\n" \
    "v_mfma_f32_16x16x32_bf16 v[148:151], a[72:75], v[196:199], v[148:151]\n" \
    "v_mfma_f32_16x16x32_bf16 v[152:155], a[136:139], v[196:199], v[152:155]\n" \
    "v_mfma_f32_16x16x32_bf16 v[156:159], a[200:203], v[196:199], v[156:159]\n" \
    "s_waitcnt lgkmcnt(2)\n" \
    "v_mfma_f32_16x16x32_bf16 v[128:131], a[12:15], v[200:203], v[128:131]\n" \
    "ds_read_b128 v[196:199], v208 offset:20480\n" \
    "v_mfma_f32_16x16x32_bf16 v[132:135], a[76:79], v[200:203], v[132:135]\n" \
    "v_mfma_f32_16x16x32_bf16 v[136:139], a[140:143], v[200:203], v[136:139]\n" \
    "s_waitcnt vmcnt(0)\n" \
    "s_barrier\n" \
    "s_sub_i32 s54, s44, 1\n" \
    "s_cmp_lt_i32 s54, 1\n" \
    "s_cbranch_scc1 FA%=\n" \
    "s_lshl_b32 s54, s54, 1\n" \
    "v_mov_b32 v209, s54\n" \
    "buffer_store_dword v209, v252, s[36:39], s48 offen sc1\n" \
    "FA%=:\n" \
    "v_mfma_f32_16x16x32_bf16 v[140:143], a[204:207], v[200:203], v[140:143]\n" \
    "s_waitcnt lgkmcnt(2)\n" \
    "v_mfma_f32_16x16x32_bf16 v[144:147], a[12:15], v[204:207], v[144:147]\n" \
    "ds_read_b128 v[200:203], v208 offset:5120\n" \
    "v_mfma_f32_16x16x32_bf16 v[148:151], a[76:79], v[204:207], v[148:151]\n" \
    "v_mfma_f32_16x16x32_bf16 v[152:155], a[140:143], v[204:207], v[152:155]\n" \
    "s_add_u32 m0, s46, 0\n" \
    "s_add_u32 s54, s45, 0\n" \
    "buffer_load_dwordx4 v253, s[36:39], s54 offen sc1 lds\n" \
    "v_mfma_f32_16x16x32_bf16 v[156:159], a[204:207], v[204:207], v[156:159]\n" \
    "s_waitcnt lgkmcnt(2)\n" \
    "v_mfma_f32_16x16x32_bf16 v[128:131], a[16:19], v[192:195], v[128:131]\n" \
    "ds_read_b128 v[204:207], v208 offset:21504\n" \
    "v_mfma_f32_16x16x32_bf16 v[132:135], a[80:83], v[192:195], v[132:135]\n" \
    "v_mfma_f32_16x16x32_bf16 v[136:139], a[144:147], v[192:195], v[136:139]\n" \
    "s_add_u32 m0, s46, 1024\n" \
    "s_add_u32 s54, s45, 1024\n" \
    "buffer_load_dwordx4 v253, s[36:39], s54 offen sc1 lds\n" \
    "v_mfma_f32_16x16x32_bf16 v[140:143], a[208:211], v[192:195], v[140:143]\n" \
    "s_waitcnt lgkmcnt(2)\n" \
    "v_mfma_f32_16x16x32_bf16 v[144:147], a[16:19], v[196:199], v[144:147]\n" \
    "ds_read_b128 v[192:195], v208 offset:6144\n" \
    "v_mfma_f32_16x16x32_bf16 v[148:151], a[80:83], v[196:199], v[148:151]\n" \
    "v_mfma_f32_16x16x32_bf16 v[152:155], a[144:147], v[196:199], v[152:155]\n" \
    "s_add_u32 m0, s46, 2048\n" \
    "s_add_u32 s54, s45, 2048\n" \
    "buffer_load_dwordx4 v253, s[36:39], s54 offen sc1 lds\n" \
    "v_mfma_f32_16x16x32_bf16 v[156:159], a[208:211], v[196:199], v[156:159]\n" \
    "s_waitcnt lgkmcnt(2)\n" \
    "v_mfma_f32_16x16x32_bf16 v[128:131], a[20:23], v[200:203], v[128:131]\n" \
    "ds_read_b128 v[196:199], v208 offset:22528\n" \
    "v_mfma_f32_16x16x32_bf16 v[132:135], a[84:87], v[200:203], v[132:135]\n" \
    "v_mfma_f32_16x16x32_bf16 v[136:139], a[148:151], v[200:203], v[136:139]\n" \
    "s_add_u32 m0, s46, 3072\n" \
    "s_add_u32 s54, s45, 3072\n" \
    "buffer_load_dwordx4 v253, s[36:39], s54 offen sc1 lds\n" \
    "v_mfma_f32_16x16x32_bf16 v[140:143], a[212:215], v[200:203], v[140:143]\n" \
    "s_waitcnt lgkmcnt(2)\n" \
    "v_mfma_f32_16x16x32_bf16 v[144:147], a[20:23], v[204:207], v[144:147]\n" \
    "ds_read_b128 v[200:203], v208 offset:7168\n" \
    "v_mfma_f32_16x16x32_bf16 v[148:151], a[84:87], v[204:207], v[148:151]\n" \
    "v_mfma_f32_16x16x32_bf16 v[152:155], a[148:151], v[204:207], v[152:155]\n" \
    "s_add_u32 m0, s46, 4096\n" \
    "s_add_u32 s54, s45, 4096\n" \
    "buffer_load_dwordx4 v253, s[36:39], s54 offen sc1 lds\n" \
    "v_mfma_f32_16x16x32_bf16 v[156:159], a[212:215], v[204:207], v[156:159]\n" \
    "s_waitcnt lgkmcnt(2)\n" \
    "v_mfma_f32_16x16x32_bf16 v[128:131], a[24:27], v[192:195], v[128:131]\n" \
    "ds_read_b128 v[204:207], v208 offset:23552\n" \
    "v_mfma_f32_16x16x32_bf16 v[132:135], a[88:91], v[192:195], v[132:135]\n" \
    "v_mfma_f32_16x16x32_bf16 v[136:139], a[152:155], v[192:195], v[136:139]\n" \
    "s_add_u32 m0, s46, 5120\n" \
    "s_add_u32 s54, s45, 5120\n" \
    "buffer_load_dwordx4 v253, s[36:39], s54 offen sc1 lds\n" \
    "v_mfma_f32_16x16x32_bf16 v[140:143], a[216:219], v[192:195], v[140:143]\n" \
    "s_waitcnt lgkmcnt(2)\n" \
    "v_mfma_f32_16x16x32_bf16 v[144:147], a[24:27], v[196:199], v[144:147]\n" \
    "ds_read_b128 v[192:195], v208 offset:8192\n" \
    "v_mfma_f32_16x16x32_bf16 v[148:151], a[88:91], v[196:199], v[148:151]\n" \
    "v_mfma_f32_16x16x32_bf16 v[152:155], a[152:155], v[196:199], v[152:155]\n" \
    "s_add_u32 m0, s46, 6144\n" \
    "s_add_u32 s54, s45, 6144\n" \
    "buffer_load_dwordx4 v253, s[36:39], s54 offen sc1 lds\n" \
    "v_mfma_f32_16x16x32_bf16 v[156:159], a[216:219], v[196:199], v[156:159]\n" \
    "s_waitcnt lgkmcnt(2)\n" \
    "v_mfma_f32_16x16x32_bf16 v[128:131], a[28:31], v[200:203], v[128:131]\n" \
    "ds_read_b128 v[196:199], v208 offset:24576\n" \
    "v_mfma_f32_16x16x32_bf16 v[132:135], a[92:95], v[200:203], v[132:135]\n" \
    "v_mfma_f32_16x16x32_bf16 v[136:139], a[156:159], v[200:203], v[136:139]\n" \
    "s_add_u32 m0, s46, 7168\n" \
    "s_add_u32 s54, s45, 7168\n" \
    "buffer_load_dwordx4 v253, s[36:39], s54 offen sc1 lds\n" \
    "v_mfma_f32_16x16x32_bf16 v[140:143], a[220:223], v[200:203], v[140:143]\n" \
    "s_waitcnt lgkmcnt(2)\n" \
    "v_mfma_f32_16x16x32_bf16 v[144:147], a[28:31], v[204:207], v[144:147]\n" \
    "ds_read_b128 v[200:203], v208 offset:9216\n" \
    "v_mfma_f32_16x16x32_bf16 v[148:151], a[92:95], v[204:207], v[148:151]\n" \
    "v_mfma_f32_16x16x32_bf16 v[152:155], a[156:159], v[204:207], v[152:155]\n" \
    "v_mfma_f32_16x16x32_bf16 v[156:159], a[220:223], v[204:207], v[156:159]\n" \
    "s_waitcnt lgkmcnt(2)\n" \
    "v_mfma_f32_16x16x32_bf16 v[128:131], a[32:35], v[192:195], v[128:131]\n" \
    "ds_read_b128 v[204:207], v208 offset:25600\n" \
    "v_mfma_f32_16x16x32_bf16 v[132:135], a[96:99], v[192:195], v[132:135]\n" \
    "v_mfma_f32_16x16x32_bf16 v[136:139], a[160:163], v[192:195], v[136:139]\n" \
    "v_mfma_f32_16x16x32_bf16 v[140:143], a[224:227], v[192:195], v[140:143]\n" \
    "s_waitcnt lgkmcnt(2)\n" \
    "v_mfma_f32_16x16x32_bf16 v[144:147], a[32:35], v[196:199], v[144:147]\n" \
    "ds_read_b128 v[192:195], v208 offset:10240\n" \
    "v_mfma_f32_16x16x32_bf16 v[148:151], a[96:99], v[196:199], v[148:151]\n" \
    "v_mfma_f32_16x16x32_bf16 v[152:155], a[160:163], v[196:199], v[152:155]\n" \
    "v_mfma_f32_16x16x32_bf16 v[156:159], a[224:227], v[196:199], v[156:159]\n" \
    "s_waitcnt lgkmcnt(2)\n" \
    "v_mfma_f32_16x16x32_bf16 v[128:131], a[36:39], v[200:203], v[128:131]\n" \
    "ds_read_b128 v[196:199], v208 offset:26624\n" \
    "v_mfma_f32_16x16x32_bf16 v[132:135], a[100:103], v[200:203], v[132:135]\n" \
    "v_mfma_f32_16x16x32_bf16 v[136:139], a[164:167], v[200:203], v[136:139]\n" \
    "v_mfma_f32_16x16x32_bf16 v[140:143], a[228:231], v[200:203], v[140:143]\n" \
    "s_waitcnt lgkmcnt(2)\n" \
    "v_mfma_f32_16x16x32_bf16 v[144:147], a[36:39], v[204:207], v[144:147]\n" \
    "ds_read_b128 v[200:203], v208 offset:11264\n" \
    "v_mfma_f32_16x16x32_bf16 v[148:151], a[100:103], v[204:207], v[148:151]\n" \
    "v_mfma_f32_16x16x32_bf16 v[152:155], a[164:167], v[204:207], v[152:155]\n" \
    "v_mfma_f32_16x16x32_bf16 v[156:159], a[228:231], v[204:207], v[156:159]\n" \
    "s_waitcnt lgkmcnt(2)\n" \
    "v_mfma_f32_16x16x32_bf16 v[128:131], a[40:43], v[192:195], v[128:131]\n" \
    "ds_read_b128 v[204:207], v208 offset:27648\n" \
    "v_mfma_f32_16x16x32_bf16 v[132:135], a[104:107], v[192:195], v[132:135]\n" \
    "v_mfma_f32_16x16x32_bf16 v[136:139], a[168:171], v[192:195], v[136:139]\n" \
    "v_mfma_f32_16x16x32_bf16 v[140:143], a[232:235], v[192:195], v[140:143]\n" \
    "s_waitcnt lgkmcnt(2)\n" \
    "v_mfma_f32_16x16x32_bf16 v[144:147], a[40:43], v[196:199], v[144:147]\n" \
    "ds_read_b128 v[192:195], v208 offset:12288\n" \
    "v_mfma_f32_16x16x32_bf16 v[148:151], a[104:107], v[196:199], v[148:151]\n" \
    "v_mfma_f32_16x16x32_bf16 v[152:155], a[168:171], v[196:199], v[152:155]\n" \
    "v_mfma_f32_16x16x32_bf16 v[156:159], a[232:235], v[196:199], v[156:159]\n" \
    "s_waitcnt lgkmcnt(2)\n" \
    "v_mfma_f32_16x16x32_bf16 v[128:131], a[44:47], v[200:203], v[128:131]\n" \
    "ds_read_b128 v[196:199], v208 offset:28672\n" \
    "v_mfma_f32_16x16x32_bf16 v[132:135], a[108:111], v[200:203], v[132:135]\n" \
    "v_mfma_f32_16x16x32_bf16 v[136:139], a[172:175], v[200:203], v[136:139]\n" \
    "v_mfma_f32_16x16x32_bf16 v[140:143], a[236:239], v[200:203], v[140:143]\n" \
    "s_waitcnt lgkmcnt(2)\n" \
    "v_mfma_f32_16x16x32_bf16 v[144:147], a[44:47], v[204:207], v[144:147]\n" \
    "ds_read_b128 v[200:203], v208 offset:13312\n" \
    "v_mfma_f32_16x16x32_bf16 v[148:151], a[108:111], v[204:207], v[148:151]\n" \
    "v_mfma_f32_16x16x32_bf16 v[152:155], a[172:175], v[204:207], v[152:155]\n" \
    "v_mfma_f32_16x16x32_bf16 v[156:159], a[236:239], v[204:207], v[156:159]\n" \
    "s_waitcnt lgkmcnt(2)\n" \
    "v_mfma_f32_16x16x32_bf16 v[128:131], a[48:51], v[192:195], v[128:131]\n" \
    "ds_read_b128 v[204:207], v208 offset:29696\n" \
    "v_mfma_f32_16x16x32_bf16 v[132:135], a[112:115], v[192:195], v[132:135]\n" \
    "v_mfma_f32_16x16x32_bf16 v[136:139], a[176:179], v[192:195], v[136:139]\n" \
    "v_mfma_f32_16x16x32_bf16 v[140:143], a[240:243], v[192:195], v[140:143]\n" \
    "s_waitcnt lgkmcnt(2)\n" \
    "v_mfma_f32_16x16x32_bf16 v[144:147], a[48:51], v[196:199], v[144:147]\n" \
    "ds_read_b128 v[192:195], v208 offset:14336\n" \
    "v_mfma_f32_16x16x32_bf16 v[148:151], a[112:115], v[196:199], v[148:151]\n" \
    "v_mfma_f32_16x16x32_bf16 v[152:155], a[176:179], v[196:199], v[152:155]\n" \
    "v_mfma_f32_16x16x32_bf16 v[156:159], a[240:243], v[196:199], v[156:159]\n" \
    "s_waitcnt lgkmcnt(2)\n" \
    "v_mfma_f32_16x16x32_bf16 v[128:131], a[52:55], v[200:203], v[128:131]\n" \
    "ds_read_b128 v[196:199], v208 offset:30720\n" \
    "v_mfma_f32_16x16x32_bf16 v[132:135], a[116:119], v[200:203], v[132:135]\n" \
    "v_mfma_f32_16x16x32_bf16 v[136:139], a[180:183], v[200:203], v[136:139]\n" \
    "v_mfma_f32_16x16x32_bf16 v[140:143], a[244:247], v[200:203], v[140:143]\n" \
    "s_waitcnt lgkmcnt(2)\n" \
    "v_mfma_f32_16x16x32_bf16 v[144:147], a[52:55], v[204:207], v[144:147]\n" \
    "ds_read_b128 v[200:203], v208 offset:15360\n" \
    "v_mfma_f32_16x16x32_bf16 v[148:151], a[116:119], v[204:207], v[148:151]\n" \
    "v_mfma_f32_16x16x32_bf16 v[152:155], a[180:183], v[204:207], v[152:155]\n" \
    "v_mfma_f32_16x16x32_bf16 v[156:159], a[244:247], v[204:207], v[156:159]\n" \
    "s_waitcnt lgkmcnt(2)\n" \
    "v_mfma_f32_16x16x32_bf16 v[128:131], a[56:59], v[192:195], v[128:131]\n" \
    "ds_read_b128 v[204:207], v208 offset:31744\n" \
    "v_mfma_f32_16x16x32_bf16 v[132:135], a[120:123], v[192:195], v[132:135]\n" \
    "v_mfma_f32_16x16x32_bf16 v[136:139], a[184:187], v[192:195], v[136:139]\n" \
    "v_mfma_f32_16x16x32_bf16 v[140:143], a[248:251], v[192:195], v[140:143]\n" \
    "s_waitcnt lgkmcnt(2)\n" \
    "v_mfma_f32_16x16x32_bf16 v[144:147], a[56:59], v[196:199], v[144:147]\n" \
    "v_mfma_f32_16x16x32_bf16 v[148:151], a[120:123], v[196:199], v[148:151]\n" \
    "v_mfma_f32_16x16x32_bf16 v[152:155], a[184:187], v[196:199], v[152:155]\n" \
    "s_waitcnt vmcnt(0)\n" \
    "v_mfma_f32_16x16x32_bf16 v[156:159], a[248:251], v[196:199], v[156:159]\n" \
    "s_waitcnt lgkmcnt(1)\n" \
    "v_mfma_f32_16x16x32_bf16 v[128:131], a[60:63], v[200:203], v[128:131]\n" \
    "v_mfma_f32_16x16x32_bf16 v[132:135], a[124:127], v[200:203], v[132:135]\n" \
    "v_mfma_f32_16x16x32_bf16 v[136:139], a[188:191], v[200:203], v[136:139]\n" \
    "v_mfma_f32_16x16x32_bf16 v[140:143], a[252:255], v[200:203], v[140:143]\n" \
    "s_waitcnt lgkmcnt(0)\n" \
    "v_mfma_f32_16x16x32_bf16 v[144:147], a[60:63], v[204:207], v[144:147]\n" \
    "v_mfma_f32_16x16x32_bf16 v[148:151], a[124:127], v[204:207], v[148:151]\n" \
    "v_mfma_f32_16x16x32_bf16 v[152:155], a[188:191], v[204:207], v[152:155]\n" \
    "v_mfma_f32_16x16x32_bf16 v[156:159], a[252:255], v[204:207], v[156:159]\n" \
    "s_add_u32 s44, s44, 1\n" \
    "FL%=:\n" \
    "s_and_b32 s55, s46, 32768\n" \
    "v_add_u32 v208, s55, v253\n" \
    "s_xor_b32 s46, s46, 32768\n" \
    "s_add_u32 s45, s45, 131072\n" \
    "s_barrier\n" \
    "ds_read_b128 v[192:195], v208\n" \
    "ds_read_b128 v[196:199], v208 offset:16384\n" \
    "ds_read_b128 v[200:203], v208 offset:1024\n" \
    "s_waitcnt lgkmcnt(2)\n" \
    "v_mfma_f32_16x16x32_bf16 v[160:163], a[0:3], v[192:195], 0\n" \
    "ds_read_b128 v[204:207], v208 offset:17408\n" \
    "v_mfma_f32_16x16x32_bf16 v[164:167], a[64:67], v[192:195], 0\n" \
    "v_mfma_f32_16x16x32_bf16 v[168:171], a[128:131], v[192:195], 0\n" \
    "v_mfma_f32_16x16x32_bf16 v[172:175], a[192:195], v[192:195], 0\n" \
    "s_waitcnt lgkmcnt(2)\n" \
    "v_mfma_f32_16x16x32_bf16 v[176:179], a[0:3], v[196:199], 0\n" \
    "ds_read_b128 v[192:195], v208 offset:2048\n" \
    "v_mfma_f32_16x16x32_bf16 v[180:183], a[64:67], v[196:199], 0\n" \
    "v_mfma_f32_16x16x32_bf16 v[184:187], a[128:131], v[196:199], 0\n" \
    "v_mfma_f32_16x16x32_bf16 v[188:191], a[192:195], v[196:199], 0\n" \
    "s_waitcnt lgkmcnt(2)\n" \
    "v_mfma_f32_16x16x32_bf16 v[160:163], a[4:7], v[200:203], v[160:163]\n" \
    "ds_read_b128 v[196:199], v208 offset:18432\n" \
    "v_mul_f32 v210, 0xbfb8aa3b, v128\n" \
    "v_mfma_f32_16x16x32_bf16 v[164:167], a[68:71], v[200:203], v[164:167]\n" \
    "v_mul_f32 v211, 0xbfb8aa3b, v129\n" \
    "v_mul_f32 v212, 0xbfb8aa3b, v130\n" \
    "v_mfma_f32_16x16x32_bf16 v[168:171], a[132:135], v[200:203], v[168:171]\n" \
    "v_mul_f32 v213, 0xbfb8aa3b, v131\n" \
    "v_mfma_f32_16x16x32_bf16 v[172:175], a[196:199], v[200:203], v[172:175]\n" \
    "v_mul_f32 v214, 0xbfb8aa3b, v132\n" \
    "s_waitcnt lgkmcnt(2)\n" \
    "v_mfma_f32_16x16x32_bf16 v[176:179], a[4:7], v[204:207], v[176:179]\n" \
    "ds_read_b128 v[200:203], v208 offset:3072\n" \
    "v_mul_f32 v215, 0xbfb8aa3b, v133\n" \
    "v_mfma_f32_16x16x32_bf16 v[180:183], a[68:71], v[204:207], v[180:183]\n" \
    "v_mul_f32 v216, 0xbfb8aa3b, v134\n" \
    "v_mul_f32 v217, 0xbfb8aa3b, v135\n" \
    "v_mfma_f32_16x16x32_bf16 v[184:187], a[132:135], v[204:207], v[184:187]\n" \
    "v_exp_f32 v210, v210\n" \
    "v_mfma_f32_16x16x32_bf16 v[188:191], a[196:199], v[204:207], v[188:191]\n" \
    "v_exp_f32 v211, v211\n" \
    "s_waitcnt lgkmcnt(2)\n" \
    "v_mfma_f32_16x16x32_bf16 v[160:163], a[8:11], v[192:195], v[160:163]\n" \
    "ds_read_b128 v[204:207], v208 offset:19456\n" \
    "v_exp_f32 v212, v212\n" \
    "v_mfma_f32_16x16x32_bf16 v[164:167], a[72:75], v[192:195], v[164:167]\n" \
    "v_exp_f32 v213, v213\n" \
    "v_exp_f32 v214, v214\n" \
    "v_mfma_f32_16x16x32_bf16 v[168:171], a[136:139], v[192:195], v[168:171]\n" \
    "v_exp_f32 v215, v215\n" \
    "v_mfma_f32_16x16x32_bf16 v[172:175], a[200:203], v[192:195], v[172:175]\n" \
    "v_exp_f32 v216, v216\n" \
    "s_waitcnt lgkmcnt(2)\n" \
    "v_mfma_f32_16x16x32_bf16 v[176:179], a[8:11], v[196:199], v[176:179]\n" \
    "ds_read_b128 v[192:195], v208 offset:4096\n" \
    "v_exp_f32 v217, v217\n" \
    "v_mfma_f32_16x16x32_bf16 v[180:183], a[72:75], v[196:199], v[180:183]\n" \
    "v_mul_f32 v128, v128, v136\n" \
    "v_mul_f32 v129, v129, v137\n" \
    "v_mfma_f32_16x16x32_bf16 v[184:187], a[136:139], v[196:199], v[184:187]\n" \
    "v_mul_f32 v130, v130, v138\n" \
    "v_mfma_f32_16x16x32_bf16 v[188:191], a[200:203], v[196:199], v[188:191]\n" \
    "v_mul_f32 v131, v131, v139\n" \
    "s_waitcnt lgkmcnt(2)\n" \
    "v_mfma_f32_16x16x32_bf16 v[160:163], a[12:15], v[200:203], v[160:163]\n" \
    "ds_read_b128 v[196:199], v208 offset:20480\n" \
    "v_mul_f32 v132, v132, v140\n" \
    "v_mfma_f32_16x16x32_bf16 v[164:167], a[76:79], v[200:203], v[164:167]\n" \
    "v_mul_f32 v133, v133, v141\n" \
    "v_mfma_f32_16x16x32_bf16 v[168:171], a[140:143], v[200:203], v[168:171]\n" \
    "s_waitcnt vmcnt(0)\n" \
    "s_barrier\n" \
    "s_sub_i32 s54, s44, 1\n" \
    "s_cmp_lt_i32 s54, 1\n" \
    "s_cbranch_scc1 FB%=\n" \
    "s_lshl_b32 s54, s54, 1\n" \
    "v_mov_b32 v209, s54\n" \
    "buffer_store_dword v209, v252, s[36:39], s48 offen sc1\n" \
    "FB%=:\n" \
    "v_mfma_f32_16x16x32_bf16 v[172:175], a[204:207], v[200:203], v[172:175]\n" \
    "v_mul_f32 v134, v134, v142\n" \
    "s_waitcnt lgkmcnt(2)\n" \
    "v_mfma_f32_16x16x32_bf16 v[176:179], a[12:15], v[204:207], v[176:179]\n" \
    "ds_read_b128 v[200:203], v208 offset:5120\n" \
    "v_mul_f32 v135, v135, v143\n" \
    "v_mfma_f32_16x16x32_bf16 v[180:183], a[76:79], v[204:207], v[180:183]\n" \
    "v_add_f32 v210, 1.0, v210\n" \
    "v_mfma_f32_16x16x32_bf16 v[184:187], a[140:143], v[204:207], v[184:187]\n" \
    "s_add_u32 m0, s46, 0\n" \
    "s_add_u32 s54, s45, 0\n" \
    "buffer_load_dwordx4 v253, s[36:39], s54 offen sc1 lds\n" \
    "v_mfma_f32_16x16x32_bf16 v[188:191], a[204:207], v[204:207], v[188:191]\n" \
    "v_add_f32 v211, 1.0, v211\n" \
    "s_waitcnt lgkmcnt(2)\n" \
    "v_mfma_f32_16x16x32_bf16 v[160:163], a[16:19], v[192:195], v[160:163]\n" \
    "ds_read_b128 v[204:207], v208 offset:21504\n" \
    "v_add_f32 v212, 1.0, v212\n" \
    "v_mfma_f32_16x16x32_bf16 v[164:167], a[80:83], v[192:195], v[164:167]\n" \
    "v_add_f32 v213, 1.0, v213\n" \
    "v_mfma_f32_16x16x32_bf16 v[168:171], a[144:147], v[192:195], v[168:171]\n" \
    "s_add_u32 m0, s46, 1024\n" \
    "s_add_u32 s54, s45, 1024\n" \
    "buffer_load_dwordx4 v253, s[36:39], s54 offen sc1 lds\n" \
    "v_mfma_f32_16x16x32_bf16 v[172:175], a[208:211], v[192:195], v[172:175]\n" \
    "v_add_f32 v214, 1.0, v214\n" \
    "s_waitcnt lgkmcnt(2)\n" \
    "v_mfma_f32_16x16x32_bf16 v[176:179], a[16:19], v[196:199], v[176:179]\n" \
    "ds_read_b128 v[192:195], v208 offset:6144\n" \
    "v_add_f32 v215, 1.0, v215\n" \
    "v_mfma_f32_16x16x32_bf16 v[180:183], a[80:83], v[196:199], v[180:183]\n" \
    "v_add_f32 v216, 1.0, v216\n" \
    "v_mfma_f32_16x16x32_bf16 v[184:187], a[144:147], v[196:199], v[184:187]\n" \
    "s_add_u32 m0, s46, 2048\n" \
    "s_add_u32 s54, s45, 2048\n" \
    "buffer_load_dwordx4 v253, s[36:39], s54 offen sc1 lds\n" \
    "v_mfma_f32_16x16x32_bf16 v[188:191], a[208:211], v[196:199], v[188:191]\n" \
    "v_add_f32 v217, 1.0, v217\n" \
    "s_waitcnt lgkmcnt(2)\n" \
    "v_mfma_f32_16x16x32_bf16 v[160:163], a[20:23], v[200:203], v[160:163]\n" \
    "ds_read_b128 v[196:199], v208 offset:22528\n" \
    "v_rcp_f32 v210, v210\n" \
    "v_mfma_f32_16x16x32_bf16 v[164:167], a[84:87], v[200:203], v[164:167]\n" \
    "v_rcp_f32 v211, v211\n" \
    "v_mfma_f32_16x16x32_bf16 v[168:171], a[148:151], v[200:203], v[168:171]\n" \
    "s_add_u32 m0, s46, 3072\n" \
    "s_add_u32 s54, s45, 3072\n" \
    "buffer_load_dwordx4 v253, s[36:39], s54 offen sc1 lds\n" \
    "v_mfma_f32_16x16x32_bf16 v[172:175], a[212:215], v[200:203], v[172:175]\n" \
    "v_rcp_f32 v212, v212\n" \
    "s_waitcnt lgkmcnt(2)\n" \
    "v_mfma_f32_16x16x32_bf16 v[176:179], a[20:23], v[204:207], v[176:179]\n" \
    "ds_read_b128 v[200:203], v208 offset:7168\n" \
    "v_rcp_f32 v213, v213\n" \
    "v_mfma_f32_16x16x32_bf16 v[180:183], a[84:87], v[204:207], v[180:183]\n" \
    "v_rcp_f32 v214, v214\n" \
    "v_mfma_f32_16x16x32_bf16 v[184:187], a[148:151], v[204:207], v[184:187]\n" \
    "s_add_u32 m0, s46, 4096\n" \
    "s_add_u32 s54, s45, 4096\n" \
    "buffer_load_dwordx4 v253, s[36:39], s54 offen sc1 lds\n" \
    "v_mfma_f32_16x16x32_bf16 v[188:191], a[212:215], v[204:207], v[188:191]\n" \
    "v_rcp_f32 v215, v215\n" \
    "s_waitcnt lgkmcnt(2)\n" \
    "v_mfma_f32_16x16x32_bf16 v[160:163], a[24:27], v[192:195], v[160:163]\n" \
    "ds_read_b128 v[204:207], v208 offset:23552\n" \
    "v_rcp_f32 v216, v216\n" \
    "v_mfma_f32_16x16x32_bf16 v[164:167], a[88:91], v[192:195], v[164:167]\n" \
    "v_rcp_f32 v217, v217\n" \
    "v_mfma_f32_16x16x32_bf16 v[168:171], a[152:155], v[192:195], v[168:171]\n" \
    "s_add_u32 m0, s46, 5120\n" \
    "s_add_u32 s54, s45, 5120\n" \
    "buffer_load_dwordx4 v253, s[36:39], s54 offen sc1 lds\n" \
    "v_mfma_f32_16x16x32_bf16 v[172:175], a[216:219], v[192:195], v[172:175]\n" \
    "v_mul_f32 v128, v128, v210\n" \
    "s_waitcnt lgkmcnt(2)\n" \
    "v_mfma_f32_16x16x32_bf16 v[176:179], a[24:27], v[196:199], v[176:179]\n" \
    "ds_read_b128 v[192:195], v208 offset:8192\n" \
    "v_mul_f32 v129, v129, v211\n" \
    "v_mfma_f32_16x16x32_bf16 v[180:183], a[88:91], v[196:199], v[180:183]\n" \
    "v_mul_f32 v130, v130, v212\n" \
    "v_mfma_f32_16x16x32_bf16 v[184:187], a[152:155], v[196:199], v[184:187]\n" \
    "s_add_u32 m0, s46, 6144\n" \
    "s_add_u32 s54, s45, 6144\n" \
    "buffer_load_dwordx4 v253, s[36:39], s54 offen sc1 lds\n" \
    "v_mfma_f32_16x16x32_bf16 v[188:191], a[216:219], v[196:199], v[188:191]\n" \
    "v_mul_f32 v131, v131, v213\n" \
    "s_waitcnt lgkmcnt(2)\n" \
    "v_mfma_f32_16x16x32_bf16 v[160:163], a[28:31], v[200:203], v[160:163]\n" \
    "ds_read_b128 v[196:199], v208 offset:24576\n" \
    "v_mul_f32 v132, v132, v214\n" \
    "v_mfma_f32_16x16x32_bf16 v[164:167], a[92:95], v[200:203], v[164:167]\n" \
    "v_mul_f32 v133, v133, v215\n" \
    "v_mfma_f32_16x16x32_bf16 v[168:171], a[156:159], v[200:203], v[168:171]\n" \
    "s_add_u32 m0, s46, 7168\n" \
    "s_add_u32 s54, s45, 7168\n" \
    "buffer_load_dwordx4 v253, s[36:39], s54 offen sc1 lds\n" \
    "v_mfma_f32_16x16x32_bf16 v[172:175], a[220:223], v[200:203], v[172:175]\n" \
    "v_mul_f32 v134, v134, v216\n" \
    "s_waitcnt lgkmcnt(2)\n" \
    "v_mfma_f32_16x16x32_bf16 v[176:179], a[28:31], v[204:207], v[176:179]\n" \
    "ds_read_b128 v[200:203], v208 offset:9216\n" \
    "v_mul_f32 v135, v135, v217\n" \
    "v_mfma_f32_16x16x32_bf16 v[180:183], a[92:95], v[204:207], v[180:183]\n" \
    "v_cvt_pk_bf16_f32 v226, v128, v129\n" \
    "v_cvt_pk_bf16_f32 v227, v130, v131\n" \
    "v_mfma_f32_16x16x32_bf16 v[184:187], a[156:159], v[204:207], v[184:187]\n" \
    "v_cvt_pk_bf16_f32 v228, v132, v133\n" \
    "v_mfma_f32_16x16x32_bf16 v[188:191], a[220:223], v[204:207], v[188:191]\n" \
    "v_cvt_pk_bf16_f32 v229, v134, v135\n" \
    "s_waitcnt lgkmcnt(2)\n" \
    "v_mfma_f32_16x16x32_bf16 v[160:163], a[32:35], v[192:195], v[160:163]\n" \
    "ds_read_b128 v[204:207], v208 offset:25600\n" \
    "v_mul_f32 v218, 0xbfb8aa3b, v144\n" \
    "v_mfma_f32_16x16x32_bf16 v[164:167], a[96:99], v[192:195], v[164:167]\n" \
    "v_mul_f32 v219, 0xbfb8aa3b, v145\n" \
    "v_mul_f32 v220, 0xbfb8aa3b, v146\n" \
    "v_mfma_f32_16x16x32_bf16 v[168:171], a[160:163], v[192:195], v[168:171]\n" \
    "v_mul_f32 v221, 0xbfb8aa3b, v147\n" \
    "v_mfma_f32_16x16x32_bf16 v[172:175], a[224:227], v[192:195], v[172:175]\n" \
    "v_mul_f32 v222, 0xbfb8aa3b, v148\n" \
    "s_waitcnt lgkmcnt(2)\n" \
    "v_mfma_f32_16x16x32_bf16 v[176:179], a[32:35], v[196:199], v[176:179]\n" \
    "ds_read_b128 v[192:195], v208 offset:10240\n" \
    "v_mul_f32 v223, 0xbfb8aa3b, v149\n" \
    "v_mfma_f32_16x16x32_bf16 v[180:183], a[96:99], v[196:199], v[180:183]\n" \
    "v_mul_f32 v224, 0xbfb8aa3b, v150\n" \
    "v_mul_f32 v225, 0xbfb8aa3b, v151\n" \
    "v_mfma_f32_16x16x32_bf16 v[184:187], a[160:163], v[196:199], v[184:187]\n" \
    "v_exp_f32 v218, v218\n" \
    "v_mfma_f32_16x16x32_bf16 v[188:191], a[224:227], v[196:199], v[188:191]\n" \
    "v_exp_f32 v219, v219\n" \
    "s_waitcnt lgkmcnt(2)\n" \
    "v_mfma_f32_16x16x32_bf16 v[160:163], a[36:39], v[200:203], v[160:163]\n" \
    "ds_read_b128 v[196:199], v208 offset:26624\n" \
    "v_exp_f32 v220, v220\n" \
    "v_mfma_f32_16x16x32_bf16 v[164:167], a[100:103], v[200:203], v[164:167]\n" \
    "v_exp_f32 v221, v221\n" \
    "v_exp_f32 v222, v222\n" \
    "v_mfma_f32_16x16x32_bf16 v[168:171], a[164:167], v[200:203], v[168:171]\n" \
    "v_exp_f32 v223, v223\n" \
    "v_mfma_f32_16x16x32_bf16 v[172:175], a[228:231], v[200:203], v[172:175]\n" \
    "v_exp_f32 v224, v224\n" \
    "s_waitcnt lgkmcnt(2)\n" \
    "v_mfma_f32_16x16x32_bf16 v[176:179], a[36:39], v[204:207], v[176:179]\n" \
    "ds_read_b128 v[200:203], v208 offset:11264\n" \
    "v_exp_f32 v225, v225\n" \
    "v_mfma_f32_16x16x32_bf16 v[180:183], a[100:103], v[204:207], v[180:183]\n" \
    "v_mul_f32 v144, v144, v152\n" \
    "v_mul_f32 v145, v145, v153\n" \
    "v_mfma_f32_16x16x32_bf16 v[184:187], a[164:167], v[204:207], v[184:187]\n" \
    "v_mul_f32 v146, v146, v154\n" \
    "v_mfma_f32_16x16x32_bf16 v[188:191], a[228:231], v[204:207], v[188:191]\n" \
    "v_mul_f32 v147, v147, v155\n" \
    "s_waitcnt lgkmcnt(2)\n" \
    "v_mfma_f32_16x16x32_bf16 v[160:163], a[40:43], v[192:195], v[160:163]\n" \
    "ds_read_b128 v[204:207], v208 offset:27648\n" \
    "v_mul_f32 v148, v148, v156\n" \
    "v_mfma_f32_16x16x32_bf16 v[164:167], a[104:107], v[192:195], v[164:167]\n" \
    "v_mul_f32 v149, v149, v157\n" \
    "v_mul_f32 v150, v150, v158\n" \
    "v_mfma_f32_16x16x32_bf16 v[168:171], a[168:171], v[192:195], v[168:171]\n" \
    "v_mul_f32 v151, v151, v159\n" \
    "v_mfma_f32_16x16x32_bf16 v[172:175], a[232:235], v[192:195], v[172:175]\n" \
    "v_add_f32 v218, 1.0, v218\n" \
    "s_waitcnt lgkmcnt(2)\n" \
    "v_mfma_f32_16x16x32_bf16 v[176:179], a[40:43], v[196:199], v[176:179]\n" \
    "ds_read_b128 v[192:195], v208 offset:12288\n" \
    "v_add_f32 v219, 1.0, v219\n" \
    "v_mfma_f32_16x16x32_bf16 v[180:183], a[104:107], v[196:199], v[180:183]\n" \
    "v_add_f32 v220, 1.0, v220\n" \
    "v_add_f32 v221, 1.0, v221\n" \
    "v_mfma_f32_16x16x32_bf16 v[184:187], a[168:171], v[196:199], v[184:187]\n" \
    "v_add_f32 v222, 1.0, v222\n" \
    "v_mfma_f32_16x16x32_bf16 v[188:191], a[232:235], v[196:199], v[188:191]\n" \
    "v_add_f32 v223, 1.0, v223\n" \
    "s_waitcnt lgkmcnt(2)\n" \
    "v_mfma_f32_16x16x32_bf16 v[160:163], a[44:47], v[200:203], v[160:163]\n" \
    "ds_read_b128 v[196:199], v208 offset:28672\n" \
    "v_add_f32 v224, 1.0, v224\n" \
    "v_mfma_f32_16x16x32_bf16 v[164:167], a[108:111], v[200:203], v[164:167]\n" \
    "v_add_f32 v225, 1.0, v225\n" \
    "v_rcp_f32 v218, v218\n" \
    "v_mfma_f32_16x16x32_bf16 v[168:171], a[172:175], v[200:203], v[168:171]\n" \
    "v_rcp_f32 v219, v219\n" \
    "v_mfma_f32_16x16x32_bf16 v[172:175], a[236:239], v[200:203], v[172:175]\n" \
    "v_rcp_f32 v220, v220\n" \
    "s_waitcnt lgkmcnt(2)\n" \
    "v_mfma_f32_16x16x32_bf16 v[176:179], a[44:47], v[204:207], v[176:179]\n" \
    "ds_read_b128 v[200:203], v208 offset:13312\n" \
    "v_rcp_f32 v221, v221\n" \
    "v_mfma_f32_16x16x32_bf16 v[180:183], a[108:111], v[204:207], v[180:183]\n" \
    "v_rcp_f32 v222, v222\n" \
    "v_rcp_f32 v223, v223\n" \
    "v_mfma_f32_16x16x32_bf16 v[184:187], a[172:175], v[204:207], v[184:187]\n" \
    "v_rcp_f32 v224, v224\n" \
    "v_mfma_f32_16x16x32_bf16 v[188:191], a[236:239], v[204:207], v[188:191]\n" \
    "v_rcp_f32 v225, v225\n" \
    "s_waitcnt lgkmcnt(2)\n" \
    "v_mfma_f32_16x16x32_bf16 v[160:163], a[48:51], v[192:195], v[160:163]\n" \
    "ds_read_b128 v[204:207], v208 offset:29696\n" \
    "v_mul_f32 v144, v144, v218\n" \
    "v_mfma_f32_16x16x32_bf16 v[164:167], a[112:115], v[192:195], v[164:167]\n" \
    "v_mul_f32 v145, v145, v219\n" \
    "v_mul_f32 v146, v146, v220\n" \
    "v_mfma_f32_16x16x32_bf16 v[168:171], a[176:179], v[192:195], v[168:171]\n" \
    "v_mul_f32 v147, v147, v221\n" \
    "v_mfma_f32_16x16x32_bf16 v[172:175], a[240:243], v[192:195], v[172:175]\n" \
    "v_mul_f32 v148, v148, v222\n" \
    "s_waitcnt lgkmcnt(2)\n" \
    "v_mfma_f32_16x16x32_bf16 v[176:179], a[48:51], v[196:199], v[176:179]\n" \
    "ds_read_b128 v[192:195], v208 offset:14336\n" \
    "v_mul_f32 v149, v149, v223\n" \
    "v_mfma_f32_16x16x32_bf16 v[180:183], a[112:115], v[196:199], v[180:183]\n" \
    "v_mul_f32 v150, v150, v224\n" \
    "v_mul_f32 v151, v151, v225\n" \
    "v_mfma_f32_16x16x32_bf16 v[184:187], a[176:179], v[196:199], v[184:187]\n" \
    "v_cvt_pk_bf16_f32 v230, v144, v145\n" \
    "v_mfma_f32_16x16x32_bf16 v[188:191], a[240:243], v[196:199], v[188:191]\n" \
    "v_cvt_pk_bf16_f32 v231, v146, v147\n" \
    "s_waitcnt lgkmcnt(2)\n" \
    "v_mfma_f32_16x16x32_bf16 v[160:163], a[52:55], v[200:203], v[160:163]\n" \
    "ds_read_b128 v[196:199], v208 offset:30720\n" \
    "v_cvt_pk_bf16_f32 v232, v148, v149\n" \
    "v_mfma_f32_16x16x32_bf16 v[164:167], a[116:119], v[200:203], v[164:167]\n" \
    "v_cvt_pk_bf16_f32 v233, v150, v151\n" \
    "v_mfma_f32_16x16x32_bf16 v[168:171], a[180:183], v[200:203], v[168:171]\n" \
    "v_mfma_f32_16x16x32_bf16 v[172:175], a[244:247], v[200:203], v[172:175]\n" \
    "s_waitcnt lgkmcnt(2)\n" \
    "v_mfma_f32_16x16x32_bf16 v[176:179], a[52:55], v[204:207], v[176:179]\n" \
    "ds_read_b128 v[200:203], v208 offset:15360\n" \
    "v_mfma_f32_16x16x32_bf16 v[180:183], a[116:119], v[204:207], v[180:183]\n" \
    "v_mfma_f32_16x16x32_bf16 v[184:187], a[180:183], v[204:207], v[184:187]\n" \
    "v_mfma_f32_16x16x32_bf16 v[188:191], a[244:247], v[204:207], v[188:191]\n" \
    "s_waitcnt lgkmcnt(2)\n" \
    "v_mfma_f32_16x16x32_bf16 v[160:163], a[56:59], v[192:195], v[160:163]\n" \
    "ds_read_b128 v[204:207], v208 offset:31744\n" \
    "v_mfma_f32_16x16x32_bf16 v[164:167], a[120:123], v[192:195], v[164:167]\n" \
    "v_mfma_f32_16x16x32_bf16 v[168:171], a[184:187], v[192:195], v[168:171]\n" \
    "v_mfma_f32_16x16x32_bf16 v[172:175], a[248:251], v[192:195], v[172:175]\n" \
    "s_waitcnt lgkmcnt(2)\n" \
    "v_mfma_f32_16x16x32_bf16 v[176:179], a[56:59], v[196:199], v[176:179]\n" \
    "v_mfma_f32_16x16x32_bf16 v[180:183], a[120:123], v[196:199], v[180:183]\n" \
    "v_mfma_f32_16x16x32_bf16 v[184:187], a[184:187], v[196:199], v[184:187]\n" \
    "s_waitcnt vmcnt(0)\n" \
    "v_mfma_f32_16x16x32_bf16 v[188:191], a[248:251], v[196:199], v[188:191]\n" \
    "s_waitcnt lgkmcnt(1)\n" \
    "v_mfma_f32_16x16x32_bf16 v[160:163], a[60:63], v[200:203], v[160:163]\n" \
    "v_mfma_f32_16x16x32_bf16 v[164:167], a[124:127], v[200:203], v[164:167]\n" \
    "v_mfma_f32_16x16x32_bf16 v[168:171], a[188:191], v[200:203], v[168:171]\n" \
    "buffer_store_dwordx4 v[226:229], v253, s[36:39], s47 offen sc1\n" \
    "s_add_u32 s54, s47, 57344\n" \
    "buffer_store_dwordx4 v[230:233], v253, s[36:39], s54 offen sc1\n" \
    "s_add_u32 s47, s47, 458752\n" \
    "v_mfma_f32_16x16x32_bf16 v[172:175], a[252:255], v[200:203], v[172:175]\n" \
    "s_waitcnt lgkmcnt(0)\n" \
    "v_mfma_f32_16x16x32_bf16 v[176:179], a[60:63], v[204:207], v[176:179]\n" \
    "v_mfma_f32_16x16x32_bf16 v[180:183], a[124:127], v[204:207], v[180:183]\n" \
    "v_mfma_f32_16x16x32_bf16 v[184:187], a[188:191], v[204:207], v[184:187]\n" \
    "v_mfma_f32_16x16x32_bf16 v[188:191], a[252:255], v[204:207], v[188:191]\n" \
    "s_add_u32 s44, s44, 1\n" \
    "s_cmp_eq_u32 s44, 16\n" \
    "s_cbranch_scc1 FT%=\n" \
    "s_and_b32 s55, s46, 32768\n" \
    "v_add_u32 v208, s55, v253\n" \
    "s_xor_b32 s46, s46, 32768\n" \
    "s_add_u32 s45, s45, 131072\n" \
    "s_barrier\n" \
    "ds_read_b128 v[192:195], v208\n" \
    "ds_read_b128 v[196:199], v208 offset:16384\n" \
    "ds_read_b128 v[200:203], v208 offset:1024\n" \
    "s_waitcnt lgkmcnt(2)\n" \
    "v_mfma_f32_16x16x32_bf16 v[128:131], a[0:3], v[192:195], 0\n" \
    "ds_read_b128 v[204:207], v208 offset:17408\n" \
    "v_mfma_f32_16x16x32_bf16 v[132:135], a[64:67], v[192:195], 0\n" \
    "v_mfma_f32_16x16x32_bf16 v[136:139], a[128:131], v[192:195], 0\n" \
    "v_mfma_f32_16x16x32_bf16 v[140:143], a[192:195], v[192:195], 0\n" \
    "s_waitcnt lgkmcnt(2)\n" \
    "v_mfma_f32_16x16x32_bf16 v[144:147], a[0:3], v[196:199], 0\n" \
    "ds_read_b128 v[192:195], v208 offset:2048\n" \
    "v_mfma_f32_16x16x32_bf16 v[148:151], a[64:67], v[196:199], 0\n" \
    "v_mfma_f32_16x16x32_bf16 v[152:155], a[128:131], v[196:199], 0\n" \
    "v_mfma_f32_16x16x32_bf16 v[156:159], a[192:195], v[196:199], 0\n" \
    "s_waitcnt lgkmcnt(2)\n" \
    "v_mfma_f32_16x16x32_bf16 v[128:131], a[4:7], v[200:203], v[128:131]\n" \
    "ds_read_b128 v[196:199], v208 offset:18432\n" \
    "v_mul_f32 v210, 0xbfb8aa3b, v160\n" \
    "v_mfma_f32_16x16x32_bf16 v[132:135], a[68:71], v[200:203], v[132:135]\n" \
    "v_mul_f32 v211, 0xbfb8aa3b, v161\n" \
    "v_mul_f32 v212, 0xbfb8aa3b, v162\n" \
    "v_mfma_f32_16x16x32_bf16 v[136:139], a[132:135], v[200:203], v[136:139]\n" \
    "v_mul_f32 v213, 0xbfb8aa3b, v163\n" \
    "v_mfma_f32_16x16x32_bf16 v[140:143], a[196:199], v[200:203], v[140:143]\n" \
    "v_mul_f32 v214, 0xbfb8aa3b, v164\n" \
    "s_waitcnt lgkmcnt(2)\n" \
    "v_mfma_f32_16x16x32_bf16 v[144:147], a[4:7], v[204:207], v[144:147]\n" \
    "ds_read_b128 v[200:203], v208 offset:3072\n" \
    "v_mul_f32 v215, 0xbfb8aa3b, v165\n" \
    "v_mfma_f32_16x16x32_bf16 v[148:151], a[68:71], v[204:207], v[148:151]\n" \
    "v_mul_f32 v216, 0xbfb8aa3b, v166\n" \
    "v_mul_f32 v217, 0xbfb8aa3b, v167\n" \
    "v_mfma_f32_16x16x32_bf16 v[152:155], a[132:135], v[204:207], v[152:155]\n" \
    "v_exp_f32 v210, v210\n" \
    "v_mfma_f32_16x16x32_bf16 v[156:159], a[196:199], v[204:207], v[156:159]\n" \
    "v_exp_f32 v211, v211\n" \
    "s_waitcnt lgkmcnt(2)\n" \
    "v_mfma_f32_16x16x32_bf16 v[128:131], a[8:11], v[192:195], v[128:131]\n" \
    "ds_read_b128 v[204:207], v208 offset:19456\n" \
    "v_exp_f32 v212, v212\n" \
    "v_mfma_f32_16x16x32_bf16 v[132:135], a[72:75], v[192:195], v[132:135]\n" \
    "v_exp_f32 v213, v213\n" \
    "v_exp_f32 v214, v214\n" \
    "v_mfma_f32_16x16x32_bf16 v[136:139], a[136:139], v[192:195], v[136:139]\n" \
    "v_exp_f32 v215, v215\n" \
    "v_mfma_f32_16x16x32_bf16 v[140:143], a[200:203], v[192:195], v[140:143]\n" \
    "v_exp_f32 v216, v216\n" \
    "s_waitcnt lgkmcnt(2)\n" \
    "v_mfma_f32_16x16x32_bf16 v[144:147], a[8:11], v[196:199], v[144:147]\n" \
    "ds_read_b128 v[192:195], v208 offset:4096\n" \
    "v_exp_f32 v217, v217\n" \
    "v_mfma_f32_16x16x32_bf16 v[148:151], a[72:75], v[196:199], v[148:151]\n" \
    "v_mul_f32 v160, v160, v168\n" \
    "v_mul_f32 v161, v161, v169\n" \
    "v_mfma_f32_16x16x32_bf16 v[152:155], a[136:139], v[196:199], v[152:155]\n" \
    "v_mul_f32 v162, v162, v170\n" \
    "v_mfma_f32_16x16x32_bf16 v[156:159], a[200:203], v[196:199], v[156:159]\n" \
    "v_mul_f32 v163, v163, v171\n" \
    "s_waitcnt lgkmcnt(2)\n" \
    "v_mfma_f32_16x16x32_bf16 v[128:131], a[12:15], v[200:203], v[128:131]\n" \
    "ds_read_b128 v[196:199], v208 offset:20480\n" \
    "v_mul_f32 v164, v164, v172\n" \
    "v_mfma_f32_16x16x32_bf16 v[132:135], a[76:79], v[200:203], v[132:135]\n" \
    "v_mul_f32 v165, v165, v173\n" \
    "v_mfma_f32_16x16x32_bf16 v[136:139], a[140:143], v[200:203], v[136:139]\n" \
    "s_waitcnt vmcnt(0)\n" \
    "s_barrier\n" \
    "s_sub_i32 s54, s44, 1\n" \
    "s_cmp_lt_i32 s54, 1\n" \
    "s_cbranch_scc1 FC%=\n" \
    "s_lshl_b32 s54, s54, 1\n" \
    "v_mov_b32 v209, s54\n" \
    "buffer_store_dword v209, v252, s[36:39], s48 offen sc1\n" \
    "FC%=:\n" \
    "v_mfma_f32_16x16x32_bf16 v[140:143], a[204:207], v[200:203], v[140:143]\n" \
    "v_mul_f32 v166, v166, v174\n" \
    "s_waitcnt lgkmcnt(2)\n" \
    "v_mfma_f32_16x16x32_bf16 v[144:147], a[12:15], v[204:207], v[144:147]\n" \
    "ds_read_b128 v[200:203], v208 offset:5120\n" \
    "v_mul_f32 v167, v167, v175\n" \
    "v_mfma_f32_16x16x32_bf16 v[148:151], a[76:79], v[204:207], v[148:151]\n" \
    "v_add_f32 v210, 1.0, v210\n" \
    "v_mfma_f32_16x16x32_bf16 v[152:155], a[140:143], v[204:207], v[152:155]\n" \
    "s_add_u32 m0, s46, 0\n" \
    "s_add_u32 s54, s45, 0\n" \
    "buffer_load_dwordx4 v253, s[36:39], s54 offen sc1 lds\n" \
    "v_mfma_f32_16x16x32_bf16 v[156:159], a[204:207], v[204:207], v[156:159]\n" \
    "v_add_f32 v211, 1.0, v211\n" \
    "s_waitcnt lgkmcnt(2)\n" \
    "v_mfma_f32_16x16x32_bf16 v[128:131], a[16:19], v[192:195], v[128:131]\n" \
    "ds_read_b128 v[204:207], v208 offset:21504\n" \
    "v_add_f32 v212, 1.0, v212\n" \
    "v_mfma_f32_16x16x32_bf16 v[132:135], a[80:83], v[192:195], v[132:135]\n" \
    "v_add_f32 v213, 1.0, v213\n" \
    "v_mfma_f32_16x16x32_bf16 v[136:139], a[144:147], v[192:195], v[136:139]\n" \
    "s_add_u32 m0, s46, 1024\n" \
    "s_add_u32 s54, s45, 1024\n" \
    "buffer_load_dwordx4 v253, s[36:39], s54 offen sc1 lds\n" \
    "v_mfma_f32_16x16x32_bf16 v[140:143], a[208:211], v[192:195], v[140:143]\n" \
    "v_add_f32 v214, 1.0, v214\n" \
    "s_waitcnt lgkmcnt(2)\n" \
    "v_mfma_f32_16x16x32_bf16 v[144:147], a[16:19], v[196:199], v[144:147]\n" \
    "ds_read_b128 v[192:195], v208 offset:6144\n" \
    "v_add_f32 v215, 1.0, v215\n" \
    "v_mfma_f32_16x16x32_bf16 v[148:151], a[80:83], v[196:199], v[148:151]\n" \
    "v_add_f32 v216, 1.0, v216\n" \
    "v_mfma_f32_16x16x32_bf16 v[152:155], a[144:147], v[196:199], v[152:155]\n" \
    "s_add_u32 m0, s46, 2048\n" \
    "s_add_u32 s54, s45, 2048\n" \
    "buffer_load_dwordx4 v253, s[36:39], s54 offen sc1 lds\n" \
    "v_mfma_f32_16x16x32_bf16 v[156:159], a[208:211], v[196:199], v[156:159]\n" \
    "v_add_f32 v217, 1.0, v217\n" \
    "s_waitcnt lgkmcnt(2)\n" \
    "v_mfma_f32_16x16x32_bf16 v[128:131], a[20:23], v[200:203], v[128:131]\n" \
    "ds_read_b128 v[196:199], v208 offset:22528\n" \
    "v_rcp_f32 v210, v210\n" \
    "v_mfma_f32_16x16x32_bf16 v[132:135], a[84:87], v[200:203], v[132:135]\n" \
    "v_rcp_f32 v211, v211\n" \
    "v_mfma_f32_16x16x32_bf16 v[136:139], a[148:151], v[200:203], v[136:139]\n" \
    "s_add_u32 m0, s46, 3072\n" \
    "s_add_u32 s54, s45, 3072\n" \
    "buffer_load_dwordx4 v253, s[36:39], s54 offen sc1 lds\n" \
    "v_mfma_f32_16x16x32_bf16 v[140:143], a[212:215], v[200:203], v[140:143]\n" \
    "v_rcp_f32 v212, v212\n" \
    "s_waitcnt lgkmcnt(2)\n" \
    "v_mfma_f32_16x16x32_bf16 v[144:147], a[20:23], v[204:207], v[144:147]\n" \
    "ds_read_b128 v[200:203], v208 offset:7168\n" \
    "v_rcp_f32 v213, v213\n" \
    "v_mfma_f32_16x16x32_bf16 v[148:151], a[84:87], v[204:207], v[148:151]\n" \
    "v_rcp_f32 v214, v214\n" \
    "v_mfma_f32_16x16x32_bf16 v[152:155], a[148:151], v[204:207], v[152:155]\n" \
    "s_add_u32 m0, s46, 4096\n" \
    "s_add_u32 s54, s45, 4096\n" \
    "buffer_load_dwordx4 v253, s[36:39], s54 offen sc1 lds\n" \
    "v_mfma_f32_16x16x32_bf16 v[156:159], a[212:215], v[204:207], v[156:159]\n" \
    "v_rcp_f32 v215, v215\n" \
    "s_waitcnt lgkmcnt(2)\n" \
    "v_mfma_f32_16x16x32_bf16 v[128:131], a[24:27], v[192:195], v[128:131]\n" \
    "ds_read_b128 v[204:207], v208 offset:23552\n" \
    "v_rcp_f32 v216, v216\n" \
    "v_mfma_f32_16x16x32_bf16 v[132:135], a[88:91], v[192:195], v[132:135]\n" \
    "v_rcp_f32 v217, v217\n" \
    "v_mfma_f32_16x16x32_bf16 v[136:139], a[152:155], v[192:195], v[136:139]\n" \
    "s_add_u32 m0, s46, 5120\n" \
    "s_add_u32 s54, s45, 5120\n" \
    "buffer_load_dwordx4 v253, s[36:39], s54 offen sc1 lds\n" \
    "v_mfma_f32_16x16x32_bf16 v[140:143], a[216:219], v[192:195], v[140:143]\n" \
    "v_mul_f32 v160, v160, v210\n" \
    "s_waitcnt lgkmcnt(2)\n" \
    "v_mfma_f32_16x16x32_bf16 v[144:147], a[24:27], v[196:199], v[144:147]\n" \
    "ds_read_b128 v[192:195], v208 offset:8192\n" \
    "v_mul_f32 v161, v161, v211\n" \
    "v_mfma_f32_16x16x32_bf16 v[148:151], a[88:91], v[196:199], v[148:151]\n" \
    "v_mul_f32 v162, v162, v212\n" \
    "v_mfma_f32_16x16x32_bf16 v[152:155], a[152:155], v[196:199], v[152:155]\n" \
    "s_add_u32 m0, s46, 6144\n" \
    "s_add_u32 s54, s45, 6144\n" \
    "buffer_load_dwordx4 v253, s[36:39], s54 offen sc1 lds\n" \
    "v_mfma_f32_16x16x32_bf16 v[156:159], a[216:219], v[196:199], v[156:159]\n" \
    "v_mul_f32 v163, v163, v213\n" \
    "s_waitcnt lgkmcnt(2)\n" \
    "v_mfma_f32_16x16x32_bf16 v[128:131], a[28:31], v[200:203], v[128:131]\n" \
    "ds_read_b128 v[196:199], v208 offset:24576\n" \
    "v_mul_f32 v164, v164, v214\n" \
    "v_mfma_f32_16x16x32_bf16 v[132:135], a[92:95], v[200:203], v[132:135]\n" \
    "v_mul_f32 v165, v165, v215\n" \
    "v_mfma_f32_16x16x32_bf16 v[136:139], a[156:159], v[200:203], v[136:139]\n" \
    "s_add_u32 m0, s46, 7168\n" \
    "s_add_u32 s54, s45, 7168\n" \
    "buffer_load_dwordx4 v253, s[36:39], s54 offen sc1 lds\n" \
    "v_mfma_f32_16x16x32_bf16 v[140:143], a[220:223], v[200:203], v[140:143]\n" \
    "v_mul_f32 v166, v166, v216\n" \
    "s_waitcnt lgkmcnt(2)\n" \
    "v_mfma_f32_16x16x32_bf16 v[144:147], a[28:31], v[204:207], v[144:147]\n" \
    "ds_read_b128 v[200:203], v208 offset:9216\n" \
    "v_mul_f32 v167, v167, v217\n" \
    "v_mfma_f32_16x16x32_bf16 v[148:151], a[92:95], v[204:207], v[148:151]\n" \
    "v_cvt_pk_bf16_f32 v226, v160, v161\n" \
    "v_cvt_pk_bf16_f32 v227, v162, v163\n" \
    "v_mfma_f32_16x16x32_bf16 v[152:155], a[156:159], v[204:207], v[152:155]\n" \
    "v_cvt_pk_bf16_f32 v228, v164, v165\n" \
    "v_mfma_f32_16x16x32_bf16 v[156:159], a[220:223], v[204:207], v[156:159]\n" \
    "v_cvt_pk_bf16_f32 v229, v166, v167\n" \
    "s_waitcnt lgkmcnt(2)\n" \
    "v_mfma_f32_16x16x32_bf16 v[128:131], a[32:35], v[192:195], v[128:131]\n" \
    "ds_read_b128 v[204:207], v208 offset:25600\n" \
    "v_mul_f32 v218, 0xbfb8aa3b, v176\n" \
    "v_mfma_f32_16x16x32_bf16 v[132:135], a[96:99], v[192:195], v[132:135]\n" \
    "v_mul_f32 v219, 0xbfb8aa3b, v177\n" \
    "v_mul_f32 v220, 0xbfb8aa3b, v178\n" \
    "v_mfma_f32_16x16x32_bf16 v[136:139], a[160:163], v[192:195], v[136:139]\n" \
    "v_mul_f32 v221, 0xbfb8aa3b, v179\n" \
    "v_mfma_f32_16x16x32_bf16 v[140:143], a[224:227], v[192:195], v[140:143]\n" \
    "v_mul_f32 v222, 0xbfb8aa3b, v180\n" \
    "s_waitcnt lgkmcnt(2)\n" \
    "v_mfma_f32_16x16x32_bf16 v[144:147], a[32:35], v[196:199], v[144:147]\n" \
    "ds_read_b128 v[192:195], v208 offset:10240\n" \
    "v_mul_f32 v223, 0xbfb8aa3b, v181\n" \
    "v_mfma_f32_16x16x32_bf16 v[148:151], a[96:99], v[196:199], v[148:151]\n" \
    "v_mul_f32 v224, 0xbfb8aa3b, v182\n" \
    "v_mul_f32 v225, 0xbfb8aa3b, v183\n" \
    "v_mfma_f32_16x16x32_bf16 v[152:155], a[160:163], v[196:199], v[152:155]\n" \
    "v_exp_f32 v218, v218\n" \
    "v_mfma_f32_16x16x32_bf16 v[156:159], a[224:227], v[196:199], v[156:159]\n" \
    "v_exp_f32 v219, v219\n" \
    "s_waitcnt lgkmcnt(2)\n" \
    "v_mfma_f32_16x16x32_bf16 v[128:131], a[36:39], v[200:203], v[128:131]\n" \
    "ds_read_b128 v[196:199], v208 offset:26624\n" \
    "v_exp_f32 v220, v220\n" \
    "v_mfma_f32_16x16x32_bf16 v[132:135], a[100:103], v[200:203], v[132:135]\n" \
    "v_exp_f32 v221, v221\n" \
    "v_exp_f32 v222, v222\n" \
    "v_mfma_f32_16x16x32_bf16 v[136:139], a[164:167], v[200:203], v[136:139]\n" \
    "v_exp_f32 v223, v223\n" \
    "v_mfma_f32_16x16x32_bf16 v[140:143], a[228:231], v[200:203], v[140:143]\n" \
    "v_exp_f32 v224, v224\n" \
    "s_waitcnt lgkmcnt(2)\n" \
    "v_mfma_f32_16x16x32_bf16 v[144:147], a[36:39], v[204:207], v[144:147]\n" \
    "ds_read_b128 v[200:203], v208 offset:11264\n" \
    "v_exp_f32 v225, v225\n" \
    "v_mfma_f32_16x16x32_bf16 v[148:151], a[100:103], v[204:207], v[148:151]\n" \
    "v_mul_f32 v176, v176, v184\n" \
    "v_mul_f32 v177, v177, v185\n" \
    "v_mfma_f32_16x16x32_bf16 v[152:155], a[164:167], v[204:207], v[152:155]\n" \
    "v_mul_f32 v178, v178, v186\n" \
    "v_mfma_f32_16x16x32_bf16 v[156:159], a[228:231], v[204:207], v[156:159]\n" \
    "v_mul_f32 v179, v179, v187\n" \
    "s_waitcnt lgkmcnt(2)\n" \
    "v_mfma_f32_16x16x32_bf16 v[128:131], a[40:43], v[192:195], v[128:131]\n" \
    "ds_read_b128 v[204:207], v208 offset:27648\n" \
    "v_mul_f32 v180, v180, v188\n" \
    "v_mfma_f32_16x16x32_bf16 v[132:135], a[104:107], v[192:195], v[132:135]\n" \
    "v_mul_f32 v181, v181, v189\n" \
    "v_mul_f32 v182, v182, v190\n" \
    "v_mfma_f32_16x16x32_bf16 v[136:139], a[168:171], v[192:195], v[136:139]\n" \
    "v_mul_f32 v183, v183, v191\n" \
    "v_mfma_f32_16x16x32_bf16 v[140:143], a[232:235], v[192:195], v[140:143]\n" \
    "v_add_f32 v218, 1.0, v218\n" \
    "s_waitcnt lgkmcnt(2)\n" \
    "v_mfma_f32_16x16x32_bf16 v[144:147], a[40:43], v[196:199], v[144:147]\n" \
    "ds_read_b128 v[192:195], v208 offset:12288\n" \
    "v_add_f32 v219, 1.0, v219\n" \
    "v_mfma_f32_16x16x32_bf16 v[148:151], a[104:107], v[196:199], v[148:151]\n" \
    "v_add_f32 v220, 1.0, v220\n" \
    "v_add_f32 v221, 1.0, v221\n" \
    "v_mfma_f32_16x16x32_bf16 v[152:155], a[168:171], v[196:199], v[152:155]\n" \
    "v_add_f32 v222, 1.0, v222\n" \
    "v_mfma_f32_16x16x32_bf16 v[156:159], a[232:235], v[196:199], v[156:159]\n" \
    "v_add_f32 v223, 1.0, v223\n" \
    "s_waitcnt lgkmcnt(2)\n" \
    "v_mfma_f32_16x16x32_bf16 v[128:131], a[44:47], v[200:203], v[128:131]\n" \
    "ds_read_b128 v[196:199], v208 offset:28672\n" \
    "v_add_f32 v224, 1.0, v224\n" \
    "v_mfma_f32_16x16x32_bf16 v[132:135], a[108:111], v[200:203], v[132:135]\n" \
    "v_add_f32 v225, 1.0, v225\n" \
    "v_rcp_f32 v218, v218\n" \
    "v_mfma_f32_16x16x32_bf16 v[136:139], a[172:175], v[200:203], v[136:139]\n" \
    "v_rcp_f32 v219, v219\n" \
    "v_mfma_f32_16x16x32_bf16 v[140:143], a[236:239], v[200:203], v[140:143]\n" \
    "v_rcp_f32 v220, v220\n" \
    "s_waitcnt lgkmcnt(2)\n" \
    "v_mfma_f32_16x16x32_bf16 v[144:147], a[44:47], v[204:207], v[144:147]\n" \
    "ds_read_b128 v[200:203], v208 offset:13312\n" \
    "v_rcp_f32 v221, v221\n" \
    "v_mfma_f32_16x16x32_bf16 v[148:151], a[108:111], v[204:207], v[148:151]\n" \
    "v_rcp_f32 v222, v222\n" \
    "v_rcp_f32 v223, v223\n" \
    "v_mfma_f32_16x16x32_bf16 v[152:155], a[172:175], v[204:207], v[152:155]\n" \
    "v_rcp_f32 v224, v224\n" \
    "v_mfma_f32_16x16x32_bf16 v[156:159], a[236:239], v[204:207], v[156:159]\n" \
    "v_rcp_f32 v225, v225\n" \
    "s_waitcnt lgkmcnt(2)\n" \
    "v_mfma_f32_16x16x32_bf16 v[128:131], a[48:51], v[192:195], v[128:131]\n" \
    "ds_read_b128 v[204:207], v208 offset:29696\n" \
    "v_mul_f32 v176, v176, v218\n" \
    "v_mfma_f32_16x16x32_bf16 v[132:135], a[112:115], v[192:195], v[132:135]\n" \
    "v_mul_f32 v177, v177, v219\n" \
    "v_mul_f32 v178, v178, v220\n" \
    "v_mfma_f32_16x16x32_bf16 v[136:139], a[176:179], v[192:195], v[136:139]\n" \
    "v_mul_f32 v179, v179, v221\n" \
    "v_mfma_f32_16x16x32_bf16 v[140:143], a[240:243], v[192:195], v[140:143]\n" \
    "v_mul_f32 v180, v180, v222\n" \
    "s_waitcnt lgkmcnt(2)\n" \
    "v_mfma_f32_16x16x32_bf16 v[144:147], a[48:51], v[196:199], v[144:147]\n" \
    "ds_read_b128 v[192:195], v208 offset:14336\n" \
    "v_mul_f32 v181, v181, v223\n" \
    "v_mfma_f32_16x16x32_bf16 v[148:151], a[112:115], v[196:199], v[148:151]\n" \
    "v_mul_f32 v182, v182, v224\n" \
    "v_mul_f32 v183, v183, v225\n" \
    "v_mfma_f32_16x16x32_bf16 v[152:155], a[176:179], v[196:199], v[152:155]\n" \
    "v_cvt_pk_bf16_f32 v230, v176, v177\n" \
    "v_mfma_f32_16x16x32_bf16 v[156:159], a[240:243], v[196:199], v[156:159]\n" \
    "v_cvt_pk_bf16_f32 v231, v178, v179\n" \
    "s_waitcnt lgkmcnt(2)\n" \
    "v_mfma_f32_16x16x32_bf16 v[128:131], a[52:55], v[200:203], v[128:131]\n" \
    "ds_read_b128 v[196:199], v208 offset:30720\n" \
    "v_cvt_pk_bf16_f32 v232, v180, v181\n" \
    "v_mfma_f32_16x16x32_bf16 v[132:135], a[116:119], v[200:203], v[132:135]\n" \
    "v_cvt_pk_bf16_f32 v233, v182, v183\n" \
    "v_mfma_f32_16x16x32_bf16 v[136:139], a[180:183], v[200:203], v[136:139]\n" \
    "v_mfma_f32_16x16x32_bf16 v[140:143], a[244:247], v[200:203], v[140:143]\n" \
    "s_waitcnt lgkmcnt(2)\n" \
    "v_mfma_f32_16x16x32_bf16 v[144:147], a[52:55], v[204:207], v[144:147]\n" \
    "ds_read_b128 v[200:203], v208 offset:15360\n" \
    "v_mfma_f32_16x16x32_bf16 v[148:151], a[116:119], v[204:207], v[148:151]\n" \
    "v_mfma_f32_16x16x32_bf16 v[152:155], a[180:183], v[204:207], v[152:155]\n" \
    "v_mfma_f32_16x16x32_bf16 v[156:159], a[244:247], v[204:207], v[156:159]\n" \
    "s_waitcnt lgkmcnt(2)\n" \
    "v_mfma_f32_16x16x32_bf16 v[128:131], a[56:59], v[192:195], v[128:131]\n" \
    "ds_read_b128 v[204:207], v208 offset:31744\n" \
    "v_mfma_f32_16x16x32_bf16 v[132:135], a[120:123], v[192:195], v[132:135]\n" \
    "v_mfma_f32_16x16x32_bf16 v[136:139], a[184:187], v[192:195], v[136:139]\n" \
    "v_mfma_f32_16x16x32_bf16 v[140:143], a[248:251], v[192:195], v[140:143]\n" \
    "s_waitcnt lgkmcnt(2)\n" \
    "v_mfma_f32_16x16x32_bf16 v[144:147], a[56:59], v[196:199], v[144:147]\n" \
    "v_mfma_f32_16x16x32_bf16 v[148:151], a[120:123], v[196:199], v[148:151]\n" \
    "v_mfma_f32_16x16x32_bf16 v[152:155], a[184:187], v[196:199], v[152:155]\n" \
    "s_waitcnt vmcnt(0)\n" \
    "v_mfma_f32_16x16x32_bf16 v[156:159], a[248:251], v[196:199], v[156:159]\n" \
    "s_waitcnt lgkmcnt(1)\n" \
    "v_mfma_f32_16x16x32_bf16 v[128:131], a[60:63], v[200:203], v[128:131]\n" \
    "v_mfma_f32_16x16x32_bf16 v[132:135], a[124:127], v[200:203], v[132:135]\n" \
    "v_mfma_f32_16x16x32_bf16 v[136:139], a[188:191], v[200:203], v[136:139]\n" \
    "buffer_store_dwordx4 v[226:229], v253, s[36:39], s47 offen sc1\n" \
    "s_add_u32 s54, s47, 57344\n" \
    "buffer_store_dwordx4 v[230:233], v253, s[36:39], s54 offen sc1\n" \
    "s_add_u32 s47, s47, 458752\n" \
    "v_mfma_f32_16x16x32_bf16 v[140:143], a[252:255], v[200:203], v[140:143]\n" \
    "s_waitcnt lgkmcnt(0)\n" \
    "v_mfma_f32_16x16x32_bf16 v[144:147], a[60:63], v[204:207], v[144:147]\n" \
    "v_mfma_f32_16x16x32_bf16 v[148:151], a[124:127], v[204:207], v[148:151]\n" \
    "v_mfma_f32_16x16x32_bf16 v[152:155], a[188:191], v[204:207], v[152:155]\n" \
    "v_mfma_f32_16x16x32_bf16 v[156:159], a[252:255], v[204:207], v[156:159]\n" \
    "s_add_u32 s44, s44, 1\n" \
    "s_branch FL%=\n" \
    "FT%=:\n" \
    "s_nop 7\n" \
    "v_mul_f32 v210, 0xbfb8aa3b, v160\n" \
    "v_mul_f32 v211, 0xbfb8aa3b, v161\n" \
    "v_mul_f32 v212, 0xbfb8aa3b, v162\n" \
    "v_mul_f32 v213, 0xbfb8aa3b, v163\n" \
    "v_mul_f32 v214, 0xbfb8aa3b, v164\n" \
    "v_mul_f32 v215, 0xbfb8aa3b, v165\n" \
    "v_mul_f32 v216, 0xbfb8aa3b, v166\n" \
    "v_mul_f32 v217, 0xbfb8aa3b, v167\n" \
    "v_exp_f32 v210, v210\n" \
    "v_exp_f32 v211, v211\n" \
    "v_exp_f32 v212, v212\n" \
    "v_exp_f32 v213, v213\n" \
    "v_exp_f32 v214, v214\n" \
    "v_exp_f32 v215, v215\n" \
    "v_exp_f32 v216, v216\n" \
    "v_exp_f32 v217, v217\n" \
    "v_mul_f32 v160, v160, v168\n" \
    "v_mul_f32 v161, v161, v169\n" \
    "v_mul_f32 v162, v162, v170\n" \
    "v_mul_f32 v163, v163, v171\n" \
    "v_mul_f32 v164, v164, v172\n" \
    "v_mul_f32 v165, v165, v173\n" \
    "v_mul_f32 v166, v166, v174\n" \
    "v_mul_f32 v167, v167, v175\n" \
    "v_add_f32 v210, 1.0, v210\n" \
    "v_add_f32 v211, 1.0, v211\n" \
    "v_add_f32 v212, 1.0, v212\n" \
    "v_add_f32 v213, 1.0, v213\n" \
    "v_add_f32 v214, 1.0, v214\n" \
    "v_add_f32 v215, 1.0, v215\n" \
    "v_add_f32 v216, 1.0, v216\n" \
    "v_add_f32 v217, 1.0, v217\n" \
    "v_rcp_f32 v210, v210\n" \
    "v_rcp_f32 v211, v211\n" \
    "v_rcp_f32 v212, v212\n" \
    "v_rcp_f32 v213, v213\n" \
    "v_rcp_f32 v214, v214\n" \
    "v_rcp_f32 v215, v215\n" \
    "v_rcp_f32 v216, v216\n" \
    "v_rcp_f32 v217, v217\n" \
    "v_mul_f32 v160, v160, v210\n" \
    "v_mul_f32 v161, v161, v211\n" \
    "v_mul_f32 v162, v162, v212\n" \
    "v_mul_f32 v163, v163, v213\n" \
    "v_mul_f32 v164, v164, v214\n" \
    "v_mul_f32 v165, v165, v215\n" \
    "v_mul_f32 v166, v166, v216\n" \
    "v_mul_f32 v167, v167, v217\n" \
    "v_cvt_pk_bf16_f32 v226, v160, v161\n" \
    "v_cvt_pk_bf16_f32 v227, v162, v163\n" \
    "v_cvt_pk_bf16_f32 v228, v164, v165\n" \
    "v_cvt_pk_bf16_f32 v229, v166, v167\n" \
    "v_mul_f32 v218, 0xbfb8aa3b, v176\n" \
    "v_mul_f32 v219, 0xbfb8aa3b, v177\n" \
    "v_mul_f32 v220, 0xbfb8aa3b, v178\n" \
    "v_mul_f32 v221, 0xbfb8aa3b, v179\n" \
    "v_mul_f32 v222, 0xbfb8aa3b, v180\n" \
    "v_mul_f32 v223, 0xbfb8aa3b, v181\n" \
    "v_mul_f32 v224, 0xbfb8aa3b, v182\n" \
    "v_mul_f32 v225, 0xbfb8aa3b, v183\n" \
    "v_exp_f32 v218, v218\n" \
    "v_exp_f32 v219, v219\n" \
    "v_exp_f32 v220, v220\n" \
    "v_exp_f32 v221, v221\n" \
    "v_exp_f32 v222, v222\n" \
    "v_exp_f32 v223, v223\n" \
    "v_exp_f32 v224, v224\n" \
    "v_exp_f32 v225, v225\n" \
    "v_mul_f32 v176, v176, v184\n" \
    "v_mul_f32 v177, v177, v185\n" \
    "v_mul_f32 v178, v178, v186\n" \
    "v_mul_f32 v179, v179, v187\n" \
    "v_mul_f32 v180, v180, v188\n" \
    "v_mul_f32 v181, v181, v189\n" \
    "v_mul_f32 v182, v182, v190\n" \
    "v_mul_f32 v183, v183, v191\n" \
    "v_add_f32 v218, 1.0, v218\n" \
    "v_add_f32 v219, 1.0, v219\n" \
    "v_add_f32 v220, 1.0, v220\n" \
    "v_add_f32 v221, 1.0, v221\n" \
    "v_add_f32 v222, 1.0, v222\n" \
    "v_add_f32 v223, 1.0, v223\n" \
    "v_add_f32 v224, 1.0, v224\n" \
    "v_add_f32 v225, 1.0, v225\n" \
    "v_rcp_f32 v218, v218\n" \
    "v_rcp_f32 v219, v219\n" \
    "v_rcp_f32 v220, v220\n" \
    "v_rcp_f32 v221, v221\n" \
    "v_rcp_f32 v222, v222\n" \
    "v_rcp_f32 v223, v223\n" \
    "v_rcp_f32 v224, v224\n" \
    "v_rcp_f32 v225, v225\n" \
    "v_mul_f32 v176, v176, v218\n" \
    "v_mul_f32 v177, v177, v219\n" \
    "v_mul_f32 v178, v178, v220\n" \
    "v_mul_f32 v179, v179, v221\n" \
    "v_mul_f32 v180, v180, v222\n" \
    "v_mul_f32 v181, v181, v223\n" \
    "v_mul_f32 v182, v182, v224\n" \
    "v_mul_f32 v183, v183, v225\n" \
    "v_cvt_pk_bf16_f32 v230, v176, v177\n" \
    "v_cvt_pk_bf16_f32 v231, v178, v179\n" \
    "v_cvt_pk_bf16_f32 v232, v180, v181\n" \
    "v_cvt_pk_bf16_f32 v233, v182, v183\n" \
    "buffer_store_dwordx4 v[226:229], v253, s[36:39], s47 offen sc1\n" \
    "s_add_u32 s54, s47, 57344\n" \
    "buffer_store_dwordx4 v[230:233], v253, s[36:39], s54 offen sc1\n" \
    "s_add_u32 s47, s47, 458752\n" \
    "s_waitcnt vmcnt(0)\n" \
    "s_barrier\n" \
    "s_sub_i32 s54, s44, 0\n" \
    "s_cmp_lt_i32 s54, 1\n" \
    "s_cbranch_scc1 FD%=\n" \
    "s_lshl_b32 s54, s54, 1\n" \
    "v_mov_b32 v209, s54\n" \
    "buffer_store_dword v209, v252, s[36:39], s48 offen sc1\n" \
    "FD%=:\n" \
    "FE%=:\n" \
    "s_waitcnt vmcnt(0)\n" \
    "s_barrier\n" \
    "s_mul_i32 s46, s51, 40\n" \
    "s_mov_b32 s43, 0x20000\n" \
    "s_mov_b32 s40, s12\n" \
    "s_and_b32 s41, s13, 0xffff\n" \
    "s_mov_b32 s42, s14\n" \
    "s_add_u32 m0, s46, 0\n" \
    "s_add_u32 s54, s15, 0\n" \
    "buffer_load_dwordx4 v244, s[40:43], s54 offen lds\n" \
    "s_sleep 3\n" \
    "s_add_u32 m0, s46, 1024\n" \
    "s_add_u32 s54, s15, 8192\n" \
    "buffer_load_dwordx4 v244, s[40:43], s54 offen lds\n" \
    "s_sleep 3\n" \
    "s_add_u32 m0, s46, 2048\n" \
    "s_add_u32 s54, s15, 16384\n" \
    "buffer_load_dwordx4 v244, s[40:43], s54 offen lds\n" \
    "s_sleep 3\n" \
    "s_add_u32 m0, s46, 3072\n" \
    "s_add_u32 s54, s15, 24576\n" \
    "buffer_load_dwordx4 v244, s[40:43], s54 offen lds\n" \
    "s_sleep 3\n" \
    "s_add_u32 m0, s46, 4096\n" \
    "s_add_u32 s54, s15, 32768\n" \
    "buffer_load_dwordx4 v244, s[40:43], s54 offen lds\n" \
    "s_sleep 3\n" \
    "s_add_u32 m0, s46, 5120\n" \
    "s_add_u32 s54, s15, 40960\n" \
    "buffer_load_dwordx4 v244, s[40:43], s54 offen lds\n" \
    "s_sleep 3\n" \
    "s_add_u32 m0, s46, 6144\n" \
    "s_add_u32 s54, s15, 49152\n" \
    "buffer_load_dwordx4 v244, s[40:43], s54 offen lds\n" \
    "s_sleep 3\n" \
    "s_add_u32 m0, s46, 7168\n" \
    "s_add_u32 s54, s15, 57344\n" \
    "buffer_load_dwordx4 v244, s[40:43], s54 offen lds\n" \
    "s_sleep 3\n" \
    "s_add_u32 m0, s46, 8192\n" \
    "s_add_u32 s54, s15, 65536\n" \
    "buffer_load_dwordx4 v244, s[40:43], s54 offen lds\n" \
    "s_sleep 3\n" \
    "s_add_u32 m0, s46, 9216\n" \
    "s_add_u32 s54, s15, 73728\n" \
    "buffer_load_dwordx4 v244, s[40:43], s54 offen lds\n" \
    "s_sleep 3\n" \
    "s_add_u32 m0, s46, 10240\n" \
    "s_add_u32 s54, s15, 81920\n" \
    "buffer_load_dwordx4 v244, s[40:43], s54 offen lds\n" \
    "s_sleep 3\n" \
    "s_add_u32 m0, s46, 11264\n" \
    "s_add_u32 s54, s15, 90112\n" \
    "buffer_load_dwordx4 v244, s[40:43], s54 offen lds\n" \
    "s_sleep 3\n" \
    "s_add_u32 m0, s46, 12288\n" \
    "s_add_u32 s54, s15, 98304\n" \
    "buffer_load_dwordx4 v244, s[40:43], s54 offen lds\n" \
    "s_sleep 3\n" \
    "s_add_u32 m0, s46, 13312\n" \
    "s_add_u32 s54, s15, 106496\n" \
    "buffer_load_dwordx4 v244, s[40:43], s54 offen lds\n" \
    "s_sleep 3\n" \
    "s_add_u32 m0, s46, 14336\n" \
    "s_add_u32 s54, s15, 114688\n" \
    "buffer_load_dwordx4 v244, s[40:43], s54 offen lds\n" \
    "s_sleep 3\n" \
    "s_add_u32 m0, s46, 15360\n" \
    "s_add_u32 s54, s15, 122880\n" \
    "buffer_load_dwordx4 v244, s[40:43], s54 offen lds\n" \
    "s_sleep 3\n" \
    "s_add_u32 m0, s46, 16384\n" \
    "s_add_u32 s54, s15, 131072\n" \
    "buffer_load_dwordx4 v244, s[40:43], s54 offen lds\n" \
    "s_sleep 3\n" \
    "s_add_u32 m0, s46, 17408\n" \
    "s_add_u32 s54, s15, 139264\n" \
    "buffer_load_dwordx4 v244, s[40:43], s54 offen lds\n" \
    "s_sleep 3\n" \
    "s_add_u32 m0, s46, 18432\n" \
    "s_add_u32 s54, s15, 147456\n" \
    "buffer_load_dwordx4 v244, s[40:43], s54 offen lds\n" \
    "s_sleep 3\n" \
    "s_add_u32 m0, s46, 19456\n" \
    "s_add_u32 s54, s15, 155648\n" \
    "buffer_load_dwordx4 v244, s[40:43], s54 offen lds\n" \
    "s_sleep 3\n" \
    "s_add_u32 m0, s46, 20480\n" \
    "s_add_u32 s54, s15, 163840\n" \
    "buffer_load_dwordx4 v244, s[40:43], s54 offen lds\n" \
    "s_sleep 3\n" \
    "s_add_u32 m0, s46, 21504\n" \
    "s_add_u32 s54, s15, 172032\n" \
    "buffer_load_dwordx4 v244, s[40:43], s54 offen lds\n" \
    "s_sleep 3\n" \
    "s_add_u32 m0, s46, 22528\n" \
    "s_add_u32 s54, s15, 180224\n" \
    "buffer_load_dwordx4 v244, s[40:43], s54 offen lds\n" \
    "s_sleep 3\n" \
    "s_add_u32 m0, s46, 23552\n" \
    "s_add_u32 s54, s15, 188416\n" \
    "buffer_load_dwordx4 v244, s[40:43], s54 offen lds\n" \
    "s_sleep 3\n" \
    "s_add_u32 m0, s46, 24576\n" \
    "s_add_u32 s54, s15, 196608\n" \
    "buffer_load_dwordx4 v244, s[40:43], s54 offen lds\n" \
    "s_sleep 3\n" \
    "s_add_u32 m0, s46, 25600\n" \
    "s_add_u32 s54, s15, 204800\n" \
    "buffer_load_dwordx4 v244, s[40:43], s54 offen lds\n" \
    "s_sleep 3\n" \
    "s_add_u32 m0, s46, 26624\n" \
    "s_add_u32 s54, s15, 212992\n" \
    "buffer_load_dwordx4 v244, s[40:43], s54 offen lds\n" \
    "s_sleep 3\n" \
    "s_add_u32 m0, s46, 27648\n" \
    "s_add_u32 s54, s15, 221184\n" \
    "buffer_load_dwordx4 v244, s[40:43], s54 offen lds\n" \
    "s_sleep 3\n" \
    "s_add_u32 m0, s46, 28672\n" \
    "s_add_u32 s54, s15, 229376\n" \
    "buffer_load_dwordx4 v244, s[40:43], s54 offen lds\n" \
    "s_sleep 3\n" \
    "s_add_u32 m0, s46, 29696\n" \
    "s_add_u32 s54, s15, 237568\n" \
    "buffer_load_dwordx4 v244, s[40:43], s54 offen lds\n" \
    "s_sleep 3\n" \
    "s_add_u32 m0, s46, 30720\n" \
    "s_add_u32 s54, s15, 245760\n" \
    "buffer_load_dwordx4 v244, s[40:43], s54 offen lds\n" \
    "s_sleep 3\n" \
    "s_add_u32 m0, s46, 31744\n" \
    "s_add_u32 s54, s15, 253952\n" \
    "buffer_load_dwordx4 v244, s[40:43], s54 offen lds\n" \
    "s_sleep 3\n" \
    "s_add_u32 m0, s46, 32768\n" \
    "s_add_u32 s54, s15, 262144\n" \
    "buffer_load_dwordx4 v244, s[40:43], s54 offen lds\n" \
    "s_sleep 3\n" \
    "s_add_u32 m0, s46, 33792\n" \
    "s_add_u32 s54, s15, 270336\n" \
    "buffer_load_dwordx4 v244, s[40:43], s54 offen lds\n" \
    "s_sleep 3\n" \
    "s_add_u32 m0, s46, 34816\n" \
    "s_add_u32 s54, s15, 278528\n" \
    "buffer_load_dwordx4 v244, s[40:43], s54 offen lds\n" \
    "s_sleep 3\n" \
    "s_add_u32 m0, s46, 35840\n" \
    "s_add_u32 s54, s15, 286720\n" \
    "buffer_load_dwordx4 v244, s[40:43], s54 offen lds\n" \
    "s_sleep 3\n" \
    "s_add_u32 m0, s46, 36864\n" \
    "s_add_u32 s54, s15, 294912\n" \
    "buffer_load_dwordx4 v244, s[40:43], s54 offen lds\n" \
    "s_sleep 3\n" \
    "s_add_u32 m0, s46, 37888\n" \
    "s_add_u32 s54, s15, 303104\n" \
    "buffer_load_dwordx4 v244, s[40:43], s54 offen lds\n" \
    "s_sleep 3\n" \
    "s_add_u32 m0, s46, 38912\n" \
    "s_add_u32 s54, s15, 311296\n" \
    "buffer_load_dwordx4 v244, s[40:43], s54 offen lds\n" \
    "s_sleep 3\n" \
    "s_add_u32 m0, s46, 39936\n" \
    "s_add_u32 s54, s15, 319488\n" \
    "buffer_load_dwordx4 v244, s[40:43], s54 offen lds\n" \
    "s_sleep 3\n" \
    "v_xor_b32 v128, 0, v245\n" \
    "v_xor_b32 v129, 32, v245\n" \
    "v_xor_b32 v130, 64, v245\n" \
    "v_xor_b32 v131, 96, v245\n" \
    "s_waitcnt vmcnt(20)\n" \
    "ds_read_b64_tr_b16 a[0:1], v128\n" \
    "ds_read_b64_tr_b16 a[2:3], v128 offset:2048\n" \
    "ds_read_b64_tr_b16 a[4:5], v129\n" \
    "ds_read_b64_tr_b16 a[6:7], v129 offset:2048\n" \
    "ds_read_b64_tr_b16 a[8:9], v130\n" \
    "ds_read_b64_tr_b16 a[10:11], v130 offset:2048\n" \
    "ds_read_b64_tr_b16 a[12:13], v131\n" \
    "ds_read_b64_tr_b16 a[14:15], v131 offset:2048\n" \
    "s_waitcnt lgkmcnt(0)\n" \
    "ds_read_b64_tr_b16 a[16:17], v128 offset:4096\n" \
    "ds_read_b64_tr_b16 a[18:19], v128 offset:6144\n" \
    "ds_read_b64_tr_b16 a[20:21], v129 offset:4096\n" \
    "ds_read_b64_tr_b16 a[22:23], v129 offset:6144\n" \
    "ds_read_b64_tr_b16 a[24:25], v130 offset:4096\n" \
    "ds_read_b64_tr_b16 a[26:27], v130 offset:6144\n" \
    "ds_read_b64_tr_b16 a[28:29], v131 offset:4096\n" \
    "ds_read_b64_tr_b16 a[30:31], v131 offset:6144\n" \
    "s_waitcnt lgkmcnt(0)\n" \
    "ds_read_b64_tr_b16 a[32:33], v128 offset:8192\n" \
    "ds_read_b64_tr_b16 a[34:35], v128 offset:10240\n" \
    "ds_read_b64_tr_b16 a[36:37], v129 offset:8192\n" \
    "ds_read_b64_tr_b16 a[38:39], v129 offset:10240\n" \
    "ds_read_b64_tr_b16 a[40:41], v130 offset:8192\n" \
    "ds_read_b64_tr_b16 a[42:43], v130 offset:10240\n" \
    "ds_read_b64_tr_b16 a[44:45], v131 offset:8192\n" \
    "ds_read_b64_tr_b16 a[46:47], v131 offset:10240\n" \
    "s_waitcnt lgkmcnt(0)\n" \
    "ds_read_b64_tr_b16 a[48:49], v128 offset:12288\n" \
    "ds_read_b64_tr_b16 a[50:51], v128 offset:14336\n" \
    "ds_read_b64_tr_b16 a[52:53], v129 offset:12288\n" \
    "ds_read_b64_tr_b16 a[54:55], v129 offset:14336\n" \
    "ds_read_b64_tr_b16 a[56:57], v130 offset:12288\n" \
    "ds_read_b64_tr_b16 a[58:59], v130 offset:14336\n" \
    "ds_read_b64_tr_b16 a[60:61], v131 offset:12288\n" \
    "ds_read_b64_tr_b16 a[62:63], v131 offset:14336\n" \
    "s_waitcnt lgkmcnt(0)\n" \
    "ds_read_b64_tr_b16 a[64:65], v128 offset:16384\n" \
    "ds_read_b64_tr_b16 a[66:67], v128 offset:18432\n" \
    "ds_read_b64_tr_b16 a[68:69], v129 offset:16384\n" \
    "ds_read_b64_tr_b16 a[70:71], v129 offset:18432\n" \
    "ds_read_b64_tr_b16 a[72:73], v130 offset:16384\n" \
    "ds_read_b64_tr_b16 a[74:75], v130 offset:18432\n" \
    "ds_read_b64_tr_b16 a[76:77], v131 offset:16384\n" \
    "ds_read_b64_tr_b16 a[78:79], v131 offset:18432\n" \
    "s_waitcnt lgkmcnt(0)\n" \
    "s_add_u32 m0, s46, 0\n" \
    "s_add_u32 s54, s15, 327680\n" \
    "buffer_load_dwordx4 v244, s[40:43], s54 offen lds\n" \
    "s_sleep 3\n" \
    "s_add_u32 m0, s46, 1024\n" \
    "s_add_u32 s54, s15, 335872\n" \
    "buffer_load_dwordx4 v244, s[40:43], s54 offen lds\n" \
    "s_sleep 3\n" \
    "s_add_u32 m0, s46, 2048\n" \
    "s_add_u32 s54, s15, 344064\n" \
    "buffer_load_dwordx4 v244, s[40:43], s54 offen lds\n" \
    "s_sleep 3\n" \
    "s_add_u32 m0, s46, 3072\n" \
    "s_add_u32 s54, s15, 352256\n" \
    "buffer_load_dwordx4 v244, s[40:43], s54 offen lds\n" \
    "s_sleep 3\n" \
    "s_add_u32 m0, s46, 4096\n" \
    "s_add_u32 s54, s15, 360448\n" \
    "buffer_load_dwordx4 v244, s[40:43], s54 offen lds\n" \
    "s_sleep 3\n" \
    "s_add_u32 m0, s46, 5120\n" \
    "s_add_u32 s54, s15, 368640\n" \
    "buffer_load_dwordx4 v244, s[40:43], s54 offen lds\n" \
    "s_sleep 3\n" \
    "s_add_u32 m0, s46, 6144\n" \
    "s_add_u32 s54, s15, 376832\n" \
    "buffer_load_dwordx4 v244, s[40:43], s54 offen lds\n" \
    "s_sleep 3\n" \
    "s_add_u32 m0, s46, 7168\n" \
    "s_add_u32 s54, s15, 385024\n" \
    "buffer_load_dwordx4 v244, s[40:43], s54 offen lds\n" \
    "s_sleep 3\n" \
    "s_add_u32 m0, s46, 8192\n" \
    "s_add_u32 s54, s15, 393216\n" \
    "buffer_load_dwordx4 v244, s[40:43], s54 offen lds\n" \
    "s_sleep 3\n" \
    "s_add_u32 m0, s46, 9216\n" \
    "s_add_u32 s54, s15, 401408\n" \
    "buffer_load_dwordx4 v244, s[40:43], s54 offen lds\n" \
    "s_sleep 3\n" \
    "s_add_u32 m0, s46, 10240\n" \
    "s_add_u32 s54, s15, 409600\n" \
    "buffer_load_dwordx4 v244, s[40:43], s54 offen lds\n" \
    "s_sleep 3\n" \
    "s_add_u32 m0, s46, 11264\n" \
    "s_add_u32 s54, s15, 417792\n" \
    "buffer_load_dwordx4 v244, s[40:43], s54 offen lds\n" \
    "s_sleep 3\n" \
    "s_add_u32 m0, s46, 12288\n" \
    "s_add_u32 s54, s15, 425984\n" \
    "buffer_load_dwordx4 v244, s[40:43], s54 offen lds\n" \
    "s_sleep 3\n" \
    "s_add_u32 m0, s46, 13312\n" \
    "s_add_u32 s54, s15, 434176\n" \
    "buffer_load_dwordx4 v244, s[40:43], s54 offen lds\n" \
    "s_sleep 3\n" \
    "s_add_u32 m0, s46, 14336\n" \
    "s_add_u32 s54, s15, 442368\n" \
    "buffer_load_dwordx4 v244, s[40:43], s54 offen lds\n" \
    "s_sleep 3\n" \
    "s_add_u32 m0, s46, 15360\n" \
    "s_add_u32 s54, s15, 450560\n" \
    "buffer_load_dwordx4 v244, s[40:43], s54 offen lds\n" \
    "s_sleep 3\n" \
    "s_mov_b32 s40, s16\n" \
    "s_and_b32 s41, s17, 0xffff\n" \
    "s_mov_b32 s42, s18\n" \
    "s_add_u32 m0, s46, 16384\n" \
    "s_add_u32 s54, s19, 0\n" \
    "buffer_load_dwordx4 v244, s[40:43], s54 offen lds\n" \
    "s_sleep 3\n" \
    "s_add_u32 m0, s46, 17408\n" \
    "s_add_u32 s54, s19, 8192\n" \
    "buffer_load_dwordx4 v244, s[40:43], s54 offen lds\n" \
    "s_sleep 3\n" \
    "s_add_u32 m0, s46, 18432\n" \
    "s_add_u32 s54, s19, 16384\n" \
    "buffer_load_dwordx4 v244, s[40:43], s54 offen lds\n" \
    "s_sleep 3\n" \
    "s_add_u32 m0, s46, 19456\n" \
    "s_add_u32 s54, s19, 24576\n" \
    "buffer_load_dwordx4 v244, s[40:43], s54 offen lds\n" \
    "s_sleep 3\n" \
    "s_waitcnt vmcnt(20)\n" \
    "ds_read_b64_tr_b16 a[80:81], v128 offset:20480\n" \
    "ds_read_b64_tr_b16 a[82:83], v128 offset:22528\n" \
    "ds_read_b64_tr_b16 a[84:85], v129 offset:20480\n" \
    "ds_read_b64_tr_b16 a[86:87], v129 offset:22528\n" \
    "ds_read_b64_tr_b16 a[88:89], v130 offset:20480\n" \
    "ds_read_b64_tr_b16 a[90:91], v130 offset:22528\n" \
    "ds_read_b64_tr_b16 a[92:93], v131 offset:20480\n" \
    "ds_read_b64_tr_b16 a[94:95], v131 offset:22528\n" \
    "s_waitcnt lgkmcnt(0)\n" \
    "ds_read_b64_tr_b16 a[96:97], v128 offset:24576\n" \
    "ds_read_b64_tr_b16 a[98:99], v128 offset:26624\n" \
    "ds_read_b64_tr_b16 a[100:101], v129 offset:24576\n" \
    "ds_read_b64_tr_b16 a[102:103], v129 offset:26624\n" \
    "ds_read_b64_tr_b16 a[104:105], v130 offset:24576\n" \
    "ds_read_b64_tr_b16 a[106:107], v130 offset:26624\n" \
    "ds_read_b64_tr_b16 a[108:109], v131 offset:24576\n" \
    "ds_read_b64_tr_b16 a[110:111], v131 offset:26624\n" \
    "s_waitcnt lgkmcnt(0)\n" \
    "ds_read_b64_tr_b16 a[112:113], v128 offset:28672\n" \
    "ds_read_b64_tr_b16 a[114:115], v128 offset:30720\n" \
    "ds_read_b64_tr_b16 a[116:117], v129 offset:28672\n" \
    "ds_read_b64_tr_b16 a[118:119], v129 offset:30720\n" \
    "ds_read_b64_tr_b16 a[120:121], v130 offset:28672\n" \
    "ds_read_b64_tr_b16 a[122:123], v130 offset:30720\n" \
    "ds_read_b64_tr_b16 a[124:125], v131 offset:28672\n" \
    "ds_read_b64_tr_b16 a[126:127], v131 offset:30720\n" \
    "s_waitcnt lgkmcnt(0)\n" \
    "ds_read_b64_tr_b16 a[128:129], v128 offset:32768\n" \
    "ds_read_b64_tr_b16 a[130:131], v128 offset:34816\n" \
    "ds_read_b64_tr_b16 a[132:133], v129 offset:32768\n" \
    "ds_read_b64_tr_b16 a[134:135], v129 offset:34816\n" \
    "ds_read_b64_tr_b16 a[136:137], v130 offset:32768\n" \
    "ds_read_b64_tr_b16 a[138:139], v130 offset:34816\n" \
    "ds_read_b64_tr_b16 a[140:141], v131 offset:32768\n" \
    "ds_read_b64_tr_b16 a[142:143], v131 offset:34816\n" \
    "s_waitcnt lgkmcnt(0)\n" \
    "ds_read_b64_tr_b16 a[144:145], v128 offset:36864\n" \
    "ds_read_b64_tr_b16 a[146:147], v128 offset:38912\n" \
    "ds_read_b64_tr_b16 a[148:149], v129 offset:36864\n" \
    "ds_read_b64_tr_b16 a[150:151], v129 offset:38912\n" \
    "ds_read_b64_tr_b16 a[152:153], v130 offset:36864\n" \
    "ds_read_b64_tr_b16 a[154:155], v130 offset:38912\n" \
    "ds_read_b64_tr_b16 a[156:157], v131 offset:36864\n" \
    "ds_read_b64_tr_b16 a[158:159], v131 offset:38912\n" \
    "s_waitcnt lgkmcnt(0)\n" \
    "s_add_u32 m0, s46, 20480\n" \
    "s_add_u32 s54, s19, 32768\n" \
    "buffer_load_dwordx4 v244, s[40:43], s54 offen lds\n" \
    "s_sleep 3\n" \
    "s_add_u32 m0, s46, 21504\n" \
    "s_add_u32 s54, s19, 40960\n" \
    "buffer_load_dwordx4 v244, s[40:43], s54 offen lds\n" \
    "s_sleep 3\n" \
    "s_add_u32 m0, s46, 22528\n" \
    "s_add_u32 s54, s19, 49152\n" \
    "buffer_load_dwordx4 v244, s[40:43], s54 offen lds\n" \
    "s_sleep 3\n" \
    "s_add_u32 m0, s46, 23552\n" \
    "s_add_u32 s54, s19, 57344\n" \
    "buffer_load_dwordx4 v244, s[40:43], s54 offen lds\n" \
    "s_sleep 3\n" \
    "s_add_u32 m0, s46, 24576\n" \
    "s_add_u32 s54, s19, 65536\n" \
    "buffer_load_dwordx4 v244, s[40:43], s54 offen lds\n" \
    "s_sleep 3\n" \
    "s_add_u32 m0, s46, 25600\n" \
    "s_add_u32 s54, s19, 73728\n" \
    "buffer_load_dwordx4 v244, s[40:43], s54 offen lds\n" \
    "s_sleep 3\n" \
    "s_add_u32 m0, s46, 26624\n" \
    "s_add_u32 s54, s19, 81920\n" \
    "buffer_load_dwordx4 v244, s[40:43], s54 offen lds\n" \
    "s_sleep 3\n" \
    "s_add_u32 m0, s46, 27648\n" \
    "s_add_u32 s54, s19, 90112\n" \
    "buffer_load_dwordx4 v244, s[40:43], s54 offen lds\n" \
    "s_sleep 3\n" \
    "s_add_u32 m0, s46, 28672\n" \
    "s_add_u32 s54, s19, 98304\n" \
    "buffer_load_dwordx4 v244, s[40:43], s54 offen lds\n" \
    "s_sleep 3\n" \
    "s_add_u32 m0, s46, 29696\n" \
    "s_add_u32 s54, s19, 106496\n" \
    "buffer_load_dwordx4 v244, s[40:43], s54 offen lds\n" \
    "s_sleep 3\n" \
    "s_add_u32 m0, s46, 30720\n" \
    "s_add_u32 s54, s19, 114688\n" \
    "buffer_load_dwordx4 v244, s[40:43], s54 offen lds\n" \
    "s_sleep 3\n" \
    "s_add_u32 m0, s46, 31744\n" \
    "s_add_u32 s54, s19, 122880\n" \
    "buffer_load_dwordx4 v244, s[40:43], s54 offen lds\n" \
    "s_sleep 3\n" \
    "s_add_u32 m0, s46, 32768\n" \
    "s_add_u32 s54, s19, 131072\n" \
    "buffer_load_dwordx4 v244, s[40:43], s54 offen lds\n" \
    "s_sleep 3\n" \
    "s_add_u32 m0, s46, 33792\n" \
    "s_add_u32 s54, s19, 139264\n" \
    "buffer_load_dwordx4 v244, s[40:43], s54 offen lds\n" \
    "s_sleep 3\n" \
    "s_add_u32 m0, s46, 34816\n" \
    "s_add_u32 s54, s19, 147456\n" \
    "buffer_load_dwordx4 v244, s[40:43], s54 offen lds\n" \
    "s_sleep 3\n" \
    "s_add_u32 m0, s46, 35840\n" \
    "s_add_u32 s54, s19, 155648\n" \
    "buffer_load_dwordx4 v244, s[40:43], s54 offen lds\n" \
    "s_sleep 3\n" \
    "s_add_u32 m0, s46, 36864\n" \
    "s_add_u32 s54, s19, 163840\n" \
    "buffer_load_dwordx4 v244, s[40:43], s54 offen lds\n" \
    "s_sleep 3\n" \
    "s_add_u32 m0, s46, 37888\n" \
    "s_add_u32 s54, s19, 172032\n" \
    "buffer_load_dwordx4 v244, s[40:43], s54 offen lds\n" \
    "s_sleep 3\n" \
    "s_add_u32 m0, s46, 38912\n" \
    "s_add_u32 s54, s19, 180224\n" \
    "buffer_load_dwordx4 v244, s[40:43], s54 offen lds\n" \
    "s_sleep 3\n" \
    "s_add_u32 m0, s46, 39936\n" \
    "s_add_u32 s54, s19, 188416\n" \
    "buffer_load_dwordx4 v244, s[40:43], s54 offen lds\n" \
    "s_sleep 3\n" \
    "s_waitcnt vmcnt(20)\n" \
    "ds_read_b64_tr_b16 a[160:161], v128\n" \
    "ds_read_b64_tr_b16 a[162:163], v128 offset:2048\n" \
    "ds_read_b64_tr_b16 a[164:165], v129\n" \
    "ds_read_b64_tr_b16 a[166:167], v129 offset:2048\n" \
    "ds_read_b64_tr_b16 a[168:169], v130\n" \
    "ds_read_b64_tr_b16 a[170:171], v130 offset:2048\n" \
    "ds_read_b64_tr_b16 a[172:173], v131\n" \
    "ds_read_b64_tr_b16 a[174:175], v131 offset:2048\n" \
    "s_waitcnt lgkmcnt(0)\n" \
    "ds_read_b64_tr_b16 a[176:177], v128 offset:4096\n" \
    "ds_read_b64_tr_b16 a[178:179], v128 offset:6144\n" \
    "ds_read_b64_tr_b16 a[180:181], v129 offset:4096\n" \
    "ds_read_b64_tr_b16 a[182:183], v129 offset:6144\n" \
    "ds_read_b64_tr_b16 a[184:185], v130 offset:4096\n" \
    "ds_read_b64_tr_b16 a[186:187], v130 offset:6144\n" \
    "ds_read_b64_tr_b16 a[188:189], v131 offset:4096\n" \
    "ds_read_b64_tr_b16 a[190:191], v131 offset:6144\n" \
    "s_waitcnt lgkmcnt(0)\n" \
    "ds_read_b64_tr_b16 a[192:193], v128 offset:8192\n" \
    "ds_read_b64_tr_b16 a[194:195], v128 offset:10240\n" \
    "ds_read_b64_tr_b16 a[196:197], v129 offset:8192\n" \
    "ds_read_b64_tr_b16 a[198:199], v129 offset:10240\n" \
    "ds_read_b64_tr_b16 a[200:201], v130 offset:8192\n" \
    "ds_read_b64_tr_b16 a[202:203], v130 offset:10240\n" \
    "ds_read_b64_tr_b16 a[204:205], v131 offset:8192\n" \
    "ds_read_b64_tr_b16 a[206:207], v131 offset:10240\n" \
    "s_waitcnt lgkmcnt(0)\n" \
    "ds_read_b64_tr_b16 a[208:209], v128 offset:12288\n" \
    "ds_read_b64_tr_b16 a[210:211], v128 offset:14336\n" \
    "ds_read_b64_tr_b16 a[212:213], v129 offset:12288\n" \
    "ds_read_b64_tr_b16 a[214:215], v129 offset:14336\n" \
    "ds_read_b64_tr_b16 a[216:217], v130 offset:12288\n" \
    "ds_read_b64_tr_b16 a[218:219], v130 offset:14336\n" \
    "ds_read_b64_tr_b16 a[220:221], v131 offset:12288\n" \
    "ds_read_b64_tr_b16 a[222:223], v131 offset:14336\n" \
    "s_waitcnt lgkmcnt(0)\n" \
    "ds_read_b64_tr_b16 a[224:225], v128 offset:16384\n" \
    "ds_read_b64_tr_b16 a[226:227], v128 offset:18432\n" \
    "ds_read_b64_tr_b16 a[228:229], v129 offset:16384\n" \
    "ds_read_b64_tr_b16 a[230:231], v129 offset:18432\n" \
    "ds_read_b64_tr_b16 a[232:233], v130 offset:16384\n" \
    "ds_read_b64_tr_b16 a[234:235], v130 offset:18432\n" \
    "ds_read_b64_tr_b16 a[236:237], v131 offset:16384\n" \
    "ds_read_b64_tr_b16 a[238:239], v131 offset:18432\n" \
    "s_waitcnt lgkmcnt(0)\n" \
    "s_add_u32 m0, s46, 0\n" \
    "s_add_u32 s54, s19, 196608\n" \
    "buffer_load_dwordx4 v244, s[40:43], s54 offen lds\n" \
    "s_sleep 3\n" \
    "s_add_u32 m0, s46, 1024\n" \
    "s_add_u32 s54, s19, 204800\n" \
    "buffer_load_dwordx4 v244, s[40:43], s54 offen lds\n" \
    "s_sleep 3\n" \
    "s_add_u32 m0, s46, 2048\n" \
    "s_add_u32 s54, s19, 212992\n" \
    "buffer_load_dwordx4 v244, s[40:43], s54 offen lds\n" \
    "s_sleep 3\n" \
    "s_add_u32 m0, s46, 3072\n" \
    "s_add_u32 s54, s19, 221184\n" \
    "buffer_load_dwordx4 v244, s[40:43], s54 offen lds\n" \
    "s_sleep 3\n" \
    "s_add_u32 m0, s46, 4096\n" \
    "s_add_u32 s54, s19, 229376\n" \
    "buffer_load_dwordx4 v244, s[40:43], s54 offen lds\n" \
    "s_sleep 3\n" \
    "s_add_u32 m0, s46, 5120\n" \
    "s_add_u32 s54, s19, 237568\n" \
    "buffer_load_dwordx4 v244, s[40:43], s54 offen lds\n" \
    "s_sleep 3\n" \
    "s_add_u32 m0, s46, 6144\n" \
    "s_add_u32 s54, s19, 245760\n" \
    "buffer_load_dwordx4 v244, s[40:43], s54 offen lds\n" \
    "s_sleep 3\n" \
    "s_add_u32 m0, s46, 7168\n" \
    "s_add_u32 s54, s19, 253952\n" \
    "buffer_load_dwordx4 v244, s[40:43], s54 offen lds\n" \
    "s_sleep 3\n" \
    "s_add_u32 m0, s46, 8192\n" \
    "s_add_u32 s54, s19, 262144\n" \
    "buffer_load_dwordx4 v244, s[40:43], s54 offen lds\n" \
    "s_sleep 3\n" \
    "s_add_u32 m0, s46, 9216\n" \
    "s_add_u32 s54, s19, 270336\n" \
    "buffer_load_dwordx4 v244, s[40:43], s54 offen lds\n" \
    "s_sleep 3\n" \
    "s_add_u32 m0, s46, 10240\n" \
    "s_add_u32 s54, s19, 278528\n" \
    "buffer_load_dwordx4 v244, s[40:43], s54 offen lds\n" \
    "s_sleep 3\n" \
    "s_add_u32 m0, s46, 11264\n" \
    "s_add_u32 s54, s19, 286720\n" \
    "buffer_load_dwordx4 v244, s[40:43], s54 offen lds\n" \
    "s_sleep 3\n" \
    "s_waitcnt vmcnt(12)\n" \
    "ds_read_b64_tr_b16 a[240:241], v128 offset:20480\n" \
    "ds_read_b64_tr_b16 a[242:243], v128 offset:22528\n" \
    "ds_read_b64_tr_b16 a[244:245], v129 offset:20480\n" \
    "ds_read_b64_tr_b16 a[246:247], v129 offset:22528\n" \
    "ds_read_b64_tr_b16 a[248:249], v130 offset:20480\n" \
    "ds_read_b64_tr_b16 a[250:251], v130 offset:22528\n" \
    "ds_read_b64_tr_b16 a[252:253], v131 offset:20480\n" \
    "ds_read_b64_tr_b16 a[254:255], v131 offset:22528\n" \
    "s_waitcnt lgkmcnt(0)\n" \
    "ds_read_b64_tr_b16 v[0:1], v128 offset:24576\n" \
    "ds_read_b64_tr_b16 v[2:3], v128 offset:26624\n" \
    "ds_read_b64_tr_b16 v[4:5], v129 offset:24576\n" \
    "ds_read_b64_tr_b16 v[6:7], v129 offset:26624\n" \
    "ds_read_b64_tr_b16 v[8:9], v130 offset:24576\n" \
    "ds_read_b64_tr_b16 v[10:11], v130 offset:26624\n" \
    "ds_read_b64_tr_b16 v[12:13], v131 offset:24576\n" \
    "ds_read_b64_tr_b16 v[14:15], v131 offset:26624\n" \
    "s_waitcnt lgkmcnt(0)\n" \
    "ds_read_b64_tr_b16 v[16:17], v128 offset:28672\n" \
    "ds_read_b64_tr_b16 v[18:19], v128 offset:30720\n" \
    "ds_read_b64_tr_b16 v[20:21], v129 offset:28672\n" \
    "ds_read_b64_tr_b16 v[22:23], v129 offset:30720\n" \
    "ds_read_b64_tr_b16 v[24:25], v130 offset:28672\n" \
    "ds_read_b64_tr_b16 v[26:27], v130 offset:30720\n" \
    "ds_read_b64_tr_b16 v[28:29], v131 offset:28672\n" \
    "ds_read_b64_tr_b16 v[30:31], v131 offset:30720\n" \
    "s_waitcnt lgkmcnt(0)\n" \
    "ds_read_b64_tr_b16 v[32:33], v128 offset:32768\n" \
    "ds_read_b64_tr_b16 v[34:35], v128 offset:34816\n" \
    "ds_read_b64_tr_b16 v[36:37], v129 offset:32768\n" \
    "ds_read_b64_tr_b16 v[38:39], v129 offset:34816\n" \
    "ds_read_b64_tr_b16 v[40:41], v130 offset:32768\n" \
    "ds_read_b64_tr_b16 v[42:43], v130 offset:34816\n" \
    "ds_read_b64_tr_b16 v[44:45], v131 offset:32768\n" \
    "ds_read_b64_tr_b16 v[46:47], v131 offset:34816\n" \
    "s_waitcnt lgkmcnt(0)\n" \
    "ds_read_b64_tr_b16 v[48:49], v128 offset:36864\n" \
    "ds_read_b64_tr_b16 v[50:51], v128 offset:38912\n" \
    "ds_read_b64_tr_b16 v[52:53], v129 offset:36864\n" \
    "ds_read_b64_tr_b16 v[54:55], v129 offset:38912\n" \
    "ds_read_b64_tr_b16 v[56:57], v130 offset:36864\n" \
    "ds_read_b64_tr_b16 v[58:59], v130 offset:38912\n" \
    "ds_read_b64_tr_b16 v[60:61], v131 offset:36864\n" \
    "ds_read_b64_tr_b16 v[62:63], v131 offset:38912\n" \
    "s_waitcnt lgkmcnt(0)\n" \
    "s_waitcnt vmcnt(0)\n" \
    "ds_read_b64_tr_b16 v[64:65], v128\n" \
    "ds_read_b64_tr_b16 v[66:67], v128 offset:2048\n" \
    "ds_read_b64_tr_b16 v[68:69], v129\n" \
    "ds_read_b64_tr_b16 v[70:71], v129 offset:2048\n" \
    "ds_read_b64_tr_b16 v[72:73], v130\n" \
    "ds_read_b64_tr_b16 v[74:75], v130 offset:2048\n" \
    "ds_read_b64_tr_b16 v[76:77], v131\n" \
    "ds_read_b64_tr_b16 v[78:79], v131 offset:2048\n" \
    "s_waitcnt lgkmcnt(0)\n" \
    "ds_read_b64_tr_b16 v[80:81], v128 offset:4096\n" \
    "ds_read_b64_tr_b16 v[82:83], v128 offset:6144\n" \
    "ds_read_b64_tr_b16 v[84:85], v129 offset:4096\n" \
    "ds_read_b64_tr_b16 v[86:87], v129 offset:6144\n" \
    "ds_read_b64_tr_b16 v[88:89], v130 offset:4096\n" \
    "ds_read_b64_tr_b16 v[90:91], v130 offset:6144\n" \
    "ds_read_b64_tr_b16 v[92:93], v131 offset:4096\n" \
    "ds_read_b64_tr_b16 v[94:95], v131 offset:6144\n" \
    "s_waitcnt lgkmcnt(0)\n" \
    "ds_read_b64_tr_b16 v[96:97], v128 offset:8192\n" \
    "ds_read_b64_tr_b16 v[98:99], v128 offset:10240\n" \
    "ds_read_b64_tr_b16 v[100:101], v129 offset:8192\n" \
    "ds_read_b64_tr_b16 v[102:103], v129 offset:10240\n" \
    "ds_read_b64_tr_b16 v[104:105], v130 offset:8192\n" \
    "ds_read_b64_tr_b16 v[106:107], v130 offset:10240\n" \
    "ds_read_b64_tr_b16 v[108:109], v131 offset:8192\n" \
    "ds_read_b64_tr_b16 v[110:111], v131 offset:10240\n" \
    "s_waitcnt lgkmcnt(0)\n" \
    "s_barrier\n" \
    "s_mov_b32 s40, s70\n" \
    "s_and_b32 s41, s71, 0xffff\n" \
    "s_mov_b32 s42, 0x200000\n" \
    "s_mov_b32 s76, s72\n" \
    "s_and_b32 s77, s73, 0xffff\n" \
    "s_mov_b32 s78, 0x200000\n" \
    "s_mov_b32 s79, 0x20000\n" \
    "s_mov_b32 s44, 0\n" \
    "s_mov_b32 s25, 0\n" \
    "v_mov_b32 v245, 1\n" \
    "s_lshl_b32 s54, s20, 31\n" \
    "v_mov_b32 v244, s54\n" \
    "v_or_b32 v228, s54, v253\n" \
    "s_branch OI%=\n" \
    "OL%=:\n" \
    "s_waitcnt vmcnt(22)\n" \
    "v_cmp_gt_u32 vcc, v245, v242\n" \
    "s_cmp_eq_u64 vcc, 0\n" \
    "s_cselect_b32 s26, 1, 0\n" \
    "v_min_u32 v238, v238, v239\n" \
    "v_cmp_ge_u32 vcc, v245, v238\n" \
    "s_cbranch_vccnz OS%=\n" \
    "s_cmp_ge_u32 s44, 30\n" \
    "s_cbranch_scc1 OS%=\n" \
    "s_add_u32 s52, s52, s23\n" \
    "s_add_u32 s50, s50, 524288\n" \
    "buffer_load_dword v238, v247, s[36:39], s57 offen sc1\n" \
    "buffer_load_dword v239, v246, s[36:39], s53 offen sc1\n" \
    "buffer_load_dword v242, v244, s[36:39], s22 offen sc1\n" \
    "buffer_load_dwordx2 v[236:237], v250, s[40:43], s58 offen\n" \
    "buffer_load_dwordx4 v[220:223], v228, s[36:39], s21 offen sc1\n" \
    "v_mfma_f32_16x16x32_bf16 v[112:115], a[0:3], v[128:131], 0\n" \
    "v_mfma_f32_16x16x32_bf16 v[116:119], a[4:7], v[128:131], 0\n" \
    "v_mfma_f32_16x16x32_bf16 v[120:123], a[8:11], v[128:131], 0\n" \
    "v_mfma_f32_16x16x32_bf16 v[124:127], a[12:15], v[128:131], 0\n" \
    "s_add_u32 s54, s52, 0\n" \
    "buffer_load_dwordx4 v[128:131], v253, s[36:39], s54 offen sc1\n" \
    "s_waitcnt vmcnt(27)\n" \
    "v_mfma_f32_16x16x32_bf16 v[112:115], a[16:19], v[132:135], v[112:115]\n" \
    "v_mfma_f32_16x16x32_bf16 v[116:119], a[20:23], v[132:135], v[116:119]\n" \
    "v_mfma_f32_16x16x32_bf16 v[120:123], a[24:27], v[132:135], v[120:123]\n" \
    "v_mfma_f32_16x16x32_bf16 v[124:127], a[28:31], v[132:135], v[124:127]\n" \
    "s_add_u32 s54, s52, 1024\n" \
    "buffer_load_dwordx4 v[132:135], v253, s[36:39], s54 offen sc1\n" \
    "s_waitcnt vmcnt(27)\n" \
    "v_mfma_f32_16x16x32_bf16 v[112:115], a[32:35], v[136:139], v[112:115]\n" \
    "v_mfma_f32_16x16x32_bf16 v[116:119], a[36:39], v[136:139], v[116:119]\n" \
    "v_mfma_f32_16x16x32_bf16 v[120:123], a[40:43], v[136:139], v[120:123]\n" \
    "v_mfma_f32_16x16x32_bf16 v[124:127], a[44:47], v[136:139], v[124:127]\n" \
    "s_add_u32 s54, s52, 2048\n" \
    "buffer_load_dwordx4 v[136:139], v253, s[36:39], s54 offen sc1\n" \
    "s_waitcnt vmcnt(27)\n" \
    "v_mfma_f32_16x16x32_bf16 v[112:115], a[48:51], v[140:143], v[112:115]\n" \
    "v_mfma_f32_16x16x32_bf16 v[116:119], a[52:55], v[140:143], v[116:119]\n" \
    "v_mfma_f32_16x16x32_bf16 v[120:123], a[56:59], v[140:143], v[120:123]\n" \
    "v_mfma_f32_16x16x32_bf16 v[124:127], a[60:63], v[140:143], v[124:127]\n" \
    "s_add_u32 s54, s52, 3072\n" \
    "buffer_load_dwordx4 v[140:143], v253, s[36:39], s54 offen sc1\n" \
    "s_waitcnt vmcnt(27)\n" \
    "v_mfma_f32_16x16x32_bf16 v[112:115], a[64:67], v[144:147], v[112:115]\n" \
    "v_mfma_f32_16x16x32_bf16 v[116:119], a[68:71], v[144:147], v[116:119]\n" \
    "v_mfma_f32_16x16x32_bf16 v[120:123], a[72:75], v[144:147], v[120:123]\n" \
    "v_mfma_f32_16x16x32_bf16 v[124:127], a[76:79], v[144:147], v[124:127]\n" \
    "s_add_u32 s54, s52, 4096\n" \
    "buffer_load_dwordx4 v[144:147], v253, s[36:39], s54 offen sc1\n" \
    "s_waitcnt vmcnt(27)\n" \
    "v_mfma_f32_16x16x32_bf16 v[112:115], a[80:83], v[148:151], v[112:115]\n" \
    "v_mfma_f32_16x16x32_bf16 v[116:119], a[84:87], v[148:151], v[116:119]\n" \
    "v_mfma_f32_16x16x32_bf16 v[120:123], a[88:91], v[148:151], v[120:123]\n" \
    "v_mfma_f32_16x16x32_bf16 v[124:127], a[92:95], v[148:151], v[124:127]\n" \
    "s_add_u32 s54, s52, 5120\n" \
    "buffer_load_dwordx4 v[148:151], v253, s[36:39], s54 offen sc1\n" \
    "s_waitcnt vmcnt(27)\n" \
    "v_mfma_f32_16x16x32_bf16 v[112:115], a[96:99], v[152:155], v[112:115]\n" \
    "v_mfma_f32_16x16x32_bf16 v[116:119], a[100:103], v[152:155], v[116:119]\n" \
    "v_mfma_f32_16x16x32_bf16 v[120:123], a[104:107], v[152:155], v[120:123]\n" \
    "v_mfma_f32_16x16x32_bf16 v[124:127], a[108:111], v[152:155], v[124:127]\n" \
    "s_add_u32 s54, s52, 6144\n" \
    "buffer_load_dwordx4 v[152:155], v253, s[36:39], s54 offen sc1\n" \
    "s_waitcnt vmcnt(27)\n" \
    "v_mfma_f32_16x16x32_bf16 v[112:115], a[112:115], v[156:159], v[112:115]\n" \
    "v_mfma_f32_16x16x32_bf16 v[116:119], a[116:119], v[156:159], v[116:119]\n" \
    "v_mfma_f32_16x16x32_bf16 v[120:123], a[120:123], v[156:159], v[120:123]\n" \
    "v_mfma_f32_16x16x32_bf16 v[124:127], a[124:127], v[156:159], v[124:127]\n" \
    "s_add_u32 s54, s52, 7168\n" \
    "buffer_load_dwordx4 v[156:159], v253, s[36:39], s54 offen sc1\n" \
    "s_waitcnt vmcnt(27)\n" \
    "v_mfma_f32_16x16x32_bf16 v[112:115], a[128:131], v[160:163], v[112:115]\n" \
    "v_mfma_f32_16x16x32_bf16 v[116:119], a[132:135], v[160:163], v[116:119]\n" \
    "v_mfma_f32_16x16x32_bf16 v[120:123], a[136:139], v[160:163], v[120:123]\n" \
    "v_mfma_f32_16x16x32_bf16 v[124:127], a[140:143], v[160:163], v[124:127]\n" \
    "s_add_u32 s54, s52, 8192\n" \
    "buffer_load_dwordx4 v[160:163], v253, s[36:39], s54 offen sc1\n" \
    "s_waitcnt vmcnt(27)\n" \
    "v_mfma_f32_16x16x32_bf16 v[112:115], a[144:147], v[164:167], v[112:115]\n" \
    "v_mfma_f32_16x16x32_bf16 v[116:119], a[148:151], v[164:167], v[116:119]\n" \
    "v_mfma_f32_16x16x32_bf16 v[120:123], a[152:155], v[164:167], v[120:123]\n" \
    "v_mfma_f32_16x16x32_bf16 v[124:127], a[156:159], v[164:167], v[124:127]\n" \
    "s_add_u32 s54, s52, 9216\n" \
    "buffer_load_dwordx4 v[164:167], v253, s[36:39], s54 offen sc1\n" \
    "s_waitcnt vmcnt(27)\n" \
    "v_mfma_f32_16x16x32_bf16 v[112:115], a[160:163], v[168:171], v[112:115]\n" \
    "v_mfma_f32_16x16x32_bf16 v[116:119], a[164:167], v[168:171], v[116:119]\n" \
    "v_mfma_f32_16x16x32_bf16 v[120:123], a[168:171], v[168:171], v[120:123]\n" \
    "v_mfma_f32_16x16x32_bf16 v[124:127], a[172:175], v[168:171], v[124:127]\n" \
    "s_add_u32 s54, s52, 10240\n" \
    "buffer_load_dwordx4 v[168:171], v253, s[36:39], s54 offen sc1\n" \
    "s_waitcnt vmcnt(27)\n" \
    "v_mfma_f32_16x16x32_bf16 v[112:115], a[176:179], v[172:175], v[112:115]\n" \
    "v_mfma_f32_16x16x32_bf16 v[116:119], a[180:183], v[172:175], v[116:119]\n" \
    "v_mfma_f32_16x16x32_bf16 v[120:123], a[184:187], v[172:175], v[120:123]\n" \
    "v_mfma_f32_16x16x32_bf16 v[124:127], a[188:191], v[172:175], v[124:127]\n" \
    "s_add_u32 s54, s52, 11264\n" \
    "buffer_load_dwordx4 v[172:175], v253, s[36:39], s54 offen sc1\n" \
    "s_waitcnt vmcnt(27)\n" \
    "v_mfma_f32_16x16x32_bf16 v[112:115], a[192:195], v[176:179], v[112:115]\n" \
    "v_mfma_f32_16x16x32_bf16 v[116:119], a[196:199], v[176:179], v[116:119]\n" \
    "v_mfma_f32_16x16x32_bf16 v[120:123], a[200:203], v[176:179], v[120:123]\n" \
    "v_mfma_f32_16x16x32_bf16 v[124:127], a[204:207], v[176:179], v[124:127]\n" \
    "s_add_u32 s54, s52, 12288\n" \
    "buffer_load_dwordx4 v[176:179], v253, s[36:39], s54 offen sc1\n" \
    "s_waitcnt vmcnt(27)\n" \
    "v_mfma_f32_16x16x32_bf16 v[112:115], a[208:211], v[180:183], v[112:115]\n" \
    "v_mfma_f32_16x16x32_bf16 v[116:119], a[212:215], v[180:183], v[116:119]\n" \
    "v_mfma_f32_16x16x32_bf16 v[120:123], a[216:219], v[180:183], v[120:123]\n" \
    "v_mfma_f32_16x16x32_bf16 v[124:127], a[220:223], v[180:183], v[124:127]\n" \
    "s_add_u32 s54, s52, 13312\n" \
    "buffer_load_dwordx4 v[180:183], v253, s[36:39], s54 offen sc1\n" \
    "s_waitcnt vmcnt(27)\n" \
    "v_mfma_f32_16x16x32_bf16 v[112:115], a[224:227], v[184:187], v[112:115]\n" \
    "v_mfma_f32_16x16x32_bf16 v[116:119], a[228:231], v[184:187], v[116:119]\n" \
    "v_mfma_f32_16x16x32_bf16 v[120:123], a[232:235], v[184:187], v[120:123]\n" \
    "v_mfma_f32_16x16x32_bf16 v[124:127], a[236:239], v[184:187], v[124:127]\n" \
    "s_add_u32 s54, s50, 0\n" \
    "buffer_load_dwordx4 v[184:187], v253, s[36:39], s54 offen sc1\n" \
    "s_waitcnt vmcnt(27)\n" \
    "v_mfma_f32_16x16x32_bf16 v[112:115], a[240:243], v[188:191], v[112:115]\n" \
    "v_mfma_f32_16x16x32_bf16 v[116:119], a[244:247], v[188:191], v[116:119]\n" \
    "v_mfma_f32_16x16x32_bf16 v[120:123], a[248:251], v[188:191], v[120:123]\n" \
    "v_mfma_f32_16x16x32_bf16 v[124:127], a[252:255], v[188:191], v[124:127]\n" \
    "s_add_u32 s54, s50, 1024\n" \
    "buffer_load_dwordx4 v[188:191], v253, s[36:39], s54 offen sc1\n" \
    "s_waitcnt vmcnt(27)\n" \
    "v_mfma_f32_16x16x32_bf16 v[112:115], v[0:3], v[192:195], v[112:115]\n" \
    "v_mfma_f32_16x16x32_bf16 v[116:119], v[4:7], v[192:195], v[116:119]\n" \
    "v_mfma_f32_16x16x32_bf16 v[120:123], v[8:11], v[192:195], v[120:123]\n" \
    "v_mfma_f32_16x16x32_bf16 v[124:127], v[12:15], v[192:195], v[124:127]\n" \
    "s_add_u32 s54, s50, 2048\n" \
    "buffer_load_dwordx4 v[192:195], v253, s[36:39], s54 offen sc1\n" \
    "s_waitcnt vmcnt(27)\n" \
    "v_mfma_f32_16x16x32_bf16 v[112:115], v[16:19], v[196:199], v[112:115]\n" \
    "v_mfma_f32_16x16x32_bf16 v[116:119], v[20:23], v[196:199], v[116:119]\n" \
    "v_mfma_f32_16x16x32_bf16 v[120:123], v[24:27], v[196:199], v[120:123]\n" \
    "v_mfma_f32_16x16x32_bf16 v[124:127], v[28:31], v[196:199], v[124:127]\n" \
    "s_add_u32 s54, s50, 3072\n" \
    "buffer_load_dwordx4 v[196:199], v253, s[36:39], s54 offen sc1\n" \
    "s_waitcnt vmcnt(27)\n" \
    "v_mfma_f32_16x16x32_bf16 v[112:115], v[32:35], v[200:203], v[112:115]\n" \
    "v_mfma_f32_16x16x32_bf16 v[116:119], v[36:39], v[200:203], v[116:119]\n" \
    "v_mfma_f32_16x16x32_bf16 v[120:123], v[40:43], v[200:203], v[120:123]\n" \
    "v_mfma_f32_16x16x32_bf16 v[124:127], v[44:47], v[200:203], v[124:127]\n" \
    "s_add_u32 s54, s50, 4096\n" \
    "buffer_load_dwordx4 v[200:203], v253, s[36:39], s54 offen sc1\n" \
    "s_waitcnt vmcnt(27)\n" \
    "v_mfma_f32_16x16x32_bf16 v[112:115], v[48:51], v[204:207], v[112:115]\n" \
    "v_mfma_f32_16x16x32_bf16 v[116:119], v[52:55], v[204:207], v[116:119]\n" \
    "v_mfma_f32_16x16x32_bf16 v[120:123], v[56:59], v[204:207], v[120:123]\n" \
    "v_mfma_f32_16x16x32_bf16 v[124:127], v[60:63], v[204:207], v[124:127]\n" \
    "s_add_u32 s54, s50, 5120\n" \
    "buffer_load_dwordx4 v[204:207], v253, s[36:39], s54 offen sc1\n" \
    "s_waitcnt vmcnt(27)\n" \
    "v_mfma_f32_16x16x32_bf16 v[112:115], v[64:67], v[208:211], v[112:115]\n" \
    "v_mfma_f32_16x16x32_bf16 v[116:119], v[68:71], v[208:211], v[116:119]\n" \
    "v_mfma_f32_16x16x32_bf16 v[120:123], v[72:75], v[208:211], v[120:123]\n" \
    "v_mfma_f32_16x16x32_bf16 v[124:127], v[76:79], v[208:211], v[124:127]\n" \
    "s_add_u32 s54, s50, 6144\n" \
    "buffer_load_dwordx4 v[208:211], v253, s[36:39], s54 offen sc1\n" \
    "s_waitcnt vmcnt(27)\n" \
    "v_mfma_f32_16x16x32_bf16 v[112:115], v[80:83], v[212:215], v[112:115]\n" \
    "v_mfma_f32_16x16x32_bf16 v[116:119], v[84:87], v[212:215], v[116:119]\n" \
    "v_mfma_f32_16x16x32_bf16 v[120:123], v[88:91], v[212:215], v[120:123]\n" \
    "v_mfma_f32_16x16x32_bf16 v[124:127], v[92:95], v[212:215], v[124:127]\n" \
    "s_add_u32 s54, s50, 7168\n" \
    "buffer_load_dwordx4 v[212:215], v253, s[36:39], s54 offen sc1\n" \
    "s_waitcnt vmcnt(27)\n" \
    "v_mfma_f32_16x16x32_bf16 v[112:115], v[96:99], v[216:219], v[112:115]\n" \
    "v_mfma_f32_16x16x32_bf16 v[116:119], v[100:103], v[216:219], v[116:119]\n" \
    "v_mfma_f32_16x16x32_bf16 v[120:123], v[104:107], v[216:219], v[120:123]\n" \
    "v_mfma_f32_16x16x32_bf16 v[124:127], v[108:111], v[216:219], v[124:127]\n" \
    "s_add_u32 s54, s50, 8192\n" \
    "buffer_load_dwordx4 v[216:219], v253, s[36:39], s54 offen sc1\n" \
    "s_waitcnt vmcnt(23)\n" \
    "s_cmp_eq_u32 s25, 0\n" \
    "s_cbranch_scc1 OF3%=\n" \
    "v_mov_b32 v224, s44\n" \
    "buffer_store_dword v224, v255, s[36:39], s22 offen sc1\n" \
    "s_mov_b32 s25, 0\n" \
    "OF3%=:\n" \
    "s_nop 3\n" \
    "ds_write_b128 v248, v[112:115]\n" \
    "ds_write_b128 v248, v[116:119] offset:1024\n" \
    "ds_write_b128 v248, v[120:123] offset:2048\n" \
    "ds_write_b128 v248, v[124:127] offset:3072\n" \
    "v_lshlrev_b32 v224, 16, v236\n" \
    "v_and_b32 v225, 0xffff0000, v236\n" \
    "v_lshlrev_b32 v226, 16, v237\n" \
    "v_and_b32 v227, 0xffff0000, v237\n" \
    "s_waitcnt lgkmcnt(0)\n" \
    "s_barrier\n" \
    "ds_read_b128 v[112:115], v249\n" \
    "ds_read_b128 v[116:119], v249 offset:4096\n" \
    "ds_read_b128 v[120:123], v249 offset:8192\n" \
    "ds_read_b128 v[124:127], v249 offset:12288\n" \
    "v_xor_b32 v248, 16384, v248\n" \
    "v_xor_b32 v249, 16384, v249\n" \
    "s_waitcnt lgkmcnt(0)\n" \
    "v_pk_add_f32 v[112:113], v[112:113], v[116:117]\n" \
    "v_pk_add_f32 v[120:121], v[120:121], v[124:125]\n" \
    "v_pk_add_f32 v[114:115], v[114:115], v[118:119]\n" \
    "v_pk_add_f32 v[122:123], v[122:123], v[126:127]\n" \
    "v_pk_add_f32 v[112:113], v[112:113], v[120:121]\n" \
    "v_pk_add_f32 v[114:115], v[114:115], v[122:123]\n" \
    "s_cmp_eq_u32 s20, 0\n" \
    "s_cbranch_scc1 OF0%=\n" \
    "v_pk_add_f32 v[224:225], v[224:225], v[112:113]\n" \
    "v_pk_add_f32 v[226:227], v[226:227], v[114:115]\n" \
    "s_nop 0\n" \
    "buffer_store_dwordx4 v[224:227], v253, s[36:39], s21 offen sc1\n" \
    "s_mov_b32 s25, 1\n" \
    "s_branch OF1%=\n" \
    "OF0%=:\n" \
    "s_cmp_eq_u32 s26, 1\n" \
    "s_cbranch_scc1 OF2%=\n" \
    "OFwa%=:\n" \
    "buffer_load_dword v224, v244, s[36:39], s22 offen sc1\n" \
    "s_waitcnt vmcnt(0)\n" \
    "v_cmp_gt_u32 vcc, v245, v224\n" \
    "s_cbranch_vccz OFwb%=\n" \
    "s_sleep 1\n" \
    "s_add_u32 s56, s56, 1\n" \
    "s_cmp_lt_u32 s56, 262144\n" \
    "s_cbranch_scc1 OFwa%=\n" \
    "OFwb%=:\n" \
    "buffer_load_dwordx4 v[220:223], v253, s[36:39], s21 offen sc1\n" \
    "s_waitcnt vmcnt(0)\n" \
    "OF2%=:\n" \
    "v_pk_add_f32 v[112:113], v[112:113], v[220:221]\n" \
    "v_pk_add_f32 v[114:115], v[114:115], v[222:223]\n" \
    "v_cvt_pk_bf16_f32 v224, v112, v113\n" \
    "v_cvt_pk_bf16_f32 v225, v114, v115\n" \
    "buffer_store_dwordx2 v[224:225], v250, s[76:79], s58 offen\n" \
    "OF1%=:\n" \
    "s_add_u32 s58, s58, 65536\n" \
    "s_add_u32 s21, s21, 131072\n" \
    "v_add_u32 v245, 1, v245\n" \
    "s_add_u32 s44, s44, 1\n" \
    "s_sleep 6\n" \
    "s_branch OL%=\n" \
    "OS%=:\n" \
    "buffer_load_dwordx2 v[236:237], v250, s[40:43], s58 offen\n" \
    "buffer_load_dwordx4 v[220:223], v228, s[36:39], s21 offen sc1\n" \
    "v_mfma_f32_16x16x32_bf16 v[112:115], a[0:3], v[128:131], 0\n" \
    "v_mfma_f32_16x16x32_bf16 v[116:119], a[4:7], v[128:131], 0\n" \
    "v_mfma_f32_16x16x32_bf16 v[120:123], a[8:11], v[128:131], 0\n" \
    "v_mfma_f32_16x16x32_bf16 v[124:127], a[12:15], v[128:131], 0\n" \
    "s_waitcnt vmcnt(23)\n" \
    "v_mfma_f32_16x16x32_bf16 v[112:115], a[16:19], v[132:135], v[112:115]\n" \
    "v_mfma_f32_16x16x32_bf16 v[116:119], a[20:23], v[132:135], v[116:119]\n" \
    "v_mfma_f32_16x16x32_bf16 v[120:123], a[24:27], v[132:135], v[120:123]\n" \
    "v_mfma_f32_16x16x32_bf16 v[124:127], a[28:31], v[132:135], v[124:127]\n" \
    "s_waitcnt vmcnt(22)\n" \
    "v_mfma_f32_16x16x32_bf16 v[112:115], a[32:35], v[136:139], v[112:115]\n" \
    "v_mfma_f32_16x16x32_bf16 v[116:119], a[36:39], v[136:139], v[116:119]\n" \
    "v_mfma_f32_16x16x32_bf16 v[120:123], a[40:43], v[136:139], v[120:123]\n" \
    "v_mfma_f32_16x16x32_bf16 v[124:127], a[44:47], v[136:139], v[124:127]\n" \
    "s_waitcnt vmcnt(21)\n" \
    "v_mfma_f32_16x16x32_bf16 v[112:115], a[48:51], v[140:143], v[112:115]\n" \
    "v_mfma_f32_16x16x32_bf16 v[116:119], a[52:55], v[140:143], v[116:119]\n" \
    "v_mfma_f32_16x16x32_bf16 v[120:123], a[56:59], v[140:143], v[120:123]\n" \
    "v_mfma_f32_16x16x32_bf16 v[124:127], a[60:63], v[140:143], v[124:127]\n" \
    "s_waitcnt vmcnt(20)\n" \
    "v_mfma_f32_16x16x32_bf16 v[112:115], a[64:67], v[144:147], v[112:115]\n" \
    "v_mfma_f32_16x16x32_bf16 v[116:119], a[68:71], v[144:147], v[116:119]\n" \
    "v_mfma_f32_16x16x32_bf16 v[120:123], a[72:75], v[144:147], v[120:123]\n" \
    "v_mfma_f32_16x16x32_bf16 v[124:127], a[76:79], v[144:147], v[124:127]\n" \
    "s_waitcnt vmcnt(19)\n" \
    "v_mfma_f32_16x16x32_bf16 v[112:115], a[80:83], v[148:151], v[112:115]\n" \
    "v_mfma_f32_16x16x32_bf16 v[116:119], a[84:87], v[148:151], v[116:119]\n" \
    "v_mfma_f32_16x16x32_bf16 v[120:123], a[88:91], v[148:151], v[120:123]\n" \
    "v_mfma_f32_16x16x32_bf16 v[124:127], a[92:95], v[148:151], v[124:127]\n" \
    "s_waitcnt vmcnt(18)\n" \
    "v_mfma_f32_16x16x32_bf16 v[112:115], a[96:99], v[152:155], v[112:115]\n" \
    "v_mfma_f32_16x16x32_bf16 v[116:119], a[100:103], v[152:155], v[116:119]\n" \
    "v_mfma_f32_16x16x32_bf16 v[120:123], a[104:107], v[152:155], v[120:123]\n" \
    "v_mfma_f32_16x16x32_bf16 v[124:127], a[108:111], v[152:155], v[124:127]\n" \
    "s_waitcnt vmcnt(17)\n" \
    "v_mfma_f32_16x16x32_bf16 v[112:115], a[112:115], v[156:159], v[112:115]\n" \
    "v_mfma_f32_16x16x32_bf16 v[116:119], a[116:119], v[156:159], v[116:119]\n" \
    "v_mfma_f32_16x16x32_bf16 v[120:123], a[120:123], v[156:159], v[120:123]\n" \
    "v_mfma_f32_16x16x32_bf16 v[124:127], a[124:127], v[156:159], v[124:127]\n" \
    "s_waitcnt vmcnt(16)\n" \
    "v_mfma_f32_16x16x32_bf16 v[112:115], a[128:131], v[160:163], v[112:115]\n" \
    "v_mfma_f32_16x16x32_bf16 v[116:119], a[132:135], v[160:163], v[116:119]\n" \
    "v_mfma_f32_16x16x32_bf16 v[120:123], a[136:139], v[160:163], v[120:123]\n" \
    "v_mfma_f32_16x16x32_bf16 v[124:127], a[140:143], v[160:163], v[124:127]\n" \
    "s_waitcnt vmcnt(15)\n" \
    "v_mfma_f32_16x16x32_bf16 v[112:115], a[144:147], v[164:167], v[112:115]\n" \
    "v_mfma_f32_16x16x32_bf16 v[116:119], a[148:151], v[164:167], v[116:119]\n" \
    "v_mfma_f32_16x16x32_bf16 v[120:123], a[152:155], v[164:167], v[120:123]\n" \
    "v_mfma_f32_16x16x32_bf16 v[124:127], a[156:159], v[164:167], v[124:127]\n" \
    "s_waitcnt vmcnt(14)\n" \
    "v_mfma_f32_16x16x32_bf16 v[112:115], a[160:163], v[168:171], v[112:115]\n" \
    "v_mfma_f32_16x16x32_bf16 v[116:119], a[164:167], v[168:171], v[116:119]\n" \
    "v_mfma_f32_16x16x32_bf16 v[120:123], a[168:171], v[168:171], v[120:123]\n" \
    "v_mfma_f32_16x16x32_bf16 v[124:127], a[172:175], v[168:171], v[124:127]\n" \
    "s_waitcnt vmcnt(13)\n" \
    "v_mfma_f32_16x16x32_bf16 v[112:115], a[176:179], v[172:175], v[112:115]\n" \
    "v_mfma_f32_16x16x32_bf16 v[116:119], a[180:183], v[172:175], v[116:119]\n" \
    "v_mfma_f32_16x16x32_bf16 v[120:123], a[184:187], v[172:175], v[120:123]\n" \
    "v_mfma_f32_16x16x32_bf16 v[124:127], a[188:191], v[172:175], v[124:127]\n" \
    "s_waitcnt vmcnt(12)\n" \
    "v_mfma_f32_16x16x32_bf16 v[112:115], a[192:195], v[176:179], v[112:115]\n" \
    "v_mfma_f32_16x16x32_bf16 v[116:119], a[196:199], v[176:179], v[116:119]\n" \
    "v_mfma_f32_16x16x32_bf16 v[120:123], a[200:203], v[176:179], v[120:123]\n" \
    "v_mfma_f32_16x16x32_bf16 v[124:127], a[204:207], v[176:179], v[124:127]\n" \
    "s_waitcnt vmcnt(11)\n" \
    "v_mfma_f32_16x16x32_bf16 v[112:115], a[208:211], v[180:183], v[112:115]\n" \
    "v_mfma_f32_16x16x32_bf16 v[116:119], a[212:215], v[180:183], v[116:119]\n" \
    "v_mfma_f32_16x16x32_bf16 v[120:123], a[216:219], v[180:183], v[120:123]\n" \
    "v_mfma_f32_16x16x32_bf16 v[124:127], a[220:223], v[180:183], v[124:127]\n" \
    "s_waitcnt vmcnt(10)\n" \
    "v_mfma_f32_16x16x32_bf16 v[112:115], a[224:227], v[184:187], v[112:115]\n" \
    "v_mfma_f32_16x16x32_bf16 v[116:119], a[228:231], v[184:187], v[116:119]\n" \
    "v_mfma_f32_16x16x32_bf16 v[120:123], a[232:235], v[184:187], v[120:123]\n" \
    "v_mfma_f32_16x16x32_bf16 v[124:127], a[236:239], v[184:187], v[124:127]\n" \
    "s_waitcnt vmcnt(9)\n" \
    "v_mfma_f32_16x16x32_bf16 v[112:115], a[240:243], v[188:191], v[112:115]\n" \
    "v_mfma_f32_16x16x32_bf16 v[116:119], a[244:247], v[188:191], v[116:119]\n" \
    "v_mfma_f32_16x16x32_bf16 v[120:123], a[248:251], v[188:191], v[120:123]\n" \
    "v_mfma_f32_16x16x32_bf16 v[124:127], a[252:255], v[188:191], v[124:127]\n" \
    "s_waitcnt vmcnt(8)\n" \
    "v_mfma_f32_16x16x32_bf16 v[112:115], v[0:3], v[192:195], v[112:115]\n" \
    "v_mfma_f32_16x16x32_bf16 v[116:119], v[4:7], v[192:195], v[116:119]\n" \
    "v_mfma_f32_16x16x32_bf16 v[120:123], v[8:11], v[192:195], v[120:123]\n" \
    "v_mfma_f32_16x16x32_bf16 v[124:127], v[12:15], v[192:195], v[124:127]\n" \
    "s_waitcnt vmcnt(7)\n" \
    "v_mfma_f32_16x16x32_bf16 v[112:115], v[16:19], v[196:199], v[112:115]\n" \
    "v_mfma_f32_16x16x32_bf16 v[116:119], v[20:23], v[196:199], v[116:119]\n" \
    "v_mfma_f32_16x16x32_bf16 v[120:123], v[24:27], v[196:199], v[120:123]\n" \
    "v_mfma_f32_16x16x32_bf16 v[124:127], v[28:31], v[196:199], v[124:127]\n" \
    "s_waitcnt vmcnt(6)\n" \
    "v_mfma_f32_16x16x32_bf16 v[112:115], v[32:35], v[200:203], v[112:115]\n" \
    "v_mfma_f32_16x16x32_bf16 v[116:119], v[36:39], v[200:203], v[116:119]\n" \
    "v_mfma_f32_16x16x32_bf16 v[120:123], v[40:43], v[200:203], v[120:123]\n" \
    "v_mfma_f32_16x16x32_bf16 v[124:127], v[44:47], v[200:203], v[124:127]\n" \
    "s_waitcnt vmcnt(5)\n" \
    "v_mfma_f32_16x16x32_bf16 v[112:115], v[48:51], v[204:207], v[112:115]\n" \
    "v_mfma_f32_16x16x32_bf16 v[116:119], v[52:55], v[204:207], v[116:119]\n" \
    "v_mfma_f32_16x16x32_bf16 v[120:123], v[56:59], v[204:207], v[120:123]\n" \
    "v_mfma_f32_16x16x32_bf16 v[124:127], v[60:63], v[204:207], v[124:127]\n" \
    "s_waitcnt vmcnt(4)\n" \
    "v_mfma_f32_16x16x32_bf16 v[112:115], v[64:67], v[208:211], v[112:115]\n" \
    "v_mfma_f32_16x16x32_bf16 v[116:119], v[68:71], v[208:211], v[116:119]\n" \
    "v_mfma_f32_16x16x32_bf16 v[120:123], v[72:75], v[208:211], v[120:123]\n" \
    "v_mfma_f32_16x16x32_bf16 v[124:127], v[76:79], v[208:211], v[124:127]\n" \
    "s_waitcnt vmcnt(3)\n" \
    "v_mfma_f32_16x16x32_bf16 v[112:115], v[80:83], v[212:215], v[112:115]\n" \
    "v_mfma_f32_16x16x32_bf16 v[116:119], v[84:87], v[212:215], v[116:119]\n" \
    "v_mfma_f32_16x16x32_bf16 v[120:123], v[88:91], v[212:215], v[120:123]\n" \
    "v_mfma_f32_16x16x32_bf16 v[124:127], v[92:95], v[212:215], v[124:127]\n" \
    "s_waitcnt vmcnt(2)\n" \
    "v_mfma_f32_16x16x32_bf16 v[112:115], v[96:99], v[216:219], v[112:115]\n" \
    "v_mfma_f32_16x16x32_bf16 v[116:119], v[100:103], v[216:219], v[116:119]\n" \
    "v_mfma_f32_16x16x32_bf16 v[120:123], v[104:107], v[216:219], v[120:123]\n" \
    "v_mfma_f32_16x16x32_bf16 v[124:127], v[108:111], v[216:219], v[124:127]\n" \
    "s_waitcnt vmcnt(0)\n" \
    "s_cmp_eq_u32 s25, 0\n" \
    "s_cbranch_scc1 OE3%=\n" \
    "v_mov_b32 v224, s44\n" \
    "buffer_store_dword v224, v255, s[36:39], s22 offen sc1\n" \
    "s_mov_b32 s25, 0\n" \
    "OE3%=:\n" \
    "s_nop 3\n" \
    "ds_write_b128 v248, v[112:115]\n" \
    "ds_write_b128 v248, v[116:119] offset:1024\n" \
    "ds_write_b128 v248, v[120:123] offset:2048\n" \
    "ds_write_b128 v248, v[124:127] offset:3072\n" \
    "v_lshlrev_b32 v224, 16, v236\n" \
    "v_and_b32 v225, 0xffff0000, v236\n" \
    "v_lshlrev_b32 v226, 16, v237\n" \
    "v_and_b32 v227, 0xffff0000, v237\n" \
    "s_waitcnt lgkmcnt(0)\n" \
    "s_barrier\n" \
    "ds_read_b128 v[112:115], v249\n" \
    "ds_read_b128 v[116:119], v249 offset:4096\n" \
    "ds_read_b128 v[120:123], v249 offset:8192\n" \
    "ds_read_b128 v[124:127], v249 offset:12288\n" \
    "v_xor_b32 v248, 16384, v248\n" \
    "v_xor_b32 v249, 16384, v249\n" \
    "s_waitcnt lgkmcnt(0)\n" \
    "v_pk_add_f32 v[112:113], v[112:113], v[116:117]\n" \
    "v_pk_add_f32 v[120:121], v[120:121], v[124:125]\n" \
    "v_pk_add_f32 v[114:115], v[114:115], v[118:119]\n" \
    "v_pk_add_f32 v[122:123], v[122:123], v[126:127]\n" \
    "v_pk_add_f32 v[112:113], v[112:113], v[120:121]\n" \
    "v_pk_add_f32 v[114:115], v[114:115], v[122:123]\n" \
    "s_cmp_eq_u32 s20, 0\n" \
    "s_cbranch_scc1 OE0%=\n" \
    "v_pk_add_f32 v[224:225], v[224:225], v[112:113]\n" \
    "v_pk_add_f32 v[226:227], v[226:227], v[114:115]\n" \
    "s_nop 0\n" \
    "buffer_store_dwordx4 v[224:227], v253, s[36:39], s21 offen sc1\n" \
    "s_waitcnt vmcnt(0)\n" \
    "buffer_store_dword v245, v255, s[36:39], s22 offen sc1\n" \
    "s_branch OE1%=\n" \
    "OE0%=:\n" \
    "s_cmp_eq_u32 s26, 1\n" \
    "s_cbranch_scc1 OE2%=\n" \
    "OEwa%=:\n" \
    "buffer_load_dword v224, v244, s[36:39], s22 offen sc1\n" \
    "s_waitcnt vmcnt(0)\n" \
    "v_cmp_gt_u32 vcc, v245, v224\n" \
    "s_cbranch_vccz OEwb%=\n" \
    "s_sleep 1\n" \
    "s_add_u32 s56, s56, 1\n" \
    "s_cmp_lt_u32 s56, 262144\n" \
    "s_cbranch_scc1 OEwa%=\n" \
    "OEwb%=:\n" \
    "buffer_load_dwordx4 v[220:223], v253, s[36:39], s21 offen sc1\n" \
    "s_waitcnt vmcnt(0)\n" \
    "OE2%=:\n" \
    "v_pk_add_f32 v[112:113], v[112:113], v[220:221]\n" \
    "v_pk_add_f32 v[114:115], v[114:115], v[222:223]\n" \
    "v_cvt_pk_bf16_f32 v224, v112, v113\n" \
    "v_cvt_pk_bf16_f32 v225, v114, v115\n" \
    "buffer_store_dwordx2 v[224:225], v250, s[76:79], s58 offen\n" \
    "OE1%=:\n" \
    "s_add_u32 s58, s58, 65536\n" \
    "s_add_u32 s21, s21, 131072\n" \
    "v_add_u32 v245, 1, v245\n" \
    "s_add_u32 s44, s44, 1\n" \
    "s_cmp_eq_u32 s44, 32\n" \
    "s_cbranch_scc1 OX%=\n" \
    "s_add_u32 s52, s52, s23\n" \
    "s_add_u32 s50, s50, 524288\n" \
    "OI%=:\n" \
    "OAa%=:\n" \
    "buffer_load_dword v238, v247, s[36:39], s57 offen sc1\n" \
    "s_waitcnt vmcnt(0)\n" \
    "v_cmp_gt_u32 vcc, v245, v238\n" \
    "s_cbranch_vccz OAb%=\n" \
    "s_sleep 4\n" \
    "s_add_u32 s56, s56, 1\n" \
    "s_cmp_lt_u32 s56, 262144\n" \
    "s_cbranch_scc1 OAa%=\n" \
    "OAb%=:\n" \
    "s_cmp_eq_u32 s20, 0\n" \
    "s_cbranch_scc1 OJ%=\n" \
    "s_add_u32 s54, s52, 0\n" \
    "buffer_load_dwordx4 v[128:131], v253, s[36:39], s54 offen sc1\n" \
    "s_add_u32 s54, s52, 1024\n" \
    "buffer_load_dwordx4 v[132:135], v253, s[36:39], s54 offen sc1\n" \
    "s_add_u32 s54, s52, 2048\n" \
    "buffer_load_dwordx4 v[136:139], v253, s[36:39], s54 offen sc1\n" \
    "s_add_u32 s54, s52, 3072\n" \
    "buffer_load_dwordx4 v[140:143], v253, s[36:39], s54 offen sc1\n" \
    "s_add_u32 s54, s52, 4096\n" \
    "buffer_load_dwordx4 v[144:147], v253, s[36:39], s54 offen sc1\n" \
    "s_add_u32 s54, s52, 5120\n" \
    "buffer_load_dwordx4 v[148:151], v253, s[36:39], s54 offen sc1\n" \
    "s_add_u32 s54, s52, 6144\n" \
    "buffer_load_dwordx4 v[152:155], v253, s[36:39], s54 offen sc1\n" \
    "s_add_u32 s54, s52, 7168\n" \
    "buffer_load_dwordx4 v[156:159], v253, s[36:39], s54 offen sc1\n" \
    "s_add_u32 s54, s52, 8192\n" \
    "buffer_load_dwordx4 v[160:163], v253, s[36:39], s54 offen sc1\n" \
    "s_add_u32 s54, s52, 9216\n" \
    "buffer_load_dwordx4 v[164:167], v253, s[36:39], s54 offen sc1\n" \
    "s_add_u32 s54, s52, 10240\n" \
    "buffer_load_dwordx4 v[168:171], v253, s[36:39], s54 offen sc1\n" \
    "s_add_u32 s54, s52, 11264\n" \
    "buffer_load_dwordx4 v[172:175], v253, s[36:39], s54 offen sc1\n" \
    "s_add_u32 s54, s52, 12288\n" \
    "buffer_load_dwordx4 v[176:179], v253, s[36:39], s54 offen sc1\n" \
    "s_add_u32 s54, s52, 13312\n" \
    "buffer_load_dwordx4 v[180:183], v253, s[36:39], s54 offen sc1\n" \
    "OBa%=:\n" \
    "buffer_load_dword v239, v246, s[36:39], s53 offen sc1\n" \
    "s_waitcnt vmcnt(0)\n" \
    "v_cmp_gt_u32 vcc, v245, v239\n" \
    "s_cbranch_vccz OBb%=\n" \
    "s_sleep 4\n" \
    "s_add_u32 s56, s56, 1\n" \
    "s_cmp_lt_u32 s56, 262144\n" \
    "s_cbranch_scc1 OBa%=\n" \
    "OBb%=:\n" \
    "s_add_u32 s54, s50, 0\n" \
    "buffer_load_dwordx4 v[184:187], v253, s[36:39], s54 offen sc1\n" \
    "s_add_u32 s54, s50, 1024\n" \
    "buffer_load_dwordx4 v[188:191], v253, s[36:39], s54 offen sc1\n" \
    "s_add_u32 s54, s50, 2048\n" \
    "buffer_load_dwordx4 v[192:195], v253, s[36:39], s54 offen sc1\n" \
    "s_add_u32 s54, s50, 3072\n" \
    "buffer_load_dwordx4 v[196:199], v253, s[36:39], s54 offen sc1\n" \
    "s_add_u32 s54, s50, 4096\n" \
    "buffer_load_dwordx4 v[200:203], v253, s[36:39], s54 offen sc1\n" \
    "s_add_u32 s54, s50, 5120\n" \
    "buffer_load_dwordx4 v[204:207], v253, s[36:39], s54 offen sc1\n" \
    "s_add_u32 s54, s50, 6144\n" \
    "buffer_load_dwordx4 v[208:211], v253, s[36:39], s54 offen sc1\n" \
    "s_add_u32 s54, s50, 7168\n" \
    "buffer_load_dwordx4 v[212:215], v253, s[36:39], s54 offen sc1\n" \
    "s_add_u32 s54, s50, 8192\n" \
    "buffer_load_dwordx4 v[216:219], v253, s[36:39], s54 offen sc1\n" \
    "s_branch OK%=\n" \
    "OJ%=:\n" \
    "v_mov_b32 v239, v238\n" \
    "v_mov_b32 v242, 0\n" \
    "s_add_u32 s54, s52, 0\n" \
    "buffer_load_dwordx4 v[128:131], v253, s[36:39], s54 offen sc1\n" \
    "s_add_u32 s54, s52, 1024\n" \
    "buffer_load_dwordx4 v[132:135], v253, s[36:39], s54 offen sc1\n" \
    "s_add_u32 s54, s52, 2048\n" \
    "buffer_load_dwordx4 v[136:139], v253, s[36:39], s54 offen sc1\n" \
    "s_add_u32 s54, s52, 3072\n" \
    "buffer_load_dwordx4 v[140:143], v253, s[36:39], s54 offen sc1\n" \
    "s_add_u32 s54, s52, 4096\n" \
    "buffer_load_dwordx4 v[144:147], v253, s[36:39], s54 offen sc1\n" \
    "s_add_u32 s54, s52, 5120\n" \
    "buffer_load_dwordx4 v[148:151], v253, s[36:39], s54 offen sc1\n" \
    "s_add_u32 s54, s52, 6144\n" \
    "buffer_load_dwordx4 v[152:155], v253, s[36:39], s54 offen sc1\n" \
    "s_add_u32 s54, s52, 7168\n" \
    "buffer_load_dwordx4 v[156:159], v253, s[36:39], s54 offen sc1\n" \
    "s_add_u32 s54, s52, 8192\n" \
    "buffer_load_dwordx4 v[160:163], v253, s[36:39], s54 offen sc1\n" \
    "s_add_u32 s54, s52, 9216\n" \
    "buffer_load_dwordx4 v[164:167], v253, s[36:39], s54 offen sc1\n" \
    "s_add_u32 s54, s52, 10240\n" \
    "buffer_load_dwordx4 v[168:171], v253, s[36:39], s54 offen sc1\n" \
    "s_add_u32 s54, s52, 11264\n" \
    "buffer_load_dwordx4 v[172:175], v253, s[36:39], s54 offen sc1\n" \
    "s_add_u32 s54, s52, 12288\n" \
    "buffer_load_dwordx4 v[176:179], v253, s[36:39], s54 offen sc1\n" \
    "s_add_u32 s54, s52, 13312\n" \
    "buffer_load_dwordx4 v[180:183], v253, s[36:39], s54 offen sc1\n" \
    "s_add_u32 s54, s50, 0\n" \
    "buffer_load_dwordx4 v[184:187], v253, s[36:39], s54 offen sc1\n" \
    "s_add_u32 s54, s50, 1024\n" \
    "buffer_load_dwordx4 v[188:191], v253, s[36:39], s54 offen sc1\n" \
    "s_add_u32 s54, s50, 2048\n" \
    "buffer_load_dwordx4 v[192:195], v253, s[36:39], s54 offen sc1\n" \
    "s_add_u32 s54, s50, 3072\n" \
    "buffer_load_dwordx4 v[196:199], v253, s[36:39], s54 offen sc1\n" \
    "s_add_u32 s54, s50, 4096\n" \
    "buffer_load_dwordx4 v[200:203], v253, s[36:39], s54 offen sc1\n" \
    "s_add_u32 s54, s50, 5120\n" \
    "buffer_load_dwordx4 v[204:207], v253, s[36:39], s54 offen sc1\n" \
    "s_add_u32 s54, s50, 6144\n" \
    "buffer_load_dwordx4 v[208:211], v253, s[36:39], s54 offen sc1\n" \
    "s_add_u32 s54, s50, 7168\n" \
    "buffer_load_dwordx4 v[212:215], v253, s[36:39], s54 offen sc1\n" \
    "s_add_u32 s54, s50, 8192\n" \
    "buffer_load_dwordx4 v[216:219], v253, s[36:39], s54 offen sc1\n" \
    "OK%=:\n" \
    "s_cmp_lg_u32 s44, 31\n" \
    "s_cbranch_scc1 OL%=\n" \
    "s_mov_b32 s26, 0\n" \
    "s_mov_b32 s24, 0\n" \
    "OV%=:\n" \
    "buffer_load_dwordx2 v[236:237], v250, s[40:43], s58 offen\n" \
    "buffer_load_dword v239, v246, s[36:39], s53 offen sc1\n" \
    "s_waitcnt vmcnt(24)\n" \
    "v_mfma_f32_16x16x32_bf16 v[112:115], a[0:3], v[128:131], 0\n" \
    "v_max3_u32 v238, v128, v129, v130\n" \
    "v_mfma_f32_16x16x32_bf16 v[116:119], a[4:7], v[128:131], 0\n" \
    "v_max3_u32 v238, v130, v131, v238\n" \
    "v_mfma_f32_16x16x32_bf16 v[120:123], a[8:11], v[128:131], 0\n" \
    "v_mfma_f32_16x16x32_bf16 v[124:127], a[12:15], v[128:131], 0\n" \
    "s_waitcnt vmcnt(23)\n" \
    "v_mfma_f32_16x16x32_bf16 v[112:115], a[16:19], v[132:135], v[112:115]\n" \
    "v_max3_u32 v238, v132, v133, v238\n" \
    "v_mfma_f32_16x16x32_bf16 v[116:119], a[20:23], v[132:135], v[116:119]\n" \
    "v_max3_u32 v238, v134, v135, v238\n" \
    "v_mfma_f32_16x16x32_bf16 v[120:123], a[24:27], v[132:135], v[120:123]\n" \
    "v_mfma_f32_16x16x32_bf16 v[124:127], a[28:31], v[132:135], v[124:127]\n" \
    "s_waitcnt vmcnt(22)\n" \
    "v_mfma_f32_16x16x32_bf16 v[112:115], a[32:35], v[136:139], v[112:115]\n" \
    "v_max3_u32 v238, v136, v137, v238\n" \
    "v_mfma_f32_16x16x32_bf16 v[116:119], a[36:39], v[136:139], v[116:119]\n" \
    "v_max3_u32 v238, v138, v139, v238\n" \
    "v_mfma_f32_16x16x32_bf16 v[120:123], a[40:43], v[136:139], v[120:123]\n" \
    "v_mfma_f32_16x16x32_bf16 v[124:127], a[44:47], v[136:139], v[124:127]\n" \
    "s_waitcnt vmcnt(21)\n" \
    "v_mfma_f32_16x16x32_bf16 v[112:115], a[48:51], v[140:143], v[112:115]\n" \
    "v_max3_u32 v238, v140, v141, v238\n" \
    "v_mfma_f32_16x16x32_bf16 v[116:119], a[52:55], v[140:143], v[116:119]\n" \
    "v_max3_u32 v238, v142, v143, v238\n" \
    "v_mfma_f32_16x16x32_bf16 v[120:123], a[56:59], v[140:143], v[120:123]\n" \
    "v_mfma_f32_16x16x32_bf16 v[124:127], a[60:63], v[140:143], v[124:127]\n" \
    "s_waitcnt vmcnt(20)\n" \
    "v_mfma_f32_16x16x32_bf16 v[112:115], a[64:67], v[144:147], v[112:115]\n" \
    "v_max3_u32 v238, v144, v145, v238\n" \
    "v_mfma_f32_16x16x32_bf16 v[116:119], a[68:71], v[144:147], v[116:119]\n" \
    "v_max3_u32 v238, v146, v147, v238\n" \
    "v_mfma_f32_16x16x32_bf16 v[120:123], a[72:75], v[144:147], v[120:123]\n" \
    "v_mfma_f32_16x16x32_bf16 v[124:127], a[76:79], v[144:147], v[124:127]\n" \
    "s_waitcnt vmcnt(19)\n" \
    "v_mfma_f32_16x16x32_bf16 v[112:115], a[80:83], v[148:151], v[112:115]\n" \
    "v_max3_u32 v238, v148, v149, v238\n" \
    "v_mfma_f32_16x16x32_bf16 v[116:119], a[84:87], v[148:151], v[116:119]\n" \
    "v_max3_u32 v238, v150, v151, v238\n" \
    "v_mfma_f32_16x16x32_bf16 v[120:123], a[88:91], v[148:151], v[120:123]\n" \
    "v_mfma_f32_16x16x32_bf16 v[124:127], a[92:95], v[148:151], v[124:127]\n" \
    "s_waitcnt vmcnt(18)\n" \
    "v_mfma_f32_16x16x32_bf16 v[112:115], a[96:99], v[152:155], v[112:115]\n" \
    "v_max3_u32 v238, v152, v153, v238\n" \
    "v_mfma_f32_16x16x32_bf16 v[116:119], a[100:103], v[152:155], v[116:119]\n" \
    "v_max3_u32 v238, v154, v155, v238\n" \
    "v_mfma_f32_16x16x32_bf16 v[120:123], a[104:107], v[152:155], v[120:123]\n" \
    "v_mfma_f32_16x16x32_bf16 v[124:127], a[108:111], v[152:155], v[124:127]\n" \
    "s_waitcnt vmcnt(17)\n" \
    "v_mfma_f32_16x16x32_bf16 v[112:115], a[112:115], v[156:159], v[112:115]\n" \
    "v_max3_u32 v238, v156, v157, v238\n" \
    "v_mfma_f32_16x16x32_bf16 v[116:119], a[116:119], v[156:159], v[116:119]\n" \
    "v_max3_u32 v238, v158, v159, v238\n" \
    "v_mfma_f32_16x16x32_bf16 v[120:123], a[120:123], v[156:159], v[120:123]\n" \
    "v_mfma_f32_16x16x32_bf16 v[124:127], a[124:127], v[156:159], v[124:127]\n" \
    "s_waitcnt vmcnt(16)\n" \
    "v_mfma_f32_16x16x32_bf16 v[112:115], a[128:131], v[160:163], v[112:115]\n" \
    "v_max3_u32 v238, v160, v161, v238\n" \
    "v_mfma_f32_16x16x32_bf16 v[116:119], a[132:135], v[160:163], v[116:119]\n" \
    "v_max3_u32 v238, v162, v163, v238\n" \
    "v_mfma_f32_16x16x32_bf16 v[120:123], a[136:139], v[160:163], v[120:123]\n" \
    "v_mfma_f32_16x16x32_bf16 v[124:127], a[140:143], v[160:163], v[124:127]\n" \
    "s_waitcnt vmcnt(15)\n" \
    "v_mfma_f32_16x16x32_bf16 v[112:115], a[144:147], v[164:167], v[112:115]\n" \
    "v_max3_u32 v238, v164, v165, v238\n" \
    "v_mfma_f32_16x16x32_bf16 v[116:119], a[148:151], v[164:167], v[116:119]\n" \
    "v_max3_u32 v238, v166, v167, v238\n" \
    "v_mfma_f32_16x16x32_bf16 v[120:123], a[152:155], v[164:167], v[120:123]\n" \
    "v_mfma_f32_16x16x32_bf16 v[124:127], a[156:159], v[164:167], v[124:127]\n" \
    "s_waitcnt vmcnt(14)\n" \
    "v_mfma_f32_16x16x32_bf16 v[112:115], a[160:163], v[168:171], v[112:115]\n" \
    "v_max3_u32 v238, v168, v169, v238\n" \
    "v_mfma_f32_16x16x32_bf16 v[116:119], a[164:167], v[168:171], v[116:119]\n" \
    "v_max3_u32 v238, v170, v171, v238\n" \
    "v_mfma_f32_16x16x32_bf16 v[120:123], a[168:171], v[168:171], v[120:123]\n" \
    "v_mfma_f32_16x16x32_bf16 v[124:127], a[172:175], v[168:171], v[124:127]\n" \
    "s_waitcnt vmcnt(13)\n" \
    "v_mfma_f32_16x16x32_bf16 v[112:115], a[176:179], v[172:175], v[112:115]\n" \
    "v_max3_u32 v238, v172, v173, v238\n" \
    "v_mfma_f32_16x16x32_bf16 v[116:119], a[180:183], v[172:175], v[116:119]\n" \
    "v_max3_u32 v238, v174, v175, v238\n" \
    "v_mfma_f32_16x16x32_bf16 v[120:123], a[184:187], v[172:175], v[120:123]\n" \
    "v_mfma_f32_16x16x32_bf16 v[124:127], a[188:191], v[172:175], v[124:127]\n" \
    "s_waitcnt vmcnt(12)\n" \
    "v_mfma_f32_16x16x32_bf16 v[112:115], a[192:195], v[176:179], v[112:115]\n" \
    "v_max3_u32 v238, v176, v177, v238\n" \
    "v_mfma_f32_16x16x32_bf16 v[116:119], a[196:199], v[176:179], v[116:119]\n" \
    "v_max3_u32 v238, v178, v179, v238\n" \
    "v_mfma_f32_16x16x32_bf16 v[120:123], a[200:203], v[176:179], v[120:123]\n" \
    "v_mfma_f32_16x16x32_bf16 v[124:127], a[204:207], v[176:179], v[124:127]\n" \
    "s_waitcnt vmcnt(11)\n" \
    "v_mfma_f32_16x16x32_bf16 v[112:115], a[208:211], v[180:183], v[112:115]\n" \
    "v_max3_u32 v238, v180, v181, v238\n" \
    "v_mfma_f32_16x16x32_bf16 v[116:119], a[212:215], v[180:183], v[116:119]\n" \
    "v_max3_u32 v238, v182, v183, v238\n" \
    "v_mfma_f32_16x16x32_bf16 v[120:123], a[216:219], v[180:183], v[120:123]\n" \
    "v_mfma_f32_16x16x32_bf16 v[124:127], a[220:223], v[180:183], v[124:127]\n" \
    "s_waitcnt vmcnt(10)\n" \
    "v_mfma_f32_16x16x32_bf16 v[112:115], a[224:227], v[184:187], v[112:115]\n" \
    "v_max3_u32 v238, v184, v185, v238\n" \
    "v_mfma_f32_16x16x32_bf16 v[116:119], a[228:231], v[184:187], v[116:119]\n" \
    "v_max3_u32 v238, v186, v187, v238\n" \
    "v_mfma_f32_16x16x32_bf16 v[120:123], a[232:235], v[184:187], v[120:123]\n" \
    "v_mfma_f32_16x16x32_bf16 v[124:127], a[236:239], v[184:187], v[124:127]\n" \
    "s_waitcnt vmcnt(9)\n" \
    "v_mfma_f32_16x16x32_bf16 v[112:115], a[240:243], v[188:191], v[112:115]\n" \
    "v_max3_u32 v238, v188, v189, v238\n" \
    "v_mfma_f32_16x16x32_bf16 v[116:119], a[244:247], v[188:191], v[116:119]\n" \
    "v_max3_u32 v238, v190, v191, v238\n" \
    "v_mfma_f32_16x16x32_bf16 v[120:123], a[248:251], v[188:191], v[120:123]\n" \
    "v_mfma_f32_16x16x32_bf16 v[124:127], a[252:255], v[188:191], v[124:127]\n" \
    "s_waitcnt vmcnt(8)\n" \
    "v_mfma_f32_16x16x32_bf16 v[112:115], v[0:3], v[192:195], v[112:115]\n" \
    "v_max3_u32 v238, v192, v193, v238\n" \
    "v_mfma_f32_16x16x32_bf16 v[116:119], v[4:7], v[192:195], v[116:119]\n" \
    "v_max3_u32 v238, v194, v195, v238\n" \
    "v_mfma_f32_16x16x32_bf16 v[120:123], v[8:11], v[192:195], v[120:123]\n" \
    "v_mfma_f32_16x16x32_bf16 v[124:127], v[12:15], v[192:195], v[124:127]\n" \
    "buffer_load_dwordx4 v[220:223], v228, s[36:39], s21 offen sc1\n" \
    "s_waitcnt vmcnt(8)\n" \
    "v_mfma_f32_16x16x32_bf16 v[112:115], v[16:19], v[196:199], v[112:115]\n" \
    "v_max3_u32 v238, v196, v197, v238\n" \
    "v_mfma_f32_16x16x32_bf16 v[116:119], v[20:23], v[196:199], v[116:119]\n" \
    "v_max3_u32 v238, v198, v199, v238\n" \
    "v_mfma_f32_16x16x32_bf16 v[120:123], v[24:27], v[196:199], v[120:123]\n" \
    "v_mfma_f32_16x16x32_bf16 v[124:127], v[28:31], v[196:199], v[124:127]\n" \
    "s_waitcnt vmcnt(7)\n" \
    "v_mfma_f32_16x16x32_bf16 v[112:115], v[32:35], v[200:203], v[112:115]\n" \
    "v_max3_u32 v238, v200, v201, v238\n" \
    "v_mfma_f32_16x16x32_bf16 v[116:119], v[36:39], v[200:203], v[116:119]\n" \
    "v_max3_u32 v238, v202, v203, v238\n" \
    "v_mfma_f32_16x16x32_bf16 v[120:123], v[40:43], v[200:203], v[120:123]\n" \
    "v_mfma_f32_16x16x32_bf16 v[124:127], v[44:47], v[200:203], v[124:127]\n" \
    "s_waitcnt vmcnt(6)\n" \
    "v_mfma_f32_16x16x32_bf16 v[112:115], v[48:51], v[204:207], v[112:115]\n" \
    "v_max3_u32 v238, v204, v205, v238\n" \
    "v_mfma_f32_16x16x32_bf16 v[116:119], v[52:55], v[204:207], v[116:119]\n" \
    "v_max3_u32 v238, v206, v207, v238\n" \
    "v_mfma_f32_16x16x32_bf16 v[120:123], v[56:59], v[204:207], v[120:123]\n" \
    "v_mfma_f32_16x16x32_bf16 v[124:127], v[60:63], v[204:207], v[124:127]\n" \
    "s_waitcnt vmcnt(5)\n" \
    "v_mfma_f32_16x16x32_bf16 v[112:115], v[64:67], v[208:211], v[112:115]\n" \
    "v_max3_u32 v238, v208, v209, v238\n" \
    "v_mfma_f32_16x16x32_bf16 v[116:119], v[68:71], v[208:211], v[116:119]\n" \
    "v_max3_u32 v238, v210, v211, v238\n" \
    "v_mfma_f32_16x16x32_bf16 v[120:123], v[72:75], v[208:211], v[120:123]\n" \
    "v_mfma_f32_16x16x32_bf16 v[124:127], v[76:79], v[208:211], v[124:127]\n" \
    "s_waitcnt vmcnt(4)\n" \
    "v_mfma_f32_16x16x32_bf16 v[112:115], v[80:83], v[212:215], v[112:115]\n" \
    "v_max3_u32 v238, v212, v213, v238\n" \
    "v_mfma_f32_16x16x32_bf16 v[116:119], v[84:87], v[212:215], v[116:119]\n" \
    "v_max3_u32 v238, v214, v215, v238\n" \
    "v_mfma_f32_16x16x32_bf16 v[120:123], v[88:91], v[212:215], v[120:123]\n" \
    "v_mfma_f32_16x16x32_bf16 v[124:127], v[92:95], v[212:215], v[124:127]\n" \
    "s_waitcnt vmcnt(3)\n" \
    "v_mfma_f32_16x16x32_bf16 v[112:115], v[96:99], v[216:219], v[112:115]\n" \
    "v_max3_u32 v238, v216, v217, v238\n" \
    "v_mfma_f32_16x16x32_bf16 v[116:119], v[100:103], v[216:219], v[116:119]\n" \
    "v_max3_u32 v238, v218, v219, v238\n" \
    "v_mfma_f32_16x16x32_bf16 v[120:123], v[104:107], v[216:219], v[120:123]\n" \
    "v_mfma_f32_16x16x32_bf16 v[124:127], v[108:111], v[216:219], v[124:127]\n" \
    "s_waitcnt vmcnt(1)\n" \
    "v_cmp_eq_u32 vcc, -1, v238\n" \
    "s_cbranch_vccz OW%=\n" \
    "s_cmp_eq_u32 s24, 1\n" \
    "s_cbranch_scc1 OW%=\n" \
    "v_cmp_ge_u32 vcc, v245, v239\n" \
    "s_cmp_eq_u64 vcc, 0\n" \
    "s_cselect_b32 s24, 1, 0\n" \
    "s_waitcnt vmcnt(0)\n" \
    "s_add_u32 s54, s52, 0\n" \
    "buffer_load_dwordx4 v[128:131], v253, s[36:39], s54 offen sc1\n" \
    "s_add_u32 s54, s52, 1024\n" \
    "buffer_load_dwordx4 v[132:135], v253, s[36:39], s54 offen sc1\n" \
    "s_add_u32 s54, s52, 2048\n" \
    "buffer_load_dwordx4 v[136:139], v253, s[36:39], s54 offen sc1\n" \
    "s_add_u32 s54, s52, 3072\n" \
    "buffer_load_dwordx4 v[140:143], v253, s[36:39], s54 offen sc1\n" \
    "s_add_u32 s54, s52, 4096\n" \
    "buffer_load_dwordx4 v[144:147], v253, s[36:39], s54 offen sc1\n" \
    "s_add_u32 s54, s52, 5120\n" \
    "buffer_load_dwordx4 v[148:151], v253, s[36:39], s54 offen sc1\n" \
    "s_add_u32 s54, s52, 6144\n" \
    "buffer_load_dwordx4 v[152:155], v253, s[36:39], s54 offen sc1\n" \
    "s_add_u32 s54, s52, 7168\n" \
    "buffer_load_dwordx4 v[156:159], v253, s[36:39], s54 offen sc1\n" \
    "s_add_u32 s54, s52, 8192\n" \
    "buffer_load_dwordx4 v[160:163], v253, s[36:39], s54 offen sc1\n" \
    "s_add_u32 s54, s52, 9216\n" \
    "buffer_load_dwordx4 v[164:167], v253, s[36:39], s54 offen sc1\n" \
    "s_add_u32 s54, s52, 10240\n" \
    "buffer_load_dwordx4 v[168:171], v253, s[36:39], s54 offen sc1\n" \
    "s_add_u32 s54, s52, 11264\n" \
    "buffer_load_dwordx4 v[172:175], v253, s[36:39], s54 offen sc1\n" \
    "s_add_u32 s54, s52, 12288\n" \
    "buffer_load_dwordx4 v[176:179], v253, s[36:39], s54 offen sc1\n" \
    "s_add_u32 s54, s52, 13312\n" \
    "buffer_load_dwordx4 v[180:183], v253, s[36:39], s54 offen sc1\n" \
    "s_add_u32 s54, s50, 0\n" \
    "buffer_load_dwordx4 v[184:187], v253, s[36:39], s54 offen sc1\n" \
    "s_add_u32 s54, s50, 1024\n" \
    "buffer_load_dwordx4 v[188:191], v253, s[36:39], s54 offen sc1\n" \
    "s_add_u32 s54, s50, 2048\n" \
    "buffer_load_dwordx4 v[192:195], v253, s[36:39], s54 offen sc1\n" \
    "s_add_u32 s54, s50, 3072\n" \
    "buffer_load_dwordx4 v[196:199], v253, s[36:39], s54 offen sc1\n" \
    "s_add_u32 s54, s50, 4096\n" \
    "buffer_load_dwordx4 v[200:203], v253, s[36:39], s54 offen sc1\n" \
    "s_add_u32 s54, s50, 5120\n" \
    "buffer_load_dwordx4 v[204:207], v253, s[36:39], s54 offen sc1\n" \
    "s_add_u32 s54, s50, 6144\n" \
    "buffer_load_dwordx4 v[208:211], v253, s[36:39], s54 offen sc1\n" \
    "s_add_u32 s54, s50, 7168\n" \
    "buffer_load_dwordx4 v[212:215], v253, s[36:39], s54 offen sc1\n" \
    "s_add_u32 s54, s50, 8192\n" \
    "buffer_load_dwordx4 v[216:219], v253, s[36:39], s54 offen sc1\n" \
    "s_add_u32 s56, s56, 1\n" \
    "s_cmp_lt_u32 s56, 262144\n" \
    "s_cbranch_scc1 OV%=\n" \
    "OW%=:\n" \
    "v_mov_b32 v242, 0\n" \
    "s_cmp_eq_u32 s25, 0\n" \
    "s_cbranch_scc1 OZ3%=\n" \
    "v_mov_b32 v224, s44\n" \
    "buffer_store_dword v224, v255, s[36:39], s22 offen sc1\n" \
    "s_mov_b32 s25, 0\n" \
    "OZ3%=:\n" \
    "s_nop 3\n" \
    "ds_write_b128 v248, v[112:115]\n" \
    "ds_write_b128 v248, v[116:119] offset:1024\n" \
    "ds_write_b128 v248, v[120:123] offset:2048\n" \
    "ds_write_b128 v248, v[124:127] offset:3072\n" \
    "v_lshlrev_b32 v224, 16, v236\n" \
    "v_and_b32 v225, 0xffff0000, v236\n" \
    "v_lshlrev_b32 v226, 16, v237\n" \
    "v_and_b32 v227, 0xffff0000, v237\n" \
    "s_waitcnt lgkmcnt(0)\n" \
    "s_barrier\n" \
    "ds_read_b128 v[112:115], v249\n" \
    "ds_read_b128 v[116:119], v249 offset:4096\n" \
    "ds_read_b128 v[120:123], v249 offset:8192\n" \
    "ds_read_b128 v[124:127], v249 offset:12288\n" \
    "v_xor_b32 v248, 16384, v248\n" \
    "v_xor_b32 v249, 16384, v249\n" \
    "s_waitcnt lgkmcnt(0)\n" \
    "v_pk_add_f32 v[112:113], v[112:113], v[116:117]\n" \
    "v_pk_add_f32 v[120:121], v[120:121], v[124:125]\n" \
    "v_pk_add_f32 v[114:115], v[114:115], v[118:119]\n" \
    "v_pk_add_f32 v[122:123], v[122:123], v[126:127]\n" \
    "v_pk_add_f32 v[112:113], v[112:113], v[120:121]\n" \
    "v_pk_add_f32 v[114:115], v[114:115], v[122:123]\n" \
    "s_cmp_eq_u32 s20, 0\n" \
    "s_cbranch_scc1 OZ0%=\n" \
    "v_pk_add_f32 v[224:225], v[224:225], v[112:113]\n" \
    "v_pk_add_f32 v[226:227], v[226:227], v[114:115]\n" \
    "s_nop 0\n" \
    "buffer_store_dwordx4 v[224:227], v253, s[36:39], s21 offen sc1\n" \
    "s_waitcnt vmcnt(0)\n" \
    "buffer_store_dword v245, v255, s[36:39], s22 offen sc1\n" \
    "s_branch OZ1%=\n" \
    "OZ0%=:\n" \
    "s_waitcnt vmcnt(0)\n" \
    "s_cmp_eq_u32 s26, 1\n" \
    "s_cbranch_scc1 OZ2%=\n" \
    "OZ4%=:\n" \
    "v_max3_u32 v224, v220, v221, v222\n" \
    "v_max_u32 v224, v224, v223\n" \
    "v_cmp_eq_u32 vcc, -1, v224\n" \
    "s_cbranch_vccz OZ2%=\n" \
    "v_cmp_gt_u32 vcc, v245, v242\n" \
    "s_cmp_eq_u64 vcc, 0\n" \
    "s_cselect_b32 s26, 1, 0\n" \
    "buffer_load_dwordx4 v[220:223], v253, s[36:39], s21 offen sc1\n" \
    "buffer_load_dword v242, v244, s[36:39], s22 offen sc1\n" \
    "s_waitcnt vmcnt(0)\n" \
    "s_cmp_eq_u32 s26, 1\n" \
    "s_cbranch_scc1 OZ2%=\n" \
    "s_add_u32 s56, s56, 1\n" \
    "s_cmp_lt_u32 s56, 262144\n" \
    "s_cbranch_scc1 OZ4%=\n" \
    "OZ2%=:\n" \
    "v_pk_add_f32 v[112:113], v[112:113], v[220:221]\n" \
    "v_pk_add_f32 v[114:115], v[114:115], v[222:223]\n" \
    "v_cvt_pk_bf16_f32 v224, v112, v113\n" \
    "v_cvt_pk_bf16_f32 v225, v114, v115\n" \
    "buffer_store_dwordx2 v[224:225], v250, s[76:79], s58 offen\n" \
    "OZ1%=:\n" \
    "s_add_u32 s58, s58, 65536\n" \
    "s_add_u32 s21, s21, 131072\n" \
    "v_add_u32 v245, 1, v245\n" \
    "s_add_u32 s44, s44, 1\n" \
    "OX%=:\n" \
    "s_mov_b32 s54, 2162688\n" \
    "buffer_store_dwordx2 v[240:241], v254, s[36:39], s54 offen sc1\n" \
    "s_waitcnt vmcnt(0)\n" \
    "s_endpgm\n" \
    "RE_fo:\n"

#define ENTRY_PF \
    "s_nop 0\n"

typedef unsigned v16u __attribute__((ext_vector_type(16)));
#define CHAIN_CLOB "memory", "vcc", "scc", "a255", "s99"
#define WORK_CLOB CHAIN_CLOB

template <class V> DEVI void pin(V& v) {
    for (int i = 0; i < (int)(sizeof(V) / 4); ++i) { unsigned e = __builtin_amdgcn_readfirstlane(v[i]); asm("" : "+s"(e)); v[i] = e; }
}
DEVI unsigned pcls(unsigned x) { return ((0x5140u >> (4 * x)) & 7u) << 12; }

DEVI void chain_role(const Params& p, const int c, const int hx, const int w, const int lane) {
    const int h = c >> 1, a = c & 1;
    const unsigned g = lane >> 4, q4 = (lane & 15) >> 2, p4 = lane & 3, n = lane & 15;
    const unsigned hashl = (q4 & 1) | ((g & 1) << 1) | ((q4 >> 1) << 2);
    const unsigned rowl = 16 * (g >> 1) + 4 * (g & 1) + (q4 & 1) + 8 * (q4 >> 1);
    const unsigned hashw = (q4 & 1) | ((q4 >> 1) << 1) | ((g & 1) << 2);
    const unsigned roww = 16 * (g & 1) + 8 * (g >> 1) + 4 * (q4 >> 1) + (q4 & 1);
    const unsigned OOB = 0x80000000u;
    const unsigned l16 = lane * 16;
    v8u pv;
    pv[0] = (4 * w + g) * 8192u + h * 256u + ((n ^ (((g & 1) | (w << 1)) << 1)) * 16u);
    pv[1] = (rowl * 256 + p4 * 8 + (((2 * w) ^ hashl) << 5)) | ((w & 1) << 15);
    pv[2] = (rowl * 256 + p4 * 8 + (((2 * w + 1) ^ hashl) << 5)) | ((w & 1) << 15);
    pv[3] = (h * 128 + 2 * w + (lane >> 5)) * 1024u + a * 512u + (((lane & 31) ^ (((lane >> 5) | ((w >> 1) << 1)) << 1)) * 16u);
    pv[4] = 32768 + roww * 512 + p4 * 8 + (((4 * w) ^ hashw) << 5);
    pv[5] = 98304 + w * 1024 + p4 * 16384 + (g * 16 + q4) * 16;
    pv[6] = 0; pv[7] = 0;
    const v4u cv = {0x3F803F80u, 0x3F803F80u, 0x3F803F80u, 0x3F803F80u};
    v16u qv;
    qv[0] = 0; qv[1] = 0; qv[2] = 0; qv[3] = 0;
    qv[4] = 1;
    qv[5] = lane < 32 ? (w >> 1) * 32768 + (pcls(hx) ^ 0x4000) + lane * 128 : (unsigned)WS_CTRL + CT_KVF + h * 256 + 8 - 0x80000;
    qv[6] = w == 3 ? (lane < 4 ? a * 32768 + (pcls(lane) ^ 0x4000) + h * 128 : OOB) : (lane == 0 ? 0 : OOB);
    qv[7] = 98304 + w * 1024 + l16;
    qv[8] = g * 256 + (n >> 3) * 128 + ((n >> 1) & 1) * 64 + ((n >> 2) & 1) * 32 + (n & 1) * 16;
    qv[9] = 28672 + ((n & 2) ? 0 : 2048) + (w >> 1) * 1024 + ((2 * (w & 1) + (g >> 1)) * 16 + 4 * q4 + 2 * (n & 1) + ((g ^ (n >> 1) ^ 1) & 1)) * 16;
    qv[10] = 24576 + (4 * g + q4) * 32 + (p4 ^ g) * 8;
    qv[11] = 24576 + w * 1024 + n * 32 + (g ^ q4) * 8;
    qv[12] = (2 * q4 + (g >> 1)) * 32768 + (g & 1) * 64 + p4 * 16;
    qv[13] = l16;
    qv[14] = (lane & 1) ? 0xEEEE : 0x4444;
    qv[15] = w * 2048 + l16;
    const bf16* mp = p.mem0 + (n >> 1) * DM + 128 * w + 16 * g + 8 * (n & 1);
    const v4u rw = {(unsigned)(uintptr_t)p.ws, (unsigned)((uintptr_t)p.ws >> 32) & 0xffffu, 128u << 20, 0x00020000u};
    v16u sv;
    sv[0] = 0;
    sv[1] = __builtin_amdgcn_readfirstlane((1u << 20) + (w & 1) * 262144 + (pcls((2 * a + (w >> 1)) ^ 2) ^ 0x1000) + h * 128);
    sv[2] = __builtin_amdgcn_readfirstlane((w >> 1) * 32768 + pcls(hx) + (w & 1) * 2048);
    sv[3] = 0;
    sv[4] = 0;
    sv[5] = (unsigned)WS_KV + h * 64 * 16384;
    sv[6] = (unsigned)WS_KM + h * 64 * 8192 + (a ? 4096 + w * 512 : w * 1024);
    sv[7] = w * 1024;
    sv[8] = 0;
    sv[9] = __float_as_uint(0.08838834764831845f * 1.4426950408889634f);
    sv[10] = 0; sv[11] = 0; sv[12] = 0; sv[13] = 0; sv[14] = a; sv[15] = 0x80000;
    v8u wp = {(unsigned)(uintptr_t)p.Wq, (unsigned)((uintptr_t)p.Wq >> 32), (unsigned)(uintptr_t)p.Wk, (unsigned)((uintptr_t)p.Wk >> 32),
              (unsigned)(uintptr_t)p.Wv, (unsigned)((uintptr_t)p.Wv >> 32), (unsigned)(uintptr_t)p.Wo, (unsigned)((uintptr_t)p.Wo >> 32)};
    const unsigned dbg = (unsigned)WS_DBG + (c * 8 + w) * 64;
    const unsigned kmf = (unsigned)WS_CTRL + CT_KMF + c * 128, kvf = (unsigned)WS_CTRL + CT_KVF + h * 256;
    pin(sv);
#define CHAIN_IN "{v[232:233]}"(mp), "{v[216:223]}"(pv), "{v[236:239]}"(cv), "{v[240:255]}"(qv), "{s[36:39]}"(rw), "{s[44:59]}"(sv), "{s[60:67]}"(wp), "{s35}"(kmf), "{s68}"(kvf), "{s69}"(dbg)
    if (w == 0) asm volatile(CHAIN_W0 :: CHAIN_IN : CHAIN_CLOB);
    else if (w == 1) asm volatile(CHAIN_W1 :: CHAIN_IN : CHAIN_CLOB);
    else if (w == 2) asm volatile(CHAIN_W2 :: CHAIN_IN : CHAIN_CLOB);
    else asm volatile(CHAIN_W3 :: CHAIN_IN : CHAIN_CLOB);
    __builtin_unreachable();
}

#define LO(x) ((unsigned)(uintptr_t)(x))
#define HI(x) ((unsigned)((uintptr_t)(x) >> 32))
DEVI void red_role(const Params& p, const int x, const int r, const int w, const int lane) {
    const int u = (x ^ 2) * 16 + ((r * CHAIN_UPC + w) & 15), h = x * 8 + (r >> 1), sg = r & 1;
    const unsigned g = lane >> 4, q4 = (lane & 15) >> 2, p4 = lane & 3, n = lane & 15, OOB = 0x80000000u;
    const unsigned hash = q4 | ((g & 1) << 2);
    v4u pv;
    pv[0] = (4 * w + g) * 8192u + h * 256u + ((n ^ ((g | ((w >> 1) << 2)) << 1)) * 16u);
    pv[1] = (8 * g + q4) * 256 + p4 * 8 + (((2 * w) ^ hash) << 5);
    pv[2] = (8 * g + q4) * 256 + p4 * 8 + (((2 * w + 1) ^ hash) << 5);
    pv[3] = 0;
    v4u cv;
    const unsigned j = 2 * (n & 3) + ((n >> 2) & 1), one = (j & 1) ? 0x3F800000u : 0x3F80u;
    cv[0] = (j >> 1) == 0 ? one : 0u; cv[1] = (j >> 1) == 1 ? one : 0u; cv[2] = (j >> 1) == 2 ? one : 0u; cv[3] = (j >> 1) == 3 ? one : 0u;
    v16u qv;
    for (int i = 0; i < 16; ++i) qv[i] = 0;
    qv[3] = (lane >> 5) * 65536 + pcls((lane >> 3) & 3) + (lane & 7) * 16;
    qv[4] = pcls((n >> 3) + 2 * (g >> 1)) + (g & 1) * 64 + (n & 7) * 8;
    qv[5] = lane < 32 ? (u >> 5) * 32768 + (pcls(x) ^ 0x4000) + lane * 128 : OOB;
    qv[12] = (w == 0 && lane == 0) ? 0 : OOB;
    qv[13] = lane * 16;
    const v4u rw = {LO(p.ws), HI(p.ws) & 0xffffu, 128u << 20, 0x00020000u};
    v16u sv;
    for (int i = 0; i < 16; ++i) sv[i] = 0;
    sv[1] = __builtin_amdgcn_readfirstlane((1u << 20) + (u & 15) * 32768 + (pcls(x) ^ 0x1000));
    sv[2] = (u >> 5) * 32768 + (u & 31) * 128;
    sv[3] = (unsigned)WS_XN + sg * 32768 + w * 8192;
    sv[4] = w * 8192;
    sv[5] = (unsigned)WS_KV + (h * 64 + sg) * 16384;
    sv[6] = (unsigned)WS_CTRL + CT_KVF + (h * 64 + sg) * 4;
    sv[7] = w * 1024;
    sv[8] = ((unsigned)(w - CHAIN_UPC) >> 31) & ((unsigned)(r - 16 / CHAIN_UPC) >> 31);
    sv[15] = 0x80000;
    const v8u wp = {0, 0, LO(p.Wk), HI(p.Wk), LO(p.Wv), HI(p.Wv), 0, 0};
    const unsigned dbg = (unsigned)WS_DBG + (u * 8 + 4) * 64;
    pin(sv);
    asm volatile(CHAIN_RED :: "{v[216:219]}"(pv), "{v[236:239]}"(cv), "{v[240:255]}"(qv), "{s[36:39]}"(rw), "{s[44:59]}"(sv), "{s[60:67]}"(wp), "{s69}"(dbg) : CHAIN_CLOB);
    __builtin_unreachable();
}

DEVI void h_role(const Params& p, const int y, const int rk, const int w, const int lane) {
    const int h = HMAP ? (y & 1) * 16 + rk : y * 8 + (rk >> 1), r = HMAP ? 1 - (y >> 1) : rk & 1;
    const unsigned g = lane >> 4, q4 = (lane & 15) >> 2, p4 = lane & 3, n = lane & 15, OOB = 0x80000000u, l16 = lane * 16;
    const unsigned hash = q4 | ((g & 1) << 2);
    v8u pv;
    pv[0] = (4 * w + g) * 8192u + h * 256u + ((n ^ ((g | ((w >> 1) << 2)) << 1)) * 16u);
    pv[1] = (8 * g + q4) * 256 + p4 * 8 + (((2 * w) ^ hash) << 5);
    pv[2] = (8 * g + q4) * 256 + p4 * 8 + (((2 * w + 1) ^ hash) << 5);
    const unsigned W = rk * 4 + w, hq = HMAP ? (y & 1) * 2 + (y >> 1) : y;
    pv[3] = (W * 4 + g) * 8192u + hq * 2048u + n * 128u;
    pv[4] = hq * 1048576u + W * 8192u + lane * 128u;
    pv[5] = 0; pv[6] = 0; pv[7] = 0;
    const v4u cv = {0x3F803F80u, 0x3F803F80u, 0x3F803F80u, 0x3F803F80u};
    const int qt = w >> 1, eh = w & 1;
    v16u qv;
    qv[0] = 0; qv[1] = 0;
    qv[2] = l16 + eh * 4096;
    qv[3] = (w == 0 && lane < 2) ? lane * 4096 : OOB;
    for (int k = 0; k < 4; ++k) {
        const unsigned diag = (4 * g + k <= n) ? 0u : 0xFF800000u;
        qv[4 + k] = qt == 0 ? diag : 0u;
        qv[8 + k] = qt == 0 ? 0xFF800000u : diag;
    }
    qv[12] = (w == 0 && lane == 0) ? 0 : OOB;
    qv[13] = l16;
    qv[14] = (lane & 1) * 128;
    qv[15] = l16 + qt * 4096;
    const v4u rw = {LO(p.ws), HI(p.ws) & 0xffffu, 128u << 20, 0x00020000u};
    v16u sv;
    sv[0] = 0;
    sv[1] = (unsigned)WS_XN + (r + 2 * E_N) * 32768 + w * 8192;
    sv[2] = w * 8192;
    sv[3] = (unsigned)WS_KV + (h * 64 + r + 2 * E_N) * 16384;
    sv[4] = (unsigned)WS_XN + r * 32768 + w * 8192;
    sv[5] = (unsigned)WS_KV + (h * 64 + r) * 16384 + w * 4096;
    sv[6] = (unsigned)WS_KM + (h * 64 + r) * 8192;
    sv[7] = w * 1024;
    sv[8] = r + 2 + KM_LATE;
    sv[9] = __float_as_uint(0.08838834764831845f * 1.4426950408889634f);
    sv[10] = 0; sv[11] = 0; sv[12] = 0;
    sv[13] = (unsigned)WS_CTRL + CT_KVF + (h * 64 + r + 2 * E_N) * 4;
    sv[14] = (unsigned)WS_CTRL + CT_KMF + h * 256;
    sv[15] = (unsigned)WS_O + (r * 2 + qt) * 131072 + (h * 4 + 2 * eh) * 1024;
    const v8u wp = {LO(p.Wq), HI(p.Wq), LO(p.Wk), HI(p.Wk), LO(p.Wv), HI(p.Wv), LO(p.Wo), HI(p.Wo)};
    const unsigned off = (unsigned)WS_CTRL + CT_OFL + r * 8192 + h * 128;
    pin(sv);
    asm volatile(ROLE_H :: "{v[216:223]}"(pv), "{v[236:239]}"(cv), "{v[240:255]}"(qv), "{s[36:39]}"(rw), "{s[44:59]}"(sv), "{s[60:67]}"(wp), "{s68}"(off), "{s69}"((unsigned)WS_DBG + 65536 + ((h * 2 + r) * 4 + w) * 32) : WORK_CLOB);
    __builtin_unreachable();
}

DEVI void fo_role(const Params& p, const int gam, const int cg, const int w, const int lane, const v2u gen) {
    const unsigned g = lane >> 4, q4 = (lane & 15) >> 2, p4 = lane & 3, n = lane & 15, OOB = 0x80000000u, l16 = lane * 16;
    const unsigned hash = q4 | ((g & 1) << 2);
    const int sl = cg, q = gam;
    v8u pv;
    const int h = gam * 8 + (cg >> 1), sg = 2 * E_N + (cg & 1);
    pv[4] = (4 * w + g) * 8192u + h * 256u + ((n ^ ((g | ((w >> 1) << 2)) << 1)) * 16u);
    pv[5] = 0; pv[6] = 0; pv[7] = 0;
    const v8u ev = {(unsigned)WS_XN + sg * 32768 + w * 8192, (unsigned)w * 8192, (unsigned)WS_KV + (h * 64 + sg) * 16384, (unsigned)WS_CTRL + CT_KVF + (h * 64 + sg) * 4, LO(p.Wk), HI(p.Wk), LO(p.Wv), HI(p.Wv)};
    pv[0] = (4 * w + g) * 3584u + sl * 256u + ((n ^ ((g | ((w >> 1) << 2)) << 1)) * 16u);
    pv[1] = (8 * g + q4) * 256 + p4 * 8 + (((2 * w) ^ hash) << 5);
    pv[2] = (8 * g + q4) * 256 + p4 * 8 + (((2 * w + 1) ^ hash) << 5);
    pv[3] = (((gam * 16 + cg) * 4 + w) * 64 + lane) * 128u;
    v16u qv;
    for (int k = 0; k < 16; ++k) qv[k] = 0;
    qv[0] = gen.x; qv[1] = gen.y;
    const int kh = cg & 1, c64 = cg >> 1, go = gam ^ kh;
    qv[4] = (lane >> 3) * 1024 + c64 * 128 + (((lane & 7) ^ (((lane >> 4) & 3) << 1)) * 16);
    qv[5] = w * 40960 + (4 * g + q4) * 128 + (((2 * g + (q4 >> 1)) & 3) << 5) + p4 * 8;
    const unsigned k0 = kh ? 92 + 9 * w : 23 * w, k1 = kh ? k0 + 8 : k0 + 22;
    qv[6] = min((k0 >> 2) + (lane & 7), k1 >> 2) * 128;
    qv[7] = kh ? ((lane & 1) * 32 + min((14 * w >> 2) + ((lane >> 1) & 7), (14 * w + 13) >> 2)) * 128 : qv[6];
    qv[8] = w * 4096 + l16;
    qv[9] = w * 1024 + l16;
    qv[10] = n * 1024 + (64 * c64 + 16 * w + 4 * g) * 2;
    qv[12] = (w == 0 && lane == 0) ? 0 : OOB;
    qv[13] = l16;
    qv[14] = (gam == 3 && cg == 0 && w == 0 && lane == 0) ? 0 : OOB;
    qv[15] = lane == 0 ? 0 : OOB;
    const v4u rw = {LO(p.ws), HI(p.ws) & 0xffffu, 128u << 20, 0x00020000u};
    v16u sv;
    sv[0] = 0;
    sv[1] = (unsigned)WS_XN + q * 32768 + w * 8192;
    sv[2] = w * 8192;
    sv[3] = (unsigned)WS_HM + (q * 2 * 56 + sl * 4 + w) * 1024;
    sv[4] = (unsigned)WS_CTRL + CT_HFL + (q * 16 + sl) * 128;
    sv[5] = (unsigned)(sl - 14) >> 31;
    sv[6] = (unsigned)WS_O + go * 131072 + (kh ? 92 + 9 * w : 23 * w + 14) * 1024;
    sv[7] = w * 1024;
    sv[8] = kh ? (unsigned)WS_HM + (go * 56 + w * 14) * 1024 : (unsigned)WS_O + go * 131072 + 23 * w * 1024;
    sv[9] = (unsigned)WS_CTRL + CT_OFL + gam * 4096;
    sv[10] = 0; sv[11] = 0; sv[12] = 0;
    sv[13] = (unsigned)WS_CTRL + (kh ? CT_HFL + (gam >> 1) * 2048 : CT_OFL + gam * 4096);
    sv[14] = go * 16384;
    sv[15] = 0;
    const v8u wp = {LO(p.Wg), HI(p.Wg), LO(p.Wu), HI(p.Wu), LO(p.Wo), HI(p.Wo), LO(p.Wd), HI(p.Wd)};
    const bf16* wa = kh ? p.Wd : p.Wo;
    const v8u ow = {LO(wa), HI(wa), kh ? 1792u * 1024u : 0x400000u, (unsigned)(kh ? 14 : 23) * w * 32768, LO(p.Wo), HI(p.Wo), 0x400000u, (unsigned)(kh ? 92 + 9 * w : 23 * w + 14) * 32768};
    const v8u ov = {(unsigned)kh, (unsigned)WS_PB + (go * 8 + c64) * 4096 + w * 1024, (unsigned)WS_CTRL + CT_PF + ((go * 8 + c64) * 4 + w) * 128, kh ? 4u * 56 * 1024 : 4u * 131072, kh ? 128u : 256u, 0, 0, 0};
    const v2u xp = {LO(p.x), HI(p.x)}, op = {LO(p.out), HI(p.out)};
    pin(sv);
    asm volatile(ROLE_FO :: "{s[4:11]}"(ev), "{v[216:223]}"(pv), "{v[240:255]}"(qv), "{s[36:39]}"(rw), "{s[44:59]}"(sv), "{s[60:67]}"(wp), "{s[12:19]}"(ow), "{s[20:27]}"(ov), "{s69}"((unsigned)WS_DBG + 65536 + 8192 + ((gam * 16 + cg) * 4 + w) * 32), "{s[70:71]}"(xp), "{s[72:73]}"(op) : WORK_CLOB);
    __builtin_unreachable();
}

#define DBGM(k)
DEVI unsigned store_time(char* a) {
    unsigned long long t1, t2;
    asm volatile("s_memtime %0\n\ts_waitcnt lgkmcnt(0)\n\tglobal_store_dword %2, %3, off sc1\n\ts_waitcnt vmcnt(0)\n\ts_memtime %1\n\ts_waitcnt lgkmcnt(0)" : "=&s"(t1), "=&s"(t2) : "v"(a), "v"(SENT) : "memory");
    return (unsigned)(t2 - t1);
}
DEVI v2u setup(const Params& p, const int i, const int t) {
    const __amdgpu_buffer_rsrc_t rs = __builtin_amdgcn_make_buffer_rsrc(p.ws, 0, 128 << 20, 0x00020000);
    const int w = t >> 6, l = t & 63;
    v4u a[8];
    const bf16* xr = p.x + (i * 32 + (w & 1) * 16 + (l & 15)) * DM + (w >> 1) * 256 + (l >> 4) * 8;
    for (int k = 0; k < 8; ++k) a[k] = *(const v4u*)(xr + k * 32);
    const v2u gen = __builtin_amdgcn_raw_buffer_load_b64(rs, 0, WS_SYNC, 16);
    if (t < 64) {
        const unsigned o = i * 1024 + t * 16;
        const v4u z = o == CT_KVF + 8192 ? (v4u){1, 1, 1, 1} : (v4u){0, 0, 0, 0};
        __builtin_amdgcn_raw_buffer_store_b128(z, rs, (unsigned)WS_CTRL + o, 0, 16);
    }
    float v[64];
    float s = 0;
    for (int k = 0; k < 8; ++k) for (int e = 0; e < 4; ++e) { v[k * 8 + 2 * e] = __uint_as_float(a[k][e] << 16); v[k * 8 + 2 * e + 1] = __uint_as_float(a[k][e] & 0xffff0000u); s += v[k * 8 + 2 * e] + v[k * 8 + 2 * e + 1]; }
    DBGM(0);
    const unsigned sa = ((w & 1) * 16 + (l & 15)) * 8;
    lwr32(sa + (w >> 1) * 4, __float_as_uint(xsum(s)));
    bar();
    const v2u s2 = lrd64(sa);
    const float mean = (__uint_as_float(s2.x) + __uint_as_float(s2.y)) * (1.f / DM);
    float q = 0;
    for (int e = 0; e < 64; ++e) { v[e] -= mean; q += v[e] * v[e]; }
    lwr32(256 + sa + (w >> 1) * 4, __float_as_uint(xsum(q)));
    bar();
    const v2u q2 = lrd64(256 + sa);
    const float rstd = rsqrtf((__uint_as_float(q2.x) + __uint_as_float(q2.y)) * (1.f / DM) + 1e-5f);
    for (int k = 0; k < 8; ++k) {
        v4u o;
        for (int e = 0; e < 4; ++e) o[e] = pk2(v[k * 8 + 2 * e] * rstd, v[k * 8 + 2 * e + 1] * rstd);
        __builtin_amdgcn_raw_buffer_store_b128(o, rs, (unsigned)WS_XN + i * 32768 + ((w & 1) * 16 + (w >> 1) * 8 + k) * 1024 + l * 16, 0, 16);
    }
    DBGM(1);
    asm volatile("s_waitcnt vmcnt(0)\n\ts_barrier" ::: "memory");
    if (t == 0) __builtin_amdgcn_raw_buffer_store_b64((v2u){gen.x + 1, gen.y + 8}, rs, 0, WS_SYNC + 1024 + i * 128, 16);
    DBGM(2);
    const v4u sent = {SENT, SENT, SENT, SENT};
    for (int k = 0; k < 8; ++k) __builtin_amdgcn_raw_buffer_store_b128(sent, rs, (unsigned)WS_COMM + i * 32768 + t * 16 + k * 4096, 0, 16);
    for (int k = 0; k < 2; ++k) {
        for (int m = 0; m < 3; ++m) if (k == 0 || t < 128) __builtin_amdgcn_raw_buffer_store_b128(sent, rs, (unsigned)WS_KM + ((i >> 1) * 64 + 58 + 2 * m + (i & 1)) * 8192 + t * 16 + k * 4096, 0, 16);
        __builtin_amdgcn_raw_buffer_store_b128(sent, rs, (unsigned)WS_O + 62 * 262144 + i * 8192 + t * 16 + k * 4096, 0, 16);
    }
    if (t < 128) __builtin_amdgcn_raw_buffer_store_b128(sent, rs, (unsigned)WS_PB + 124 * 32768 + i * 2048 + t * 16, 0, 16);
    asm volatile("s_waitcnt vmcnt(0)\n\ts_barrier" ::: "memory");
    if (t == 0) {
        char* base = p.ws + WS_COMM + 0xF0000 + (i >> 5) * 0x8000 + (i & 31) * 128;
        unsigned L[8];
        for (int k = 0; k < 8; ++k) L[k] = store_time(base + k * 4096);
        const bool b1 = min(L[2], L[3]) + min(L[6], L[7]) < min(L[0], L[1]) + min(L[4], L[5]);
        const unsigned n0 = b1 ? L[2] : L[0], n1 = b1 ? L[3] : L[1], n2 = b1 ? L[6] : L[4], n3 = b1 ? L[7] : L[5];
        const unsigned best = (n2 + n3 < n0 + n1 ? 4u : 0u) | (b1 ? 2u : 0u) | (n1 + n3 < n0 + n2 ? 1u : 0u);
        const unsigned p0 = best ^ 4u ^ 2u ^ (pcls(i & 3) >> 12) ^ PXOR;
        __builtin_amdgcn_raw_buffer_store_b64((v2u){gen.x + 1, ((gen.y + 8) & ~7u) | p0}, rs, 0, WS_SYNC + 16384 + i * 128, 16);
    }
    asm volatile("s_barrier" ::: "memory");
    return gen;
}

__global__ void __launch_bounds__(256, 1) main_kernel(Params p) {
    asm volatile(ENTRY_PF :: "v"(threadIdx.x * 128) : "s29", "s30", "s31", "v200", "a253", "a254", "a255");
    const int b = blockIdx.x, x = b & 7, r = b >> 3;
    const int w = __builtin_amdgcn_readfirstlane(threadIdx.x >> 6), lane = threadIdx.x & 63;
    if (x < 4) {
        if (r < 16) chain_role(p, x * 16 + r, x, w, lane);
        else red_role(p, x, r - 16, w, lane);
    } else if (r < 16) h_role(p, x - 4, r, w, lane);
    else { const v2u gen = setup(p, (r - 16) * 4 + (x - 4), threadIdx.x); fo_role(p, x - 4, r - 16, w, lane, gen); }
}

extern "C" void kernel_launch(void* const* d_in, const int* in_sizes, int n_in, void* d_out, int out_size, void* d_ws, size_t ws_size, hipStream_t stream) {
    Params p;
    p.x = (const bf16*)d_in[0]; p.mem0 = (const bf16*)d_in[1]; p.Wq = (const bf16*)d_in[2]; p.Wk = (const bf16*)d_in[3]; p.Wv = (const bf16*)d_in[4]; p.Wo = (const bf16*)d_in[5];
    p.Wg = (const bf16*)d_in[8]; p.Wu = (const bf16*)d_in[9]; p.Wd = (const bf16*)d_in[10];
    p.out = (bf16*)d_out;
    p.ws = (char*)(((uintptr_t)d_ws + (2u << 20) - 1) & ~(uintptr_t)((2u << 20) - 1));
    (void)hipFuncSetAttribute((const void*)main_kernel, hipFuncAttributeMaxDynamicSharedMemorySize, LDS_BYTES);
    main_kernel<<<256, 256, LDS_BYTES, stream>>>(p);
}
```
